```python
import torch

def _softmax(x, axis=-1):
    return torch.softmax(x, dim=axis)
def _gelu(x):
    return torch.nn.functional.gelu(x, approximate="tanh")
def _silu(x):
    return torch.nn.functional.silu(x)
def _sigmoid(x):
    return torch.sigmoid(x)
def _layernorm(x, axis=-1, eps=1e-5):
    m = x.mean(dim=axis, keepdim=True)
    v = x.var(dim=axis, keepdim=True, unbiased=False)
    return (x - m) / torch.sqrt(v + eps)
def _scatter_add(tgt, idx, src, axis=0):
    return tgt.clone().index_add_(axis, idx, src)
def _segment_sum(src, seg, n):
    out = torch.zeros((n,) + src.shape[1:], dtype=src.dtype)
    return out.index_add_(0, seg, src)
def _pad(x, pw):
    flat = tuple(v for pair in reversed(pw) for v in pair)
    return torch.nn.functional.pad(x, flat)

kernel_name = 'parallel_sum__compressed_kv_cla__swiglu_ffn__prompt'

def setup_inputs():

    _w = (lambda a: (lambda t: t.to(torch.bfloat16) if t.is_floating_point() else t)(torch.as_tensor(a)))
    def _A_setup():
        B, H, Sq, Sk, D, M = 1, 32, 2048, 2048, 128, 512
        Dc = 4 * D
        return {
            "x": _w((lambda *s: torch.randn(*s, dtype=torch.bfloat16) * 0.05)(B, Sq, M)),
            "c_kv": _w((lambda *s: torch.randn(*s, dtype=torch.bfloat16) * 1.6)(B, Sk, Dc)),
            "w_q": _w((lambda *s: torch.randn(*s, dtype=torch.bfloat16) * 0.8)(M, H, D) * (M ** -0.5)),
            "w_uk": _w((lambda *s: torch.randn(*s, dtype=torch.bfloat16) * 0.8)(Dc, H, D) * (Dc ** -0.5)),
            "w_uv": _w((lambda *s: torch.randn(*s, dtype=torch.bfloat16) * 1.6)(Dc, H, D) * (Dc ** -0.5)),
            "w_o": _w((lambda *s: torch.randn(*s, dtype=torch.bfloat16) * 1.6)(H, D, M) * ((H * D) ** -0.5)),
        }
    _A_in = _A_setup()

    def _B_setup():
        B, S, D, F = 1, 2048, 512, 1792
        return {
            "x": _w((lambda *s: torch.randn(*s, dtype=torch.bfloat16) * 0.05)(B, S, D)),
            "w_gate": _w((lambda *s: torch.randn(*s, dtype=torch.bfloat16) * 0.05)(D, F)),
            "w_up": _w((lambda *s: torch.randn(*s, dtype=torch.bfloat16) * 0.05)(D, F)),
            "w_down": _w((lambda *s: torch.randn(*s, dtype=torch.bfloat16) * 0.05)(F, D)),
        }
    _B_in = _B_setup()

    return {
        "A_x": _A_in["x"],
        "A_c_kv": _A_in["c_kv"],
        "A_w_q": _A_in["w_q"],
        "A_w_uk": _A_in["w_uk"],
        "A_w_uv": _A_in["w_uv"],
        "A_w_o": _A_in["w_o"],
        "B_x": _B_in["x"],
        "B_w_gate": _B_in["w_gate"],
        "B_w_up": _B_in["w_up"],
        "B_w_down": _B_in["w_down"],
    }

def reference(A_x, A_c_kv, A_w_q, A_w_uk, A_w_uv, A_w_o, B_x, B_w_gate, B_w_up, B_w_down):
    def _A_ref(x, c_kv, w_q, w_uk, w_uv, w_o):
        B, Sq, M = x.shape[0], x.shape[1], x.shape[2]
        Sk = c_kv.shape[1]
        H, D = w_q.shape[1], w_q.shape[2]
        q = torch.einsum("bqm,mhd->bhqd", x, w_q)
        k = torch.einsum("bkc,chd->bhkd", c_kv, w_uk)
        v = torch.einsum("bkc,chd->bhkd", c_kv, w_uv)
        scores = torch.einsum("bhqd,bhkd->bhqk", q, k) * (D ** -0.5)
        off = Sk - Sq
        qi = torch.arange(Sq)[:, None]
        kj = torch.arange(Sk)[None, :]
        cmask = kj <= (qi + off)
        scores = torch.where(cmask[None, None], scores, -1e9)
        p = _softmax(scores, axis=-1)
        ctx = torch.einsum("bhqk,bhkd->bhqd", p, v)
        return torch.einsum("bhqd,hdm->bqm", ctx, w_o)

    def _B_ref(x, w_gate, w_up, w_down):
        g = _silu(x @ w_gate)
        u = x @ w_up
        return (g * u) @ w_down

    x = A_x
    xn = _layernorm(x, axis=-1)
    return x + _A_ref(xn, A_c_kv, A_w_q, A_w_uk, A_w_uv, A_w_o) + _B_ref(xn, B_w_gate, B_w_up, B_w_down)
```

```cpp
#include <hip/hip_runtime.h>
#include <hip/hip_bf16.h>

extern "C" __global__ __launch_bounds__(256, 1) void mega_kernel(const void* x, const void* ckv, const void* wq, const void* wuk, const void* wuv, const void* wo,
        const void* wg, const void* wu, const void* wd, void* out, void* ws) {
    asm volatile(
R"ASM(L_CODE0:
s_mov_b64 s[4:5], %0
s_load_dwordx16 s[52:67], s[4:5], 0x0
s_load_dwordx2 s[48:49], s[4:5], 0x50
s_mov_b32 s6, %1
v_mov_b32 v2, %2
v_and_b32 v1, 63, v2
v_lshrrev_b32 v3, 6, v2
s_nop 0
v_readfirstlane_b32 s7, v3
s_getpc_b64 s[40:41]
L_PC:
s_lshr_b32 s20, s6, 3
s_lshl_b32 s20, s20, 2
s_add_u32 s20, s20, s7
s_lshl_b32 s20, s20, 10
v_lshlrev_b32 v4, 4, v1
v_add_u32 v4, s20, v4
v_min_u32 v4, L_CODE_END-L_PC-16, v4
global_load_dwordx4 v[252:255], v4, s[40:41]
s_and_b32 s20, s6, 7
s_lshr_b32 s21, s6, 3
s_lshr_b32 s22, s21, 3
s_lshl_b32 s45, s20, 2
s_add_u32 s45, s45, s22
s_and_b32 s44, s21, 7
s_and_b32 s22, s45, 7
s_and_b32 s23, s44, 4
s_xor_b32 s22, s22, s23
s_cmp_eq_u32 s22, 3
s_cselect_b32 s74, 1, 0
s_cmp_eq_u32 s74, 1
s_cbranch_scc1 L_LN
L_A0:
s_and_b32 s20, s6, 7
s_lshr_b32 s21, s6, 3
s_and_b32 s22, s21, 7
s_lshr_b32 s23, s21, 3
s_lshr_b32 s36, s23, 1
s_and_b32 s26, s23, 1
s_lshl_b32 s26, s26, 1
s_lshl_b32 s25, s20, 2
s_add_u32 s25, s25, s26
v_lshrrev_b32 v29, 5, v1
s_and_b32 s26, s7, 1
s_lshl_b32 s26, s26, 2
v_add_u32 v30, 0, v29
v_or_b32 v30, s26, v30
v_lshlrev_b32 v30, 1, v30
v_and_b32 v31, 31, v1
v_xor_b32 v31, v31, v30
v_lshlrev_b32 v31, 4, v31
v_lshlrev_b32 v32, 13, v29
v_add_u32 v31, v31, v32
v_add_u32 v4, 0, v31
v_add_u32 v30, 2, v29
v_or_b32 v30, s26, v30
v_lshlrev_b32 v30, 1, v30
v_and_b32 v31, 31, v1
v_xor_b32 v31, v31, v30
v_lshlrev_b32 v31, 4, v31
v_lshlrev_b32 v32, 13, v29
v_add_u32 v31, v31, v32
v_add_u32 v5, 15360, v31
v_add_u32 v30, 0, v29
v_or_b32 v30, s26, v30
v_lshlrev_b32 v30, 1, v30
v_and_b32 v31, 31, v1
v_xor_b32 v31, v31, v30
v_lshlrev_b32 v31, 4, v31
v_lshlrev_b32 v32, 13, v29
v_add_u32 v31, v31, v32
v_add_u32 v6, 30720, v31
v_add_u32 v30, 2, v29
v_or_b32 v30, s26, v30
v_lshlrev_b32 v30, 1, v30
v_and_b32 v31, 31, v1
v_xor_b32 v31, v31, v30
v_lshlrev_b32 v31, 4, v31
v_lshlrev_b32 v32, 13, v29
v_add_u32 v31, v31, v32
v_add_u32 v7, 46080, v31
v_lshrrev_b32 v29, 4, v1
v_add_u32 v29, 0, v29
v_and_b32 v30, 7, v1
v_xor_b32 v30, v30, v29
v_lshlrev_b32 v30, 4, v30
v_lshrrev_b32 v31, 3, v1
v_lshlrev_b32 v31, 10, v31
v_add_u32 v29, v30, v31
v_add_u32 v8, 0, v29
v_add_u32 v37, 14336, v29
v_lshrrev_b32 v29, 4, v1
v_add_u32 v29, 4, v29
v_and_b32 v30, 7, v1
v_xor_b32 v30, v30, v29
v_lshlrev_b32 v30, 4, v30
v_lshrrev_b32 v31, 3, v1
v_lshlrev_b32 v31, 10, v31
v_add_u32 v29, v30, v31
v_add_u32 v9, 7168, v29
v_add_u32 v38, 21504, v29
v_lshrrev_b32 v33, 4, v1
v_and_b32 v34, 15, v1
v_lshrrev_b32 v34, 2, v34
v_and_b32 v35, 3, v1
v_and_b32 v36, 1, v33
v_lshlrev_b32 v36, 2, v36
v_or_b32 v36, v36, v34
v_lshlrev_b32 v29, 3, v33
v_add_u32 v29, v29, v34
v_lshlrev_b32 v29, 9, v29
v_lshlrev_b32 v30, 3, v35
v_add_u32 v29, v29, v30
v_xor_b32 v30, 0, v36
v_lshlrev_b32 v30, 5, v30
v_add_u32 v10, v29, v30
v_add_u32 v18, 65536, v10
v_xor_b32 v30, 1, v36
v_lshlrev_b32 v30, 5, v30
v_add_u32 v11, v29, v30
v_add_u32 v19, 65536, v11
v_xor_b32 v30, 2, v36
v_lshlrev_b32 v30, 5, v30
v_add_u32 v12, v29, v30
v_add_u32 v20, 65536, v12
v_xor_b32 v30, 3, v36
v_lshlrev_b32 v30, 5, v30
v_add_u32 v13, v29, v30
v_add_u32 v21, 65536, v13
v_xor_b32 v30, 4, v36
v_lshlrev_b32 v30, 5, v30
v_add_u32 v14, v29, v30
v_add_u32 v22, 65536, v14
v_xor_b32 v30, 5, v36
v_lshlrev_b32 v30, 5, v30
v_add_u32 v15, v29, v30
v_add_u32 v23, 65536, v15
v_xor_b32 v30, 6, v36
v_lshlrev_b32 v30, 5, v30
v_add_u32 v16, v29, v30
v_add_u32 v24, 65536, v16
v_xor_b32 v30, 7, v36
v_lshlrev_b32 v30, 5, v30
v_add_u32 v17, v29, v30
v_add_u32 v25, 65536, v17
s_mul_i32 s35, s7, 20480
s_add_u32 s35, s35, 81920
v_and_b32 v29, 15, v1
v_lshrrev_b32 v30, 1, v29
v_lshlrev_b32 v29, 7, v29
v_add_u32 v29, s35, v29
v_add_u32 v31, 0, v33
v_xor_b32 v31, v31, v30
v_lshlrev_b32 v31, 4, v31
v_add_u32 v26, v29, v31
v_add_u32 v31, 4, v33
v_xor_b32 v31, v31, v30
v_lshlrev_b32 v31, 4, v31
v_add_u32 v27, v29, v31
v_lshlrev_b32 v28, 4, v1
s_waitcnt lgkmcnt(0)
s_mov_b64 s[42:43], s[54:55]
s_cmp_eq_u32 s36, 0
s_cselect_b64 s[40:41], s[58:59], s[60:61]
s_load_dwordx2 s[98:99], s[48:49], 0x0
s_lshl_b32 s26, s25, 8
s_add_u32 s40, s40, s26
s_addc_u32 s41, s41, 0
s_sub_u32 s27, 4194304, s26
s_mov_b32 s8, s40
s_and_b32 s9, s41, 65535
s_mov_b32 s10, s27
s_mov_b32 s11, 131072
s_lshl_b32 s26, s22, 18
s_lshl_b32 s27, s7, 16
s_add_u32 s26, s26, s27
s_add_u32 s42, s42, s26
s_addc_u32 s43, s43, 0
s_mov_b32 s12, s42
s_and_b32 s13, s43, 65535
s_mov_b32 s14, 65536
s_mov_b32 s15, 131072
s_mul_i32 s26, s36, 16777216
s_add_u32 s26, s26, 5242880
s_lshl_b32 s27, s25, 19
s_add_u32 s26, s26, s27
s_lshl_b32 s27, s22, 16
s_add_u32 s26, s26, s27
s_lshl_b32 s27, s7, 14
s_add_u32 s26, s26, s27
s_add_u32 s44, s48, s26
s_addc_u32 s45, s49, 0
s_mov_b32 s92, s44
s_and_b32 s93, s45, 65535
s_mov_b32 s94, 1048576
s_mov_b32 s95, 131072
s_lshl_b32 s34, s7, 12
s_lshl_b32 s37, s7, 16
s_add_u32 s28, s37, 0
s_add_u32 m0, s34, 0
s_nop 0
buffer_load_dwordx4 v4, s[8:11], s28 offen lds
buffer_load_dwordx4 v5, s[8:11], s28 offen offset:1024 lds
buffer_load_dwordx4 v6, s[8:11], s28 offen offset:2048 lds
buffer_load_dwordx4 v7, s[8:11], s28 offen offset:3072 lds
s_mov_b32 s29, 0
s_add_u32 m0, s35, 0
s_nop 0
buffer_load_dwordx4 v8, s[12:15], s29 offen lds
buffer_load_dwordx4 v9, s[12:15], s29 offen offset:1024 lds
buffer_load_dwordx4 v37, s[12:15], s29 offen offset:2048 lds
buffer_load_dwordx4 v38, s[12:15], s29 offen offset:3072 lds
s_mov_b32 s29, 32768
s_add_u32 m0, s35, 4096
s_nop 0
buffer_load_dwordx4 v8, s[12:15], s29 offen lds
buffer_load_dwordx4 v9, s[12:15], s29 offen offset:1024 lds
buffer_load_dwordx4 v37, s[12:15], s29 offen offset:2048 lds
buffer_load_dwordx4 v38, s[12:15], s29 offen offset:3072 lds
s_add_u32 s28, s37, 262144
s_add_u32 m0, s34, 16384
s_nop 0
buffer_load_dwordx4 v4, s[8:11], s28 offen lds
buffer_load_dwordx4 v5, s[8:11], s28 offen offset:1024 lds
buffer_load_dwordx4 v6, s[8:11], s28 offen offset:2048 lds
buffer_load_dwordx4 v7, s[8:11], s28 offen offset:3072 lds
s_mov_b32 s29, 128
s_add_u32 m0, s35, 8192
s_nop 0
buffer_load_dwordx4 v8, s[12:15], s29 offen lds
buffer_load_dwordx4 v9, s[12:15], s29 offen offset:1024 lds
buffer_load_dwordx4 v37, s[12:15], s29 offen offset:2048 lds
buffer_load_dwordx4 v38, s[12:15], s29 offen offset:3072 lds
s_add_u32 s28, s37, 524288
s_add_u32 m0, s34, 32768
s_nop 0
buffer_load_dwordx4 v4, s[8:11], s28 offen lds
buffer_load_dwordx4 v5, s[8:11], s28 offen offset:1024 lds
buffer_load_dwordx4 v6, s[8:11], s28 offen offset:2048 lds
buffer_load_dwordx4 v7, s[8:11], s28 offen offset:3072 lds
s_mov_b32 s29, 32896
s_add_u32 m0, s35, 12288
s_nop 0
buffer_load_dwordx4 v8, s[12:15], s29 offen lds
buffer_load_dwordx4 v9, s[12:15], s29 offen offset:1024 lds
buffer_load_dwordx4 v37, s[12:15], s29 offen offset:2048 lds
buffer_load_dwordx4 v38, s[12:15], s29 offen offset:3072 lds
s_mov_b32 s29, 256
s_add_u32 m0, s35, 16384
s_nop 0
buffer_load_dwordx4 v8, s[12:15], s29 offen lds
buffer_load_dwordx4 v9, s[12:15], s29 offen offset:1024 lds
buffer_load_dwordx4 v37, s[12:15], s29 offen offset:2048 lds
buffer_load_dwordx4 v38, s[12:15], s29 offen offset:3072 lds
s_cmp_eq_u32 s36, 0
s_cbranch_scc0 L_A_V
s_waitcnt lgkmcnt(0)
s_add_u32 s98, s98, 1
s_addc_u32 s99, s99, 0
s_waitcnt vmcnt(20)
s_barrier
ds_read_b128 v[72:75], v26 offset:0
ds_read_b128 v[76:79], v26 offset:2048
ds_read_b128 v[80:83], v26 offset:4096
ds_read_b128 v[84:87], v26 offset:6144
ds_read_b128 v[88:91], v27 offset:0
ds_read_b128 v[92:95], v27 offset:2048
ds_read_b128 v[96:99], v27 offset:4096
ds_read_b128 v[100:103], v27 offset:6144
ds_read_b64_tr_b16 v[40:41], v10 offset:0
ds_read_b64_tr_b16 v[42:43], v10 offset:2048
ds_read_b64_tr_b16 v[44:45], v11 offset:0
ds_read_b64_tr_b16 v[46:47], v11 offset:2048
ds_read_b64_tr_b16 v[48:49], v12 offset:0
ds_read_b64_tr_b16 v[50:51], v12 offset:2048
ds_read_b64_tr_b16 v[52:53], v13 offset:0
ds_read_b64_tr_b16 v[54:55], v13 offset:2048
s_waitcnt lgkmcnt(6)
v_mfma_f32_16x16x32_bf16 a[0:3], v[40:43], v[72:75], 0
ds_read_b64_tr_b16 v[56:57], v14 offset:0
ds_read_b64_tr_b16 v[58:59], v14 offset:2048
v_mfma_f32_16x16x32_bf16 a[4:7], v[40:43], v[76:79], 0
s_add_u32 s28, s37, 786432
v_mfma_f32_16x16x32_bf16 a[8:11], v[40:43], v[80:83], 0
s_add_u32 m0, s34, 49152
v_mfma_f32_16x16x32_bf16 a[12:15], v[40:43], v[84:87], 0
buffer_load_dwordx4 v4, s[8:11], s28 offen lds
s_waitcnt lgkmcnt(6)
v_mfma_f32_16x16x32_bf16 a[16:19], v[44:47], v[72:75], 0
ds_read_b64_tr_b16 v[60:61], v15 offset:0
v_mfma_f32_16x16x32_bf16 a[20:23], v[44:47], v[76:79], 0
ds_read_b64_tr_b16 v[62:63], v15 offset:2048
v_mfma_f32_16x16x32_bf16 a[24:27], v[44:47], v[80:83], 0
buffer_load_dwordx4 v5, s[8:11], s28 offen offset:1024 lds
v_mfma_f32_16x16x32_bf16 a[28:31], v[44:47], v[84:87], 0
s_waitcnt lgkmcnt(6)
v_mfma_f32_16x16x32_bf16 a[32:35], v[48:51], v[72:75], 0
ds_read_b64_tr_b16 v[64:65], v16 offset:0
v_mfma_f32_16x16x32_bf16 a[36:39], v[48:51], v[76:79], 0
v_mfma_f32_16x16x32_bf16 a[40:43], v[48:51], v[80:83], 0
ds_read_b64_tr_b16 v[66:67], v16 offset:2048
v_mfma_f32_16x16x32_bf16 a[44:47], v[48:51], v[84:87], 0
s_waitcnt lgkmcnt(6)
v_mfma_f32_16x16x32_bf16 a[48:51], v[52:55], v[72:75], 0
ds_read_b64_tr_b16 v[68:69], v17 offset:0
v_mfma_f32_16x16x32_bf16 a[52:55], v[52:55], v[76:79], 0
ds_read_b64_tr_b16 v[70:71], v17 offset:2048
v_mfma_f32_16x16x32_bf16 a[56:59], v[52:55], v[80:83], 0
buffer_load_dwordx4 v6, s[8:11], s28 offen offset:2048 lds
v_mfma_f32_16x16x32_bf16 a[60:63], v[52:55], v[84:87], 0
s_waitcnt lgkmcnt(6)
v_mfma_f32_16x16x32_bf16 a[64:67], v[56:59], v[72:75], 0
ds_read_b64_tr_b16 v[40:41], v10 offset:256
ds_read_b64_tr_b16 v[42:43], v10 offset:2304
v_mfma_f32_16x16x32_bf16 a[68:71], v[56:59], v[76:79], 0
buffer_load_dwordx4 v7, s[8:11], s28 offen offset:3072 lds
v_mfma_f32_16x16x32_bf16 a[72:75], v[56:59], v[80:83], 0
s_and_b32 s20, s6, 7
v_mfma_f32_16x16x32_bf16 a[76:79], v[56:59], v[84:87], 0
s_lshr_b32 s21, s6, 3
s_waitcnt lgkmcnt(6)
v_mfma_f32_16x16x32_bf16 a[80:83], v[60:63], v[72:75], 0
ds_read_b64_tr_b16 v[44:45], v11 offset:256
v_mfma_f32_16x16x32_bf16 a[84:87], v[60:63], v[76:79], 0
ds_read_b64_tr_b16 v[46:47], v11 offset:2304
v_mfma_f32_16x16x32_bf16 a[88:91], v[60:63], v[80:83], 0
s_lshr_b32 s22, s21, 3
v_mfma_f32_16x16x32_bf16 a[92:95], v[60:63], v[84:87], 0
s_waitcnt lgkmcnt(6)
v_mfma_f32_16x16x32_bf16 a[96:99], v[64:67], v[72:75], 0
ds_read_b64_tr_b16 v[48:49], v12 offset:256
ds_read_b64_tr_b16 v[50:51], v12 offset:2304
v_mfma_f32_16x16x32_bf16 a[100:103], v[64:67], v[76:79], 0
s_mov_b32 s29, 33024
s_add_u32 m0, s35, 0
v_mfma_f32_16x16x32_bf16 a[104:107], v[64:67], v[80:83], 0
buffer_load_dwordx4 v8, s[12:15], s29 offen lds
s_lshl_b32 s45, s20, 2
v_mfma_f32_16x16x32_bf16 a[108:111], v[64:67], v[84:87], 0
s_add_u32 s45, s45, s22
s_waitcnt lgkmcnt(6)
v_mfma_f32_16x16x32_bf16 a[112:115], v[68:71], v[72:75], 0
ds_read_b64_tr_b16 v[52:53], v13 offset:256
v_mfma_f32_16x16x32_bf16 a[116:119], v[68:71], v[76:79], 0
ds_read_b64_tr_b16 v[54:55], v13 offset:2304
v_mfma_f32_16x16x32_bf16 a[120:123], v[68:71], v[80:83], 0
buffer_load_dwordx4 v9, s[12:15], s29 offen offset:1024 lds
v_mfma_f32_16x16x32_bf16 a[124:127], v[68:71], v[84:87], 0
s_and_b32 s44, s21, 7
s_waitcnt lgkmcnt(6)
v_mfma_f32_16x16x32_bf16 a[128:131], v[40:43], v[72:75], 0
ds_read_b64_tr_b16 v[56:57], v14 offset:256
v_mfma_f32_16x16x32_bf16 a[132:135], v[40:43], v[76:79], 0
ds_read_b64_tr_b16 v[58:59], v14 offset:2304
v_mfma_f32_16x16x32_bf16 a[136:139], v[40:43], v[80:83], 0
s_and_b32 s22, s45, 7
v_mfma_f32_16x16x32_bf16 a[140:143], v[40:43], v[84:87], 0
s_and_b32 s23, s44, 4
s_waitcnt lgkmcnt(6)
v_mfma_f32_16x16x32_bf16 a[144:147], v[44:47], v[72:75], 0
ds_read_b64_tr_b16 v[60:61], v15 offset:256
v_mfma_f32_16x16x32_bf16 a[148:151], v[44:47], v[76:79], 0
ds_read_b64_tr_b16 v[62:63], v15 offset:2304
v_mfma_f32_16x16x32_bf16 a[152:155], v[44:47], v[80:83], 0
buffer_load_dwordx4 v37, s[12:15], s29 offen offset:2048 lds
v_mfma_f32_16x16x32_bf16 a[156:159], v[44:47], v[84:87], 0
s_xor_b32 s22, s22, s23
s_waitcnt lgkmcnt(6)
v_mfma_f32_16x16x32_bf16 a[160:163], v[48:51], v[72:75], 0
ds_read_b64_tr_b16 v[64:65], v16 offset:256
ds_read_b64_tr_b16 v[66:67], v16 offset:2304
v_mfma_f32_16x16x32_bf16 a[164:167], v[48:51], v[76:79], 0
buffer_load_dwordx4 v38, s[12:15], s29 offen offset:3072 lds
v_mfma_f32_16x16x32_bf16 a[168:171], v[48:51], v[80:83], 0
s_cmp_eq_u32 s22, 3
s_cselect_b32 s74, 1, 0
v_mfma_f32_16x16x32_bf16 a[172:175], v[48:51], v[84:87], 0
s_and_b32 s75, s7, 1
s_waitcnt lgkmcnt(6)
v_mfma_f32_16x16x32_bf16 a[176:179], v[52:55], v[72:75], 0
ds_read_b64_tr_b16 v[68:69], v17 offset:256
v_mfma_f32_16x16x32_bf16 a[180:183], v[52:55], v[76:79], 0
ds_read_b64_tr_b16 v[70:71], v17 offset:2304
v_mfma_f32_16x16x32_bf16 a[184:187], v[52:55], v[80:83], 0
s_sub_u32 s22, 15, s44
v_mfma_f32_16x16x32_bf16 a[188:191], v[52:55], v[84:87], 0
s_waitcnt vmcnt(24)
s_barrier
s_waitcnt lgkmcnt(6)
v_mfma_f32_16x16x32_bf16 a[192:195], v[56:59], v[72:75], 0
ds_read_b64_tr_b16 v[40:41], v10 offset:16384
v_mfma_f32_16x16x32_bf16 a[196:199], v[56:59], v[76:79], 0
ds_read_b64_tr_b16 v[42:43], v10 offset:18432
v_mfma_f32_16x16x32_bf16 a[200:203], v[56:59], v[80:83], 0
s_cmp_lt_u32 s7, 2
v_mfma_f32_16x16x32_bf16 a[204:207], v[56:59], v[84:87], 0
s_cselect_b32 s22, s44, s22
s_waitcnt lgkmcnt(6)
v_mfma_f32_16x16x32_bf16 a[208:211], v[60:63], v[72:75], 0
ds_read_b64_tr_b16 v[44:45], v11 offset:16384
v_mfma_f32_16x16x32_bf16 a[212:215], v[60:63], v[76:79], 0
ds_read_b64_tr_b16 v[46:47], v11 offset:18432
v_mfma_f32_16x16x32_bf16 a[216:219], v[60:63], v[80:83], 0
s_lshl_b32 s22, s22, 3
v_mfma_f32_16x16x32_bf16 a[220:223], v[60:63], v[84:87], 0
s_lshl_b32 s23, s75, 0
s_waitcnt lgkmcnt(6)
v_mfma_f32_16x16x32_bf16 a[224:227], v[64:67], v[72:75], 0
ds_read_b64_tr_b16 v[48:49], v12 offset:16384
v_mfma_f32_16x16x32_bf16 a[228:231], v[64:67], v[76:79], 0
ds_read_b64_tr_b16 v[50:51], v12 offset:18432
v_mfma_f32_16x16x32_bf16 a[232:235], v[64:67], v[80:83], 0
s_add_u32 s50, s22, s23
v_mfma_f32_16x16x32_bf16 a[236:239], v[64:67], v[84:87], 0
s_waitcnt lgkmcnt(6)
v_mfma_f32_16x16x32_bf16 a[240:243], v[68:71], v[72:75], 0
ds_read_b64_tr_b16 v[52:53], v13 offset:16384
v_mfma_f32_16x16x32_bf16 a[244:247], v[68:71], v[76:79], 0
ds_read_b64_tr_b16 v[54:55], v13 offset:18432
v_mfma_f32_16x16x32_bf16 a[248:251], v[68:71], v[80:83], 0
v_lshrrev_b32 v131, 4, v1
v_mfma_f32_16x16x32_bf16 a[252:255], v[68:71], v[84:87], 0
s_lshl_b32 s24, s75, 2
s_waitcnt lgkmcnt(6)
v_mfma_f32_16x16x32_bf16 a[0:3], v[40:43], v[88:91], a[0:3]
ds_read_b64_tr_b16 v[56:57], v14 offset:16384
ds_read_b64_tr_b16 v[58:59], v14 offset:18432
v_mfma_f32_16x16x32_bf16 a[4:7], v[40:43], v[92:95], a[4:7]
s_add_u32 s28, s37, 1048576
v_mfma_f32_16x16x32_bf16 a[8:11], v[40:43], v[96:99], a[8:11]
s_add_u32 m0, s34, 65536
s_nop 0
buffer_load_dwordx4 v4, s[8:11], s28 offen lds
v_mfma_f32_16x16x32_bf16 a[12:15], v[40:43], v[100:103], a[12:15]
v_or_b32 v128, s24, v131
s_waitcnt lgkmcnt(6)
v_mfma_f32_16x16x32_bf16 a[16:19], v[44:47], v[88:91], a[16:19]
ds_read_b64_tr_b16 v[60:61], v15 offset:16384
ds_read_b64_tr_b16 v[62:63], v15 offset:18432
v_mfma_f32_16x16x32_bf16 a[20:23], v[44:47], v[92:95], a[20:23]
buffer_load_dwordx4 v5, s[8:11], s28 offen offset:1024 lds
v_mfma_f32_16x16x32_bf16 a[24:27], v[44:47], v[96:99], a[24:27]
v_lshlrev_b32 v128, 1, v128
v_mfma_f32_16x16x32_bf16 a[28:31], v[44:47], v[100:103], a[28:31]
v_and_b32 v129, 15, v1
s_waitcnt lgkmcnt(6)
v_mfma_f32_16x16x32_bf16 a[32:35], v[48:51], v[88:91], a[32:35]
ds_read_b64_tr_b16 v[64:65], v16 offset:16384
v_mfma_f32_16x16x32_bf16 a[36:39], v[48:51], v[92:95], a[36:39]
ds_read_b64_tr_b16 v[66:67], v16 offset:18432
v_mfma_f32_16x16x32_bf16 a[40:43], v[48:51], v[96:99], a[40:43]
v_xor_b32 v129, v129, v128
v_mfma_f32_16x16x32_bf16 a[44:47], v[48:51], v[100:103], a[44:47]
s_waitcnt lgkmcnt(6)
v_mfma_f32_16x16x32_bf16 a[48:51], v[52:55], v[88:91], a[48:51]
ds_read_b64_tr_b16 v[68:69], v17 offset:16384
ds_read_b64_tr_b16 v[70:71], v17 offset:18432
v_mfma_f32_16x16x32_bf16 a[52:55], v[52:55], v[92:95], a[52:55]
s_waitcnt vmcnt(14)
v_mfma_f32_16x16x32_bf16 a[56:59], v[52:55], v[96:99], a[56:59]
buffer_load_dwordx4 v6, s[8:11], s28 offen offset:2048 lds
v_mfma_f32_16x16x32_bf16 a[60:63], v[52:55], v[100:103], a[60:63]
v_lshlrev_b32 v129, 4, v129
s_waitcnt lgkmcnt(6)
v_mfma_f32_16x16x32_bf16 a[64:67], v[56:59], v[88:91], a[64:67]
ds_read_b64_tr_b16 v[40:41], v10 offset:16640
ds_read_b64_tr_b16 v[42:43], v10 offset:18688
v_mfma_f32_16x16x32_bf16 a[68:71], v[56:59], v[92:95], a[68:71]
ds_read_b128 v[104:107], v26 offset:8192
v_mfma_f32_16x16x32_bf16 a[72:75], v[56:59], v[96:99], a[72:75]
buffer_load_dwordx4 v7, s[8:11], s28 offen offset:3072 lds
v_lshlrev_b32 v130, 13, v131
v_mfma_f32_16x16x32_bf16 a[76:79], v[56:59], v[100:103], a[76:79]
v_add_u32 v29, v129, v130
s_waitcnt lgkmcnt(7)
v_mfma_f32_16x16x32_bf16 a[80:83], v[60:63], v[88:91], a[80:83]
ds_read_b64_tr_b16 v[44:45], v11 offset:16640
v_mfma_f32_16x16x32_bf16 a[84:87], v[60:63], v[92:95], a[84:87]
ds_read_b64_tr_b16 v[46:47], v11 offset:18688
v_mfma_f32_16x16x32_bf16 a[88:91], v[60:63], v[96:99], a[88:91]
ds_read_b128 v[108:111], v26 offset:10240
v_mfma_f32_16x16x32_bf16 a[92:95], v[60:63], v[100:103], a[92:95]
v_and_b32 v132, 15, v1
s_waitcnt lgkmcnt(8)
v_mfma_f32_16x16x32_bf16 a[96:99], v[64:67], v[88:91], a[96:99]
ds_read_b64_tr_b16 v[48:49], v12 offset:16640
ds_read_b64_tr_b16 v[50:51], v12 offset:18688
v_mfma_f32_16x16x32_bf16 a[100:103], v[64:67], v[92:95], a[100:103]
ds_read_b128 v[112:115], v26 offset:12288
s_mov_b32 s29, 384
v_mfma_f32_16x16x32_bf16 a[104:107], v[64:67], v[96:99], a[104:107]
s_add_u32 m0, s35, 4096
s_nop 0
buffer_load_dwordx4 v8, s[12:15], s29 offen lds
v_mfma_f32_16x16x32_bf16 a[108:111], v[64:67], v[100:103], a[108:111]
v_lshrrev_b32 v132, 2, v132
v_and_b32 v133, 3, v1
s_waitcnt lgkmcnt(9)
v_mfma_f32_16x16x32_bf16 a[112:115], v[68:71], v[88:91], a[112:115]
ds_read_b64_tr_b16 v[52:53], v13 offset:16640
ds_read_b64_tr_b16 v[54:55], v13 offset:18688
v_mfma_f32_16x16x32_bf16 a[116:119], v[68:71], v[92:95], a[116:119]
ds_read_b128 v[116:119], v26 offset:14336
v_mfma_f32_16x16x32_bf16 a[120:123], v[68:71], v[96:99], a[120:123]
buffer_load_dwordx4 v9, s[12:15], s29 offen offset:1024 lds
v_mfma_f32_16x16x32_bf16 a[124:127], v[68:71], v[100:103], a[124:127]
v_and_b32 v134, 1, v131
s_waitcnt lgkmcnt(10)
v_mfma_f32_16x16x32_bf16 a[128:131], v[40:43], v[88:91], a[128:131]
ds_read_b64_tr_b16 v[56:57], v14 offset:16640
ds_read_b64_tr_b16 v[58:59], v14 offset:18688
v_mfma_f32_16x16x32_bf16 a[132:135], v[40:43], v[92:95], a[132:135]
ds_read_b128 v[72:75], v27 offset:8192
v_mfma_f32_16x16x32_bf16 a[136:139], v[40:43], v[96:99], a[136:139]
v_lshlrev_b32 v134, 2, v134
v_mfma_f32_16x16x32_bf16 a[140:143], v[40:43], v[100:103], a[140:143]
v_or_b32 v134, v134, v132
s_waitcnt lgkmcnt(10)
v_mfma_f32_16x16x32_bf16 a[144:147], v[44:47], v[88:91], a[144:147]
ds_read_b64_tr_b16 v[60:61], v15 offset:16640
ds_read_b64_tr_b16 v[62:63], v15 offset:18688
v_mfma_f32_16x16x32_bf16 a[148:151], v[44:47], v[92:95], a[148:151]
ds_read_b128 v[76:79], v27 offset:10240
v_mfma_f32_16x16x32_bf16 a[152:155], v[44:47], v[96:99], a[152:155]
buffer_load_dwordx4 v37, s[12:15], s29 offen offset:2048 lds
v_mfma_f32_16x16x32_bf16 a[156:159], v[44:47], v[100:103], a[156:159]
v_lshlrev_b32 v128, 4, v131
s_waitcnt lgkmcnt(10)
v_mfma_f32_16x16x32_bf16 a[160:163], v[48:51], v[88:91], a[160:163]
ds_read_b64_tr_b16 v[64:65], v16 offset:16640
ds_read_b64_tr_b16 v[66:67], v16 offset:18688
v_mfma_f32_16x16x32_bf16 a[164:167], v[48:51], v[92:95], a[164:167]
ds_read_b128 v[80:83], v27 offset:12288
v_mfma_f32_16x16x32_bf16 a[168:171], v[48:51], v[96:99], a[168:171]
buffer_load_dwordx4 v38, s[12:15], s29 offen offset:3072 lds
v_add_u32 v128, v128, v132
v_mfma_f32_16x16x32_bf16 a[172:175], v[48:51], v[100:103], a[172:175]
v_lshlrev_b32 v128, 8, v128
s_waitcnt lgkmcnt(10)
v_mfma_f32_16x16x32_bf16 a[176:179], v[52:55], v[88:91], a[176:179]
ds_read_b64_tr_b16 v[68:69], v17 offset:16640
v_mfma_f32_16x16x32_bf16 a[180:183], v[52:55], v[92:95], a[180:183]
ds_read_b64_tr_b16 v[70:71], v17 offset:18688
v_mfma_f32_16x16x32_bf16 a[184:187], v[52:55], v[96:99], a[184:187]
ds_read_b128 v[84:87], v27 offset:14336
v_mfma_f32_16x16x32_bf16 a[188:191], v[52:55], v[100:103], a[188:191]
v_lshlrev_b32 v129, 3, v133
s_barrier
s_waitcnt lgkmcnt(10)
v_mfma_f32_16x16x32_bf16 a[192:195], v[56:59], v[88:91], a[192:195]
ds_read_b64_tr_b16 v[40:41], v10 offset:32768
v_mfma_f32_16x16x32_bf16 a[196:199], v[56:59], v[92:95], a[196:199]
ds_read_b64_tr_b16 v[42:43], v10 offset:34816
v_mfma_f32_16x16x32_bf16 a[200:203], v[56:59], v[96:99], a[200:203]
v_add_u32 v128, v128, v129
v_mfma_f32_16x16x32_bf16 a[204:207], v[56:59], v[100:103], a[204:207]
s_waitcnt lgkmcnt(9)
v_mfma_f32_16x16x32_bf16 a[208:211], v[60:63], v[88:91], a[208:211]
ds_read_b64_tr_b16 v[44:45], v11 offset:32768
v_mfma_f32_16x16x32_bf16 a[212:215], v[60:63], v[92:95], a[212:215]
ds_read_b64_tr_b16 v[46:47], v11 offset:34816
v_mfma_f32_16x16x32_bf16 a[216:219], v[60:63], v[96:99], a[216:219]
v_add_u32 v128, 16384, v128
v_mfma_f32_16x16x32_bf16 a[220:223], v[60:63], v[100:103], a[220:223]
v_lshlrev_b32 v129, 5, v134
s_waitcnt lgkmcnt(8)
v_mfma_f32_16x16x32_bf16 a[224:227], v[64:67], v[88:91], a[224:227]
ds_read_b64_tr_b16 v[48:49], v12 offset:32768
v_mfma_f32_16x16x32_bf16 a[228:231], v[64:67], v[92:95], a[228:231]
ds_read_b64_tr_b16 v[50:51], v12 offset:34816
v_mfma_f32_16x16x32_bf16 a[232:235], v[64:67], v[96:99], a[232:235]
v_add_u32 v31, v128, v129
v_mfma_f32_16x16x32_bf16 a[236:239], v[64:67], v[100:103], a[236:239]
s_waitcnt lgkmcnt(7)
v_mfma_f32_16x16x32_bf16 a[240:243], v[68:71], v[88:91], a[240:243]
ds_read_b64_tr_b16 v[52:53], v13 offset:32768
v_mfma_f32_16x16x32_bf16 a[244:247], v[68:71], v[92:95], a[244:247]
ds_read_b64_tr_b16 v[54:55], v13 offset:34816
v_mfma_f32_16x16x32_bf16 a[248:251], v[68:71], v[96:99], a[248:251]
v_lshrrev_b32 v128, 3, v1
v_mfma_f32_16x16x32_bf16 a[252:255], v[68:71], v[100:103], a[252:255]
v_lshrrev_b32 v129, 1, v128
s_waitcnt lgkmcnt(6)
v_mfma_f32_16x16x32_bf16 a[0:3], v[40:43], v[104:107], a[0:3]
ds_read_b64_tr_b16 v[56:57], v14 offset:32768
ds_read_b64_tr_b16 v[58:59], v14 offset:34816
v_mfma_f32_16x16x32_bf16 a[4:7], v[40:43], v[108:111], a[4:7]
s_add_u32 s28, s37, 1310720
v_mfma_f32_16x16x32_bf16 a[8:11], v[40:43], v[112:115], a[8:11]
s_add_u32 m0, s34, 0
s_nop 0
buffer_load_dwordx4 v4, s[8:11], s28 offen lds
v_mfma_f32_16x16x32_bf16 a[12:15], v[40:43], v[116:119], a[12:15]
v_and_b32 v129, 1, v129
s_waitcnt lgkmcnt(6)
v_mfma_f32_16x16x32_bf16 a[16:19], v[44:47], v[104:107], a[16:19]
ds_read_b64_tr_b16 v[60:61], v15 offset:32768
ds_read_b64_tr_b16 v[62:63], v15 offset:34816
v_mfma_f32_16x16x32_bf16 a[20:23], v[44:47], v[108:111], a[20:23]
buffer_load_dwordx4 v5, s[8:11], s28 offen offset:1024 lds
v_mfma_f32_16x16x32_bf16 a[24:27], v[44:47], v[112:115], a[24:27]
s_lshl_b32 s26, s75, 1
v_mfma_f32_16x16x32_bf16 a[28:31], v[44:47], v[116:119], a[28:31]
v_or_b32 v129, s26, v129
s_waitcnt lgkmcnt(6)
v_mfma_f32_16x16x32_bf16 a[32:35], v[48:51], v[104:107], a[32:35]
ds_read_b64_tr_b16 v[64:65], v16 offset:32768
v_mfma_f32_16x16x32_bf16 a[36:39], v[48:51], v[108:111], a[36:39]
ds_read_b64_tr_b16 v[66:67], v16 offset:34816
v_mfma_f32_16x16x32_bf16 a[40:43], v[48:51], v[112:115], a[40:43]
v_lshlrev_b32 v129, 1, v129
v_mfma_f32_16x16x32_bf16 a[44:47], v[48:51], v[116:119], a[44:47]
s_waitcnt lgkmcnt(6)
v_mfma_f32_16x16x32_bf16 a[48:51], v[52:55], v[104:107], a[48:51]
ds_read_b64_tr_b16 v[68:69], v17 offset:32768
v_mfma_f32_16x16x32_bf16 a[52:55], v[52:55], v[108:111], a[52:55]
ds_read_b64_tr_b16 v[70:71], v17 offset:34816
v_mfma_f32_16x16x32_bf16 a[56:59], v[52:55], v[112:115], a[56:59]
buffer_load_dwordx4 v6, s[8:11], s28 offen offset:2048 lds
v_mfma_f32_16x16x32_bf16 a[60:63], v[52:55], v[116:119], a[60:63]
v_and_b32 v130, 7, v1
s_waitcnt lgkmcnt(6)
v_mfma_f32_16x16x32_bf16 a[64:67], v[56:59], v[104:107], a[64:67]
ds_read_b64_tr_b16 v[40:41], v10 offset:33024
ds_read_b64_tr_b16 v[42:43], v10 offset:35072
v_mfma_f32_16x16x32_bf16 a[68:71], v[56:59], v[108:111], a[68:71]
buffer_load_dwordx4 v7, s[8:11], s28 offen offset:3072 lds
v_mfma_f32_16x16x32_bf16 a[72:75], v[56:59], v[112:115], a[72:75]
v_xor_b32 v130, v130, v129
v_mfma_f32_16x16x32_bf16 a[76:79], v[56:59], v[116:119], a[76:79]
v_lshlrev_b32 v130, 4, v130
s_waitcnt lgkmcnt(6)
v_mfma_f32_16x16x32_bf16 a[80:83], v[60:63], v[104:107], a[80:83]
ds_read_b64_tr_b16 v[44:45], v11 offset:33024
v_mfma_f32_16x16x32_bf16 a[84:87], v[60:63], v[108:111], a[84:87]
ds_read_b64_tr_b16 v[46:47], v11 offset:35072
v_mfma_f32_16x16x32_bf16 a[88:91], v[60:63], v[112:115], a[88:91]
v_mul_u32_u24 v128, 3584, v128
v_mfma_f32_16x16x32_bf16 a[92:95], v[60:63], v[116:119], a[92:95]
s_waitcnt lgkmcnt(6)
v_mfma_f32_16x16x32_bf16 a[96:99], v[64:67], v[104:107], a[96:99]
ds_read_b64_tr_b16 v[48:49], v12 offset:33024
ds_read_b64_tr_b16 v[50:51], v12 offset:35072
v_mfma_f32_16x16x32_bf16 a[100:103], v[64:67], v[108:111], a[100:103]
s_mov_b32 s29, 33152
s_add_u32 m0, s35, 8192
v_mfma_f32_16x16x32_bf16 a[104:107], v[64:67], v[112:115], a[104:107]
buffer_load_dwordx4 v8, s[12:15], s29 offen lds
v_add_u32 v30, v128, v130
v_mfma_f32_16x16x32_bf16 a[108:111], v[64:67], v[116:119], a[108:111]
v_and_b32 v134, 1, v131
s_waitcnt lgkmcnt(6)
v_mfma_f32_16x16x32_bf16 a[112:115], v[68:71], v[104:107], a[112:115]
ds_read_b64_tr_b16 v[52:53], v13 offset:33024
v_mfma_f32_16x16x32_bf16 a[116:119], v[68:71], v[108:111], a[116:119]
ds_read_b64_tr_b16 v[54:55], v13 offset:35072
v_mfma_f32_16x16x32_bf16 a[120:123], v[68:71], v[112:115], a[120:123]
buffer_load_dwordx4 v9, s[12:15], s29 offen offset:1024 lds
v_mfma_f32_16x16x32_bf16 a[124:127], v[68:71], v[116:119], a[124:127]
v_lshlrev_b32 v134, 1, v134
s_waitcnt lgkmcnt(6)
v_mfma_f32_16x16x32_bf16 a[128:131], v[40:43], v[104:107], a[128:131]
ds_read_b64_tr_b16 v[56:57], v14 offset:33024
v_mfma_f32_16x16x32_bf16 a[132:135], v[40:43], v[108:111], a[132:135]
ds_read_b64_tr_b16 v[58:59], v14 offset:35072
v_mfma_f32_16x16x32_bf16 a[136:139], v[40:43], v[112:115], a[136:139]
v_lshrrev_b32 v128, 1, v132
v_mfma_f32_16x16x32_bf16 a[140:143], v[40:43], v[116:119], a[140:143]
v_or_b32 v134, v134, v128
s_waitcnt lgkmcnt(6)
v_mfma_f32_16x16x32_bf16 a[144:147], v[44:47], v[104:107], a[144:147]
ds_read_b64_tr_b16 v[60:61], v15 offset:33024
v_mfma_f32_16x16x32_bf16 a[148:151], v[44:47], v[108:111], a[148:151]
ds_read_b64_tr_b16 v[62:63], v15 offset:35072
v_mfma_f32_16x16x32_bf16 a[152:155], v[44:47], v[112:115], a[152:155]
buffer_load_dwordx4 v37, s[12:15], s29 offen offset:2048 lds
v_mfma_f32_16x16x32_bf16 a[156:159], v[44:47], v[116:119], a[156:159]
v_lshlrev_b32 v128, 5, v131
s_waitcnt lgkmcnt(6)
v_mfma_f32_16x16x32_bf16 a[160:163], v[48:51], v[104:107], a[160:163]
ds_read_b64_tr_b16 v[64:65], v16 offset:33024
v_mfma_f32_16x16x32_bf16 a[164:167], v[48:51], v[108:111], a[164:167]
ds_read_b64_tr_b16 v[66:67], v16 offset:35072
v_mfma_f32_16x16x32_bf16 a[168:171], v[48:51], v[112:115], a[168:171]
buffer_load_dwordx4 v38, s[12:15], s29 offen offset:3072 lds
v_mfma_f32_16x16x32_bf16 a[172:175], v[48:51], v[116:119], a[172:175]
v_add_u32 v128, v128, v132
s_waitcnt lgkmcnt(6)
v_mfma_f32_16x16x32_bf16 a[176:179], v[52:55], v[104:107], a[176:179]
ds_read_b64_tr_b16 v[68:69], v17 offset:33024
v_mfma_f32_16x16x32_bf16 a[180:183], v[52:55], v[108:111], a[180:183]
ds_read_b64_tr_b16 v[70:71], v17 offset:35072
v_mfma_f32_16x16x32_bf16 a[184:187], v[52:55], v[112:115], a[184:187]
v_lshlrev_b32 v128, 7, v128
v_mfma_f32_16x16x32_bf16 a[188:191], v[52:55], v[116:119], a[188:191]
v_lshlrev_b32 v129, 3, v133
s_waitcnt vmcnt(20)
s_barrier
s_waitcnt lgkmcnt(6)
v_mfma_f32_16x16x32_bf16 a[192:195], v[56:59], v[104:107], a[192:195]
ds_read_b64_tr_b16 v[40:41], v10 offset:49152
v_mfma_f32_16x16x32_bf16 a[196:199], v[56:59], v[108:111], a[196:199]
ds_read_b64_tr_b16 v[42:43], v10 offset:51200
v_mfma_f32_16x16x32_bf16 a[200:203], v[56:59], v[112:115], a[200:203]
v_add_u32 v128, v128, v129
v_mfma_f32_16x16x32_bf16 a[204:207], v[56:59], v[116:119], a[204:207]
s_waitcnt lgkmcnt(6)
v_mfma_f32_16x16x32_bf16 a[208:211], v[60:63], v[104:107], a[208:211]
ds_read_b64_tr_b16 v[44:45], v11 offset:49152
v_mfma_f32_16x16x32_bf16 a[212:215], v[60:63], v[108:111], a[212:215]
ds_read_b64_tr_b16 v[46:47], v11 offset:51200
v_mfma_f32_16x16x32_bf16 a[216:219], v[60:63], v[112:115], a[216:219]
v_add_u32 v128, 18432, v128
v_mfma_f32_16x16x32_bf16 a[220:223], v[60:63], v[116:119], a[220:223]
v_lshlrev_b32 v129, 5, v134
s_waitcnt lgkmcnt(6)
v_mfma_f32_16x16x32_bf16 a[224:227], v[64:67], v[104:107], a[224:227]
ds_read_b64_tr_b16 v[48:49], v12 offset:49152
v_mfma_f32_16x16x32_bf16 a[228:231], v[64:67], v[108:111], a[228:231]
ds_read_b64_tr_b16 v[50:51], v12 offset:51200
v_mfma_f32_16x16x32_bf16 a[232:235], v[64:67], v[112:115], a[232:235]
v_add_u32 v32, v128, v129
v_mfma_f32_16x16x32_bf16 a[236:239], v[64:67], v[116:119], a[236:239]
s_waitcnt lgkmcnt(6)
v_mfma_f32_16x16x32_bf16 a[240:243], v[68:71], v[104:107], a[240:243]
ds_read_b64_tr_b16 v[52:53], v13 offset:49152
ds_read_b64_tr_b16 v[54:55], v13 offset:51200
v_mfma_f32_16x16x32_bf16 a[244:247], v[68:71], v[108:111], a[244:247]
s_lshl_b32 s22, s45, 8
v_mfma_f32_16x16x32_bf16 a[248:251], v[68:71], v[112:115], a[248:251]
s_add_u32 s46, s56, s22
v_mfma_f32_16x16x32_bf16 a[252:255], v[68:71], v[116:119], a[252:255]
s_addc_u32 s47, s57, 0
s_waitcnt lgkmcnt(6)
v_mfma_f32_16x16x32_bf16 a[0:3], v[40:43], v[72:75], a[0:3]
ds_read_b64_tr_b16 v[56:57], v14 offset:49152
ds_read_b64_tr_b16 v[58:59], v14 offset:51200
v_mfma_f32_16x16x32_bf16 a[4:7], v[40:43], v[76:79], a[4:7]
s_add_u32 s28, s37, 1572864
v_mfma_f32_16x16x32_bf16 a[8:11], v[40:43], v[80:83], a[8:11]
s_add_u32 m0, s34, 16384
s_nop 0
buffer_load_dwordx4 v4, s[8:11], s28 offen lds
v_mfma_f32_16x16x32_bf16 a[12:15], v[40:43], v[84:87], a[12:15]
s_sub_u32 s23, 4194304, s22
s_waitcnt lgkmcnt(6)
v_mfma_f32_16x16x32_bf16 a[16:19], v[44:47], v[72:75], a[16:19]
ds_read_b64_tr_b16 v[60:61], v15 offset:49152
ds_read_b64_tr_b16 v[62:63], v15 offset:51200
)ASM"
R"ASM(v_mfma_f32_16x16x32_bf16 a[20:23], v[44:47], v[76:79], a[20:23]
buffer_load_dwordx4 v5, s[8:11], s28 offen offset:1024 lds
v_mfma_f32_16x16x32_bf16 a[24:27], v[44:47], v[80:83], a[24:27]
s_mov_b32 s76, s46
v_mfma_f32_16x16x32_bf16 a[28:31], v[44:47], v[84:87], a[28:31]
s_and_b32 s77, s47, 65535
s_waitcnt lgkmcnt(6)
v_mfma_f32_16x16x32_bf16 a[32:35], v[48:51], v[72:75], a[32:35]
ds_read_b64_tr_b16 v[64:65], v16 offset:49152
v_mfma_f32_16x16x32_bf16 a[36:39], v[48:51], v[76:79], a[36:39]
ds_read_b64_tr_b16 v[66:67], v16 offset:51200
v_mfma_f32_16x16x32_bf16 a[40:43], v[48:51], v[80:83], a[40:43]
s_mov_b32 s78, s23
v_mfma_f32_16x16x32_bf16 a[44:47], v[48:51], v[84:87], a[44:47]
s_waitcnt lgkmcnt(6)
v_mfma_f32_16x16x32_bf16 a[48:51], v[52:55], v[72:75], a[48:51]
ds_read_b64_tr_b16 v[68:69], v17 offset:49152
ds_read_b64_tr_b16 v[70:71], v17 offset:51200
v_mfma_f32_16x16x32_bf16 a[52:55], v[52:55], v[76:79], a[52:55]
s_waitcnt vmcnt(18)
v_mfma_f32_16x16x32_bf16 a[56:59], v[52:55], v[80:83], a[56:59]
buffer_load_dwordx4 v6, s[8:11], s28 offen offset:2048 lds
v_mfma_f32_16x16x32_bf16 a[60:63], v[52:55], v[84:87], a[60:63]
s_mov_b32 s79, 131072
s_waitcnt lgkmcnt(6)
v_mfma_f32_16x16x32_bf16 a[64:67], v[56:59], v[72:75], a[64:67]
ds_read_b64_tr_b16 v[40:41], v10 offset:49408
ds_read_b64_tr_b16 v[42:43], v10 offset:51456
v_mfma_f32_16x16x32_bf16 a[68:71], v[56:59], v[76:79], a[68:71]
ds_read_b128 v[88:91], v26 offset:16384
v_mfma_f32_16x16x32_bf16 a[72:75], v[56:59], v[80:83], a[72:75]
buffer_load_dwordx4 v7, s[8:11], s28 offen offset:3072 lds
s_and_b32 s24, s44, 4
v_mfma_f32_16x16x32_bf16 a[76:79], v[56:59], v[84:87], a[76:79]
s_xor_b32 s24, s24, 4
s_waitcnt lgkmcnt(7)
v_mfma_f32_16x16x32_bf16 a[80:83], v[60:63], v[72:75], a[80:83]
ds_read_b64_tr_b16 v[44:45], v11 offset:49408
v_mfma_f32_16x16x32_bf16 a[84:87], v[60:63], v[76:79], a[84:87]
ds_read_b64_tr_b16 v[46:47], v11 offset:51456
v_mfma_f32_16x16x32_bf16 a[88:91], v[60:63], v[80:83], a[88:91]
ds_read_b128 v[92:95], v26 offset:18432
v_mfma_f32_16x16x32_bf16 a[92:95], v[60:63], v[84:87], a[92:95]
s_add_u32 s24, s45, s24
s_waitcnt lgkmcnt(8)
v_mfma_f32_16x16x32_bf16 a[96:99], v[64:67], v[72:75], a[96:99]
ds_read_b64_tr_b16 v[48:49], v12 offset:49408
ds_read_b64_tr_b16 v[50:51], v12 offset:51456
v_mfma_f32_16x16x32_bf16 a[100:103], v[64:67], v[76:79], a[100:103]
ds_read_b128 v[96:99], v26 offset:0
s_mov_b32 s29, 512
v_mfma_f32_16x16x32_bf16 a[104:107], v[64:67], v[80:83], a[104:107]
s_add_u32 m0, s35, 12288
s_nop 0
buffer_load_dwordx4 v8, s[12:15], s29 offen lds
v_mfma_f32_16x16x32_bf16 a[108:111], v[64:67], v[84:87], a[108:111]
s_lshr_b32 s24, s24, 3
s_sub_u32 s24, s45, s24
s_waitcnt lgkmcnt(9)
v_mfma_f32_16x16x32_bf16 a[112:115], v[68:71], v[72:75], a[112:115]
ds_read_b64_tr_b16 v[52:53], v13 offset:49408
ds_read_b64_tr_b16 v[54:55], v13 offset:51456
v_mfma_f32_16x16x32_bf16 a[116:119], v[68:71], v[76:79], a[116:119]
ds_read_b128 v[100:103], v26 offset:2048
v_mfma_f32_16x16x32_bf16 a[120:123], v[68:71], v[80:83], a[120:123]
buffer_load_dwordx4 v9, s[12:15], s29 offen offset:1024 lds
v_mfma_f32_16x16x32_bf16 a[124:127], v[68:71], v[84:87], a[124:127]
s_min_u32 s24, s24, 27
s_waitcnt lgkmcnt(10)
v_mfma_f32_16x16x32_bf16 a[128:131], v[40:43], v[72:75], a[128:131]
ds_read_b64_tr_b16 v[56:57], v14 offset:49408
ds_read_b64_tr_b16 v[58:59], v14 offset:51456
v_mfma_f32_16x16x32_bf16 a[132:135], v[40:43], v[76:79], a[132:135]
ds_read_b128 v[104:107], v27 offset:16384
v_mfma_f32_16x16x32_bf16 a[136:139], v[40:43], v[80:83], a[136:139]
s_lshl_b32 s22, s24, 7
v_mfma_f32_16x16x32_bf16 a[140:143], v[40:43], v[84:87], a[140:143]
s_sub_u32 s23, 1835008, s22
s_waitcnt lgkmcnt(10)
v_mfma_f32_16x16x32_bf16 a[144:147], v[44:47], v[72:75], a[144:147]
ds_read_b64_tr_b16 v[60:61], v15 offset:49408
ds_read_b64_tr_b16 v[62:63], v15 offset:51456
v_mfma_f32_16x16x32_bf16 a[148:151], v[44:47], v[76:79], a[148:151]
ds_read_b128 v[108:111], v27 offset:18432
v_mfma_f32_16x16x32_bf16 a[152:155], v[44:47], v[80:83], a[152:155]
buffer_load_dwordx4 v37, s[12:15], s29 offen offset:2048 lds
s_add_u32 s46, s64, s22
v_mfma_f32_16x16x32_bf16 a[156:159], v[44:47], v[84:87], a[156:159]
s_addc_u32 s47, s65, 0
s_waitcnt lgkmcnt(10)
v_mfma_f32_16x16x32_bf16 a[160:163], v[48:51], v[72:75], a[160:163]
ds_read_b64_tr_b16 v[64:65], v16 offset:49408
ds_read_b64_tr_b16 v[66:67], v16 offset:51456
v_mfma_f32_16x16x32_bf16 a[164:167], v[48:51], v[76:79], a[164:167]
ds_read_b128 v[112:115], v27 offset:0
v_mfma_f32_16x16x32_bf16 a[168:171], v[48:51], v[80:83], a[168:171]
buffer_load_dwordx4 v38, s[12:15], s29 offen offset:3072 lds
s_sub_u32 s46, s46, 2048
v_mfma_f32_16x16x32_bf16 a[172:175], v[48:51], v[84:87], a[172:175]
s_subb_u32 s47, s47, 0
s_waitcnt lgkmcnt(10)
v_mfma_f32_16x16x32_bf16 a[176:179], v[52:55], v[72:75], a[176:179]
ds_read_b64_tr_b16 v[68:69], v17 offset:49408
ds_read_b64_tr_b16 v[70:71], v17 offset:51456
v_mfma_f32_16x16x32_bf16 a[180:183], v[52:55], v[76:79], a[180:183]
ds_read_b128 v[116:119], v27 offset:2048
v_mfma_f32_16x16x32_bf16 a[184:187], v[52:55], v[80:83], a[184:187]
s_add_u32 s25, s23, 2048
v_mfma_f32_16x16x32_bf16 a[188:191], v[52:55], v[84:87], a[188:191]
s_mov_b32 s80, s46
s_waitcnt vmcnt(20)
s_barrier
s_waitcnt lgkmcnt(10)
v_mfma_f32_16x16x32_bf16 a[192:195], v[56:59], v[72:75], a[192:195]
ds_read_b64_tr_b16 v[40:41], v18 offset:0
v_mfma_f32_16x16x32_bf16 a[196:199], v[56:59], v[76:79], a[196:199]
ds_read_b64_tr_b16 v[42:43], v18 offset:2048
v_mfma_f32_16x16x32_bf16 a[200:203], v[56:59], v[80:83], a[200:203]
s_and_b32 s81, s47, 65535
v_mfma_f32_16x16x32_bf16 a[204:207], v[56:59], v[84:87], a[204:207]
s_waitcnt lgkmcnt(9)
v_mfma_f32_16x16x32_bf16 a[208:211], v[60:63], v[72:75], a[208:211]
ds_read_b64_tr_b16 v[44:45], v19 offset:0
v_mfma_f32_16x16x32_bf16 a[212:215], v[60:63], v[76:79], a[212:215]
ds_read_b64_tr_b16 v[46:47], v19 offset:2048
v_mfma_f32_16x16x32_bf16 a[216:219], v[60:63], v[80:83], a[216:219]
s_mov_b32 s82, s25
v_mfma_f32_16x16x32_bf16 a[220:223], v[60:63], v[84:87], a[220:223]
s_mov_b32 s83, 131072
s_waitcnt lgkmcnt(8)
v_mfma_f32_16x16x32_bf16 a[224:227], v[64:67], v[72:75], a[224:227]
ds_read_b64_tr_b16 v[48:49], v20 offset:0
v_mfma_f32_16x16x32_bf16 a[228:231], v[64:67], v[76:79], a[228:231]
ds_read_b64_tr_b16 v[50:51], v20 offset:2048
v_mfma_f32_16x16x32_bf16 a[232:235], v[64:67], v[80:83], a[232:235]
s_add_u32 s46, s66, s22
v_mfma_f32_16x16x32_bf16 a[236:239], v[64:67], v[84:87], a[236:239]
s_addc_u32 s47, s67, 0
s_waitcnt lgkmcnt(7)
v_mfma_f32_16x16x32_bf16 a[240:243], v[68:71], v[72:75], a[240:243]
ds_read_b64_tr_b16 v[52:53], v21 offset:0
ds_read_b64_tr_b16 v[54:55], v21 offset:2048
v_mfma_f32_16x16x32_bf16 a[244:247], v[68:71], v[76:79], a[244:247]
s_sub_u32 s46, s46, 3072
v_mfma_f32_16x16x32_bf16 a[248:251], v[68:71], v[80:83], a[248:251]
s_subb_u32 s47, s47, 0
v_mfma_f32_16x16x32_bf16 a[252:255], v[68:71], v[84:87], a[252:255]
s_add_u32 s25, s23, 3072
s_waitcnt lgkmcnt(6)
v_mfma_f32_16x16x32_bf16 a[0:3], v[40:43], v[88:91], a[0:3]
ds_read_b64_tr_b16 v[56:57], v22 offset:0
ds_read_b64_tr_b16 v[58:59], v22 offset:2048
v_mfma_f32_16x16x32_bf16 a[4:7], v[40:43], v[92:95], a[4:7]
s_add_u32 s28, s37, 1835008
v_mfma_f32_16x16x32_bf16 a[8:11], v[40:43], v[96:99], a[8:11]
s_add_u32 m0, s34, 32768
s_nop 0
buffer_load_dwordx4 v4, s[8:11], s28 offen lds
v_mfma_f32_16x16x32_bf16 a[12:15], v[40:43], v[100:103], a[12:15]
s_mov_b32 s84, s46
s_waitcnt lgkmcnt(6)
v_mfma_f32_16x16x32_bf16 a[16:19], v[44:47], v[88:91], a[16:19]
ds_read_b64_tr_b16 v[60:61], v23 offset:0
v_mfma_f32_16x16x32_bf16 a[20:23], v[44:47], v[92:95], a[20:23]
ds_read_b64_tr_b16 v[62:63], v23 offset:2048
v_mfma_f32_16x16x32_bf16 a[24:27], v[44:47], v[96:99], a[24:27]
buffer_load_dwordx4 v5, s[8:11], s28 offen offset:1024 lds
v_mfma_f32_16x16x32_bf16 a[28:31], v[44:47], v[100:103], a[28:31]
s_and_b32 s85, s47, 65535
s_waitcnt lgkmcnt(6)
v_mfma_f32_16x16x32_bf16 a[32:35], v[48:51], v[88:91], a[32:35]
ds_read_b64_tr_b16 v[64:65], v24 offset:0
v_mfma_f32_16x16x32_bf16 a[36:39], v[48:51], v[92:95], a[36:39]
ds_read_b64_tr_b16 v[66:67], v24 offset:2048
v_mfma_f32_16x16x32_bf16 a[40:43], v[48:51], v[96:99], a[40:43]
s_mov_b32 s86, s25
v_mfma_f32_16x16x32_bf16 a[44:47], v[48:51], v[100:103], a[44:47]
s_mov_b32 s87, 131072
s_waitcnt lgkmcnt(6)
v_mfma_f32_16x16x32_bf16 a[48:51], v[52:55], v[88:91], a[48:51]
ds_read_b64_tr_b16 v[68:69], v25 offset:0
v_mfma_f32_16x16x32_bf16 a[52:55], v[52:55], v[92:95], a[52:55]
ds_read_b64_tr_b16 v[70:71], v25 offset:2048
v_mfma_f32_16x16x32_bf16 a[56:59], v[52:55], v[96:99], a[56:59]
buffer_load_dwordx4 v6, s[8:11], s28 offen offset:2048 lds
v_mfma_f32_16x16x32_bf16 a[60:63], v[52:55], v[100:103], a[60:63]
s_lshl_b32 s22, s50, 14
s_waitcnt lgkmcnt(6)
v_mfma_f32_16x16x32_bf16 a[64:67], v[56:59], v[88:91], a[64:67]
ds_read_b64_tr_b16 v[40:41], v18 offset:256
ds_read_b64_tr_b16 v[42:43], v18 offset:2304
v_mfma_f32_16x16x32_bf16 a[68:71], v[56:59], v[92:95], a[68:71]
buffer_load_dwordx4 v7, s[8:11], s28 offen offset:3072 lds
v_mfma_f32_16x16x32_bf16 a[72:75], v[56:59], v[96:99], a[72:75]
s_add_u32 s22, s22, 1048576
s_add_u32 s46, s48, s22
v_mfma_f32_16x16x32_bf16 a[76:79], v[56:59], v[100:103], a[76:79]
s_addc_u32 s47, s49, 0
s_waitcnt lgkmcnt(6)
v_mfma_f32_16x16x32_bf16 a[80:83], v[60:63], v[88:91], a[80:83]
ds_read_b64_tr_b16 v[44:45], v19 offset:256
v_mfma_f32_16x16x32_bf16 a[84:87], v[60:63], v[92:95], a[84:87]
ds_read_b64_tr_b16 v[46:47], v19 offset:2304
v_mfma_f32_16x16x32_bf16 a[88:91], v[60:63], v[96:99], a[88:91]
s_mov_b32 s52, s46
v_mfma_f32_16x16x32_bf16 a[92:95], v[60:63], v[100:103], a[92:95]
s_waitcnt lgkmcnt(6)
v_mfma_f32_16x16x32_bf16 a[96:99], v[64:67], v[88:91], a[96:99]
ds_read_b64_tr_b16 v[48:49], v20 offset:256
ds_read_b64_tr_b16 v[50:51], v20 offset:2304
v_mfma_f32_16x16x32_bf16 a[100:103], v[64:67], v[92:95], a[100:103]
s_mov_b32 s29, 33280
s_add_u32 m0, s35, 16384
v_mfma_f32_16x16x32_bf16 a[104:107], v[64:67], v[96:99], a[104:107]
buffer_load_dwordx4 v8, s[12:15], s29 offen lds
s_and_b32 s53, s47, 65535
v_mfma_f32_16x16x32_bf16 a[108:111], v[64:67], v[100:103], a[108:111]
s_mov_b32 s54, 114688
s_waitcnt lgkmcnt(6)
v_mfma_f32_16x16x32_bf16 a[112:115], v[68:71], v[88:91], a[112:115]
ds_read_b64_tr_b16 v[52:53], v21 offset:256
v_mfma_f32_16x16x32_bf16 a[116:119], v[68:71], v[92:95], a[116:119]
ds_read_b64_tr_b16 v[54:55], v21 offset:2304
v_mfma_f32_16x16x32_bf16 a[120:123], v[68:71], v[96:99], a[120:123]
buffer_load_dwordx4 v9, s[12:15], s29 offen offset:1024 lds
v_mfma_f32_16x16x32_bf16 a[124:127], v[68:71], v[100:103], a[124:127]
s_mov_b32 s55, 131072
s_waitcnt lgkmcnt(6)
v_mfma_f32_16x16x32_bf16 a[128:131], v[40:43], v[88:91], a[128:131]
ds_read_b64_tr_b16 v[56:57], v22 offset:256
v_mfma_f32_16x16x32_bf16 a[132:135], v[40:43], v[92:95], a[132:135]
ds_read_b64_tr_b16 v[58:59], v22 offset:2304
v_mfma_f32_16x16x32_bf16 a[136:139], v[40:43], v[96:99], a[136:139]
s_mul_i32 s22, s50, 184
v_mfma_f32_16x16x32_bf16 a[140:143], v[40:43], v[100:103], a[140:143]
s_lshl_b32 s23, s24, 1
s_waitcnt lgkmcnt(6)
v_mfma_f32_16x16x32_bf16 a[144:147], v[44:47], v[88:91], a[144:147]
ds_read_b64_tr_b16 v[60:61], v23 offset:256
v_mfma_f32_16x16x32_bf16 a[148:151], v[44:47], v[92:95], a[148:151]
ds_read_b64_tr_b16 v[62:63], v23 offset:2304
v_mfma_f32_16x16x32_bf16 a[152:155], v[44:47], v[96:99], a[152:155]
buffer_load_dwordx4 v37, s[12:15], s29 offen offset:2048 lds
v_mfma_f32_16x16x32_bf16 a[156:159], v[44:47], v[100:103], a[156:159]
s_add_u32 s22, s22, s23
s_waitcnt lgkmcnt(6)
v_mfma_f32_16x16x32_bf16 a[160:163], v[48:51], v[88:91], a[160:163]
ds_read_b64_tr_b16 v[64:65], v24 offset:256
v_mfma_f32_16x16x32_bf16 a[164:167], v[48:51], v[92:95], a[164:167]
ds_read_b64_tr_b16 v[66:67], v24 offset:2304
v_mfma_f32_16x16x32_bf16 a[168:171], v[48:51], v[96:99], a[168:171]
buffer_load_dwordx4 v38, s[12:15], s29 offen offset:3072 lds
v_mfma_f32_16x16x32_bf16 a[172:175], v[48:51], v[100:103], a[172:175]
s_add_u32 s22, s22, 128
s_waitcnt lgkmcnt(6)
v_mfma_f32_16x16x32_bf16 a[176:179], v[52:55], v[88:91], a[176:179]
ds_read_b64_tr_b16 v[68:69], v25 offset:256
v_mfma_f32_16x16x32_bf16 a[180:183], v[52:55], v[92:95], a[180:183]
ds_read_b64_tr_b16 v[70:71], v25 offset:2304
v_mfma_f32_16x16x32_bf16 a[184:187], v[52:55], v[96:99], a[184:187]
s_lshl_b32 s22, s22, 10
v_mfma_f32_16x16x32_bf16 a[188:191], v[52:55], v[100:103], a[188:191]
s_add_u32 s22, s22, 38797312
s_waitcnt vmcnt(20)
s_barrier
s_waitcnt lgkmcnt(6)
v_mfma_f32_16x16x32_bf16 a[192:195], v[56:59], v[88:91], a[192:195]
ds_read_b64_tr_b16 v[40:41], v10 offset:0
v_mfma_f32_16x16x32_bf16 a[196:199], v[56:59], v[92:95], a[196:199]
ds_read_b64_tr_b16 v[42:43], v10 offset:2048
v_mfma_f32_16x16x32_bf16 a[200:203], v[56:59], v[96:99], a[200:203]
s_add_u32 s46, s48, s22
v_mfma_f32_16x16x32_bf16 a[204:207], v[56:59], v[100:103], a[204:207]
s_addc_u32 s47, s49, 0
s_waitcnt lgkmcnt(6)
v_mfma_f32_16x16x32_bf16 a[208:211], v[60:63], v[88:91], a[208:211]
ds_read_b64_tr_b16 v[44:45], v11 offset:0
v_mfma_f32_16x16x32_bf16 a[212:215], v[60:63], v[92:95], a[212:215]
ds_read_b64_tr_b16 v[46:47], v11 offset:2048
v_mfma_f32_16x16x32_bf16 a[216:219], v[60:63], v[96:99], a[216:219]
s_mov_b32 s88, s46
v_mfma_f32_16x16x32_bf16 a[220:223], v[60:63], v[100:103], a[220:223]
s_and_b32 s89, s47, 65535
s_waitcnt lgkmcnt(6)
v_mfma_f32_16x16x32_bf16 a[224:227], v[64:67], v[88:91], a[224:227]
ds_read_b64_tr_b16 v[48:49], v12 offset:0
v_mfma_f32_16x16x32_bf16 a[228:231], v[64:67], v[92:95], a[228:231]
ds_read_b64_tr_b16 v[50:51], v12 offset:2048
v_mfma_f32_16x16x32_bf16 a[232:235], v[64:67], v[96:99], a[232:235]
s_mov_b32 s90, 1318912
v_mfma_f32_16x16x32_bf16 a[236:239], v[64:67], v[100:103], a[236:239]
s_waitcnt lgkmcnt(6)
v_mfma_f32_16x16x32_bf16 a[240:243], v[68:71], v[88:91], a[240:243]
ds_read_b64_tr_b16 v[52:53], v13 offset:0
v_mfma_f32_16x16x32_bf16 a[244:247], v[68:71], v[92:95], a[244:247]
ds_read_b64_tr_b16 v[54:55], v13 offset:2048
v_mfma_f32_16x16x32_bf16 a[248:251], v[68:71], v[96:99], a[248:251]
s_mov_b32 s91, 131072
v_mfma_f32_16x16x32_bf16 a[252:255], v[68:71], v[100:103], a[252:255]
s_lshl_b32 s70, s7, 12
s_waitcnt lgkmcnt(6)
v_mfma_f32_16x16x32_bf16 a[0:3], v[40:43], v[104:107], a[0:3]
ds_read_b64_tr_b16 v[56:57], v14 offset:0
ds_read_b64_tr_b16 v[58:59], v14 offset:2048
v_mfma_f32_16x16x32_bf16 a[4:7], v[40:43], v[108:111], a[4:7]
s_add_u32 s28, s37, 2097152
v_mfma_f32_16x16x32_bf16 a[8:11], v[40:43], v[112:115], a[8:11]
s_add_u32 m0, s34, 49152
s_nop 0
buffer_load_dwordx4 v4, s[8:11], s28 offen lds
v_mfma_f32_16x16x32_bf16 a[12:15], v[40:43], v[116:119], a[12:15]
s_add_u32 s70, s70, 16384
s_waitcnt lgkmcnt(6)
v_mfma_f32_16x16x32_bf16 a[16:19], v[44:47], v[104:107], a[16:19]
ds_read_b64_tr_b16 v[60:61], v15 offset:0
v_mfma_f32_16x16x32_bf16 a[20:23], v[44:47], v[108:111], a[20:23]
ds_read_b64_tr_b16 v[62:63], v15 offset:2048
v_mfma_f32_16x16x32_bf16 a[24:27], v[44:47], v[112:115], a[24:27]
buffer_load_dwordx4 v5, s[8:11], s28 offen offset:1024 lds
v_mfma_f32_16x16x32_bf16 a[28:31], v[44:47], v[116:119], a[28:31]
s_lshl_b32 s71, s7, 16
s_waitcnt lgkmcnt(6)
v_mfma_f32_16x16x32_bf16 a[32:35], v[48:51], v[104:107], a[32:35]
ds_read_b64_tr_b16 v[64:65], v16 offset:0
v_mfma_f32_16x16x32_bf16 a[36:39], v[48:51], v[108:111], a[36:39]
ds_read_b64_tr_b16 v[66:67], v16 offset:2048
v_mfma_f32_16x16x32_bf16 a[40:43], v[48:51], v[112:115], a[40:43]
s_mul_i32 s73, s7, 28672
v_mfma_f32_16x16x32_bf16 a[44:47], v[48:51], v[116:119], a[44:47]
s_lshr_b32 s22, s6, 3
s_waitcnt lgkmcnt(6)
v_mfma_f32_16x16x32_bf16 a[48:51], v[52:55], v[104:107], a[48:51]
ds_read_b64_tr_b16 v[68:69], v17 offset:0
ds_read_b64_tr_b16 v[70:71], v17 offset:2048
v_mfma_f32_16x16x32_bf16 a[52:55], v[52:55], v[108:111], a[52:55]
s_waitcnt vmcnt(18)
v_mfma_f32_16x16x32_bf16 a[56:59], v[52:55], v[112:115], a[56:59]
buffer_load_dwordx4 v6, s[8:11], s28 offen offset:2048 lds
v_mfma_f32_16x16x32_bf16 a[60:63], v[52:55], v[116:119], a[60:63]
s_lshl_b32 s22, s22, 2
s_waitcnt lgkmcnt(6)
v_mfma_f32_16x16x32_bf16 a[64:67], v[56:59], v[104:107], a[64:67]
ds_read_b64_tr_b16 v[40:41], v10 offset:256
ds_read_b64_tr_b16 v[42:43], v10 offset:2304
v_mfma_f32_16x16x32_bf16 a[68:71], v[56:59], v[108:111], a[68:71]
ds_read_b128 v[72:75], v26 offset:4096
v_mfma_f32_16x16x32_bf16 a[72:75], v[56:59], v[112:115], a[72:75]
buffer_load_dwordx4 v7, s[8:11], s28 offen offset:3072 lds
s_add_u32 s22, s22, s7
v_mfma_f32_16x16x32_bf16 a[76:79], v[56:59], v[116:119], a[76:79]
s_and_b32 s23, s6, 7
s_waitcnt lgkmcnt(7)
v_mfma_f32_16x16x32_bf16 a[80:83], v[60:63], v[104:107], a[80:83]
ds_read_b64_tr_b16 v[44:45], v11 offset:256
v_mfma_f32_16x16x32_bf16 a[84:87], v[60:63], v[108:111], a[84:87]
ds_read_b64_tr_b16 v[46:47], v11 offset:2304
v_mfma_f32_16x16x32_bf16 a[88:91], v[60:63], v[112:115], a[88:91]
ds_read_b128 v[76:79], v26 offset:6144
v_mfma_f32_16x16x32_bf16 a[92:95], v[60:63], v[116:119], a[92:95]
v_lshrrev_b32 v121, 3, v1
s_waitcnt lgkmcnt(8)
v_mfma_f32_16x16x32_bf16 a[96:99], v[64:67], v[104:107], a[96:99]
ds_read_b64_tr_b16 v[48:49], v12 offset:256
ds_read_b64_tr_b16 v[50:51], v12 offset:2304
v_mfma_f32_16x16x32_bf16 a[100:103], v[64:67], v[108:111], a[100:103]
ds_read_b128 v[80:83], v26 offset:8192
s_mov_b32 s29, 640
v_mfma_f32_16x16x32_bf16 a[104:107], v[64:67], v[112:115], a[104:107]
s_add_u32 m0, s35, 0
s_nop 0
buffer_load_dwordx4 v8, s[12:15], s29 offen lds
v_mfma_f32_16x16x32_bf16 a[108:111], v[64:67], v[116:119], a[108:111]
v_and_b32 v121, 1, v121
v_lshlrev_b32 v121, 13, v121
s_waitcnt lgkmcnt(9)
v_mfma_f32_16x16x32_bf16 a[112:115], v[68:71], v[104:107], a[112:115]
ds_read_b64_tr_b16 v[52:53], v13 offset:256
ds_read_b64_tr_b16 v[54:55], v13 offset:2304
v_mfma_f32_16x16x32_bf16 a[116:119], v[68:71], v[108:111], a[116:119]
ds_read_b128 v[84:87], v26 offset:10240
v_mfma_f32_16x16x32_bf16 a[120:123], v[68:71], v[112:115], a[120:123]
buffer_load_dwordx4 v9, s[12:15], s29 offen offset:1024 lds
v_mfma_f32_16x16x32_bf16 a[124:127], v[68:71], v[116:119], a[124:127]
v_and_b32 v128, 7, v1
s_waitcnt lgkmcnt(10)
v_mfma_f32_16x16x32_bf16 a[128:131], v[40:43], v[104:107], a[128:131]
ds_read_b64_tr_b16 v[56:57], v14 offset:256
v_mfma_f32_16x16x32_bf16 a[132:135], v[40:43], v[108:111], a[132:135]
ds_read_b64_tr_b16 v[58:59], v14 offset:2304
v_mfma_f32_16x16x32_bf16 a[136:139], v[40:43], v[112:115], a[136:139]
ds_read_b128 v[88:91], v27 offset:4096
v_mfma_f32_16x16x32_bf16 a[140:143], v[40:43], v[116:119], a[140:143]
v_lshlrev_b32 v128, 7, v128
s_waitcnt lgkmcnt(10)
v_mfma_f32_16x16x32_bf16 a[144:147], v[44:47], v[104:107], a[144:147]
ds_read_b64_tr_b16 v[60:61], v15 offset:256
ds_read_b64_tr_b16 v[62:63], v15 offset:2304
v_mfma_f32_16x16x32_bf16 a[148:151], v[44:47], v[108:111], a[148:151]
ds_read_b128 v[92:95], v27 offset:6144
v_mfma_f32_16x16x32_bf16 a[152:155], v[44:47], v[112:115], a[152:155]
buffer_load_dwordx4 v37, s[12:15], s29 offen offset:2048 lds
v_add_u32 v121, v121, v128
v_mfma_f32_16x16x32_bf16 a[156:159], v[44:47], v[116:119], a[156:159]
s_lshl_b32 s24, s23, 10
s_waitcnt lgkmcnt(10)
v_mfma_f32_16x16x32_bf16 a[160:163], v[48:51], v[104:107], a[160:163]
ds_read_b64_tr_b16 v[64:65], v16 offset:256
ds_read_b64_tr_b16 v[66:67], v16 offset:2304
v_mfma_f32_16x16x32_bf16 a[164:167], v[48:51], v[108:111], a[164:167]
ds_read_b128 v[96:99], v27 offset:8192
v_mfma_f32_16x16x32_bf16 a[168:171], v[48:51], v[112:115], a[168:171]
buffer_load_dwordx4 v38, s[12:15], s29 offen offset:3072 lds
v_mfma_f32_16x16x32_bf16 a[172:175], v[48:51], v[116:119], a[172:175]
s_lshl_b32 s25, s22, 15
s_waitcnt lgkmcnt(10)
v_mfma_f32_16x16x32_bf16 a[176:179], v[52:55], v[104:107], a[176:179]
ds_read_b64_tr_b16 v[68:69], v17 offset:256
ds_read_b64_tr_b16 v[70:71], v17 offset:2304
v_mfma_f32_16x16x32_bf16 a[180:183], v[52:55], v[108:111], a[180:183]
ds_read_b128 v[100:103], v27 offset:10240
v_mfma_f32_16x16x32_bf16 a[184:187], v[52:55], v[112:115], a[184:187]
s_add_u32 s24, s24, s25
v_mfma_f32_16x16x32_bf16 a[188:191], v[52:55], v[116:119], a[188:191]
v_add_u32 v121, s24, v121
s_waitcnt vmcnt(20)
s_barrier
s_waitcnt lgkmcnt(10)
v_mfma_f32_16x16x32_bf16 a[192:195], v[56:59], v[104:107], a[192:195]
ds_read_b64_tr_b16 v[40:41], v10 offset:16384
v_mfma_f32_16x16x32_bf16 a[196:199], v[56:59], v[108:111], a[196:199]
ds_read_b64_tr_b16 v[42:43], v10 offset:18432
v_mfma_f32_16x16x32_bf16 a[200:203], v[56:59], v[112:115], a[200:203]
v_add_u32 v124, 16384, v121
v_mfma_f32_16x16x32_bf16 a[204:207], v[56:59], v[116:119], a[204:207]
s_waitcnt lgkmcnt(9)
v_mfma_f32_16x16x32_bf16 a[208:211], v[60:63], v[104:107], a[208:211]
ds_read_b64_tr_b16 v[44:45], v11 offset:16384
v_mfma_f32_16x16x32_bf16 a[212:215], v[60:63], v[108:111], a[212:215]
ds_read_b64_tr_b16 v[46:47], v11 offset:18432
v_mfma_f32_16x16x32_bf16 a[216:219], v[60:63], v[112:115], a[216:219]
s_lshl_b32 s24, s23, 2
v_mfma_f32_16x16x32_bf16 a[220:223], v[60:63], v[116:119], a[220:223]
s_add_u32 s25, s24, 4
s_waitcnt lgkmcnt(8)
v_mfma_f32_16x16x32_bf16 a[224:227], v[64:67], v[104:107], a[224:227]
ds_read_b64_tr_b16 v[48:49], v12 offset:16384
v_mfma_f32_16x16x32_bf16 a[228:231], v[64:67], v[108:111], a[228:231]
ds_read_b64_tr_b16 v[50:51], v12 offset:18432
v_mfma_f32_16x16x32_bf16 a[232:235], v[64:67], v[112:115], a[232:235]
s_lshr_b32 s25, s25, 3
v_mfma_f32_16x16x32_bf16 a[236:239], v[64:67], v[116:119], a[236:239]
s_waitcnt lgkmcnt(7)
v_mfma_f32_16x16x32_bf16 a[240:243], v[68:71], v[104:107], a[240:243]
ds_read_b64_tr_b16 v[52:53], v13 offset:16384
v_mfma_f32_16x16x32_bf16 a[244:247], v[68:71], v[108:111], a[244:247]
ds_read_b64_tr_b16 v[54:55], v13 offset:18432
v_mfma_f32_16x16x32_bf16 a[248:251], v[68:71], v[112:115], a[248:251]
s_sub_u32 s24, s24, s25
v_mfma_f32_16x16x32_bf16 a[252:255], v[68:71], v[116:119], a[252:255]
v_and_b32 v128, 3, v1
s_waitcnt lgkmcnt(6)
v_mfma_f32_16x16x32_bf16 a[0:3], v[40:43], v[72:75], a[0:3]
ds_read_b64_tr_b16 v[56:57], v14 offset:16384
ds_read_b64_tr_b16 v[58:59], v14 offset:18432
v_mfma_f32_16x16x32_bf16 a[4:7], v[40:43], v[76:79], a[4:7]
s_add_u32 s28, s37, 2359296
v_mfma_f32_16x16x32_bf16 a[8:11], v[40:43], v[80:83], a[8:11]
s_add_u32 m0, s34, 65536
s_nop 0
buffer_load_dwordx4 v4, s[8:11], s28 offen lds
v_mfma_f32_16x16x32_bf16 a[12:15], v[40:43], v[84:87], a[12:15]
v_add_u32 v128, s24, v128
s_waitcnt lgkmcnt(6)
v_mfma_f32_16x16x32_bf16 a[16:19], v[44:47], v[72:75], a[16:19]
ds_read_b64_tr_b16 v[60:61], v15 offset:16384
v_mfma_f32_16x16x32_bf16 a[20:23], v[44:47], v[76:79], a[20:23]
ds_read_b64_tr_b16 v[62:63], v15 offset:18432
v_mfma_f32_16x16x32_bf16 a[24:27], v[44:47], v[80:83], a[24:27]
buffer_load_dwordx4 v5, s[8:11], s28 offen offset:1024 lds
v_mfma_f32_16x16x32_bf16 a[28:31], v[44:47], v[84:87], a[28:31]
v_min_u32 v128, 27, v128
s_waitcnt lgkmcnt(6)
v_mfma_f32_16x16x32_bf16 a[32:35], v[48:51], v[72:75], a[32:35]
ds_read_b64_tr_b16 v[64:65], v16 offset:16384
v_mfma_f32_16x16x32_bf16 a[36:39], v[48:51], v[76:79], a[36:39]
ds_read_b64_tr_b16 v[66:67], v16 offset:18432
v_mfma_f32_16x16x32_bf16 a[40:43], v[48:51], v[80:83], a[40:43]
v_lshlrev_b32 v128, 7, v128
v_mfma_f32_16x16x32_bf16 a[44:47], v[48:51], v[84:87], a[44:47]
v_lshrrev_b32 v125, 2, v1
s_waitcnt lgkmcnt(6)
v_mfma_f32_16x16x32_bf16 a[48:51], v[52:55], v[72:75], a[48:51]
ds_read_b64_tr_b16 v[68:69], v17 offset:16384
v_mfma_f32_16x16x32_bf16 a[52:55], v[52:55], v[76:79], a[52:55]
ds_read_b64_tr_b16 v[70:71], v17 offset:18432
v_mfma_f32_16x16x32_bf16 a[56:59], v[52:55], v[80:83], a[56:59]
buffer_load_dwordx4 v6, s[8:11], s28 offen offset:2048 lds
v_mfma_f32_16x16x32_bf16 a[60:63], v[52:55], v[84:87], a[60:63]
v_and_b32 v125, 3, v125
s_waitcnt lgkmcnt(6)
v_mfma_f32_16x16x32_bf16 a[64:67], v[56:59], v[72:75], a[64:67]
ds_read_b64_tr_b16 v[40:41], v10 offset:16640
ds_read_b64_tr_b16 v[42:43], v10 offset:18688
v_mfma_f32_16x16x32_bf16 a[68:71], v[56:59], v[76:79], a[68:71]
buffer_load_dwordx4 v7, s[8:11], s28 offen offset:3072 lds
v_mfma_f32_16x16x32_bf16 a[72:75], v[56:59], v[80:83], a[72:75]
v_mul_u32_u24 v125, 3584, v125
v_mfma_f32_16x16x32_bf16 a[76:79], v[56:59], v[84:87], a[76:79]
v_add_u32 v125, v125, v128
s_waitcnt lgkmcnt(6)
v_mfma_f32_16x16x32_bf16 a[80:83], v[60:63], v[72:75], a[80:83]
ds_read_b64_tr_b16 v[44:45], v11 offset:16640
v_mfma_f32_16x16x32_bf16 a[84:87], v[60:63], v[76:79], a[84:87]
ds_read_b64_tr_b16 v[46:47], v11 offset:18688
v_mfma_f32_16x16x32_bf16 a[88:91], v[60:63], v[80:83], a[88:91]
s_mul_i32 s25, s22, 14336
v_mfma_f32_16x16x32_bf16 a[92:95], v[60:63], v[84:87], a[92:95]
s_waitcnt lgkmcnt(6)
v_mfma_f32_16x16x32_bf16 a[96:99], v[64:67], v[72:75], a[96:99]
ds_read_b64_tr_b16 v[48:49], v12 offset:16640
ds_read_b64_tr_b16 v[50:51], v12 offset:18688
v_mfma_f32_16x16x32_bf16 a[100:103], v[64:67], v[76:79], a[100:103]
s_mov_b32 s29, 33408
s_add_u32 m0, s35, 4096
v_mfma_f32_16x16x32_bf16 a[104:107], v[64:67], v[80:83], a[104:107]
buffer_load_dwordx4 v8, s[12:15], s29 offen lds
v_add_u32 v125, s25, v125
v_mfma_f32_16x16x32_bf16 a[108:111], v[64:67], v[84:87], a[108:111]
v_and_b32 v120, 31, v1
s_waitcnt lgkmcnt(6)
v_mfma_f32_16x16x32_bf16 a[112:115], v[68:71], v[72:75], a[112:115]
ds_read_b64_tr_b16 v[52:53], v13 offset:16640
v_mfma_f32_16x16x32_bf16 a[116:119], v[68:71], v[76:79], a[116:119]
ds_read_b64_tr_b16 v[54:55], v13 offset:18688
v_mfma_f32_16x16x32_bf16 a[120:123], v[68:71], v[80:83], a[120:123]
buffer_load_dwordx4 v9, s[12:15], s29 offen offset:1024 lds
v_mfma_f32_16x16x32_bf16 a[124:127], v[68:71], v[84:87], a[124:127]
v_lshlrev_b32 v120, 6, v120
s_waitcnt lgkmcnt(6)
v_mfma_f32_16x16x32_bf16 a[128:131], v[40:43], v[72:75], a[128:131]
ds_read_b64_tr_b16 v[56:57], v14 offset:16640
v_mfma_f32_16x16x32_bf16 a[132:135], v[40:43], v[76:79], a[132:135]
ds_read_b64_tr_b16 v[58:59], v14 offset:18688
v_mfma_f32_16x16x32_bf16 a[136:139], v[40:43], v[80:83], a[136:139]
v_add_u32 v120, 16384, v120
v_mfma_f32_16x16x32_bf16 a[140:143], v[40:43], v[84:87], a[140:143]
s_waitcnt lgkmcnt(6)
v_mfma_f32_16x16x32_bf16 a[144:147], v[44:47], v[72:75], a[144:147]
ds_read_b64_tr_b16 v[60:61], v15 offset:16640
ds_read_b64_tr_b16 v[62:63], v15 offset:18688
v_mfma_f32_16x16x32_bf16 a[148:151], v[44:47], v[76:79], a[148:151]
buffer_load_dwordx4 v37, s[12:15], s29 offen offset:2048 lds
s_getpc_b64 s[46:47]
v_mfma_f32_16x16x32_bf16 a[152:155], v[44:47], v[80:83], a[152:155]
L_CP_AK:
s_add_u32 s46, s46, L_A_END-L_CP_AK
v_mfma_f32_16x16x32_bf16 a[156:159], v[44:47], v[84:87], a[156:159]
s_addc_u32 s47, s47, 0
s_lshr_b32 s22, s6, 3
s_waitcnt lgkmcnt(6)
v_mfma_f32_16x16x32_bf16 a[160:163], v[48:51], v[72:75], a[160:163]
ds_read_b64_tr_b16 v[64:65], v16 offset:16640
v_mfma_f32_16x16x32_bf16 a[164:167], v[48:51], v[76:79], a[164:167]
ds_read_b64_tr_b16 v[66:67], v16 offset:18688
v_mfma_f32_16x16x32_bf16 a[168:171], v[48:51], v[80:83], a[168:171]
buffer_load_dwordx4 v38, s[12:15], s29 offen offset:3072 lds
v_mfma_f32_16x16x32_bf16 a[172:175], v[48:51], v[84:87], a[172:175]
s_lshl_b32 s22, s22, 2
s_waitcnt lgkmcnt(6)
v_mfma_f32_16x16x32_bf16 a[176:179], v[52:55], v[72:75], a[176:179]
ds_read_b64_tr_b16 v[68:69], v17 offset:16640
v_mfma_f32_16x16x32_bf16 a[180:183], v[52:55], v[76:79], a[180:183]
ds_read_b64_tr_b16 v[70:71], v17 offset:18688
v_mfma_f32_16x16x32_bf16 a[184:187], v[52:55], v[80:83], a[184:187]
s_add_u32 s22, s22, s7
v_mfma_f32_16x16x32_bf16 a[188:191], v[52:55], v[84:87], a[188:191]
s_mul_i32 s22, s22, 256
s_waitcnt vmcnt(20)
s_barrier
s_waitcnt lgkmcnt(6)
v_mfma_f32_16x16x32_bf16 a[192:195], v[56:59], v[72:75], a[192:195]
ds_read_b64_tr_b16 v[40:41], v10 offset:32768
v_mfma_f32_16x16x32_bf16 a[196:199], v[56:59], v[76:79], a[196:199]
ds_read_b64_tr_b16 v[42:43], v10 offset:34816
v_mfma_f32_16x16x32_bf16 a[200:203], v[56:59], v[80:83], a[200:203]
v_min_u32 v126, 1, v1
v_mfma_f32_16x16x32_bf16 a[204:207], v[56:59], v[84:87], a[204:207]
s_waitcnt lgkmcnt(6)
v_mfma_f32_16x16x32_bf16 a[208:211], v[60:63], v[72:75], a[208:211]
ds_read_b64_tr_b16 v[44:45], v11 offset:32768
v_mfma_f32_16x16x32_bf16 a[212:215], v[60:63], v[76:79], a[212:215]
ds_read_b64_tr_b16 v[46:47], v11 offset:34816
v_mfma_f32_16x16x32_bf16 a[216:219], v[60:63], v[80:83], a[216:219]
v_lshlrev_b32 v126, 7, v126
v_mfma_f32_16x16x32_bf16 a[220:223], v[60:63], v[84:87], a[220:223]
v_add_u32 v126, s22, v126
s_waitcnt lgkmcnt(6)
v_mfma_f32_16x16x32_bf16 a[224:227], v[64:67], v[72:75], a[224:227]
ds_read_b64_tr_b16 v[48:49], v12 offset:32768
v_mfma_f32_16x16x32_bf16 a[228:231], v[64:67], v[76:79], a[228:231]
ds_read_b64_tr_b16 v[50:51], v12 offset:34816
v_mfma_f32_16x16x32_bf16 a[232:235], v[64:67], v[80:83], a[232:235]
v_min_u32 v126, L_CODE_END-L_A_END-4, v126
v_mfma_f32_16x16x32_bf16 a[236:239], v[64:67], v[84:87], a[236:239]
s_waitcnt lgkmcnt(6)
v_mfma_f32_16x16x32_bf16 a[240:243], v[68:71], v[72:75], a[240:243]
ds_read_b64_tr_b16 v[52:53], v13 offset:32768
v_mfma_f32_16x16x32_bf16 a[244:247], v[68:71], v[76:79], a[244:247]
ds_read_b64_tr_b16 v[54:55], v13 offset:34816
v_mfma_f32_16x16x32_bf16 a[248:251], v[68:71], v[80:83], a[248:251]
global_load_dword v127, v126, s[46:47]
v_mfma_f32_16x16x32_bf16 a[252:255], v[68:71], v[84:87], a[252:255]
s_waitcnt lgkmcnt(6)
v_mfma_f32_16x16x32_bf16 a[0:3], v[40:43], v[88:91], a[0:3]
ds_read_b64_tr_b16 v[56:57], v14 offset:32768
ds_read_b64_tr_b16 v[58:59], v14 offset:34816
v_mfma_f32_16x16x32_bf16 a[4:7], v[40:43], v[92:95], a[4:7]
s_add_u32 s28, s37, 2621440
v_mfma_f32_16x16x32_bf16 a[8:11], v[40:43], v[96:99], a[8:11]
s_add_u32 m0, s34, 0
v_mfma_f32_16x16x32_bf16 a[12:15], v[40:43], v[100:103], a[12:15]
buffer_load_dwordx4 v4, s[8:11], s28 offen lds
s_waitcnt lgkmcnt(6)
v_mfma_f32_16x16x32_bf16 a[16:19], v[44:47], v[88:91], a[16:19]
ds_read_b64_tr_b16 v[60:61], v15 offset:32768
v_mfma_f32_16x16x32_bf16 a[20:23], v[44:47], v[92:95], a[20:23]
ds_read_b64_tr_b16 v[62:63], v15 offset:34816
v_mfma_f32_16x16x32_bf16 a[24:27], v[44:47], v[96:99], a[24:27]
buffer_load_dwordx4 v5, s[8:11], s28 offen offset:1024 lds
v_mfma_f32_16x16x32_bf16 a[28:31], v[44:47], v[100:103], a[28:31]
s_waitcnt lgkmcnt(6)
v_mfma_f32_16x16x32_bf16 a[32:35], v[48:51], v[88:91], a[32:35]
)ASM"
R"ASM(ds_read_b64_tr_b16 v[64:65], v16 offset:32768
v_mfma_f32_16x16x32_bf16 a[36:39], v[48:51], v[92:95], a[36:39]
v_mfma_f32_16x16x32_bf16 a[40:43], v[48:51], v[96:99], a[40:43]
ds_read_b64_tr_b16 v[66:67], v16 offset:34816
v_mfma_f32_16x16x32_bf16 a[44:47], v[48:51], v[100:103], a[44:47]
s_waitcnt lgkmcnt(6)
v_mfma_f32_16x16x32_bf16 a[48:51], v[52:55], v[88:91], a[48:51]
ds_read_b64_tr_b16 v[68:69], v17 offset:32768
v_mfma_f32_16x16x32_bf16 a[52:55], v[52:55], v[92:95], a[52:55]
ds_read_b64_tr_b16 v[70:71], v17 offset:34816
v_mfma_f32_16x16x32_bf16 a[56:59], v[52:55], v[96:99], a[56:59]
s_waitcnt vmcnt(19)
v_mfma_f32_16x16x32_bf16 a[60:63], v[52:55], v[100:103], a[60:63]
buffer_load_dwordx4 v6, s[8:11], s28 offen offset:2048 lds
s_waitcnt lgkmcnt(6)
v_mfma_f32_16x16x32_bf16 a[64:67], v[56:59], v[88:91], a[64:67]
ds_read_b64_tr_b16 v[40:41], v10 offset:33024
v_mfma_f32_16x16x32_bf16 a[68:71], v[56:59], v[92:95], a[68:71]
ds_read_b64_tr_b16 v[42:43], v10 offset:35072
v_mfma_f32_16x16x32_bf16 a[72:75], v[56:59], v[96:99], a[72:75]
ds_read_b128 v[104:107], v26 offset:12288
v_mfma_f32_16x16x32_bf16 a[76:79], v[56:59], v[100:103], a[76:79]
buffer_load_dwordx4 v7, s[8:11], s28 offen offset:3072 lds
s_waitcnt lgkmcnt(7)
v_mfma_f32_16x16x32_bf16 a[80:83], v[60:63], v[88:91], a[80:83]
ds_read_b64_tr_b16 v[44:45], v11 offset:33024
v_mfma_f32_16x16x32_bf16 a[84:87], v[60:63], v[92:95], a[84:87]
ds_read_b64_tr_b16 v[46:47], v11 offset:35072
v_mfma_f32_16x16x32_bf16 a[88:91], v[60:63], v[96:99], a[88:91]
ds_read_b128 v[108:111], v26 offset:14336
v_mfma_f32_16x16x32_bf16 a[92:95], v[60:63], v[100:103], a[92:95]
s_waitcnt lgkmcnt(8)
v_mfma_f32_16x16x32_bf16 a[96:99], v[64:67], v[88:91], a[96:99]
ds_read_b64_tr_b16 v[48:49], v12 offset:33024
ds_read_b64_tr_b16 v[50:51], v12 offset:35072
v_mfma_f32_16x16x32_bf16 a[100:103], v[64:67], v[92:95], a[100:103]
ds_read_b128 v[112:115], v26 offset:16384
v_mfma_f32_16x16x32_bf16 a[104:107], v[64:67], v[96:99], a[104:107]
s_mov_b32 s29, 768
s_add_u32 m0, s35, 8192
v_mfma_f32_16x16x32_bf16 a[108:111], v[64:67], v[100:103], a[108:111]
buffer_load_dwordx4 v8, s[12:15], s29 offen lds
s_waitcnt lgkmcnt(9)
v_mfma_f32_16x16x32_bf16 a[112:115], v[68:71], v[88:91], a[112:115]
ds_read_b64_tr_b16 v[52:53], v13 offset:33024
v_mfma_f32_16x16x32_bf16 a[116:119], v[68:71], v[92:95], a[116:119]
ds_read_b64_tr_b16 v[54:55], v13 offset:35072
v_mfma_f32_16x16x32_bf16 a[120:123], v[68:71], v[96:99], a[120:123]
ds_read_b128 v[116:119], v26 offset:18432
v_mfma_f32_16x16x32_bf16 a[124:127], v[68:71], v[100:103], a[124:127]
buffer_load_dwordx4 v9, s[12:15], s29 offen offset:1024 lds
s_waitcnt lgkmcnt(10)
v_mfma_f32_16x16x32_bf16 a[128:131], v[40:43], v[88:91], a[128:131]
ds_read_b64_tr_b16 v[56:57], v14 offset:33024
v_mfma_f32_16x16x32_bf16 a[132:135], v[40:43], v[92:95], a[132:135]
ds_read_b64_tr_b16 v[58:59], v14 offset:35072
v_mfma_f32_16x16x32_bf16 a[136:139], v[40:43], v[96:99], a[136:139]
ds_read_b128 v[72:75], v27 offset:12288
v_mfma_f32_16x16x32_bf16 a[140:143], v[40:43], v[100:103], a[140:143]
s_waitcnt lgkmcnt(10)
v_mfma_f32_16x16x32_bf16 a[144:147], v[44:47], v[88:91], a[144:147]
ds_read_b64_tr_b16 v[60:61], v15 offset:33024
v_mfma_f32_16x16x32_bf16 a[148:151], v[44:47], v[92:95], a[148:151]
ds_read_b64_tr_b16 v[62:63], v15 offset:35072
v_mfma_f32_16x16x32_bf16 a[152:155], v[44:47], v[96:99], a[152:155]
ds_read_b128 v[76:79], v27 offset:14336
v_mfma_f32_16x16x32_bf16 a[156:159], v[44:47], v[100:103], a[156:159]
buffer_load_dwordx4 v37, s[12:15], s29 offen offset:2048 lds
s_waitcnt lgkmcnt(10)
v_mfma_f32_16x16x32_bf16 a[160:163], v[48:51], v[88:91], a[160:163]
ds_read_b64_tr_b16 v[64:65], v16 offset:33024
v_mfma_f32_16x16x32_bf16 a[164:167], v[48:51], v[92:95], a[164:167]
ds_read_b64_tr_b16 v[66:67], v16 offset:35072
v_mfma_f32_16x16x32_bf16 a[168:171], v[48:51], v[96:99], a[168:171]
ds_read_b128 v[80:83], v27 offset:16384
v_mfma_f32_16x16x32_bf16 a[172:175], v[48:51], v[100:103], a[172:175]
buffer_load_dwordx4 v38, s[12:15], s29 offen offset:3072 lds
s_waitcnt lgkmcnt(10)
v_mfma_f32_16x16x32_bf16 a[176:179], v[52:55], v[88:91], a[176:179]
ds_read_b64_tr_b16 v[68:69], v17 offset:33024
v_mfma_f32_16x16x32_bf16 a[180:183], v[52:55], v[92:95], a[180:183]
ds_read_b64_tr_b16 v[70:71], v17 offset:35072
v_mfma_f32_16x16x32_bf16 a[184:187], v[52:55], v[96:99], a[184:187]
ds_read_b128 v[84:87], v27 offset:18432
v_mfma_f32_16x16x32_bf16 a[188:191], v[52:55], v[100:103], a[188:191]
s_waitcnt vmcnt(21)
s_barrier
s_waitcnt lgkmcnt(10)
v_mfma_f32_16x16x32_bf16 a[192:195], v[56:59], v[88:91], a[192:195]
ds_read_b64_tr_b16 v[40:41], v10 offset:49152
v_mfma_f32_16x16x32_bf16 a[196:199], v[56:59], v[92:95], a[196:199]
v_mfma_f32_16x16x32_bf16 a[200:203], v[56:59], v[96:99], a[200:203]
ds_read_b64_tr_b16 v[42:43], v10 offset:51200
v_mfma_f32_16x16x32_bf16 a[204:207], v[56:59], v[100:103], a[204:207]
s_waitcnt lgkmcnt(9)
v_mfma_f32_16x16x32_bf16 a[208:211], v[60:63], v[88:91], a[208:211]
ds_read_b64_tr_b16 v[44:45], v11 offset:49152
v_mfma_f32_16x16x32_bf16 a[212:215], v[60:63], v[92:95], a[212:215]
ds_read_b64_tr_b16 v[46:47], v11 offset:51200
v_mfma_f32_16x16x32_bf16 a[216:219], v[60:63], v[96:99], a[216:219]
global_load_dword v127, v121, s[56:57]
v_mfma_f32_16x16x32_bf16 a[220:223], v[60:63], v[100:103], a[220:223]
s_waitcnt lgkmcnt(8)
v_mfma_f32_16x16x32_bf16 a[224:227], v[64:67], v[88:91], a[224:227]
ds_read_b64_tr_b16 v[48:49], v12 offset:49152
v_mfma_f32_16x16x32_bf16 a[228:231], v[64:67], v[92:95], a[228:231]
v_mfma_f32_16x16x32_bf16 a[232:235], v[64:67], v[96:99], a[232:235]
ds_read_b64_tr_b16 v[50:51], v12 offset:51200
v_mfma_f32_16x16x32_bf16 a[236:239], v[64:67], v[100:103], a[236:239]
s_waitcnt lgkmcnt(7)
v_mfma_f32_16x16x32_bf16 a[240:243], v[68:71], v[88:91], a[240:243]
ds_read_b64_tr_b16 v[52:53], v13 offset:49152
v_mfma_f32_16x16x32_bf16 a[244:247], v[68:71], v[92:95], a[244:247]
v_mfma_f32_16x16x32_bf16 a[248:251], v[68:71], v[96:99], a[248:251]
ds_read_b64_tr_b16 v[54:55], v13 offset:51200
v_mfma_f32_16x16x32_bf16 a[252:255], v[68:71], v[100:103], a[252:255]
s_waitcnt lgkmcnt(6)
v_mfma_f32_16x16x32_bf16 a[0:3], v[40:43], v[104:107], a[0:3]
ds_read_b64_tr_b16 v[56:57], v14 offset:49152
ds_read_b64_tr_b16 v[58:59], v14 offset:51200
v_mfma_f32_16x16x32_bf16 a[4:7], v[40:43], v[108:111], a[4:7]
s_add_u32 s28, s37, 2883584
v_mfma_f32_16x16x32_bf16 a[8:11], v[40:43], v[112:115], a[8:11]
s_add_u32 m0, s34, 16384
v_mfma_f32_16x16x32_bf16 a[12:15], v[40:43], v[116:119], a[12:15]
buffer_load_dwordx4 v4, s[8:11], s28 offen lds
s_waitcnt lgkmcnt(6)
v_mfma_f32_16x16x32_bf16 a[16:19], v[44:47], v[104:107], a[16:19]
ds_read_b64_tr_b16 v[60:61], v15 offset:49152
v_mfma_f32_16x16x32_bf16 a[20:23], v[44:47], v[108:111], a[20:23]
ds_read_b64_tr_b16 v[62:63], v15 offset:51200
v_mfma_f32_16x16x32_bf16 a[24:27], v[44:47], v[112:115], a[24:27]
buffer_load_dwordx4 v5, s[8:11], s28 offen offset:1024 lds
v_mfma_f32_16x16x32_bf16 a[28:31], v[44:47], v[116:119], a[28:31]
s_waitcnt lgkmcnt(6)
v_mfma_f32_16x16x32_bf16 a[32:35], v[48:51], v[104:107], a[32:35]
ds_read_b64_tr_b16 v[64:65], v16 offset:49152
v_mfma_f32_16x16x32_bf16 a[36:39], v[48:51], v[108:111], a[36:39]
v_mfma_f32_16x16x32_bf16 a[40:43], v[48:51], v[112:115], a[40:43]
ds_read_b64_tr_b16 v[66:67], v16 offset:51200
v_mfma_f32_16x16x32_bf16 a[44:47], v[48:51], v[116:119], a[44:47]
s_waitcnt lgkmcnt(6)
v_mfma_f32_16x16x32_bf16 a[48:51], v[52:55], v[104:107], a[48:51]
ds_read_b64_tr_b16 v[68:69], v17 offset:49152
v_mfma_f32_16x16x32_bf16 a[52:55], v[52:55], v[108:111], a[52:55]
ds_read_b64_tr_b16 v[70:71], v17 offset:51200
v_mfma_f32_16x16x32_bf16 a[56:59], v[52:55], v[112:115], a[56:59]
buffer_load_dwordx4 v6, s[8:11], s28 offen offset:2048 lds
v_mfma_f32_16x16x32_bf16 a[60:63], v[52:55], v[116:119], a[60:63]
s_waitcnt lgkmcnt(6)
v_mfma_f32_16x16x32_bf16 a[64:67], v[56:59], v[104:107], a[64:67]
ds_read_b64_tr_b16 v[40:41], v10 offset:49408
v_mfma_f32_16x16x32_bf16 a[68:71], v[56:59], v[108:111], a[68:71]
ds_read_b64_tr_b16 v[42:43], v10 offset:51456
v_mfma_f32_16x16x32_bf16 a[72:75], v[56:59], v[112:115], a[72:75]
buffer_load_dwordx4 v7, s[8:11], s28 offen offset:3072 lds
v_mfma_f32_16x16x32_bf16 a[76:79], v[56:59], v[116:119], a[76:79]
s_waitcnt lgkmcnt(6)
v_mfma_f32_16x16x32_bf16 a[80:83], v[60:63], v[104:107], a[80:83]
ds_read_b64_tr_b16 v[44:45], v11 offset:49408
v_mfma_f32_16x16x32_bf16 a[84:87], v[60:63], v[108:111], a[84:87]
v_mfma_f32_16x16x32_bf16 a[88:91], v[60:63], v[112:115], a[88:91]
ds_read_b64_tr_b16 v[46:47], v11 offset:51456
v_mfma_f32_16x16x32_bf16 a[92:95], v[60:63], v[116:119], a[92:95]
s_waitcnt lgkmcnt(6)
v_mfma_f32_16x16x32_bf16 a[96:99], v[64:67], v[104:107], a[96:99]
ds_read_b64_tr_b16 v[48:49], v12 offset:49408
ds_read_b64_tr_b16 v[50:51], v12 offset:51456
v_mfma_f32_16x16x32_bf16 a[100:103], v[64:67], v[108:111], a[100:103]
s_mov_b32 s29, 33536
v_mfma_f32_16x16x32_bf16 a[104:107], v[64:67], v[112:115], a[104:107]
s_add_u32 m0, s35, 12288
v_mfma_f32_16x16x32_bf16 a[108:111], v[64:67], v[116:119], a[108:111]
buffer_load_dwordx4 v8, s[12:15], s29 offen lds
s_waitcnt lgkmcnt(6)
v_mfma_f32_16x16x32_bf16 a[112:115], v[68:71], v[104:107], a[112:115]
ds_read_b64_tr_b16 v[52:53], v13 offset:49408
v_mfma_f32_16x16x32_bf16 a[116:119], v[68:71], v[108:111], a[116:119]
ds_read_b64_tr_b16 v[54:55], v13 offset:51456
v_mfma_f32_16x16x32_bf16 a[120:123], v[68:71], v[112:115], a[120:123]
buffer_load_dwordx4 v9, s[12:15], s29 offen offset:1024 lds
v_mfma_f32_16x16x32_bf16 a[124:127], v[68:71], v[116:119], a[124:127]
s_waitcnt lgkmcnt(6)
v_mfma_f32_16x16x32_bf16 a[128:131], v[40:43], v[104:107], a[128:131]
ds_read_b64_tr_b16 v[56:57], v14 offset:49408
v_mfma_f32_16x16x32_bf16 a[132:135], v[40:43], v[108:111], a[132:135]
v_mfma_f32_16x16x32_bf16 a[136:139], v[40:43], v[112:115], a[136:139]
ds_read_b64_tr_b16 v[58:59], v14 offset:51456
v_mfma_f32_16x16x32_bf16 a[140:143], v[40:43], v[116:119], a[140:143]
s_waitcnt lgkmcnt(6)
v_mfma_f32_16x16x32_bf16 a[144:147], v[44:47], v[104:107], a[144:147]
ds_read_b64_tr_b16 v[60:61], v15 offset:49408
v_mfma_f32_16x16x32_bf16 a[148:151], v[44:47], v[108:111], a[148:151]
ds_read_b64_tr_b16 v[62:63], v15 offset:51456
v_mfma_f32_16x16x32_bf16 a[152:155], v[44:47], v[112:115], a[152:155]
buffer_load_dwordx4 v37, s[12:15], s29 offen offset:2048 lds
v_mfma_f32_16x16x32_bf16 a[156:159], v[44:47], v[116:119], a[156:159]
s_waitcnt lgkmcnt(6)
v_mfma_f32_16x16x32_bf16 a[160:163], v[48:51], v[104:107], a[160:163]
ds_read_b64_tr_b16 v[64:65], v16 offset:49408
v_mfma_f32_16x16x32_bf16 a[164:167], v[48:51], v[108:111], a[164:167]
ds_read_b64_tr_b16 v[66:67], v16 offset:51456
v_mfma_f32_16x16x32_bf16 a[168:171], v[48:51], v[112:115], a[168:171]
buffer_load_dwordx4 v38, s[12:15], s29 offen offset:3072 lds
v_mfma_f32_16x16x32_bf16 a[172:175], v[48:51], v[116:119], a[172:175]
s_waitcnt lgkmcnt(6)
v_mfma_f32_16x16x32_bf16 a[176:179], v[52:55], v[104:107], a[176:179]
ds_read_b64_tr_b16 v[68:69], v17 offset:49408
v_mfma_f32_16x16x32_bf16 a[180:183], v[52:55], v[108:111], a[180:183]
v_mfma_f32_16x16x32_bf16 a[184:187], v[52:55], v[112:115], a[184:187]
ds_read_b64_tr_b16 v[70:71], v17 offset:51456
v_mfma_f32_16x16x32_bf16 a[188:191], v[52:55], v[116:119], a[188:191]
s_waitcnt vmcnt(22)
s_barrier
s_waitcnt lgkmcnt(6)
v_mfma_f32_16x16x32_bf16 a[192:195], v[56:59], v[104:107], a[192:195]
ds_read_b64_tr_b16 v[40:41], v18 offset:0
v_mfma_f32_16x16x32_bf16 a[196:199], v[56:59], v[108:111], a[196:199]
v_mfma_f32_16x16x32_bf16 a[200:203], v[56:59], v[112:115], a[200:203]
ds_read_b64_tr_b16 v[42:43], v18 offset:2048
v_mfma_f32_16x16x32_bf16 a[204:207], v[56:59], v[116:119], a[204:207]
s_waitcnt lgkmcnt(6)
v_mfma_f32_16x16x32_bf16 a[208:211], v[60:63], v[104:107], a[208:211]
ds_read_b64_tr_b16 v[44:45], v19 offset:0
v_mfma_f32_16x16x32_bf16 a[212:215], v[60:63], v[108:111], a[212:215]
ds_read_b64_tr_b16 v[46:47], v19 offset:2048
v_mfma_f32_16x16x32_bf16 a[216:219], v[60:63], v[112:115], a[216:219]
global_load_dword v127, v125, s[64:65]
v_mfma_f32_16x16x32_bf16 a[220:223], v[60:63], v[116:119], a[220:223]
s_waitcnt lgkmcnt(6)
v_mfma_f32_16x16x32_bf16 a[224:227], v[64:67], v[104:107], a[224:227]
ds_read_b64_tr_b16 v[48:49], v20 offset:0
v_mfma_f32_16x16x32_bf16 a[228:231], v[64:67], v[108:111], a[228:231]
v_mfma_f32_16x16x32_bf16 a[232:235], v[64:67], v[112:115], a[232:235]
ds_read_b64_tr_b16 v[50:51], v20 offset:2048
v_mfma_f32_16x16x32_bf16 a[236:239], v[64:67], v[116:119], a[236:239]
s_waitcnt lgkmcnt(6)
v_mfma_f32_16x16x32_bf16 a[240:243], v[68:71], v[104:107], a[240:243]
ds_read_b64_tr_b16 v[52:53], v21 offset:0
v_mfma_f32_16x16x32_bf16 a[244:247], v[68:71], v[108:111], a[244:247]
v_mfma_f32_16x16x32_bf16 a[248:251], v[68:71], v[112:115], a[248:251]
ds_read_b64_tr_b16 v[54:55], v21 offset:2048
v_mfma_f32_16x16x32_bf16 a[252:255], v[68:71], v[116:119], a[252:255]
s_waitcnt lgkmcnt(6)
v_mfma_f32_16x16x32_bf16 a[0:3], v[40:43], v[72:75], a[0:3]
ds_read_b64_tr_b16 v[56:57], v22 offset:0
ds_read_b64_tr_b16 v[58:59], v22 offset:2048
v_mfma_f32_16x16x32_bf16 a[4:7], v[40:43], v[76:79], a[4:7]
s_add_u32 s28, s37, 3145728
v_mfma_f32_16x16x32_bf16 a[8:11], v[40:43], v[80:83], a[8:11]
s_add_u32 m0, s34, 32768
v_mfma_f32_16x16x32_bf16 a[12:15], v[40:43], v[84:87], a[12:15]
buffer_load_dwordx4 v4, s[8:11], s28 offen lds
s_waitcnt lgkmcnt(6)
v_mfma_f32_16x16x32_bf16 a[16:19], v[44:47], v[72:75], a[16:19]
ds_read_b64_tr_b16 v[60:61], v23 offset:0
v_mfma_f32_16x16x32_bf16 a[20:23], v[44:47], v[76:79], a[20:23]
ds_read_b64_tr_b16 v[62:63], v23 offset:2048
v_mfma_f32_16x16x32_bf16 a[24:27], v[44:47], v[80:83], a[24:27]
buffer_load_dwordx4 v5, s[8:11], s28 offen offset:1024 lds
v_mfma_f32_16x16x32_bf16 a[28:31], v[44:47], v[84:87], a[28:31]
s_waitcnt lgkmcnt(6)
v_mfma_f32_16x16x32_bf16 a[32:35], v[48:51], v[72:75], a[32:35]
ds_read_b64_tr_b16 v[64:65], v24 offset:0
v_mfma_f32_16x16x32_bf16 a[36:39], v[48:51], v[76:79], a[36:39]
v_mfma_f32_16x16x32_bf16 a[40:43], v[48:51], v[80:83], a[40:43]
ds_read_b64_tr_b16 v[66:67], v24 offset:2048
v_mfma_f32_16x16x32_bf16 a[44:47], v[48:51], v[84:87], a[44:47]
s_waitcnt lgkmcnt(6)
v_mfma_f32_16x16x32_bf16 a[48:51], v[52:55], v[72:75], a[48:51]
ds_read_b64_tr_b16 v[68:69], v25 offset:0
v_mfma_f32_16x16x32_bf16 a[52:55], v[52:55], v[76:79], a[52:55]
ds_read_b64_tr_b16 v[70:71], v25 offset:2048
v_mfma_f32_16x16x32_bf16 a[56:59], v[52:55], v[80:83], a[56:59]
s_waitcnt vmcnt(21)
v_mfma_f32_16x16x32_bf16 a[60:63], v[52:55], v[84:87], a[60:63]
buffer_load_dwordx4 v6, s[8:11], s28 offen offset:2048 lds
s_waitcnt lgkmcnt(6)
v_mfma_f32_16x16x32_bf16 a[64:67], v[56:59], v[72:75], a[64:67]
ds_read_b64_tr_b16 v[40:41], v18 offset:256
v_mfma_f32_16x16x32_bf16 a[68:71], v[56:59], v[76:79], a[68:71]
ds_read_b64_tr_b16 v[42:43], v18 offset:2304
v_mfma_f32_16x16x32_bf16 a[72:75], v[56:59], v[80:83], a[72:75]
ds_read_b128 v[88:91], v26 offset:0
v_mfma_f32_16x16x32_bf16 a[76:79], v[56:59], v[84:87], a[76:79]
buffer_load_dwordx4 v7, s[8:11], s28 offen offset:3072 lds
s_waitcnt lgkmcnt(7)
v_mfma_f32_16x16x32_bf16 a[80:83], v[60:63], v[72:75], a[80:83]
ds_read_b64_tr_b16 v[44:45], v19 offset:256
v_mfma_f32_16x16x32_bf16 a[84:87], v[60:63], v[76:79], a[84:87]
ds_read_b64_tr_b16 v[46:47], v19 offset:2304
v_mfma_f32_16x16x32_bf16 a[88:91], v[60:63], v[80:83], a[88:91]
ds_read_b128 v[92:95], v26 offset:2048
v_mfma_f32_16x16x32_bf16 a[92:95], v[60:63], v[84:87], a[92:95]
s_waitcnt lgkmcnt(8)
v_mfma_f32_16x16x32_bf16 a[96:99], v[64:67], v[72:75], a[96:99]
ds_read_b64_tr_b16 v[48:49], v20 offset:256
ds_read_b64_tr_b16 v[50:51], v20 offset:2304
v_mfma_f32_16x16x32_bf16 a[100:103], v[64:67], v[76:79], a[100:103]
ds_read_b128 v[96:99], v26 offset:4096
v_mfma_f32_16x16x32_bf16 a[104:107], v[64:67], v[80:83], a[104:107]
s_mov_b32 s29, 896
s_add_u32 m0, s35, 16384
v_mfma_f32_16x16x32_bf16 a[108:111], v[64:67], v[84:87], a[108:111]
buffer_load_dwordx4 v8, s[12:15], s29 offen lds
s_waitcnt lgkmcnt(9)
v_mfma_f32_16x16x32_bf16 a[112:115], v[68:71], v[72:75], a[112:115]
ds_read_b64_tr_b16 v[52:53], v21 offset:256
v_mfma_f32_16x16x32_bf16 a[116:119], v[68:71], v[76:79], a[116:119]
ds_read_b64_tr_b16 v[54:55], v21 offset:2304
v_mfma_f32_16x16x32_bf16 a[120:123], v[68:71], v[80:83], a[120:123]
ds_read_b128 v[100:103], v26 offset:6144
v_mfma_f32_16x16x32_bf16 a[124:127], v[68:71], v[84:87], a[124:127]
buffer_load_dwordx4 v9, s[12:15], s29 offen offset:1024 lds
s_waitcnt lgkmcnt(10)
v_mfma_f32_16x16x32_bf16 a[128:131], v[40:43], v[72:75], a[128:131]
ds_read_b64_tr_b16 v[56:57], v22 offset:256
v_mfma_f32_16x16x32_bf16 a[132:135], v[40:43], v[76:79], a[132:135]
ds_read_b64_tr_b16 v[58:59], v22 offset:2304
v_mfma_f32_16x16x32_bf16 a[136:139], v[40:43], v[80:83], a[136:139]
ds_read_b128 v[104:107], v27 offset:0
v_mfma_f32_16x16x32_bf16 a[140:143], v[40:43], v[84:87], a[140:143]
s_waitcnt lgkmcnt(10)
v_mfma_f32_16x16x32_bf16 a[144:147], v[44:47], v[72:75], a[144:147]
ds_read_b64_tr_b16 v[60:61], v23 offset:256
v_mfma_f32_16x16x32_bf16 a[148:151], v[44:47], v[76:79], a[148:151]
ds_read_b64_tr_b16 v[62:63], v23 offset:2304
v_mfma_f32_16x16x32_bf16 a[152:155], v[44:47], v[80:83], a[152:155]
ds_read_b128 v[108:111], v27 offset:2048
v_mfma_f32_16x16x32_bf16 a[156:159], v[44:47], v[84:87], a[156:159]
buffer_load_dwordx4 v37, s[12:15], s29 offen offset:2048 lds
s_waitcnt lgkmcnt(10)
v_mfma_f32_16x16x32_bf16 a[160:163], v[48:51], v[72:75], a[160:163]
ds_read_b64_tr_b16 v[64:65], v24 offset:256
v_mfma_f32_16x16x32_bf16 a[164:167], v[48:51], v[76:79], a[164:167]
ds_read_b64_tr_b16 v[66:67], v24 offset:2304
v_mfma_f32_16x16x32_bf16 a[168:171], v[48:51], v[80:83], a[168:171]
ds_read_b128 v[112:115], v27 offset:4096
v_mfma_f32_16x16x32_bf16 a[172:175], v[48:51], v[84:87], a[172:175]
buffer_load_dwordx4 v38, s[12:15], s29 offen offset:3072 lds
s_waitcnt lgkmcnt(10)
v_mfma_f32_16x16x32_bf16 a[176:179], v[52:55], v[72:75], a[176:179]
ds_read_b64_tr_b16 v[68:69], v25 offset:256
v_mfma_f32_16x16x32_bf16 a[180:183], v[52:55], v[76:79], a[180:183]
ds_read_b64_tr_b16 v[70:71], v25 offset:2304
v_mfma_f32_16x16x32_bf16 a[184:187], v[52:55], v[80:83], a[184:187]
ds_read_b128 v[116:119], v27 offset:6144
v_mfma_f32_16x16x32_bf16 a[188:191], v[52:55], v[84:87], a[188:191]
s_waitcnt vmcnt(22)
s_barrier
s_waitcnt lgkmcnt(10)
v_mfma_f32_16x16x32_bf16 a[192:195], v[56:59], v[72:75], a[192:195]
ds_read_b64_tr_b16 v[40:41], v10 offset:0
v_mfma_f32_16x16x32_bf16 a[196:199], v[56:59], v[76:79], a[196:199]
v_mfma_f32_16x16x32_bf16 a[200:203], v[56:59], v[80:83], a[200:203]
ds_read_b64_tr_b16 v[42:43], v10 offset:2048
v_mfma_f32_16x16x32_bf16 a[204:207], v[56:59], v[84:87], a[204:207]
s_waitcnt lgkmcnt(9)
v_mfma_f32_16x16x32_bf16 a[208:211], v[60:63], v[72:75], a[208:211]
ds_read_b64_tr_b16 v[44:45], v11 offset:0
v_mfma_f32_16x16x32_bf16 a[212:215], v[60:63], v[76:79], a[212:215]
ds_read_b64_tr_b16 v[46:47], v11 offset:2048
v_mfma_f32_16x16x32_bf16 a[216:219], v[60:63], v[80:83], a[216:219]
global_load_dword v127, v124, s[56:57]
v_mfma_f32_16x16x32_bf16 a[220:223], v[60:63], v[84:87], a[220:223]
s_waitcnt lgkmcnt(8)
v_mfma_f32_16x16x32_bf16 a[224:227], v[64:67], v[72:75], a[224:227]
ds_read_b64_tr_b16 v[48:49], v12 offset:0
v_mfma_f32_16x16x32_bf16 a[228:231], v[64:67], v[76:79], a[228:231]
v_mfma_f32_16x16x32_bf16 a[232:235], v[64:67], v[80:83], a[232:235]
ds_read_b64_tr_b16 v[50:51], v12 offset:2048
v_mfma_f32_16x16x32_bf16 a[236:239], v[64:67], v[84:87], a[236:239]
s_waitcnt lgkmcnt(7)
v_mfma_f32_16x16x32_bf16 a[240:243], v[68:71], v[72:75], a[240:243]
ds_read_b64_tr_b16 v[52:53], v13 offset:0
v_mfma_f32_16x16x32_bf16 a[244:247], v[68:71], v[76:79], a[244:247]
v_mfma_f32_16x16x32_bf16 a[248:251], v[68:71], v[80:83], a[248:251]
ds_read_b64_tr_b16 v[54:55], v13 offset:2048
v_mfma_f32_16x16x32_bf16 a[252:255], v[68:71], v[84:87], a[252:255]
s_waitcnt lgkmcnt(6)
v_mfma_f32_16x16x32_bf16 a[0:3], v[40:43], v[88:91], a[0:3]
ds_read_b64_tr_b16 v[56:57], v14 offset:0
ds_read_b64_tr_b16 v[58:59], v14 offset:2048
v_mfma_f32_16x16x32_bf16 a[4:7], v[40:43], v[92:95], a[4:7]
s_add_u32 s28, s37, 3407872
v_mfma_f32_16x16x32_bf16 a[8:11], v[40:43], v[96:99], a[8:11]
s_add_u32 m0, s34, 49152
v_mfma_f32_16x16x32_bf16 a[12:15], v[40:43], v[100:103], a[12:15]
buffer_load_dwordx4 v4, s[8:11], s28 offen lds
s_waitcnt lgkmcnt(6)
v_mfma_f32_16x16x32_bf16 a[16:19], v[44:47], v[88:91], a[16:19]
ds_read_b64_tr_b16 v[60:61], v15 offset:0
v_mfma_f32_16x16x32_bf16 a[20:23], v[44:47], v[92:95], a[20:23]
ds_read_b64_tr_b16 v[62:63], v15 offset:2048
v_mfma_f32_16x16x32_bf16 a[24:27], v[44:47], v[96:99], a[24:27]
buffer_load_dwordx4 v5, s[8:11], s28 offen offset:1024 lds
v_mfma_f32_16x16x32_bf16 a[28:31], v[44:47], v[100:103], a[28:31]
s_waitcnt lgkmcnt(6)
v_mfma_f32_16x16x32_bf16 a[32:35], v[48:51], v[88:91], a[32:35]
ds_read_b64_tr_b16 v[64:65], v16 offset:0
v_mfma_f32_16x16x32_bf16 a[36:39], v[48:51], v[92:95], a[36:39]
v_mfma_f32_16x16x32_bf16 a[40:43], v[48:51], v[96:99], a[40:43]
ds_read_b64_tr_b16 v[66:67], v16 offset:2048
v_mfma_f32_16x16x32_bf16 a[44:47], v[48:51], v[100:103], a[44:47]
s_waitcnt lgkmcnt(6)
v_mfma_f32_16x16x32_bf16 a[48:51], v[52:55], v[88:91], a[48:51]
ds_read_b64_tr_b16 v[68:69], v17 offset:0
v_mfma_f32_16x16x32_bf16 a[52:55], v[52:55], v[92:95], a[52:55]
ds_read_b64_tr_b16 v[70:71], v17 offset:2048
v_mfma_f32_16x16x32_bf16 a[56:59], v[52:55], v[96:99], a[56:59]
buffer_load_dwordx4 v6, s[8:11], s28 offen offset:2048 lds
v_mfma_f32_16x16x32_bf16 a[60:63], v[52:55], v[100:103], a[60:63]
s_waitcnt lgkmcnt(6)
v_mfma_f32_16x16x32_bf16 a[64:67], v[56:59], v[88:91], a[64:67]
ds_read_b64_tr_b16 v[40:41], v10 offset:256
v_mfma_f32_16x16x32_bf16 a[68:71], v[56:59], v[92:95], a[68:71]
ds_read_b64_tr_b16 v[42:43], v10 offset:2304
v_mfma_f32_16x16x32_bf16 a[72:75], v[56:59], v[96:99], a[72:75]
buffer_load_dwordx4 v7, s[8:11], s28 offen offset:3072 lds
v_mfma_f32_16x16x32_bf16 a[76:79], v[56:59], v[100:103], a[76:79]
s_waitcnt lgkmcnt(6)
v_mfma_f32_16x16x32_bf16 a[80:83], v[60:63], v[88:91], a[80:83]
ds_read_b64_tr_b16 v[44:45], v11 offset:256
v_mfma_f32_16x16x32_bf16 a[84:87], v[60:63], v[92:95], a[84:87]
v_mfma_f32_16x16x32_bf16 a[88:91], v[60:63], v[96:99], a[88:91]
ds_read_b64_tr_b16 v[46:47], v11 offset:2304
v_mfma_f32_16x16x32_bf16 a[92:95], v[60:63], v[100:103], a[92:95]
s_waitcnt lgkmcnt(6)
v_mfma_f32_16x16x32_bf16 a[96:99], v[64:67], v[88:91], a[96:99]
ds_read_b64_tr_b16 v[48:49], v12 offset:256
ds_read_b64_tr_b16 v[50:51], v12 offset:2304
v_mfma_f32_16x16x32_bf16 a[100:103], v[64:67], v[92:95], a[100:103]
s_mov_b32 s29, 33664
v_mfma_f32_16x16x32_bf16 a[104:107], v[64:67], v[96:99], a[104:107]
s_add_u32 m0, s35, 0
v_mfma_f32_16x16x32_bf16 a[108:111], v[64:67], v[100:103], a[108:111]
buffer_load_dwordx4 v8, s[12:15], s29 offen lds
s_waitcnt lgkmcnt(6)
v_mfma_f32_16x16x32_bf16 a[112:115], v[68:71], v[88:91], a[112:115]
ds_read_b64_tr_b16 v[52:53], v13 offset:256
v_mfma_f32_16x16x32_bf16 a[116:119], v[68:71], v[92:95], a[116:119]
ds_read_b64_tr_b16 v[54:55], v13 offset:2304
v_mfma_f32_16x16x32_bf16 a[120:123], v[68:71], v[96:99], a[120:123]
buffer_load_dwordx4 v9, s[12:15], s29 offen offset:1024 lds
v_mfma_f32_16x16x32_bf16 a[124:127], v[68:71], v[100:103], a[124:127]
s_waitcnt lgkmcnt(6)
v_mfma_f32_16x16x32_bf16 a[128:131], v[40:43], v[88:91], a[128:131]
ds_read_b64_tr_b16 v[56:57], v14 offset:256
v_mfma_f32_16x16x32_bf16 a[132:135], v[40:43], v[92:95], a[132:135]
v_mfma_f32_16x16x32_bf16 a[136:139], v[40:43], v[96:99], a[136:139]
ds_read_b64_tr_b16 v[58:59], v14 offset:2304
v_mfma_f32_16x16x32_bf16 a[140:143], v[40:43], v[100:103], a[140:143]
s_waitcnt lgkmcnt(6)
v_mfma_f32_16x16x32_bf16 a[144:147], v[44:47], v[88:91], a[144:147]
ds_read_b64_tr_b16 v[60:61], v15 offset:256
v_mfma_f32_16x16x32_bf16 a[148:151], v[44:47], v[92:95], a[148:151]
ds_read_b64_tr_b16 v[62:63], v15 offset:2304
v_mfma_f32_16x16x32_bf16 a[152:155], v[44:47], v[96:99], a[152:155]
buffer_load_dwordx4 v37, s[12:15], s29 offen offset:2048 lds
v_mfma_f32_16x16x32_bf16 a[156:159], v[44:47], v[100:103], a[156:159]
s_waitcnt lgkmcnt(6)
v_mfma_f32_16x16x32_bf16 a[160:163], v[48:51], v[88:91], a[160:163]
ds_read_b64_tr_b16 v[64:65], v16 offset:256
v_mfma_f32_16x16x32_bf16 a[164:167], v[48:51], v[92:95], a[164:167]
ds_read_b64_tr_b16 v[66:67], v16 offset:2304
v_mfma_f32_16x16x32_bf16 a[168:171], v[48:51], v[96:99], a[168:171]
buffer_load_dwordx4 v38, s[12:15], s29 offen offset:3072 lds
v_mfma_f32_16x16x32_bf16 a[172:175], v[48:51], v[100:103], a[172:175]
s_waitcnt lgkmcnt(6)
v_mfma_f32_16x16x32_bf16 a[176:179], v[52:55], v[88:91], a[176:179]
ds_read_b64_tr_b16 v[68:69], v17 offset:256
v_mfma_f32_16x16x32_bf16 a[180:183], v[52:55], v[92:95], a[180:183]
v_mfma_f32_16x16x32_bf16 a[184:187], v[52:55], v[96:99], a[184:187]
ds_read_b64_tr_b16 v[70:71], v17 offset:2304
v_mfma_f32_16x16x32_bf16 a[188:191], v[52:55], v[100:103], a[188:191]
s_waitcnt vmcnt(22)
s_barrier
s_waitcnt lgkmcnt(6)
v_mfma_f32_16x16x32_bf16 a[192:195], v[56:59], v[88:91], a[192:195]
ds_read_b64_tr_b16 v[40:41], v10 offset:16384
v_mfma_f32_16x16x32_bf16 a[196:199], v[56:59], v[92:95], a[196:199]
v_mfma_f32_16x16x32_bf16 a[200:203], v[56:59], v[96:99], a[200:203]
ds_read_b64_tr_b16 v[42:43], v10 offset:18432
v_mfma_f32_16x16x32_bf16 a[204:207], v[56:59], v[100:103], a[204:207]
s_waitcnt lgkmcnt(6)
v_mfma_f32_16x16x32_bf16 a[208:211], v[60:63], v[88:91], a[208:211]
ds_read_b64_tr_b16 v[44:45], v11 offset:16384
v_mfma_f32_16x16x32_bf16 a[212:215], v[60:63], v[92:95], a[212:215]
ds_read_b64_tr_b16 v[46:47], v11 offset:18432
v_mfma_f32_16x16x32_bf16 a[216:219], v[60:63], v[96:99], a[216:219]
global_load_dword v127, v125, s[66:67]
v_mfma_f32_16x16x32_bf16 a[220:223], v[60:63], v[100:103], a[220:223]
s_waitcnt lgkmcnt(6)
v_mfma_f32_16x16x32_bf16 a[224:227], v[64:67], v[88:91], a[224:227]
ds_read_b64_tr_b16 v[48:49], v12 offset:16384
v_mfma_f32_16x16x32_bf16 a[228:231], v[64:67], v[92:95], a[228:231]
v_mfma_f32_16x16x32_bf16 a[232:235], v[64:67], v[96:99], a[232:235]
ds_read_b64_tr_b16 v[50:51], v12 offset:18432
v_mfma_f32_16x16x32_bf16 a[236:239], v[64:67], v[100:103], a[236:239]
s_waitcnt lgkmcnt(6)
v_mfma_f32_16x16x32_bf16 a[240:243], v[68:71], v[88:91], a[240:243]
ds_read_b64_tr_b16 v[52:53], v13 offset:16384
v_mfma_f32_16x16x32_bf16 a[244:247], v[68:71], v[92:95], a[244:247]
v_mfma_f32_16x16x32_bf16 a[248:251], v[68:71], v[96:99], a[248:251]
ds_read_b64_tr_b16 v[54:55], v13 offset:18432
v_mfma_f32_16x16x32_bf16 a[252:255], v[68:71], v[100:103], a[252:255]
s_waitcnt lgkmcnt(6)
v_mfma_f32_16x16x32_bf16 a[0:3], v[40:43], v[104:107], a[0:3]
ds_read_b64_tr_b16 v[56:57], v14 offset:16384
ds_read_b64_tr_b16 v[58:59], v14 offset:18432
v_mfma_f32_16x16x32_bf16 a[4:7], v[40:43], v[108:111], a[4:7]
s_add_u32 s28, s37, 3670016
v_mfma_f32_16x16x32_bf16 a[8:11], v[40:43], v[112:115], a[8:11]
s_add_u32 m0, s34, 65536
v_mfma_f32_16x16x32_bf16 a[12:15], v[40:43], v[116:119], a[12:15]
buffer_load_dwordx4 v4, s[8:11], s28 offen lds
s_waitcnt lgkmcnt(6)
v_mfma_f32_16x16x32_bf16 a[16:19], v[44:47], v[104:107], a[16:19]
ds_read_b64_tr_b16 v[60:61], v15 offset:16384
v_mfma_f32_16x16x32_bf16 a[20:23], v[44:47], v[108:111], a[20:23]
v_mfma_f32_16x16x32_bf16 a[24:27], v[44:47], v[112:115], a[24:27]
ds_read_b64_tr_b16 v[62:63], v15 offset:18432
v_mfma_f32_16x16x32_bf16 a[28:31], v[44:47], v[116:119], a[28:31]
s_waitcnt lgkmcnt(6)
v_mfma_f32_16x16x32_bf16 a[32:35], v[48:51], v[104:107], a[32:35]
ds_read_b64_tr_b16 v[64:65], v16 offset:16384
v_mfma_f32_16x16x32_bf16 a[36:39], v[48:51], v[108:111], a[36:39]
v_mfma_f32_16x16x32_bf16 a[40:43], v[48:51], v[112:115], a[40:43]
ds_read_b64_tr_b16 v[66:67], v16 offset:18432
v_mfma_f32_16x16x32_bf16 a[44:47], v[48:51], v[116:119], a[44:47]
s_waitcnt lgkmcnt(6)
v_mfma_f32_16x16x32_bf16 a[48:51], v[52:55], v[104:107], a[48:51]
ds_read_b64_tr_b16 v[68:69], v17 offset:16384
v_mfma_f32_16x16x32_bf16 a[52:55], v[52:55], v[108:111], a[52:55]
ds_read_b64_tr_b16 v[70:71], v17 offset:18432
v_mfma_f32_16x16x32_bf16 a[56:59], v[52:55], v[112:115], a[56:59]
s_waitcnt vmcnt(20)
v_mfma_f32_16x16x32_bf16 a[60:63], v[52:55], v[116:119], a[60:63]
buffer_load_dwordx4 v5, s[8:11], s28 offen offset:1024 lds
s_waitcnt lgkmcnt(6)
v_mfma_f32_16x16x32_bf16 a[64:67], v[56:59], v[104:107], a[64:67]
ds_read_b64_tr_b16 v[40:41], v10 offset:16640
v_mfma_f32_16x16x32_bf16 a[68:71], v[56:59], v[108:111], a[68:71]
ds_read_b64_tr_b16 v[42:43], v10 offset:18688
v_mfma_f32_16x16x32_bf16 a[72:75], v[56:59], v[112:115], a[72:75]
ds_read_b128 v[72:75], v26 offset:8192
v_mfma_f32_16x16x32_bf16 a[76:79], v[56:59], v[116:119], a[76:79]
s_waitcnt lgkmcnt(7)
v_mfma_f32_16x16x32_bf16 a[80:83], v[60:63], v[104:107], a[80:83]
ds_read_b64_tr_b16 v[44:45], v11 offset:16640
v_mfma_f32_16x16x32_bf16 a[84:87], v[60:63], v[108:111], a[84:87]
ds_read_b64_tr_b16 v[46:47], v11 offset:18688
v_mfma_f32_16x16x32_bf16 a[88:91], v[60:63], v[112:115], a[88:91]
ds_read_b128 v[76:79], v26 offset:10240
v_mfma_f32_16x16x32_bf16 a[92:95], v[60:63], v[116:119], a[92:95]
s_waitcnt lgkmcnt(8)
v_mfma_f32_16x16x32_bf16 a[96:99], v[64:67], v[104:107], a[96:99]
ds_read_b64_tr_b16 v[48:49], v12 offset:16640
v_mfma_f32_16x16x32_bf16 a[100:103], v[64:67], v[108:111], a[100:103]
ds_read_b64_tr_b16 v[50:51], v12 offset:18688
v_mfma_f32_16x16x32_bf16 a[104:107], v[64:67], v[112:115], a[104:107]
ds_read_b128 v[80:83], v26 offset:12288
v_mfma_f32_16x16x32_bf16 a[108:111], v[64:67], v[116:119], a[108:111]
buffer_load_dwordx4 v6, s[8:11], s28 offen offset:2048 lds
s_waitcnt lgkmcnt(9)
v_mfma_f32_16x16x32_bf16 a[112:115], v[68:71], v[104:107], a[112:115]
ds_read_b64_tr_b16 v[52:53], v13 offset:16640
v_mfma_f32_16x16x32_bf16 a[116:119], v[68:71], v[108:111], a[116:119]
ds_read_b64_tr_b16 v[54:55], v13 offset:18688
v_mfma_f32_16x16x32_bf16 a[120:123], v[68:71], v[112:115], a[120:123]
ds_read_b128 v[84:87], v26 offset:14336
v_mfma_f32_16x16x32_bf16 a[124:127], v[68:71], v[116:119], a[124:127]
s_waitcnt lgkmcnt(10)
v_mfma_f32_16x16x32_bf16 a[128:131], v[40:43], v[104:107], a[128:131]
ds_read_b64_tr_b16 v[56:57], v14 offset:16640
)ASM"
R"ASM(v_mfma_f32_16x16x32_bf16 a[132:135], v[40:43], v[108:111], a[132:135]
ds_read_b64_tr_b16 v[58:59], v14 offset:18688
v_mfma_f32_16x16x32_bf16 a[136:139], v[40:43], v[112:115], a[136:139]
ds_read_b128 v[88:91], v27 offset:8192
v_mfma_f32_16x16x32_bf16 a[140:143], v[40:43], v[116:119], a[140:143]
s_waitcnt lgkmcnt(10)
v_mfma_f32_16x16x32_bf16 a[144:147], v[44:47], v[104:107], a[144:147]
ds_read_b64_tr_b16 v[60:61], v15 offset:16640
v_mfma_f32_16x16x32_bf16 a[148:151], v[44:47], v[108:111], a[148:151]
ds_read_b64_tr_b16 v[62:63], v15 offset:18688
v_mfma_f32_16x16x32_bf16 a[152:155], v[44:47], v[112:115], a[152:155]
ds_read_b128 v[92:95], v27 offset:10240
v_mfma_f32_16x16x32_bf16 a[156:159], v[44:47], v[116:119], a[156:159]
buffer_load_dwordx4 v7, s[8:11], s28 offen offset:3072 lds
s_waitcnt lgkmcnt(10)
v_mfma_f32_16x16x32_bf16 a[160:163], v[48:51], v[104:107], a[160:163]
ds_read_b64_tr_b16 v[64:65], v16 offset:16640
v_mfma_f32_16x16x32_bf16 a[164:167], v[48:51], v[108:111], a[164:167]
ds_read_b64_tr_b16 v[66:67], v16 offset:18688
v_mfma_f32_16x16x32_bf16 a[168:171], v[48:51], v[112:115], a[168:171]
ds_read_b128 v[96:99], v27 offset:12288
v_mfma_f32_16x16x32_bf16 a[172:175], v[48:51], v[116:119], a[172:175]
s_waitcnt lgkmcnt(10)
v_mfma_f32_16x16x32_bf16 a[176:179], v[52:55], v[104:107], a[176:179]
ds_read_b64_tr_b16 v[68:69], v17 offset:16640
v_mfma_f32_16x16x32_bf16 a[180:183], v[52:55], v[108:111], a[180:183]
ds_read_b64_tr_b16 v[70:71], v17 offset:18688
v_mfma_f32_16x16x32_bf16 a[184:187], v[52:55], v[112:115], a[184:187]
ds_read_b128 v[100:103], v27 offset:14336
v_mfma_f32_16x16x32_bf16 a[188:191], v[52:55], v[116:119], a[188:191]
s_waitcnt vmcnt(18)
s_barrier
s_waitcnt lgkmcnt(10)
v_mfma_f32_16x16x32_bf16 a[192:195], v[56:59], v[104:107], a[192:195]
ds_read_b64_tr_b16 v[40:41], v10 offset:32768
v_mfma_f32_16x16x32_bf16 a[196:199], v[56:59], v[108:111], a[196:199]
v_mfma_f32_16x16x32_bf16 a[200:203], v[56:59], v[112:115], a[200:203]
ds_read_b64_tr_b16 v[42:43], v10 offset:34816
v_mfma_f32_16x16x32_bf16 a[204:207], v[56:59], v[116:119], a[204:207]
s_waitcnt lgkmcnt(9)
v_mfma_f32_16x16x32_bf16 a[208:211], v[60:63], v[104:107], a[208:211]
ds_read_b64_tr_b16 v[44:45], v11 offset:32768
v_mfma_f32_16x16x32_bf16 a[212:215], v[60:63], v[108:111], a[212:215]
ds_read_b64_tr_b16 v[46:47], v11 offset:34816
v_mfma_f32_16x16x32_bf16 a[216:219], v[60:63], v[112:115], a[216:219]
global_load_dwordx2 v[122:123], v120, s[48:49] sc1
v_mfma_f32_16x16x32_bf16 a[220:223], v[60:63], v[116:119], a[220:223]
s_waitcnt lgkmcnt(8)
v_mfma_f32_16x16x32_bf16 a[224:227], v[64:67], v[104:107], a[224:227]
ds_read_b64_tr_b16 v[48:49], v12 offset:32768
v_mfma_f32_16x16x32_bf16 a[228:231], v[64:67], v[108:111], a[228:231]
v_mfma_f32_16x16x32_bf16 a[232:235], v[64:67], v[112:115], a[232:235]
ds_read_b64_tr_b16 v[50:51], v12 offset:34816
v_mfma_f32_16x16x32_bf16 a[236:239], v[64:67], v[116:119], a[236:239]
s_waitcnt lgkmcnt(7)
v_mfma_f32_16x16x32_bf16 a[240:243], v[68:71], v[104:107], a[240:243]
ds_read_b64_tr_b16 v[52:53], v13 offset:32768
v_mfma_f32_16x16x32_bf16 a[244:247], v[68:71], v[108:111], a[244:247]
v_mfma_f32_16x16x32_bf16 a[248:251], v[68:71], v[112:115], a[248:251]
ds_read_b64_tr_b16 v[54:55], v13 offset:34816
v_mfma_f32_16x16x32_bf16 a[252:255], v[68:71], v[116:119], a[252:255]
s_waitcnt lgkmcnt(6)
v_mfma_f32_16x16x32_bf16 a[0:3], v[40:43], v[72:75], a[0:3]
ds_read_b64_tr_b16 v[56:57], v14 offset:32768
ds_read_b64_tr_b16 v[58:59], v14 offset:34816
v_mfma_f32_16x16x32_bf16 a[4:7], v[40:43], v[76:79], a[4:7]
s_add_u32 s28, s37, 3932160
v_mfma_f32_16x16x32_bf16 a[8:11], v[40:43], v[80:83], a[8:11]
s_add_u32 m0, s34, 0
v_mfma_f32_16x16x32_bf16 a[12:15], v[40:43], v[84:87], a[12:15]
buffer_load_dwordx4 v4, s[8:11], s28 offen lds
s_waitcnt lgkmcnt(6)
v_mfma_f32_16x16x32_bf16 a[16:19], v[44:47], v[72:75], a[16:19]
ds_read_b64_tr_b16 v[60:61], v15 offset:32768
v_mfma_f32_16x16x32_bf16 a[20:23], v[44:47], v[76:79], a[20:23]
v_mfma_f32_16x16x32_bf16 a[24:27], v[44:47], v[80:83], a[24:27]
ds_read_b64_tr_b16 v[62:63], v15 offset:34816
v_mfma_f32_16x16x32_bf16 a[28:31], v[44:47], v[84:87], a[28:31]
s_waitcnt lgkmcnt(6)
v_mfma_f32_16x16x32_bf16 a[32:35], v[48:51], v[72:75], a[32:35]
ds_read_b64_tr_b16 v[64:65], v16 offset:32768
v_mfma_f32_16x16x32_bf16 a[36:39], v[48:51], v[76:79], a[36:39]
v_mfma_f32_16x16x32_bf16 a[40:43], v[48:51], v[80:83], a[40:43]
ds_read_b64_tr_b16 v[66:67], v16 offset:34816
v_mfma_f32_16x16x32_bf16 a[44:47], v[48:51], v[84:87], a[44:47]
s_waitcnt lgkmcnt(6)
v_mfma_f32_16x16x32_bf16 a[48:51], v[52:55], v[72:75], a[48:51]
ds_read_b64_tr_b16 v[68:69], v17 offset:32768
v_mfma_f32_16x16x32_bf16 a[52:55], v[52:55], v[76:79], a[52:55]
ds_read_b64_tr_b16 v[70:71], v17 offset:34816
v_mfma_f32_16x16x32_bf16 a[56:59], v[52:55], v[80:83], a[56:59]
buffer_load_dwordx4 v5, s[8:11], s28 offen offset:1024 lds
v_mfma_f32_16x16x32_bf16 a[60:63], v[52:55], v[84:87], a[60:63]
s_waitcnt lgkmcnt(6)
v_mfma_f32_16x16x32_bf16 a[64:67], v[56:59], v[72:75], a[64:67]
ds_read_b64_tr_b16 v[40:41], v10 offset:33024
v_mfma_f32_16x16x32_bf16 a[68:71], v[56:59], v[76:79], a[68:71]
v_mfma_f32_16x16x32_bf16 a[72:75], v[56:59], v[80:83], a[72:75]
ds_read_b64_tr_b16 v[42:43], v10 offset:35072
v_mfma_f32_16x16x32_bf16 a[76:79], v[56:59], v[84:87], a[76:79]
s_waitcnt lgkmcnt(6)
v_mfma_f32_16x16x32_bf16 a[80:83], v[60:63], v[72:75], a[80:83]
ds_read_b64_tr_b16 v[44:45], v11 offset:33024
v_mfma_f32_16x16x32_bf16 a[84:87], v[60:63], v[76:79], a[84:87]
v_mfma_f32_16x16x32_bf16 a[88:91], v[60:63], v[80:83], a[88:91]
ds_read_b64_tr_b16 v[46:47], v11 offset:35072
v_mfma_f32_16x16x32_bf16 a[92:95], v[60:63], v[84:87], a[92:95]
s_waitcnt lgkmcnt(6)
v_mfma_f32_16x16x32_bf16 a[96:99], v[64:67], v[72:75], a[96:99]
ds_read_b64_tr_b16 v[48:49], v12 offset:33024
v_mfma_f32_16x16x32_bf16 a[100:103], v[64:67], v[76:79], a[100:103]
ds_read_b64_tr_b16 v[50:51], v12 offset:35072
v_mfma_f32_16x16x32_bf16 a[104:107], v[64:67], v[80:83], a[104:107]
buffer_load_dwordx4 v6, s[8:11], s28 offen offset:2048 lds
v_mfma_f32_16x16x32_bf16 a[108:111], v[64:67], v[84:87], a[108:111]
s_waitcnt lgkmcnt(6)
v_mfma_f32_16x16x32_bf16 a[112:115], v[68:71], v[72:75], a[112:115]
ds_read_b64_tr_b16 v[52:53], v13 offset:33024
v_mfma_f32_16x16x32_bf16 a[116:119], v[68:71], v[76:79], a[116:119]
v_mfma_f32_16x16x32_bf16 a[120:123], v[68:71], v[80:83], a[120:123]
ds_read_b64_tr_b16 v[54:55], v13 offset:35072
v_mfma_f32_16x16x32_bf16 a[124:127], v[68:71], v[84:87], a[124:127]
s_waitcnt lgkmcnt(6)
v_mfma_f32_16x16x32_bf16 a[128:131], v[40:43], v[72:75], a[128:131]
ds_read_b64_tr_b16 v[56:57], v14 offset:33024
v_mfma_f32_16x16x32_bf16 a[132:135], v[40:43], v[76:79], a[132:135]
v_mfma_f32_16x16x32_bf16 a[136:139], v[40:43], v[80:83], a[136:139]
ds_read_b64_tr_b16 v[58:59], v14 offset:35072
v_mfma_f32_16x16x32_bf16 a[140:143], v[40:43], v[84:87], a[140:143]
s_waitcnt lgkmcnt(6)
v_mfma_f32_16x16x32_bf16 a[144:147], v[44:47], v[72:75], a[144:147]
ds_read_b64_tr_b16 v[60:61], v15 offset:33024
v_mfma_f32_16x16x32_bf16 a[148:151], v[44:47], v[76:79], a[148:151]
ds_read_b64_tr_b16 v[62:63], v15 offset:35072
v_mfma_f32_16x16x32_bf16 a[152:155], v[44:47], v[80:83], a[152:155]
buffer_load_dwordx4 v7, s[8:11], s28 offen offset:3072 lds
v_mfma_f32_16x16x32_bf16 a[156:159], v[44:47], v[84:87], a[156:159]
s_waitcnt lgkmcnt(6)
v_mfma_f32_16x16x32_bf16 a[160:163], v[48:51], v[72:75], a[160:163]
ds_read_b64_tr_b16 v[64:65], v16 offset:33024
v_mfma_f32_16x16x32_bf16 a[164:167], v[48:51], v[76:79], a[164:167]
v_mfma_f32_16x16x32_bf16 a[168:171], v[48:51], v[80:83], a[168:171]
ds_read_b64_tr_b16 v[66:67], v16 offset:35072
v_mfma_f32_16x16x32_bf16 a[172:175], v[48:51], v[84:87], a[172:175]
s_waitcnt lgkmcnt(6)
v_mfma_f32_16x16x32_bf16 a[176:179], v[52:55], v[72:75], a[176:179]
ds_read_b64_tr_b16 v[68:69], v17 offset:33024
v_mfma_f32_16x16x32_bf16 a[180:183], v[52:55], v[76:79], a[180:183]
v_mfma_f32_16x16x32_bf16 a[184:187], v[52:55], v[80:83], a[184:187]
ds_read_b64_tr_b16 v[70:71], v17 offset:35072
v_mfma_f32_16x16x32_bf16 a[188:191], v[52:55], v[84:87], a[188:191]
s_waitcnt vmcnt(14)
s_barrier
s_waitcnt lgkmcnt(6)
v_mfma_f32_16x16x32_bf16 a[192:195], v[56:59], v[72:75], a[192:195]
ds_read_b64_tr_b16 v[40:41], v10 offset:49152
v_mfma_f32_16x16x32_bf16 a[196:199], v[56:59], v[76:79], a[196:199]
v_mfma_f32_16x16x32_bf16 a[200:203], v[56:59], v[80:83], a[200:203]
ds_read_b64_tr_b16 v[42:43], v10 offset:51200
v_mfma_f32_16x16x32_bf16 a[204:207], v[56:59], v[84:87], a[204:207]
s_waitcnt lgkmcnt(6)
v_mfma_f32_16x16x32_bf16 a[208:211], v[60:63], v[72:75], a[208:211]
ds_read_b64_tr_b16 v[44:45], v11 offset:49152
v_mfma_f32_16x16x32_bf16 a[212:215], v[60:63], v[76:79], a[212:215]
v_mfma_f32_16x16x32_bf16 a[216:219], v[60:63], v[80:83], a[216:219]
ds_read_b64_tr_b16 v[46:47], v11 offset:51200
v_mfma_f32_16x16x32_bf16 a[220:223], v[60:63], v[84:87], a[220:223]
s_waitcnt lgkmcnt(6)
v_mfma_f32_16x16x32_bf16 a[224:227], v[64:67], v[72:75], a[224:227]
ds_read_b64_tr_b16 v[48:49], v12 offset:49152
v_mfma_f32_16x16x32_bf16 a[228:231], v[64:67], v[76:79], a[228:231]
v_mfma_f32_16x16x32_bf16 a[232:235], v[64:67], v[80:83], a[232:235]
ds_read_b64_tr_b16 v[50:51], v12 offset:51200
v_mfma_f32_16x16x32_bf16 a[236:239], v[64:67], v[84:87], a[236:239]
s_waitcnt lgkmcnt(6)
v_mfma_f32_16x16x32_bf16 a[240:243], v[68:71], v[72:75], a[240:243]
ds_read_b64_tr_b16 v[52:53], v13 offset:49152
v_mfma_f32_16x16x32_bf16 a[244:247], v[68:71], v[76:79], a[244:247]
v_mfma_f32_16x16x32_bf16 a[248:251], v[68:71], v[80:83], a[248:251]
ds_read_b64_tr_b16 v[54:55], v13 offset:51200
v_mfma_f32_16x16x32_bf16 a[252:255], v[68:71], v[84:87], a[252:255]
s_waitcnt lgkmcnt(6)
v_mfma_f32_16x16x32_bf16 a[0:3], v[40:43], v[88:91], a[0:3]
ds_read_b64_tr_b16 v[56:57], v14 offset:49152
ds_read_b64_tr_b16 v[58:59], v14 offset:51200
v_mfma_f32_16x16x32_bf16 a[4:7], v[40:43], v[92:95], a[4:7]
s_add_u32 s28, s71, 0
v_mfma_f32_16x16x32_bf16 a[8:11], v[40:43], v[96:99], a[8:11]
s_add_u32 m0, s70, 0
v_mfma_f32_16x16x32_bf16 a[12:15], v[40:43], v[100:103], a[12:15]
buffer_load_dwordx4 v29, s[76:79], s28 offen lds
s_waitcnt lgkmcnt(6)
v_mfma_f32_16x16x32_bf16 a[16:19], v[44:47], v[88:91], a[16:19]
ds_read_b64_tr_b16 v[60:61], v15 offset:49152
v_mfma_f32_16x16x32_bf16 a[20:23], v[44:47], v[92:95], a[20:23]
v_mfma_f32_16x16x32_bf16 a[24:27], v[44:47], v[96:99], a[24:27]
ds_read_b64_tr_b16 v[62:63], v15 offset:51200
v_mfma_f32_16x16x32_bf16 a[28:31], v[44:47], v[100:103], a[28:31]
s_waitcnt lgkmcnt(6)
v_mfma_f32_16x16x32_bf16 a[32:35], v[48:51], v[88:91], a[32:35]
ds_read_b64_tr_b16 v[64:65], v16 offset:49152
v_mfma_f32_16x16x32_bf16 a[36:39], v[48:51], v[92:95], a[36:39]
v_mfma_f32_16x16x32_bf16 a[40:43], v[48:51], v[96:99], a[40:43]
ds_read_b64_tr_b16 v[66:67], v16 offset:51200
v_mfma_f32_16x16x32_bf16 a[44:47], v[48:51], v[100:103], a[44:47]
s_waitcnt lgkmcnt(6)
v_mfma_f32_16x16x32_bf16 a[48:51], v[52:55], v[88:91], a[48:51]
ds_read_b64_tr_b16 v[68:69], v17 offset:49152
ds_read_b64_tr_b16 v[70:71], v17 offset:51200
v_mfma_f32_16x16x32_bf16 a[52:55], v[52:55], v[92:95], a[52:55]
s_waitcnt vmcnt(11)
v_mfma_f32_16x16x32_bf16 a[56:59], v[52:55], v[96:99], a[56:59]
s_add_u32 s28, s28, 31744
v_mfma_f32_16x16x32_bf16 a[60:63], v[52:55], v[100:103], a[60:63]
buffer_load_dwordx4 v29, s[76:79], s28 offen offset:1024 lds
s_waitcnt lgkmcnt(6)
v_mfma_f32_16x16x32_bf16 a[64:67], v[56:59], v[88:91], a[64:67]
ds_read_b64_tr_b16 v[40:41], v10 offset:49408
v_mfma_f32_16x16x32_bf16 a[68:71], v[56:59], v[92:95], a[68:71]
ds_read_b64_tr_b16 v[42:43], v10 offset:51456
v_mfma_f32_16x16x32_bf16 a[72:75], v[56:59], v[96:99], a[72:75]
ds_read_b128 v[104:107], v26 offset:16384
v_mfma_f32_16x16x32_bf16 a[76:79], v[56:59], v[100:103], a[76:79]
s_waitcnt lgkmcnt(7)
v_mfma_f32_16x16x32_bf16 a[80:83], v[60:63], v[88:91], a[80:83]
ds_read_b64_tr_b16 v[44:45], v11 offset:49408
v_mfma_f32_16x16x32_bf16 a[84:87], v[60:63], v[92:95], a[84:87]
ds_read_b64_tr_b16 v[46:47], v11 offset:51456
v_mfma_f32_16x16x32_bf16 a[88:91], v[60:63], v[96:99], a[88:91]
ds_read_b128 v[108:111], v26 offset:18432
v_mfma_f32_16x16x32_bf16 a[92:95], v[60:63], v[100:103], a[92:95]
s_waitcnt lgkmcnt(8)
v_mfma_f32_16x16x32_bf16 a[96:99], v[64:67], v[88:91], a[96:99]
ds_read_b64_tr_b16 v[48:49], v12 offset:49408
ds_read_b64_tr_b16 v[50:51], v12 offset:51456
v_mfma_f32_16x16x32_bf16 a[100:103], v[64:67], v[92:95], a[100:103]
ds_read_b128 v[112:115], v26 offset:0
v_mfma_f32_16x16x32_bf16 a[104:107], v[64:67], v[96:99], a[104:107]
s_add_u32 s28, s73, 0
v_mfma_f32_16x16x32_bf16 a[108:111], v[64:67], v[100:103], a[108:111]
buffer_load_dwordx4 v30, s[80:83], s28 offen offset:2048 lds
s_waitcnt lgkmcnt(9)
v_mfma_f32_16x16x32_bf16 a[112:115], v[68:71], v[88:91], a[112:115]
ds_read_b64_tr_b16 v[52:53], v13 offset:49408
v_mfma_f32_16x16x32_bf16 a[116:119], v[68:71], v[92:95], a[116:119]
ds_read_b64_tr_b16 v[54:55], v13 offset:51456
v_mfma_f32_16x16x32_bf16 a[120:123], v[68:71], v[96:99], a[120:123]
ds_read_b128 v[116:119], v26 offset:2048
v_mfma_f32_16x16x32_bf16 a[124:127], v[68:71], v[100:103], a[124:127]
s_waitcnt lgkmcnt(10)
v_mfma_f32_16x16x32_bf16 a[128:131], v[40:43], v[88:91], a[128:131]
ds_read_b64_tr_b16 v[56:57], v14 offset:49408
v_mfma_f32_16x16x32_bf16 a[132:135], v[40:43], v[92:95], a[132:135]
ds_read_b64_tr_b16 v[58:59], v14 offset:51456
v_mfma_f32_16x16x32_bf16 a[136:139], v[40:43], v[96:99], a[136:139]
ds_read_b128 v[72:75], v27 offset:16384
v_mfma_f32_16x16x32_bf16 a[140:143], v[40:43], v[100:103], a[140:143]
s_waitcnt lgkmcnt(10)
v_mfma_f32_16x16x32_bf16 a[144:147], v[44:47], v[88:91], a[144:147]
ds_read_b64_tr_b16 v[60:61], v15 offset:49408
v_mfma_f32_16x16x32_bf16 a[148:151], v[44:47], v[92:95], a[148:151]
ds_read_b64_tr_b16 v[62:63], v15 offset:51456
v_mfma_f32_16x16x32_bf16 a[152:155], v[44:47], v[96:99], a[152:155]
ds_read_b128 v[76:79], v27 offset:18432
v_mfma_f32_16x16x32_bf16 a[156:159], v[44:47], v[100:103], a[156:159]
buffer_load_dwordx4 v30, s[84:87], s28 offen offset:3072 lds
s_waitcnt lgkmcnt(10)
v_mfma_f32_16x16x32_bf16 a[160:163], v[48:51], v[88:91], a[160:163]
ds_read_b64_tr_b16 v[64:65], v16 offset:49408
v_mfma_f32_16x16x32_bf16 a[164:167], v[48:51], v[92:95], a[164:167]
ds_read_b64_tr_b16 v[66:67], v16 offset:51456
v_mfma_f32_16x16x32_bf16 a[168:171], v[48:51], v[96:99], a[168:171]
ds_read_b128 v[80:83], v27 offset:0
v_mfma_f32_16x16x32_bf16 a[172:175], v[48:51], v[100:103], a[172:175]
s_waitcnt lgkmcnt(10)
v_mfma_f32_16x16x32_bf16 a[176:179], v[52:55], v[88:91], a[176:179]
ds_read_b64_tr_b16 v[68:69], v17 offset:49408
v_mfma_f32_16x16x32_bf16 a[180:183], v[52:55], v[92:95], a[180:183]
ds_read_b64_tr_b16 v[70:71], v17 offset:51456
v_mfma_f32_16x16x32_bf16 a[184:187], v[52:55], v[96:99], a[184:187]
ds_read_b128 v[84:87], v27 offset:2048
v_mfma_f32_16x16x32_bf16 a[188:191], v[52:55], v[100:103], a[188:191]
s_waitcnt vmcnt(4)
s_barrier
s_waitcnt lgkmcnt(10)
v_mfma_f32_16x16x32_bf16 a[192:195], v[56:59], v[88:91], a[192:195]
ds_read_b64_tr_b16 v[40:41], v18 offset:0
v_mfma_f32_16x16x32_bf16 a[196:199], v[56:59], v[92:95], a[196:199]
v_mfma_f32_16x16x32_bf16 a[200:203], v[56:59], v[96:99], a[200:203]
ds_read_b64_tr_b16 v[42:43], v18 offset:2048
v_mfma_f32_16x16x32_bf16 a[204:207], v[56:59], v[100:103], a[204:207]
s_waitcnt lgkmcnt(9)
v_mfma_f32_16x16x32_bf16 a[208:211], v[60:63], v[88:91], a[208:211]
ds_read_b64_tr_b16 v[44:45], v10 offset:0
v_mfma_f32_16x16x32_bf16 a[212:215], v[60:63], v[92:95], a[212:215]
v_mfma_f32_16x16x32_bf16 a[216:219], v[60:63], v[96:99], a[216:219]
ds_read_b64_tr_b16 v[46:47], v10 offset:2048
v_mfma_f32_16x16x32_bf16 a[220:223], v[60:63], v[100:103], a[220:223]
s_waitcnt lgkmcnt(8)
v_mfma_f32_16x16x32_bf16 a[224:227], v[64:67], v[88:91], a[224:227]
ds_read_b64_tr_b16 v[48:49], v19 offset:0
v_mfma_f32_16x16x32_bf16 a[228:231], v[64:67], v[92:95], a[228:231]
v_mfma_f32_16x16x32_bf16 a[232:235], v[64:67], v[96:99], a[232:235]
ds_read_b64_tr_b16 v[50:51], v19 offset:2048
v_mfma_f32_16x16x32_bf16 a[236:239], v[64:67], v[100:103], a[236:239]
s_waitcnt lgkmcnt(7)
v_mfma_f32_16x16x32_bf16 a[240:243], v[68:71], v[88:91], a[240:243]
ds_read_b64_tr_b16 v[52:53], v11 offset:0
v_mfma_f32_16x16x32_bf16 a[244:247], v[68:71], v[92:95], a[244:247]
v_mfma_f32_16x16x32_bf16 a[248:251], v[68:71], v[96:99], a[248:251]
ds_read_b64_tr_b16 v[54:55], v11 offset:2048
v_mfma_f32_16x16x32_bf16 a[252:255], v[68:71], v[100:103], a[252:255]
s_waitcnt lgkmcnt(6)
v_mfma_f32_16x16x32_bf16 a[0:3], v[40:43], v[104:107], a[0:3]
ds_read_b64_tr_b16 v[56:57], v20 offset:0
v_mfma_f32_16x16x32_bf16 a[4:7], v[40:43], v[108:111], a[4:7]
v_mfma_f32_16x16x32_bf16 a[8:11], v[40:43], v[112:115], a[8:11]
ds_read_b64_tr_b16 v[58:59], v20 offset:2048
v_mfma_f32_16x16x32_bf16 a[12:15], v[40:43], v[116:119], a[12:15]
s_waitcnt lgkmcnt(6)
v_mfma_f32_16x16x32_bf16 a[0:3], v[44:47], v[72:75], a[0:3]
ds_read_b64_tr_b16 v[60:61], v12 offset:0
ds_read_b64_tr_b16 v[62:63], v12 offset:2048
v_mfma_f32_16x16x32_bf16 a[4:7], v[44:47], v[76:79], a[4:7]
s_add_u32 s28, s71, 262144
v_mfma_f32_16x16x32_bf16 a[8:11], v[44:47], v[80:83], a[8:11]
s_add_u32 m0, s70, 16384
v_mfma_f32_16x16x32_bf16 a[12:15], v[44:47], v[84:87], a[12:15]
buffer_load_dwordx4 v29, s[76:79], s28 offen lds
s_waitcnt lgkmcnt(6)
v_mfma_f32_16x16x32_bf16 a[16:19], v[48:51], v[104:107], a[16:19]
ds_read_b64_tr_b16 v[64:65], v21 offset:0
v_mfma_f32_16x16x32_bf16 a[20:23], v[48:51], v[108:111], a[20:23]
ds_read_b64_tr_b16 v[66:67], v21 offset:2048
v_mfma_f32_16x16x32_bf16 a[24:27], v[48:51], v[112:115], a[24:27]
s_add_u32 s28, s28, 31744
v_mfma_f32_16x16x32_bf16 a[28:31], v[48:51], v[116:119], a[28:31]
buffer_load_dwordx4 v29, s[76:79], s28 offen offset:1024 lds
s_waitcnt lgkmcnt(6)
v_mfma_f32_16x16x32_bf16 a[16:19], v[52:55], v[72:75], a[16:19]
ds_read_b64_tr_b16 v[68:69], v13 offset:0
v_mfma_f32_16x16x32_bf16 a[20:23], v[52:55], v[76:79], a[20:23]
ds_read_b64_tr_b16 v[70:71], v13 offset:2048
v_mfma_f32_16x16x32_bf16 a[24:27], v[52:55], v[80:83], a[24:27]
s_add_u32 s28, s73, 114688
v_mfma_f32_16x16x32_bf16 a[28:31], v[52:55], v[84:87], a[28:31]
buffer_load_dwordx4 v30, s[80:83], s28 offen offset:2048 lds
s_waitcnt lgkmcnt(6)
v_mfma_f32_16x16x32_bf16 a[32:35], v[56:59], v[104:107], a[32:35]
ds_read_b64_tr_b16 v[40:41], v22 offset:0
v_mfma_f32_16x16x32_bf16 a[36:39], v[56:59], v[108:111], a[36:39]
ds_read_b64_tr_b16 v[42:43], v22 offset:2048
v_mfma_f32_16x16x32_bf16 a[40:43], v[56:59], v[112:115], a[40:43]
buffer_load_dwordx4 v30, s[84:87], s28 offen offset:3072 lds
v_mfma_f32_16x16x32_bf16 a[44:47], v[56:59], v[116:119], a[44:47]
L_PW_LNK:
v_cmp_ne_u64 vcc, s[98:99], v[122:123]
s_nop 4
s_cbranch_vccz L_PK_LNK
s_mov_b32 s31, 1048576
L_PL_LNK:
s_sleep 2
global_load_dwordx2 v[122:123], v120, s[48:49] sc1
s_waitcnt vmcnt(0)
v_cmp_ne_u64 vcc, s[98:99], v[122:123]
s_sub_u32 s31, s31, 1
s_cbranch_scc1 L_PK_LNK
s_nop 4
s_cbranch_vccnz L_PL_LNK
L_PK_LNK:
s_waitcnt lgkmcnt(6)
v_mfma_f32_16x16x32_bf16 a[32:35], v[60:63], v[72:75], a[32:35]
ds_read_b64_tr_b16 v[44:45], v14 offset:0
ds_read_b64_tr_b16 v[46:47], v14 offset:2048
v_accvgpr_read_b32 v33, a0
v_accvgpr_read_b32 v34, a1
v_mfma_f32_16x16x32_bf16 a[36:39], v[60:63], v[76:79], a[36:39]
v_accvgpr_read_b32 v35, a2
v_accvgpr_read_b32 v36, a3
v_cvt_pk_bf16_f32 v128, v33, v34
v_mfma_f32_16x16x32_bf16 a[40:43], v[60:63], v[80:83], a[40:43]
v_cvt_pk_bf16_f32 v129, v35, v36
v_accvgpr_read_b32 v121, a16
v_accvgpr_read_b32 v124, a17
v_accvgpr_read_b32 v125, a18
v_mfma_f32_16x16x32_bf16 a[44:47], v[60:63], v[84:87], a[44:47]
v_accvgpr_read_b32 v126, a19
v_cvt_pk_bf16_f32 v130, v121, v124
v_cvt_pk_bf16_f32 v131, v125, v126
s_waitcnt lgkmcnt(6)
v_mfma_f32_16x16x32_bf16 a[48:51], v[64:67], v[104:107], a[48:51]
ds_read_b64_tr_b16 v[48:49], v23 offset:0
ds_read_b64_tr_b16 v[50:51], v23 offset:2048
s_mov_b32 s8, 0
buffer_load_dwordx4 v[88:91], v28, s[52:55], s8 offen offset:0 sc1
v_mfma_f32_16x16x32_bf16 a[52:55], v[64:67], v[108:111], a[52:55]
v_accvgpr_read_b32 v33, a4
v_accvgpr_read_b32 v34, a5
v_accvgpr_read_b32 v35, a6
v_accvgpr_read_b32 v36, a7
v_mfma_f32_16x16x32_bf16 a[56:59], v[64:67], v[112:115], a[56:59]
v_cvt_pk_bf16_f32 v144, v33, v34
v_cvt_pk_bf16_f32 v145, v35, v36
v_accvgpr_read_b32 v121, a20
v_accvgpr_read_b32 v124, a21
v_mfma_f32_16x16x32_bf16 a[60:63], v[64:67], v[116:119], a[60:63]
v_accvgpr_read_b32 v125, a22
v_accvgpr_read_b32 v126, a23
v_cvt_pk_bf16_f32 v146, v121, v124
v_cvt_pk_bf16_f32 v147, v125, v126
s_waitcnt lgkmcnt(6)
v_mfma_f32_16x16x32_bf16 a[48:51], v[68:71], v[72:75], a[48:51]
ds_read_b64_tr_b16 v[52:53], v15 offset:0
ds_read_b64_tr_b16 v[54:55], v15 offset:2048
s_mov_b32 s9, 32768
buffer_load_dwordx4 v[92:95], v28, s[52:55], s9 offen offset:0 sc1
v_mfma_f32_16x16x32_bf16 a[52:55], v[68:71], v[76:79], a[52:55]
v_accvgpr_read_b32 v33, a8
v_accvgpr_read_b32 v34, a9
v_accvgpr_read_b32 v35, a10
v_accvgpr_read_b32 v36, a11
v_mfma_f32_16x16x32_bf16 a[56:59], v[68:71], v[80:83], a[56:59]
v_cvt_pk_bf16_f32 v160, v33, v34
v_cvt_pk_bf16_f32 v161, v35, v36
v_accvgpr_read_b32 v121, a24
v_accvgpr_read_b32 v124, a25
v_mfma_f32_16x16x32_bf16 a[60:63], v[68:71], v[84:87], a[60:63]
v_accvgpr_read_b32 v125, a26
v_accvgpr_read_b32 v126, a27
v_cvt_pk_bf16_f32 v162, v121, v124
v_cvt_pk_bf16_f32 v163, v125, v126
s_waitcnt lgkmcnt(6)
v_mfma_f32_16x16x32_bf16 a[64:67], v[40:43], v[104:107], a[64:67]
ds_read_b64_tr_b16 v[56:57], v24 offset:0
ds_read_b64_tr_b16 v[58:59], v24 offset:2048
s_mov_b32 s10, 65536
buffer_load_dwordx4 v[96:99], v28, s[52:55], s10 offen offset:0 sc1
v_mfma_f32_16x16x32_bf16 a[68:71], v[40:43], v[108:111], a[68:71]
v_accvgpr_read_b32 v33, a12
v_accvgpr_read_b32 v34, a13
v_accvgpr_read_b32 v35, a14
v_accvgpr_read_b32 v36, a15
v_mfma_f32_16x16x32_bf16 a[72:75], v[40:43], v[112:115], a[72:75]
v_cvt_pk_bf16_f32 v176, v33, v34
v_cvt_pk_bf16_f32 v177, v35, v36
v_accvgpr_read_b32 v121, a28
v_accvgpr_read_b32 v124, a29
v_mfma_f32_16x16x32_bf16 a[76:79], v[40:43], v[116:119], a[76:79]
v_accvgpr_read_b32 v125, a30
v_accvgpr_read_b32 v126, a31
v_cvt_pk_bf16_f32 v178, v121, v124
v_cvt_pk_bf16_f32 v179, v125, v126
s_waitcnt lgkmcnt(6)
v_mfma_f32_16x16x32_bf16 a[64:67], v[44:47], v[72:75], a[64:67]
ds_read_b64_tr_b16 v[60:61], v16 offset:0
ds_read_b64_tr_b16 v[62:63], v16 offset:2048
s_mov_b32 s11, 98304
buffer_load_dwordx4 v[100:103], v28, s[52:55], s11 offen offset:0 sc1
v_mfma_f32_16x16x32_bf16 a[68:71], v[44:47], v[76:79], a[68:71]
v_accvgpr_read_b32 v33, a32
v_accvgpr_read_b32 v34, a33
v_accvgpr_read_b32 v35, a34
v_accvgpr_read_b32 v36, a35
v_mfma_f32_16x16x32_bf16 a[72:75], v[44:47], v[80:83], a[72:75]
v_cvt_pk_bf16_f32 v132, v33, v34
v_cvt_pk_bf16_f32 v133, v35, v36
v_accvgpr_read_b32 v121, a48
v_accvgpr_read_b32 v124, a49
v_mfma_f32_16x16x32_bf16 a[76:79], v[44:47], v[84:87], a[76:79]
v_accvgpr_read_b32 v125, a50
v_accvgpr_read_b32 v126, a51
v_cvt_pk_bf16_f32 v134, v121, v124
v_cvt_pk_bf16_f32 v135, v125, v126
s_waitcnt lgkmcnt(6)
v_mfma_f32_16x16x32_bf16 a[80:83], v[48:51], v[104:107], a[80:83]
ds_read_b64_tr_b16 v[64:65], v25 offset:0
ds_read_b64_tr_b16 v[66:67], v25 offset:2048
v_accvgpr_read_b32 v33, a36
v_accvgpr_read_b32 v34, a37
v_mfma_f32_16x16x32_bf16 a[84:87], v[48:51], v[108:111], a[84:87]
v_accvgpr_read_b32 v35, a38
v_accvgpr_read_b32 v36, a39
v_cvt_pk_bf16_f32 v148, v33, v34
v_mfma_f32_16x16x32_bf16 a[88:91], v[48:51], v[112:115], a[88:91]
v_cvt_pk_bf16_f32 v149, v35, v36
v_accvgpr_read_b32 v121, a52
v_accvgpr_read_b32 v124, a53
v_accvgpr_read_b32 v125, a54
v_mfma_f32_16x16x32_bf16 a[92:95], v[48:51], v[116:119], a[92:95]
v_accvgpr_read_b32 v126, a55
v_cvt_pk_bf16_f32 v150, v121, v124
v_cvt_pk_bf16_f32 v151, v125, v126
s_barrier
s_waitcnt lgkmcnt(6)
v_mfma_f32_16x16x32_bf16 a[80:83], v[52:55], v[72:75], a[80:83]
ds_read_b64_tr_b16 v[68:69], v17 offset:0
ds_read_b64_tr_b16 v[70:71], v17 offset:2048
v_accvgpr_read_b32 v33, a40
v_accvgpr_read_b32 v34, a41
v_mfma_f32_16x16x32_bf16 a[84:87], v[52:55], v[76:79], a[84:87]
v_accvgpr_read_b32 v35, a42
v_accvgpr_read_b32 v36, a43
v_cvt_pk_bf16_f32 v164, v33, v34
v_mfma_f32_16x16x32_bf16 a[88:91], v[52:55], v[80:83], a[88:91]
v_cvt_pk_bf16_f32 v165, v35, v36
v_accvgpr_read_b32 v121, a56
v_accvgpr_read_b32 v124, a57
v_accvgpr_read_b32 v125, a58
v_mfma_f32_16x16x32_bf16 a[92:95], v[52:55], v[84:87], a[92:95]
v_accvgpr_read_b32 v126, a59
v_cvt_pk_bf16_f32 v166, v121, v124
v_cvt_pk_bf16_f32 v167, v125, v126
s_waitcnt lgkmcnt(6)
v_mfma_f32_16x16x32_bf16 a[96:99], v[56:59], v[104:107], a[96:99]
ds_read_b64_tr_b16 v[40:41], v18 offset:256
ds_read_b64_tr_b16 v[42:43], v18 offset:2304
s_add_u32 s28, s71, 524288
s_add_u32 m0, s70, 32768
s_nop 0
buffer_load_dwordx4 v29, s[76:79], s28 offen lds
v_mfma_f32_16x16x32_bf16 a[100:103], v[56:59], v[108:111], a[100:103]
v_accvgpr_read_b32 v33, a44
v_accvgpr_read_b32 v34, a45
v_accvgpr_read_b32 v35, a46
v_accvgpr_read_b32 v36, a47
v_mfma_f32_16x16x32_bf16 a[104:107], v[56:59], v[112:115], a[104:107]
v_cvt_pk_bf16_f32 v180, v33, v34
v_cvt_pk_bf16_f32 v181, v35, v36
v_accvgpr_read_b32 v121, a60
v_accvgpr_read_b32 v124, a61
v_mfma_f32_16x16x32_bf16 a[108:111], v[56:59], v[116:119], a[108:111]
v_accvgpr_read_b32 v125, a62
v_accvgpr_read_b32 v126, a63
v_cvt_pk_bf16_f32 v182, v121, v124
v_cvt_pk_bf16_f32 v183, v125, v126
s_waitcnt lgkmcnt(6)
v_mfma_f32_16x16x32_bf16 a[96:99], v[60:63], v[72:75], a[96:99]
ds_read_b64_tr_b16 v[44:45], v10 offset:256
ds_read_b64_tr_b16 v[46:47], v10 offset:2304
s_add_u32 s28, s28, 31744
buffer_load_dwordx4 v29, s[76:79], s28 offen offset:1024 lds
v_mfma_f32_16x16x32_bf16 a[100:103], v[60:63], v[76:79], a[100:103]
v_accvgpr_read_b32 v33, a64
v_accvgpr_read_b32 v34, a65
v_accvgpr_read_b32 v35, a66
v_accvgpr_read_b32 v36, a67
v_mfma_f32_16x16x32_bf16 a[104:107], v[60:63], v[80:83], a[104:107]
v_cvt_pk_bf16_f32 v136, v33, v34
v_cvt_pk_bf16_f32 v137, v35, v36
v_accvgpr_read_b32 v121, a80
v_accvgpr_read_b32 v124, a81
v_mfma_f32_16x16x32_bf16 a[108:111], v[60:63], v[84:87], a[108:111]
v_accvgpr_read_b32 v125, a82
v_accvgpr_read_b32 v126, a83
v_cvt_pk_bf16_f32 v138, v121, v124
v_cvt_pk_bf16_f32 v139, v125, v126
s_waitcnt lgkmcnt(6)
v_mfma_f32_16x16x32_bf16 a[112:115], v[64:67], v[104:107], a[112:115]
ds_read_b64_tr_b16 v[48:49], v19 offset:256
ds_read_b64_tr_b16 v[50:51], v19 offset:2304
s_add_u32 s28, s73, 229376
buffer_load_dwordx4 v30, s[80:83], s28 offen offset:2048 lds
v_mfma_f32_16x16x32_bf16 a[116:119], v[64:67], v[108:111], a[116:119]
v_accvgpr_read_b32 v33, a68
v_accvgpr_read_b32 v34, a69
v_accvgpr_read_b32 v35, a70
v_accvgpr_read_b32 v36, a71
v_mfma_f32_16x16x32_bf16 a[120:123], v[64:67], v[112:115], a[120:123]
v_cvt_pk_bf16_f32 v152, v33, v34
v_cvt_pk_bf16_f32 v153, v35, v36
v_accvgpr_read_b32 v121, a84
v_accvgpr_read_b32 v124, a85
v_mfma_f32_16x16x32_bf16 a[124:127], v[64:67], v[116:119], a[124:127]
v_accvgpr_read_b32 v125, a86
v_accvgpr_read_b32 v126, a87
v_cvt_pk_bf16_f32 v154, v121, v124
v_cvt_pk_bf16_f32 v155, v125, v126
s_waitcnt lgkmcnt(6)
v_mfma_f32_16x16x32_bf16 a[112:115], v[68:71], v[72:75], a[112:115]
ds_read_b64_tr_b16 v[52:53], v11 offset:256
ds_read_b64_tr_b16 v[54:55], v11 offset:2304
buffer_load_dwordx4 v30, s[84:87], s28 offen offset:3072 lds
v_accvgpr_read_b32 v33, a72
v_mfma_f32_16x16x32_bf16 a[116:119], v[68:71], v[76:79], a[116:119]
v_accvgpr_read_b32 v34, a73
v_accvgpr_read_b32 v35, a74
v_accvgpr_read_b32 v36, a75
v_cvt_pk_bf16_f32 v168, v33, v34
v_mfma_f32_16x16x32_bf16 a[120:123], v[68:71], v[80:83], a[120:123]
v_cvt_pk_bf16_f32 v169, v35, v36
v_accvgpr_read_b32 v121, a88
v_accvgpr_read_b32 v124, a89
v_accvgpr_read_b32 v125, a90
v_mfma_f32_16x16x32_bf16 a[124:127], v[68:71], v[84:87], a[124:127]
v_accvgpr_read_b32 v126, a91
v_cvt_pk_bf16_f32 v170, v121, v124
v_cvt_pk_bf16_f32 v171, v125, v126
s_waitcnt lgkmcnt(6)
v_mfma_f32_16x16x32_bf16 a[128:131], v[40:43], v[104:107], a[128:131]
ds_read_b64_tr_b16 v[56:57], v20 offset:256
ds_read_b64_tr_b16 v[58:59], v20 offset:2304
v_accvgpr_read_b32 v33, a76
v_accvgpr_read_b32 v34, a77
v_mfma_f32_16x16x32_bf16 a[132:135], v[40:43], v[108:111], a[132:135]
v_accvgpr_read_b32 v35, a78
v_accvgpr_read_b32 v36, a79
v_cvt_pk_bf16_f32 v184, v33, v34
v_mfma_f32_16x16x32_bf16 a[136:139], v[40:43], v[112:115], a[136:139]
v_cvt_pk_bf16_f32 v185, v35, v36
v_accvgpr_read_b32 v121, a92
v_accvgpr_read_b32 v124, a93
v_accvgpr_read_b32 v125, a94
v_mfma_f32_16x16x32_bf16 a[140:143], v[40:43], v[116:119], a[140:143]
v_accvgpr_read_b32 v126, a95
v_cvt_pk_bf16_f32 v186, v121, v124
v_cvt_pk_bf16_f32 v187, v125, v126
s_waitcnt lgkmcnt(6)
v_mfma_f32_16x16x32_bf16 a[128:131], v[44:47], v[72:75], a[128:131]
ds_read_b64_tr_b16 v[60:61], v12 offset:256
ds_read_b64_tr_b16 v[62:63], v12 offset:2304
v_accvgpr_read_b32 v33, a96
v_accvgpr_read_b32 v34, a97
v_mfma_f32_16x16x32_bf16 a[132:135], v[44:47], v[76:79], a[132:135]
v_accvgpr_read_b32 v35, a98
v_accvgpr_read_b32 v36, a99
v_cvt_pk_bf16_f32 v140, v33, v34
v_mfma_f32_16x16x32_bf16 a[136:139], v[44:47], v[80:83], a[136:139]
v_cvt_pk_bf16_f32 v141, v35, v36
v_accvgpr_read_b32 v121, a112
v_accvgpr_read_b32 v124, a113
v_accvgpr_read_b32 v125, a114
v_mfma_f32_16x16x32_bf16 a[140:143], v[44:47], v[84:87], a[140:143]
v_accvgpr_read_b32 v126, a115
v_cvt_pk_bf16_f32 v142, v121, v124
v_cvt_pk_bf16_f32 v143, v125, v126
s_waitcnt lgkmcnt(6)
v_mfma_f32_16x16x32_bf16 a[144:147], v[48:51], v[104:107], a[144:147]
ds_read_b64_tr_b16 v[64:65], v21 offset:256
ds_read_b64_tr_b16 v[66:67], v21 offset:2304
s_mov_b32 s31, 0
buffer_store_dwordx4 v[128:131], v28, s[92:95], s31 offen offset:0 sc1
v_mfma_f32_16x16x32_bf16 a[148:151], v[48:51], v[108:111], a[148:151]
v_accvgpr_read_b32 v33, a100
v_accvgpr_read_b32 v34, a101
v_accvgpr_read_b32 v35, a102
v_accvgpr_read_b32 v36, a103
v_mfma_f32_16x16x32_bf16 a[152:155], v[48:51], v[112:115], a[152:155]
v_cvt_pk_bf16_f32 v156, v33, v34
v_cvt_pk_bf16_f32 v157, v35, v36
v_accvgpr_read_b32 v121, a116
v_accvgpr_read_b32 v124, a117
v_mfma_f32_16x16x32_bf16 a[156:159], v[48:51], v[116:119], a[156:159]
v_accvgpr_read_b32 v125, a118
v_accvgpr_read_b32 v126, a119
v_cvt_pk_bf16_f32 v158, v121, v124
)ASM"
R"ASM(v_cvt_pk_bf16_f32 v159, v125, v126
s_waitcnt lgkmcnt(6)
v_mfma_f32_16x16x32_bf16 a[144:147], v[52:55], v[72:75], a[144:147]
ds_read_b64_tr_b16 v[68:69], v13 offset:256
ds_read_b64_tr_b16 v[70:71], v13 offset:2304
buffer_store_dwordx4 v[132:135], v28, s[92:95], s31 offen offset:1024 sc1
v_accvgpr_read_b32 v33, a104
v_mfma_f32_16x16x32_bf16 a[148:151], v[52:55], v[76:79], a[148:151]
v_accvgpr_read_b32 v34, a105
v_accvgpr_read_b32 v35, a106
v_accvgpr_read_b32 v36, a107
v_cvt_pk_bf16_f32 v172, v33, v34
v_mfma_f32_16x16x32_bf16 a[152:155], v[52:55], v[80:83], a[152:155]
v_cvt_pk_bf16_f32 v173, v35, v36
v_accvgpr_read_b32 v121, a120
v_accvgpr_read_b32 v124, a121
v_accvgpr_read_b32 v125, a122
v_mfma_f32_16x16x32_bf16 a[156:159], v[52:55], v[84:87], a[156:159]
v_accvgpr_read_b32 v126, a123
v_cvt_pk_bf16_f32 v174, v121, v124
v_cvt_pk_bf16_f32 v175, v125, v126
s_waitcnt lgkmcnt(6)
v_mfma_f32_16x16x32_bf16 a[160:163], v[56:59], v[104:107], a[160:163]
ds_read_b64_tr_b16 v[40:41], v22 offset:256
ds_read_b64_tr_b16 v[42:43], v22 offset:2304
v_accvgpr_read_b32 v33, a108
v_accvgpr_read_b32 v34, a109
v_mfma_f32_16x16x32_bf16 a[164:167], v[56:59], v[108:111], a[164:167]
v_accvgpr_read_b32 v35, a110
v_accvgpr_read_b32 v36, a111
v_cvt_pk_bf16_f32 v188, v33, v34
v_mfma_f32_16x16x32_bf16 a[168:171], v[56:59], v[112:115], a[168:171]
v_cvt_pk_bf16_f32 v189, v35, v36
v_accvgpr_read_b32 v121, a124
v_accvgpr_read_b32 v124, a125
v_accvgpr_read_b32 v125, a126
v_mfma_f32_16x16x32_bf16 a[172:175], v[56:59], v[116:119], a[172:175]
v_accvgpr_read_b32 v126, a127
v_cvt_pk_bf16_f32 v190, v121, v124
v_cvt_pk_bf16_f32 v191, v125, v126
s_waitcnt lgkmcnt(6)
v_mfma_f32_16x16x32_bf16 a[160:163], v[60:63], v[72:75], a[160:163]
ds_read_b64_tr_b16 v[44:45], v14 offset:256
ds_read_b64_tr_b16 v[46:47], v14 offset:2304
buffer_store_dwordx4 v[136:139], v28, s[92:95], s31 offen offset:2048 sc1
v_accvgpr_read_b32 v33, a128
v_mfma_f32_16x16x32_bf16 a[164:167], v[60:63], v[76:79], a[164:167]
v_accvgpr_read_b32 v34, a129
v_accvgpr_read_b32 v35, a130
v_accvgpr_read_b32 v36, a131
v_cvt_pk_bf16_f32 v192, v33, v34
v_mfma_f32_16x16x32_bf16 a[168:171], v[60:63], v[80:83], a[168:171]
v_cvt_pk_bf16_f32 v193, v35, v36
v_accvgpr_read_b32 v121, a144
v_accvgpr_read_b32 v124, a145
v_accvgpr_read_b32 v125, a146
v_mfma_f32_16x16x32_bf16 a[172:175], v[60:63], v[84:87], a[172:175]
v_accvgpr_read_b32 v126, a147
v_cvt_pk_bf16_f32 v194, v121, v124
v_cvt_pk_bf16_f32 v195, v125, v126
s_waitcnt lgkmcnt(6)
v_mfma_f32_16x16x32_bf16 a[176:179], v[64:67], v[104:107], a[176:179]
ds_read_b64_tr_b16 v[48:49], v23 offset:256
ds_read_b64_tr_b16 v[50:51], v23 offset:2304
buffer_store_dwordx4 v[140:143], v28, s[92:95], s31 offen offset:3072 sc1
v_accvgpr_read_b32 v33, a132
v_mfma_f32_16x16x32_bf16 a[180:183], v[64:67], v[108:111], a[180:183]
v_accvgpr_read_b32 v34, a133
v_accvgpr_read_b32 v35, a134
v_accvgpr_read_b32 v36, a135
v_cvt_pk_bf16_f32 v208, v33, v34
v_mfma_f32_16x16x32_bf16 a[184:187], v[64:67], v[112:115], a[184:187]
v_cvt_pk_bf16_f32 v209, v35, v36
v_accvgpr_read_b32 v121, a148
v_accvgpr_read_b32 v124, a149
v_accvgpr_read_b32 v125, a150
v_mfma_f32_16x16x32_bf16 a[188:191], v[64:67], v[116:119], a[188:191]
v_accvgpr_read_b32 v126, a151
v_cvt_pk_bf16_f32 v210, v121, v124
v_cvt_pk_bf16_f32 v211, v125, v126
s_waitcnt lgkmcnt(6)
v_mfma_f32_16x16x32_bf16 a[176:179], v[68:71], v[72:75], a[176:179]
ds_read_b64_tr_b16 v[52:53], v15 offset:256
ds_read_b64_tr_b16 v[54:55], v15 offset:2304
v_accvgpr_read_b32 v33, a136
v_accvgpr_read_b32 v34, a137
v_mfma_f32_16x16x32_bf16 a[180:183], v[68:71], v[76:79], a[180:183]
v_accvgpr_read_b32 v35, a138
v_accvgpr_read_b32 v36, a139
v_cvt_pk_bf16_f32 v224, v33, v34
v_mfma_f32_16x16x32_bf16 a[184:187], v[68:71], v[80:83], a[184:187]
v_cvt_pk_bf16_f32 v225, v35, v36
v_accvgpr_read_b32 v121, a152
v_accvgpr_read_b32 v124, a153
v_accvgpr_read_b32 v125, a154
v_mfma_f32_16x16x32_bf16 a[188:191], v[68:71], v[84:87], a[188:191]
v_accvgpr_read_b32 v126, a155
v_cvt_pk_bf16_f32 v226, v121, v124
v_cvt_pk_bf16_f32 v227, v125, v126
s_waitcnt lgkmcnt(6)
v_mfma_f32_16x16x32_bf16 a[192:195], v[40:43], v[104:107], a[192:195]
ds_read_b64_tr_b16 v[56:57], v24 offset:256
ds_read_b64_tr_b16 v[58:59], v24 offset:2304
s_mov_b32 s31, 4096
buffer_store_dwordx4 v[144:147], v28, s[92:95], s31 offen offset:0 sc1
v_mfma_f32_16x16x32_bf16 a[196:199], v[40:43], v[108:111], a[196:199]
v_accvgpr_read_b32 v33, a140
v_accvgpr_read_b32 v34, a141
v_accvgpr_read_b32 v35, a142
v_accvgpr_read_b32 v36, a143
v_mfma_f32_16x16x32_bf16 a[200:203], v[40:43], v[112:115], a[200:203]
v_cvt_pk_bf16_f32 v240, v33, v34
v_cvt_pk_bf16_f32 v241, v35, v36
v_accvgpr_read_b32 v121, a156
v_accvgpr_read_b32 v124, a157
v_mfma_f32_16x16x32_bf16 a[204:207], v[40:43], v[116:119], a[204:207]
v_accvgpr_read_b32 v125, a158
v_accvgpr_read_b32 v126, a159
v_cvt_pk_bf16_f32 v242, v121, v124
v_cvt_pk_bf16_f32 v243, v125, v126
s_waitcnt lgkmcnt(6)
v_mfma_f32_16x16x32_bf16 a[192:195], v[44:47], v[72:75], a[192:195]
ds_read_b64_tr_b16 v[60:61], v16 offset:256
ds_read_b64_tr_b16 v[62:63], v16 offset:2304
v_accvgpr_read_b32 v33, a160
v_accvgpr_read_b32 v34, a161
v_mfma_f32_16x16x32_bf16 a[196:199], v[44:47], v[76:79], a[196:199]
v_accvgpr_read_b32 v35, a162
v_accvgpr_read_b32 v36, a163
v_cvt_pk_bf16_f32 v196, v33, v34
v_mfma_f32_16x16x32_bf16 a[200:203], v[44:47], v[80:83], a[200:203]
v_cvt_pk_bf16_f32 v197, v35, v36
v_accvgpr_read_b32 v121, a176
v_accvgpr_read_b32 v124, a177
v_accvgpr_read_b32 v125, a178
v_mfma_f32_16x16x32_bf16 a[204:207], v[44:47], v[84:87], a[204:207]
v_accvgpr_read_b32 v126, a179
v_cvt_pk_bf16_f32 v198, v121, v124
v_cvt_pk_bf16_f32 v199, v125, v126
s_waitcnt lgkmcnt(6)
v_mfma_f32_16x16x32_bf16 a[208:211], v[48:51], v[104:107], a[208:211]
ds_read_b64_tr_b16 v[64:65], v25 offset:256
ds_read_b64_tr_b16 v[66:67], v25 offset:2304
buffer_store_dwordx4 v[148:151], v28, s[92:95], s31 offen offset:1024 sc1
v_accvgpr_read_b32 v33, a164
v_mfma_f32_16x16x32_bf16 a[212:215], v[48:51], v[108:111], a[212:215]
v_accvgpr_read_b32 v34, a165
v_accvgpr_read_b32 v35, a166
v_accvgpr_read_b32 v36, a167
v_cvt_pk_bf16_f32 v212, v33, v34
v_mfma_f32_16x16x32_bf16 a[216:219], v[48:51], v[112:115], a[216:219]
v_cvt_pk_bf16_f32 v213, v35, v36
v_accvgpr_read_b32 v121, a180
v_accvgpr_read_b32 v124, a181
v_accvgpr_read_b32 v125, a182
v_mfma_f32_16x16x32_bf16 a[220:223], v[48:51], v[116:119], a[220:223]
v_accvgpr_read_b32 v126, a183
v_cvt_pk_bf16_f32 v214, v121, v124
v_cvt_pk_bf16_f32 v215, v125, v126
s_waitcnt lgkmcnt(6)
v_mfma_f32_16x16x32_bf16 a[208:211], v[52:55], v[72:75], a[208:211]
ds_read_b64_tr_b16 v[68:69], v17 offset:256
ds_read_b64_tr_b16 v[70:71], v17 offset:2304
buffer_store_dwordx4 v[152:155], v28, s[92:95], s31 offen offset:2048 sc1
v_accvgpr_read_b32 v33, a168
v_mfma_f32_16x16x32_bf16 a[212:215], v[52:55], v[76:79], a[212:215]
v_accvgpr_read_b32 v34, a169
v_accvgpr_read_b32 v35, a170
v_accvgpr_read_b32 v36, a171
v_cvt_pk_bf16_f32 v228, v33, v34
v_mfma_f32_16x16x32_bf16 a[216:219], v[52:55], v[80:83], a[216:219]
v_cvt_pk_bf16_f32 v229, v35, v36
v_accvgpr_read_b32 v121, a184
v_accvgpr_read_b32 v124, a185
v_accvgpr_read_b32 v125, a186
v_mfma_f32_16x16x32_bf16 a[220:223], v[52:55], v[84:87], a[220:223]
v_accvgpr_read_b32 v126, a187
v_cvt_pk_bf16_f32 v230, v121, v124
v_cvt_pk_bf16_f32 v231, v125, v126
s_waitcnt lgkmcnt(6)
v_mfma_f32_16x16x32_bf16 a[224:227], v[56:59], v[104:107], a[224:227]
v_accvgpr_read_b32 v33, a172
v_accvgpr_read_b32 v34, a173
v_accvgpr_read_b32 v35, a174
v_mfma_f32_16x16x32_bf16 a[228:231], v[56:59], v[108:111], a[228:231]
v_accvgpr_read_b32 v36, a175
v_cvt_pk_bf16_f32 v244, v33, v34
v_cvt_pk_bf16_f32 v245, v35, v36
v_mfma_f32_16x16x32_bf16 a[232:235], v[56:59], v[112:115], a[232:235]
v_accvgpr_read_b32 v121, a188
v_accvgpr_read_b32 v124, a189
v_accvgpr_read_b32 v125, a190
v_mfma_f32_16x16x32_bf16 a[236:239], v[56:59], v[116:119], a[236:239]
v_accvgpr_read_b32 v126, a191
v_cvt_pk_bf16_f32 v246, v121, v124
v_cvt_pk_bf16_f32 v247, v125, v126
s_waitcnt lgkmcnt(4)
v_mfma_f32_16x16x32_bf16 a[224:227], v[60:63], v[72:75], a[224:227]
buffer_store_dwordx4 v[156:159], v28, s[92:95], s31 offen offset:3072 sc1
v_accvgpr_read_b32 v33, a192
v_accvgpr_read_b32 v34, a193
v_accvgpr_read_b32 v35, a194
v_mfma_f32_16x16x32_bf16 a[228:231], v[60:63], v[76:79], a[228:231]
v_accvgpr_read_b32 v36, a195
v_cvt_pk_bf16_f32 v200, v33, v34
v_cvt_pk_bf16_f32 v201, v35, v36
v_mfma_f32_16x16x32_bf16 a[232:235], v[60:63], v[80:83], a[232:235]
v_accvgpr_read_b32 v121, a208
v_accvgpr_read_b32 v124, a209
v_accvgpr_read_b32 v125, a210
v_mfma_f32_16x16x32_bf16 a[236:239], v[60:63], v[84:87], a[236:239]
v_accvgpr_read_b32 v126, a211
v_cvt_pk_bf16_f32 v202, v121, v124
v_cvt_pk_bf16_f32 v203, v125, v126
s_waitcnt lgkmcnt(2)
v_mfma_f32_16x16x32_bf16 a[240:243], v[64:67], v[104:107], a[240:243]
v_accvgpr_read_b32 v33, a196
v_accvgpr_read_b32 v34, a197
v_accvgpr_read_b32 v35, a198
v_mfma_f32_16x16x32_bf16 a[244:247], v[64:67], v[108:111], a[244:247]
v_accvgpr_read_b32 v36, a199
v_cvt_pk_bf16_f32 v216, v33, v34
v_cvt_pk_bf16_f32 v217, v35, v36
v_mfma_f32_16x16x32_bf16 a[248:251], v[64:67], v[112:115], a[248:251]
v_accvgpr_read_b32 v121, a212
v_accvgpr_read_b32 v124, a213
v_accvgpr_read_b32 v125, a214
v_mfma_f32_16x16x32_bf16 a[252:255], v[64:67], v[116:119], a[252:255]
v_accvgpr_read_b32 v126, a215
v_cvt_pk_bf16_f32 v218, v121, v124
v_cvt_pk_bf16_f32 v219, v125, v126
s_waitcnt lgkmcnt(0)
v_mfma_f32_16x16x32_bf16 a[240:243], v[68:71], v[72:75], a[240:243]
v_accvgpr_read_b32 v33, a200
v_accvgpr_read_b32 v34, a201
v_accvgpr_read_b32 v35, a202
v_mfma_f32_16x16x32_bf16 a[244:247], v[68:71], v[76:79], a[244:247]
v_accvgpr_read_b32 v36, a203
v_cvt_pk_bf16_f32 v232, v33, v34
v_cvt_pk_bf16_f32 v233, v35, v36
v_mfma_f32_16x16x32_bf16 a[248:251], v[68:71], v[80:83], a[248:251]
v_accvgpr_read_b32 v121, a216
v_accvgpr_read_b32 v124, a217
v_accvgpr_read_b32 v125, a218
v_mfma_f32_16x16x32_bf16 a[252:255], v[68:71], v[84:87], a[252:255]
v_accvgpr_read_b32 v126, a219
v_cvt_pk_bf16_f32 v234, v121, v124
v_cvt_pk_bf16_f32 v235, v125, v126
s_nop 3
v_accvgpr_read_b32 v33, a204
v_accvgpr_read_b32 v34, a205
v_accvgpr_read_b32 v35, a206
v_accvgpr_read_b32 v36, a207
v_cvt_pk_bf16_f32 v248, v33, v34
v_cvt_pk_bf16_f32 v249, v35, v36
v_accvgpr_read_b32 v121, a220
v_accvgpr_read_b32 v124, a221
v_accvgpr_read_b32 v125, a222
v_accvgpr_read_b32 v126, a223
v_cvt_pk_bf16_f32 v250, v121, v124
v_cvt_pk_bf16_f32 v251, v125, v126
v_accvgpr_read_b32 v33, a224
v_accvgpr_read_b32 v34, a225
v_accvgpr_read_b32 v35, a226
v_accvgpr_read_b32 v36, a227
v_cvt_pk_bf16_f32 v204, v33, v34
v_cvt_pk_bf16_f32 v205, v35, v36
v_accvgpr_read_b32 v121, a240
v_accvgpr_read_b32 v124, a241
v_accvgpr_read_b32 v125, a242
v_accvgpr_read_b32 v126, a243
v_cvt_pk_bf16_f32 v206, v121, v124
v_cvt_pk_bf16_f32 v207, v125, v126
v_accvgpr_read_b32 v33, a228
v_accvgpr_read_b32 v34, a229
v_accvgpr_read_b32 v35, a230
v_accvgpr_read_b32 v36, a231
v_cvt_pk_bf16_f32 v220, v33, v34
v_cvt_pk_bf16_f32 v221, v35, v36
v_accvgpr_read_b32 v121, a244
v_accvgpr_read_b32 v124, a245
v_accvgpr_read_b32 v125, a246
v_accvgpr_read_b32 v126, a247
v_cvt_pk_bf16_f32 v222, v121, v124
v_cvt_pk_bf16_f32 v223, v125, v126
v_accvgpr_read_b32 v33, a232
v_accvgpr_read_b32 v34, a233
v_accvgpr_read_b32 v35, a234
v_accvgpr_read_b32 v36, a235
v_cvt_pk_bf16_f32 v236, v33, v34
v_cvt_pk_bf16_f32 v237, v35, v36
v_accvgpr_read_b32 v121, a248
v_accvgpr_read_b32 v124, a249
v_accvgpr_read_b32 v125, a250
v_accvgpr_read_b32 v126, a251
v_cvt_pk_bf16_f32 v238, v121, v124
v_cvt_pk_bf16_f32 v239, v125, v126
v_accvgpr_read_b32 v33, a236
v_accvgpr_read_b32 v34, a237
v_accvgpr_read_b32 v35, a238
v_accvgpr_read_b32 v36, a239
v_cvt_pk_bf16_f32 v252, v33, v34
v_cvt_pk_bf16_f32 v253, v35, v36
v_accvgpr_read_b32 v121, a252
v_accvgpr_read_b32 v124, a253
v_accvgpr_read_b32 v125, a254
v_accvgpr_read_b32 v126, a255
v_cvt_pk_bf16_f32 v254, v121, v124
v_cvt_pk_bf16_f32 v255, v125, v126
v_xor_b32 v10, 0, v31
v_xor_b32 v11, 32, v31
v_xor_b32 v12, 64, v31
v_xor_b32 v13, 96, v31
v_xor_b32 v14, 128, v31
v_xor_b32 v15, 160, v31
v_xor_b32 v16, 192, v31
v_xor_b32 v17, 224, v31
v_xor_b32 v18, 0, v32
v_xor_b32 v19, 32, v32
v_xor_b32 v20, 64, v32
v_xor_b32 v21, 96, v32
buffer_load_dwordx4 v[72:75], v28, s[52:55], s8 offen offset:1024 sc1
buffer_load_dwordx4 v[76:79], v28, s[52:55], s9 offen offset:1024 sc1
buffer_load_dwordx4 v[80:83], v28, s[52:55], s10 offen offset:1024 sc1
buffer_load_dwordx4 v[84:87], v28, s[52:55], s11 offen offset:1024 sc1
buffer_load_dwordx4 v[104:107], v28, s[52:55], s8 offen offset:2048 sc1
buffer_load_dwordx4 v[108:111], v28, s[52:55], s9 offen offset:2048 sc1
buffer_load_dwordx4 v[112:115], v28, s[52:55], s10 offen offset:2048 sc1
buffer_load_dwordx4 v[116:119], v28, s[52:55], s11 offen offset:2048 sc1
s_branch L_A_END
L_LN:
s_nop 15
s_nop 3
s_waitcnt lgkmcnt(0)
s_waitcnt lgkmcnt(0)
s_load_dwordx2 s[98:99], s[48:49], 0x0
s_and_b32 s22, s6, 7
s_lshl_b32 s22, s22, 2
s_lshr_b32 s23, s6, 3
s_and_b32 s23, s23, 3
s_add_u32 s20, s22, s23
s_lshl_b32 s21, s20, 2
s_add_u32 s21, s21, s7
s_lshl_b32 s24, s21, 14
s_add_u32 s40, s52, s24
s_addc_u32 s41, s53, 0
s_mov_b32 s8, s40
s_and_b32 s9, s41, 65535
s_mov_b32 s10, 16384
s_mov_b32 s11, 131072
s_add_u32 s25, s24, 1048576
s_add_u32 s40, s48, s25
s_addc_u32 s41, s49, 0
s_mov_b32 s12, s40
s_and_b32 s13, s41, 65535
s_mov_b32 s14, 16384
s_mov_b32 s15, 131072
v_and_b32 v6, 15, v1
v_lshlrev_b32 v4, 10, v6
v_lshrrev_b32 v6, 4, v1
v_lshlrev_b32 v6, 4, v6
v_add_u32 v4, v4, v6
v_lshlrev_b32 v5, 4, v1
buffer_load_dwordx4 v[128:131], v4, s[8:11], 0 offen offset:0
buffer_load_dwordx4 v[132:135], v4, s[8:11], 0 offen offset:64
buffer_load_dwordx4 v[136:139], v4, s[8:11], 0 offen offset:128
buffer_load_dwordx4 v[140:143], v4, s[8:11], 0 offen offset:192
buffer_load_dwordx4 v[144:147], v4, s[8:11], 0 offen offset:256
buffer_load_dwordx4 v[148:151], v4, s[8:11], 0 offen offset:320
buffer_load_dwordx4 v[152:155], v4, s[8:11], 0 offen offset:384
buffer_load_dwordx4 v[156:159], v4, s[8:11], 0 offen offset:448
buffer_load_dwordx4 v[160:163], v4, s[8:11], 0 offen offset:512
buffer_load_dwordx4 v[164:167], v4, s[8:11], 0 offen offset:576
buffer_load_dwordx4 v[168:171], v4, s[8:11], 0 offen offset:640
buffer_load_dwordx4 v[172:175], v4, s[8:11], 0 offen offset:704
buffer_load_dwordx4 v[176:179], v4, s[8:11], 0 offen offset:768
buffer_load_dwordx4 v[180:183], v4, s[8:11], 0 offen offset:832
buffer_load_dwordx4 v[184:187], v4, s[8:11], 0 offen offset:896
buffer_load_dwordx4 v[188:191], v4, s[8:11], 0 offen offset:960
s_waitcnt vmcnt(15)
v_lshlrev_b32 v64, 16, v128
v_and_b32 v65, 0xffff0000, v128
v_lshlrev_b32 v66, 16, v129
v_and_b32 v67, 0xffff0000, v129
v_lshlrev_b32 v68, 16, v130
v_and_b32 v69, 0xffff0000, v130
v_lshlrev_b32 v70, 16, v131
v_and_b32 v71, 0xffff0000, v131
v_mov_b32 v8, v64
v_mov_b32 v9, v65
v_mov_b32 v10, v66
v_mov_b32 v11, v67
v_add_f32 v8, v8, v68
v_add_f32 v9, v9, v69
v_add_f32 v10, v10, v70
v_add_f32 v11, v11, v71
s_waitcnt vmcnt(14)
v_lshlrev_b32 v72, 16, v132
v_and_b32 v73, 0xffff0000, v132
v_lshlrev_b32 v74, 16, v133
v_and_b32 v75, 0xffff0000, v133
v_lshlrev_b32 v76, 16, v134
v_and_b32 v77, 0xffff0000, v134
v_lshlrev_b32 v78, 16, v135
v_and_b32 v79, 0xffff0000, v135
v_add_f32 v8, v8, v72
v_add_f32 v9, v9, v73
v_add_f32 v10, v10, v74
v_add_f32 v11, v11, v75
v_add_f32 v8, v8, v76
v_add_f32 v9, v9, v77
v_add_f32 v10, v10, v78
v_add_f32 v11, v11, v79
s_waitcnt vmcnt(13)
v_lshlrev_b32 v80, 16, v136
v_and_b32 v81, 0xffff0000, v136
v_lshlrev_b32 v82, 16, v137
v_and_b32 v83, 0xffff0000, v137
v_lshlrev_b32 v84, 16, v138
v_and_b32 v85, 0xffff0000, v138
v_lshlrev_b32 v86, 16, v139
v_and_b32 v87, 0xffff0000, v139
v_add_f32 v8, v8, v80
v_add_f32 v9, v9, v81
v_add_f32 v10, v10, v82
v_add_f32 v11, v11, v83
v_add_f32 v8, v8, v84
v_add_f32 v9, v9, v85
v_add_f32 v10, v10, v86
v_add_f32 v11, v11, v87
s_waitcnt vmcnt(12)
v_lshlrev_b32 v88, 16, v140
v_and_b32 v89, 0xffff0000, v140
v_lshlrev_b32 v90, 16, v141
v_and_b32 v91, 0xffff0000, v141
v_lshlrev_b32 v92, 16, v142
v_and_b32 v93, 0xffff0000, v142
v_lshlrev_b32 v94, 16, v143
v_and_b32 v95, 0xffff0000, v143
v_add_f32 v8, v8, v88
v_add_f32 v9, v9, v89
v_add_f32 v10, v10, v90
v_add_f32 v11, v11, v91
v_add_f32 v8, v8, v92
v_add_f32 v9, v9, v93
v_add_f32 v10, v10, v94
v_add_f32 v11, v11, v95
s_waitcnt vmcnt(11)
v_lshlrev_b32 v96, 16, v144
v_and_b32 v97, 0xffff0000, v144
v_lshlrev_b32 v98, 16, v145
v_and_b32 v99, 0xffff0000, v145
v_lshlrev_b32 v100, 16, v146
v_and_b32 v101, 0xffff0000, v146
v_lshlrev_b32 v102, 16, v147
v_and_b32 v103, 0xffff0000, v147
v_add_f32 v8, v8, v96
v_add_f32 v9, v9, v97
v_add_f32 v10, v10, v98
v_add_f32 v11, v11, v99
v_add_f32 v8, v8, v100
v_add_f32 v9, v9, v101
v_add_f32 v10, v10, v102
v_add_f32 v11, v11, v103
s_waitcnt vmcnt(10)
v_lshlrev_b32 v104, 16, v148
v_and_b32 v105, 0xffff0000, v148
v_lshlrev_b32 v106, 16, v149
v_and_b32 v107, 0xffff0000, v149
v_lshlrev_b32 v108, 16, v150
v_and_b32 v109, 0xffff0000, v150
v_lshlrev_b32 v110, 16, v151
v_and_b32 v111, 0xffff0000, v151
v_add_f32 v8, v8, v104
v_add_f32 v9, v9, v105
v_add_f32 v10, v10, v106
v_add_f32 v11, v11, v107
v_add_f32 v8, v8, v108
v_add_f32 v9, v9, v109
v_add_f32 v10, v10, v110
v_add_f32 v11, v11, v111
s_waitcnt vmcnt(9)
v_lshlrev_b32 v112, 16, v152
v_and_b32 v113, 0xffff0000, v152
v_lshlrev_b32 v114, 16, v153
v_and_b32 v115, 0xffff0000, v153
v_lshlrev_b32 v116, 16, v154
v_and_b32 v117, 0xffff0000, v154
v_lshlrev_b32 v118, 16, v155
v_and_b32 v119, 0xffff0000, v155
v_add_f32 v8, v8, v112
v_add_f32 v9, v9, v113
v_add_f32 v10, v10, v114
v_add_f32 v11, v11, v115
v_add_f32 v8, v8, v116
v_add_f32 v9, v9, v117
v_add_f32 v10, v10, v118
v_add_f32 v11, v11, v119
s_waitcnt vmcnt(8)
v_lshlrev_b32 v120, 16, v156
v_and_b32 v121, 0xffff0000, v156
v_lshlrev_b32 v122, 16, v157
v_and_b32 v123, 0xffff0000, v157
v_lshlrev_b32 v124, 16, v158
v_and_b32 v125, 0xffff0000, v158
v_lshlrev_b32 v126, 16, v159
v_and_b32 v127, 0xffff0000, v159
v_add_f32 v8, v8, v120
v_add_f32 v9, v9, v121
v_add_f32 v10, v10, v122
v_add_f32 v11, v11, v123
v_add_f32 v8, v8, v124
v_add_f32 v9, v9, v125
v_add_f32 v10, v10, v126
v_add_f32 v11, v11, v127
s_waitcnt vmcnt(7)
v_lshlrev_b32 v192, 16, v160
v_and_b32 v193, 0xffff0000, v160
v_lshlrev_b32 v194, 16, v161
v_and_b32 v195, 0xffff0000, v161
v_lshlrev_b32 v196, 16, v162
v_and_b32 v197, 0xffff0000, v162
v_lshlrev_b32 v198, 16, v163
v_and_b32 v199, 0xffff0000, v163
v_add_f32 v8, v8, v192
v_add_f32 v9, v9, v193
v_add_f32 v10, v10, v194
v_add_f32 v11, v11, v195
v_add_f32 v8, v8, v196
v_add_f32 v9, v9, v197
v_add_f32 v10, v10, v198
v_add_f32 v11, v11, v199
s_waitcnt vmcnt(6)
v_lshlrev_b32 v200, 16, v164
v_and_b32 v201, 0xffff0000, v164
v_lshlrev_b32 v202, 16, v165
v_and_b32 v203, 0xffff0000, v165
v_lshlrev_b32 v204, 16, v166
v_and_b32 v205, 0xffff0000, v166
v_lshlrev_b32 v206, 16, v167
v_and_b32 v207, 0xffff0000, v167
v_add_f32 v8, v8, v200
v_add_f32 v9, v9, v201
v_add_f32 v10, v10, v202
v_add_f32 v11, v11, v203
v_add_f32 v8, v8, v204
v_add_f32 v9, v9, v205
v_add_f32 v10, v10, v206
v_add_f32 v11, v11, v207
s_waitcnt vmcnt(5)
v_lshlrev_b32 v208, 16, v168
v_and_b32 v209, 0xffff0000, v168
v_lshlrev_b32 v210, 16, v169
v_and_b32 v211, 0xffff0000, v169
v_lshlrev_b32 v212, 16, v170
v_and_b32 v213, 0xffff0000, v170
v_lshlrev_b32 v214, 16, v171
v_and_b32 v215, 0xffff0000, v171
v_add_f32 v8, v8, v208
v_add_f32 v9, v9, v209
v_add_f32 v10, v10, v210
v_add_f32 v11, v11, v211
v_add_f32 v8, v8, v212
v_add_f32 v9, v9, v213
v_add_f32 v10, v10, v214
v_add_f32 v11, v11, v215
s_waitcnt vmcnt(4)
v_lshlrev_b32 v216, 16, v172
v_and_b32 v217, 0xffff0000, v172
v_lshlrev_b32 v218, 16, v173
v_and_b32 v219, 0xffff0000, v173
v_lshlrev_b32 v220, 16, v174
v_and_b32 v221, 0xffff0000, v174
v_lshlrev_b32 v222, 16, v175
v_and_b32 v223, 0xffff0000, v175
v_add_f32 v8, v8, v216
v_add_f32 v9, v9, v217
v_add_f32 v10, v10, v218
v_add_f32 v11, v11, v219
v_add_f32 v8, v8, v220
v_add_f32 v9, v9, v221
v_add_f32 v10, v10, v222
v_add_f32 v11, v11, v223
s_waitcnt vmcnt(3)
v_lshlrev_b32 v224, 16, v176
v_and_b32 v225, 0xffff0000, v176
v_lshlrev_b32 v226, 16, v177
v_and_b32 v227, 0xffff0000, v177
v_lshlrev_b32 v228, 16, v178
v_and_b32 v229, 0xffff0000, v178
v_lshlrev_b32 v230, 16, v179
v_and_b32 v231, 0xffff0000, v179
v_add_f32 v8, v8, v224
v_add_f32 v9, v9, v225
v_add_f32 v10, v10, v226
v_add_f32 v11, v11, v227
v_add_f32 v8, v8, v228
v_add_f32 v9, v9, v229
v_add_f32 v10, v10, v230
v_add_f32 v11, v11, v231
s_waitcnt vmcnt(2)
v_lshlrev_b32 v232, 16, v180
v_and_b32 v233, 0xffff0000, v180
v_lshlrev_b32 v234, 16, v181
v_and_b32 v235, 0xffff0000, v181
v_lshlrev_b32 v236, 16, v182
v_and_b32 v237, 0xffff0000, v182
v_lshlrev_b32 v238, 16, v183
v_and_b32 v239, 0xffff0000, v183
v_add_f32 v8, v8, v232
v_add_f32 v9, v9, v233
v_add_f32 v10, v10, v234
v_add_f32 v11, v11, v235
v_add_f32 v8, v8, v236
v_add_f32 v9, v9, v237
v_add_f32 v10, v10, v238
v_add_f32 v11, v11, v239
s_waitcnt vmcnt(1)
v_lshlrev_b32 v240, 16, v184
v_and_b32 v241, 0xffff0000, v184
v_lshlrev_b32 v242, 16, v185
v_and_b32 v243, 0xffff0000, v185
v_lshlrev_b32 v244, 16, v186
v_and_b32 v245, 0xffff0000, v186
v_lshlrev_b32 v246, 16, v187
v_and_b32 v247, 0xffff0000, v187
v_add_f32 v8, v8, v240
v_add_f32 v9, v9, v241
v_add_f32 v10, v10, v242
v_add_f32 v11, v11, v243
v_add_f32 v8, v8, v244
v_add_f32 v9, v9, v245
v_add_f32 v10, v10, v246
v_add_f32 v11, v11, v247
s_waitcnt vmcnt(0)
v_lshlrev_b32 v248, 16, v188
v_and_b32 v249, 0xffff0000, v188
v_lshlrev_b32 v250, 16, v189
v_and_b32 v251, 0xffff0000, v189
v_lshlrev_b32 v252, 16, v190
v_and_b32 v253, 0xffff0000, v190
v_lshlrev_b32 v254, 16, v191
v_and_b32 v255, 0xffff0000, v191
v_add_f32 v8, v8, v248
v_add_f32 v9, v9, v249
v_add_f32 v10, v10, v250
v_add_f32 v11, v11, v251
v_add_f32 v8, v8, v252
v_add_f32 v9, v9, v253
v_add_f32 v10, v10, v254
v_add_f32 v11, v11, v255
v_add_f32 v8, v8, v9
v_add_f32 v10, v10, v11
v_add_f32 v8, v8, v10
v_mov_b32 v12, v8
s_nop 1
v_permlane16_swap_b32 v8, v12
v_add_f32 v8, v8, v12
v_mov_b32 v12, v8
s_nop 1
v_permlane32_swap_b32 v8, v12
v_add_f32 v8, v8, v12
v_mul_f32 v13, 0x3b000000, v8
v_sub_f32 v64, v64, v13
v_sub_f32 v65, v65, v13
v_sub_f32 v66, v66, v13
v_sub_f32 v67, v67, v13
v_sub_f32 v68, v68, v13
v_sub_f32 v69, v69, v13
v_sub_f32 v70, v70, v13
v_sub_f32 v71, v71, v13
v_sub_f32 v72, v72, v13
v_sub_f32 v73, v73, v13
v_sub_f32 v74, v74, v13
v_sub_f32 v75, v75, v13
v_sub_f32 v76, v76, v13
v_sub_f32 v77, v77, v13
v_sub_f32 v78, v78, v13
v_sub_f32 v79, v79, v13
v_sub_f32 v80, v80, v13
v_sub_f32 v81, v81, v13
v_sub_f32 v82, v82, v13
v_sub_f32 v83, v83, v13
v_sub_f32 v84, v84, v13
v_sub_f32 v85, v85, v13
v_sub_f32 v86, v86, v13
v_sub_f32 v87, v87, v13
v_sub_f32 v88, v88, v13
v_sub_f32 v89, v89, v13
v_sub_f32 v90, v90, v13
v_sub_f32 v91, v91, v13
v_sub_f32 v92, v92, v13
v_sub_f32 v93, v93, v13
v_sub_f32 v94, v94, v13
v_sub_f32 v95, v95, v13
v_sub_f32 v96, v96, v13
v_sub_f32 v97, v97, v13
v_sub_f32 v98, v98, v13
v_sub_f32 v99, v99, v13
v_sub_f32 v100, v100, v13
v_sub_f32 v101, v101, v13
v_sub_f32 v102, v102, v13
v_sub_f32 v103, v103, v13
v_sub_f32 v104, v104, v13
v_sub_f32 v105, v105, v13
v_sub_f32 v106, v106, v13
v_sub_f32 v107, v107, v13
v_sub_f32 v108, v108, v13
v_sub_f32 v109, v109, v13
v_sub_f32 v110, v110, v13
v_sub_f32 v111, v111, v13
v_sub_f32 v112, v112, v13
v_sub_f32 v113, v113, v13
v_sub_f32 v114, v114, v13
v_sub_f32 v115, v115, v13
v_sub_f32 v116, v116, v13
v_sub_f32 v117, v117, v13
v_sub_f32 v118, v118, v13
v_sub_f32 v119, v119, v13
v_sub_f32 v120, v120, v13
v_sub_f32 v121, v121, v13
v_sub_f32 v122, v122, v13
v_sub_f32 v123, v123, v13
v_sub_f32 v124, v124, v13
v_sub_f32 v125, v125, v13
v_sub_f32 v126, v126, v13
v_sub_f32 v127, v127, v13
v_sub_f32 v192, v192, v13
v_sub_f32 v193, v193, v13
v_sub_f32 v194, v194, v13
v_sub_f32 v195, v195, v13
v_sub_f32 v196, v196, v13
v_sub_f32 v197, v197, v13
v_sub_f32 v198, v198, v13
v_sub_f32 v199, v199, v13
v_sub_f32 v200, v200, v13
v_sub_f32 v201, v201, v13
v_sub_f32 v202, v202, v13
v_sub_f32 v203, v203, v13
v_sub_f32 v204, v204, v13
v_sub_f32 v205, v205, v13
v_sub_f32 v206, v206, v13
v_sub_f32 v207, v207, v13
v_sub_f32 v208, v208, v13
v_sub_f32 v209, v209, v13
v_sub_f32 v210, v210, v13
v_sub_f32 v211, v211, v13
v_sub_f32 v212, v212, v13
v_sub_f32 v213, v213, v13
v_sub_f32 v214, v214, v13
v_sub_f32 v215, v215, v13
v_sub_f32 v216, v216, v13
v_sub_f32 v217, v217, v13
v_sub_f32 v218, v218, v13
v_sub_f32 v219, v219, v13
v_sub_f32 v220, v220, v13
v_sub_f32 v221, v221, v13
v_sub_f32 v222, v222, v13
v_sub_f32 v223, v223, v13
v_sub_f32 v224, v224, v13
v_sub_f32 v225, v225, v13
v_sub_f32 v226, v226, v13
v_sub_f32 v227, v227, v13
v_sub_f32 v228, v228, v13
v_sub_f32 v229, v229, v13
v_sub_f32 v230, v230, v13
v_sub_f32 v231, v231, v13
v_sub_f32 v232, v232, v13
v_sub_f32 v233, v233, v13
v_sub_f32 v234, v234, v13
v_sub_f32 v235, v235, v13
v_sub_f32 v236, v236, v13
v_sub_f32 v237, v237, v13
v_sub_f32 v238, v238, v13
v_sub_f32 v239, v239, v13
v_sub_f32 v240, v240, v13
v_sub_f32 v241, v241, v13
v_sub_f32 v242, v242, v13
v_sub_f32 v243, v243, v13
v_sub_f32 v244, v244, v13
v_sub_f32 v245, v245, v13
v_sub_f32 v246, v246, v13
v_sub_f32 v247, v247, v13
v_sub_f32 v248, v248, v13
v_sub_f32 v249, v249, v13
v_sub_f32 v250, v250, v13
v_sub_f32 v251, v251, v13
v_sub_f32 v252, v252, v13
v_sub_f32 v253, v253, v13
v_sub_f32 v254, v254, v13
v_sub_f32 v255, v255, v13
v_mul_f32 v8, v64, v64
v_mul_f32 v9, v65, v65
v_mul_f32 v10, v66, v66
v_mul_f32 v11, v67, v67
v_fma_f32 v8, v68, v68, v8
v_fma_f32 v9, v69, v69, v9
v_fma_f32 v10, v70, v70, v10
v_fma_f32 v11, v71, v71, v11
v_fma_f32 v8, v72, v72, v8
v_fma_f32 v9, v73, v73, v9
v_fma_f32 v10, v74, v74, v10
v_fma_f32 v11, v75, v75, v11
v_fma_f32 v8, v76, v76, v8
v_fma_f32 v9, v77, v77, v9
v_fma_f32 v10, v78, v78, v10
v_fma_f32 v11, v79, v79, v11
v_fma_f32 v8, v80, v80, v8
v_fma_f32 v9, v81, v81, v9
v_fma_f32 v10, v82, v82, v10
v_fma_f32 v11, v83, v83, v11
v_fma_f32 v8, v84, v84, v8
v_fma_f32 v9, v85, v85, v9
v_fma_f32 v10, v86, v86, v10
v_fma_f32 v11, v87, v87, v11
v_fma_f32 v8, v88, v88, v8
v_fma_f32 v9, v89, v89, v9
v_fma_f32 v10, v90, v90, v10
v_fma_f32 v11, v91, v91, v11
v_fma_f32 v8, v92, v92, v8
v_fma_f32 v9, v93, v93, v9
v_fma_f32 v10, v94, v94, v10
v_fma_f32 v11, v95, v95, v11
v_fma_f32 v8, v96, v96, v8
v_fma_f32 v9, v97, v97, v9
v_fma_f32 v10, v98, v98, v10
v_fma_f32 v11, v99, v99, v11
v_fma_f32 v8, v100, v100, v8
v_fma_f32 v9, v101, v101, v9
v_fma_f32 v10, v102, v102, v10
v_fma_f32 v11, v103, v103, v11
v_fma_f32 v8, v104, v104, v8
v_fma_f32 v9, v105, v105, v9
v_fma_f32 v10, v106, v106, v10
v_fma_f32 v11, v107, v107, v11
v_fma_f32 v8, v108, v108, v8
v_fma_f32 v9, v109, v109, v9
v_fma_f32 v10, v110, v110, v10
v_fma_f32 v11, v111, v111, v11
v_fma_f32 v8, v112, v112, v8
v_fma_f32 v9, v113, v113, v9
v_fma_f32 v10, v114, v114, v10
v_fma_f32 v11, v115, v115, v11
v_fma_f32 v8, v116, v116, v8
v_fma_f32 v9, v117, v117, v9
v_fma_f32 v10, v118, v118, v10
v_fma_f32 v11, v119, v119, v11
v_fma_f32 v8, v120, v120, v8
v_fma_f32 v9, v121, v121, v9
v_fma_f32 v10, v122, v122, v10
v_fma_f32 v11, v123, v123, v11
v_fma_f32 v8, v124, v124, v8
v_fma_f32 v9, v125, v125, v9
v_fma_f32 v10, v126, v126, v10
v_fma_f32 v11, v127, v127, v11
v_fma_f32 v8, v192, v192, v8
v_fma_f32 v9, v193, v193, v9
v_fma_f32 v10, v194, v194, v10
v_fma_f32 v11, v195, v195, v11
v_fma_f32 v8, v196, v196, v8
v_fma_f32 v9, v197, v197, v9
v_fma_f32 v10, v198, v198, v10
v_fma_f32 v11, v199, v199, v11
v_fma_f32 v8, v200, v200, v8
v_fma_f32 v9, v201, v201, v9
v_fma_f32 v10, v202, v202, v10
v_fma_f32 v11, v203, v203, v11
v_fma_f32 v8, v204, v204, v8
v_fma_f32 v9, v205, v205, v9
v_fma_f32 v10, v206, v206, v10
v_fma_f32 v11, v207, v207, v11
v_fma_f32 v8, v208, v208, v8
v_fma_f32 v9, v209, v209, v9
v_fma_f32 v10, v210, v210, v10
v_fma_f32 v11, v211, v211, v11
v_fma_f32 v8, v212, v212, v8
v_fma_f32 v9, v213, v213, v9
v_fma_f32 v10, v214, v214, v10
v_fma_f32 v11, v215, v215, v11
v_fma_f32 v8, v216, v216, v8
v_fma_f32 v9, v217, v217, v9
v_fma_f32 v10, v218, v218, v10
v_fma_f32 v11, v219, v219, v11
v_fma_f32 v8, v220, v220, v8
v_fma_f32 v9, v221, v221, v9
v_fma_f32 v10, v222, v222, v10
v_fma_f32 v11, v223, v223, v11
v_fma_f32 v8, v224, v224, v8
v_fma_f32 v9, v225, v225, v9
v_fma_f32 v10, v226, v226, v10
v_fma_f32 v11, v227, v227, v11
v_fma_f32 v8, v228, v228, v8
v_fma_f32 v9, v229, v229, v9
v_fma_f32 v10, v230, v230, v10
v_fma_f32 v11, v231, v231, v11
v_fma_f32 v8, v232, v232, v8
v_fma_f32 v9, v233, v233, v9
v_fma_f32 v10, v234, v234, v10
v_fma_f32 v11, v235, v235, v11
v_fma_f32 v8, v236, v236, v8
v_fma_f32 v9, v237, v237, v9
v_fma_f32 v10, v238, v238, v10
v_fma_f32 v11, v239, v239, v11
v_fma_f32 v8, v240, v240, v8
v_fma_f32 v9, v241, v241, v9
v_fma_f32 v10, v242, v242, v10
v_fma_f32 v11, v243, v243, v11
v_fma_f32 v8, v244, v244, v8
v_fma_f32 v9, v245, v245, v9
v_fma_f32 v10, v246, v246, v10
v_fma_f32 v11, v247, v247, v11
v_fma_f32 v8, v248, v248, v8
v_fma_f32 v9, v249, v249, v9
v_fma_f32 v10, v250, v250, v10
v_fma_f32 v11, v251, v251, v11
v_fma_f32 v8, v252, v252, v8
v_fma_f32 v9, v253, v253, v9
v_fma_f32 v10, v254, v254, v10
v_fma_f32 v11, v255, v255, v11
v_add_f32 v8, v8, v9
v_add_f32 v10, v10, v11
v_add_f32 v8, v8, v10
v_mov_b32 v12, v8
s_nop 1
v_permlane16_swap_b32 v8, v12
v_add_f32 v8, v8, v12
v_mov_b32 v12, v8
s_nop 1
v_permlane32_swap_b32 v8, v12
v_add_f32 v8, v8, v12
v_mov_b32 v12, 0x3727c5ac
v_mul_f32 v14, 0x3b000000, v8
v_add_f32 v14, v14, v12
v_rsq_f32 v14, v14
s_nop 0
v_mul_f32 v128, v64, v14
v_mul_f32 v129, v65, v14
v_mul_f32 v130, v66, v14
v_mul_f32 v131, v67, v14
v_mul_f32 v132, v68, v14
v_mul_f32 v133, v69, v14
v_mul_f32 v134, v70, v14
v_mul_f32 v135, v71, v14
v_cvt_pk_bf16_f32 v136, v128, v129
v_cvt_pk_bf16_f32 v137, v130, v131
v_cvt_pk_bf16_f32 v138, v132, v133
v_cvt_pk_bf16_f32 v139, v134, v135
s_mov_b32 s26, 0
buffer_store_dwordx4 v[136:139], v5, s[12:15], s26 offen offset:0 sc1
v_mul_f32 v140, v72, v14
v_mul_f32 v141, v73, v14
v_mul_f32 v142, v74, v14
v_mul_f32 v143, v75, v14
v_mul_f32 v144, v76, v14
v_mul_f32 v145, v77, v14
v_mul_f32 v146, v78, v14
v_mul_f32 v147, v79, v14
)ASM"
R"ASM(v_cvt_pk_bf16_f32 v148, v140, v141
v_cvt_pk_bf16_f32 v149, v142, v143
v_cvt_pk_bf16_f32 v150, v144, v145
v_cvt_pk_bf16_f32 v151, v146, v147
buffer_store_dwordx4 v[148:151], v5, s[12:15], s26 offen offset:1024 sc1
v_mul_f32 v152, v80, v14
v_mul_f32 v153, v81, v14
v_mul_f32 v154, v82, v14
v_mul_f32 v155, v83, v14
v_mul_f32 v156, v84, v14
v_mul_f32 v157, v85, v14
v_mul_f32 v158, v86, v14
v_mul_f32 v159, v87, v14
v_cvt_pk_bf16_f32 v160, v152, v153
v_cvt_pk_bf16_f32 v161, v154, v155
v_cvt_pk_bf16_f32 v162, v156, v157
v_cvt_pk_bf16_f32 v163, v158, v159
buffer_store_dwordx4 v[160:163], v5, s[12:15], s26 offen offset:2048 sc1
v_mul_f32 v164, v88, v14
v_mul_f32 v165, v89, v14
v_mul_f32 v166, v90, v14
v_mul_f32 v167, v91, v14
v_mul_f32 v168, v92, v14
v_mul_f32 v169, v93, v14
v_mul_f32 v170, v94, v14
v_mul_f32 v171, v95, v14
v_cvt_pk_bf16_f32 v172, v164, v165
v_cvt_pk_bf16_f32 v173, v166, v167
v_cvt_pk_bf16_f32 v174, v168, v169
v_cvt_pk_bf16_f32 v175, v170, v171
buffer_store_dwordx4 v[172:175], v5, s[12:15], s26 offen offset:3072 sc1
v_mul_f32 v128, v96, v14
v_mul_f32 v129, v97, v14
v_mul_f32 v130, v98, v14
v_mul_f32 v131, v99, v14
v_mul_f32 v132, v100, v14
v_mul_f32 v133, v101, v14
v_mul_f32 v134, v102, v14
v_mul_f32 v135, v103, v14
v_cvt_pk_bf16_f32 v136, v128, v129
v_cvt_pk_bf16_f32 v137, v130, v131
v_cvt_pk_bf16_f32 v138, v132, v133
v_cvt_pk_bf16_f32 v139, v134, v135
s_mov_b32 s26, 4096
buffer_store_dwordx4 v[136:139], v5, s[12:15], s26 offen offset:0 sc1
v_mul_f32 v140, v104, v14
v_mul_f32 v141, v105, v14
v_mul_f32 v142, v106, v14
v_mul_f32 v143, v107, v14
v_mul_f32 v144, v108, v14
v_mul_f32 v145, v109, v14
v_mul_f32 v146, v110, v14
v_mul_f32 v147, v111, v14
v_cvt_pk_bf16_f32 v148, v140, v141
v_cvt_pk_bf16_f32 v149, v142, v143
v_cvt_pk_bf16_f32 v150, v144, v145
v_cvt_pk_bf16_f32 v151, v146, v147
buffer_store_dwordx4 v[148:151], v5, s[12:15], s26 offen offset:1024 sc1
v_mul_f32 v152, v112, v14
v_mul_f32 v153, v113, v14
v_mul_f32 v154, v114, v14
v_mul_f32 v155, v115, v14
v_mul_f32 v156, v116, v14
v_mul_f32 v157, v117, v14
v_mul_f32 v158, v118, v14
v_mul_f32 v159, v119, v14
v_cvt_pk_bf16_f32 v160, v152, v153
v_cvt_pk_bf16_f32 v161, v154, v155
v_cvt_pk_bf16_f32 v162, v156, v157
v_cvt_pk_bf16_f32 v163, v158, v159
buffer_store_dwordx4 v[160:163], v5, s[12:15], s26 offen offset:2048 sc1
v_mul_f32 v164, v120, v14
v_mul_f32 v165, v121, v14
v_mul_f32 v166, v122, v14
v_mul_f32 v167, v123, v14
v_mul_f32 v168, v124, v14
v_mul_f32 v169, v125, v14
v_mul_f32 v170, v126, v14
v_mul_f32 v171, v127, v14
v_cvt_pk_bf16_f32 v172, v164, v165
v_cvt_pk_bf16_f32 v173, v166, v167
v_cvt_pk_bf16_f32 v174, v168, v169
v_cvt_pk_bf16_f32 v175, v170, v171
buffer_store_dwordx4 v[172:175], v5, s[12:15], s26 offen offset:3072 sc1
v_mul_f32 v128, v192, v14
v_mul_f32 v129, v193, v14
v_mul_f32 v130, v194, v14
v_mul_f32 v131, v195, v14
v_mul_f32 v132, v196, v14
v_mul_f32 v133, v197, v14
v_mul_f32 v134, v198, v14
v_mul_f32 v135, v199, v14
v_cvt_pk_bf16_f32 v136, v128, v129
v_cvt_pk_bf16_f32 v137, v130, v131
v_cvt_pk_bf16_f32 v138, v132, v133
v_cvt_pk_bf16_f32 v139, v134, v135
s_mov_b32 s26, 8192
buffer_store_dwordx4 v[136:139], v5, s[12:15], s26 offen offset:0 sc1
v_mul_f32 v140, v200, v14
v_mul_f32 v141, v201, v14
v_mul_f32 v142, v202, v14
v_mul_f32 v143, v203, v14
v_mul_f32 v144, v204, v14
v_mul_f32 v145, v205, v14
v_mul_f32 v146, v206, v14
v_mul_f32 v147, v207, v14
v_cvt_pk_bf16_f32 v148, v140, v141
v_cvt_pk_bf16_f32 v149, v142, v143
v_cvt_pk_bf16_f32 v150, v144, v145
v_cvt_pk_bf16_f32 v151, v146, v147
buffer_store_dwordx4 v[148:151], v5, s[12:15], s26 offen offset:1024 sc1
v_mul_f32 v152, v208, v14
v_mul_f32 v153, v209, v14
v_mul_f32 v154, v210, v14
v_mul_f32 v155, v211, v14
v_mul_f32 v156, v212, v14
v_mul_f32 v157, v213, v14
v_mul_f32 v158, v214, v14
v_mul_f32 v159, v215, v14
v_cvt_pk_bf16_f32 v160, v152, v153
v_cvt_pk_bf16_f32 v161, v154, v155
v_cvt_pk_bf16_f32 v162, v156, v157
v_cvt_pk_bf16_f32 v163, v158, v159
buffer_store_dwordx4 v[160:163], v5, s[12:15], s26 offen offset:2048 sc1
v_mul_f32 v164, v216, v14
v_mul_f32 v165, v217, v14
v_mul_f32 v166, v218, v14
v_mul_f32 v167, v219, v14
v_mul_f32 v168, v220, v14
v_mul_f32 v169, v221, v14
v_mul_f32 v170, v222, v14
v_mul_f32 v171, v223, v14
v_cvt_pk_bf16_f32 v172, v164, v165
v_cvt_pk_bf16_f32 v173, v166, v167
v_cvt_pk_bf16_f32 v174, v168, v169
v_cvt_pk_bf16_f32 v175, v170, v171
buffer_store_dwordx4 v[172:175], v5, s[12:15], s26 offen offset:3072 sc1
v_mul_f32 v128, v224, v14
v_mul_f32 v129, v225, v14
v_mul_f32 v130, v226, v14
v_mul_f32 v131, v227, v14
v_mul_f32 v132, v228, v14
v_mul_f32 v133, v229, v14
v_mul_f32 v134, v230, v14
v_mul_f32 v135, v231, v14
v_cvt_pk_bf16_f32 v136, v128, v129
v_cvt_pk_bf16_f32 v137, v130, v131
v_cvt_pk_bf16_f32 v138, v132, v133
v_cvt_pk_bf16_f32 v139, v134, v135
s_mov_b32 s26, 12288
buffer_store_dwordx4 v[136:139], v5, s[12:15], s26 offen offset:0 sc1
v_mul_f32 v140, v232, v14
v_mul_f32 v141, v233, v14
v_mul_f32 v142, v234, v14
v_mul_f32 v143, v235, v14
v_mul_f32 v144, v236, v14
v_mul_f32 v145, v237, v14
v_mul_f32 v146, v238, v14
v_mul_f32 v147, v239, v14
v_cvt_pk_bf16_f32 v148, v140, v141
v_cvt_pk_bf16_f32 v149, v142, v143
v_cvt_pk_bf16_f32 v150, v144, v145
v_cvt_pk_bf16_f32 v151, v146, v147
buffer_store_dwordx4 v[148:151], v5, s[12:15], s26 offen offset:1024 sc1
v_mul_f32 v152, v240, v14
v_mul_f32 v153, v241, v14
v_mul_f32 v154, v242, v14
v_mul_f32 v155, v243, v14
v_mul_f32 v156, v244, v14
v_mul_f32 v157, v245, v14
v_mul_f32 v158, v246, v14
v_mul_f32 v159, v247, v14
v_cvt_pk_bf16_f32 v160, v152, v153
v_cvt_pk_bf16_f32 v161, v154, v155
v_cvt_pk_bf16_f32 v162, v156, v157
v_cvt_pk_bf16_f32 v163, v158, v159
buffer_store_dwordx4 v[160:163], v5, s[12:15], s26 offen offset:2048 sc1
v_mul_f32 v164, v248, v14
v_mul_f32 v165, v249, v14
v_mul_f32 v166, v250, v14
v_mul_f32 v167, v251, v14
v_mul_f32 v168, v252, v14
v_mul_f32 v169, v253, v14
v_mul_f32 v170, v254, v14
v_mul_f32 v171, v255, v14
v_cvt_pk_bf16_f32 v172, v164, v165
v_cvt_pk_bf16_f32 v173, v166, v167
v_cvt_pk_bf16_f32 v174, v168, v169
v_cvt_pk_bf16_f32 v175, v170, v171
buffer_store_dwordx4 v[172:175], v5, s[12:15], s26 offen offset:3072 sc1
s_waitcnt vmcnt(0) lgkmcnt(0)
s_add_u32 s98, s98, 1
s_addc_u32 s99, s99, 0
s_barrier
s_mov_b32 s21, s20
s_cmp_eq_u32 s7, 0
s_cbranch_scc0 L_FS_LN
s_lshl_b32 s31, s21, 6
s_add_u32 s31, s31, 16384
v_mov_b32 v254, s31
v_mov_b32 v252, s98
v_mov_b32 v253, s99
s_mov_b64 exec, 1
global_store_dwordx2 v254, v[252:253], s[48:49] sc1
s_mov_b64 exec, -1
L_FS_LN:
s_waitcnt vmcnt(0) lgkmcnt(0)
s_nop 15
s_nop 3
s_branch L_A0
L_A_V:
s_nop 15
s_nop 3
s_waitcnt lgkmcnt(0)
s_add_u32 s98, s98, 1
s_addc_u32 s99, s99, 0
s_waitcnt vmcnt(20)
s_barrier
ds_read_b128 v[72:75], v26 offset:0
ds_read_b128 v[76:79], v26 offset:2048
ds_read_b128 v[80:83], v26 offset:4096
ds_read_b128 v[84:87], v26 offset:6144
ds_read_b128 v[88:91], v27 offset:0
ds_read_b128 v[92:95], v27 offset:2048
ds_read_b128 v[96:99], v27 offset:4096
ds_read_b128 v[100:103], v27 offset:6144
ds_read_b64_tr_b16 v[40:41], v10 offset:0
ds_read_b64_tr_b16 v[42:43], v10 offset:2048
ds_read_b64_tr_b16 v[44:45], v11 offset:0
ds_read_b64_tr_b16 v[46:47], v11 offset:2048
ds_read_b64_tr_b16 v[48:49], v12 offset:0
ds_read_b64_tr_b16 v[50:51], v12 offset:2048
ds_read_b64_tr_b16 v[52:53], v13 offset:0
ds_read_b64_tr_b16 v[54:55], v13 offset:2048
s_waitcnt lgkmcnt(6)
v_mfma_f32_16x16x32_bf16 a[0:3], v[72:75], v[40:43], 0
ds_read_b64_tr_b16 v[56:57], v14 offset:0
ds_read_b64_tr_b16 v[58:59], v14 offset:2048
v_mfma_f32_16x16x32_bf16 a[4:7], v[76:79], v[40:43], 0
s_add_u32 s28, s37, 786432
v_mfma_f32_16x16x32_bf16 a[8:11], v[80:83], v[40:43], 0
s_add_u32 m0, s34, 49152
v_mfma_f32_16x16x32_bf16 a[12:15], v[84:87], v[40:43], 0
buffer_load_dwordx4 v4, s[8:11], s28 offen lds
s_waitcnt lgkmcnt(6)
v_mfma_f32_16x16x32_bf16 a[16:19], v[72:75], v[44:47], 0
ds_read_b64_tr_b16 v[60:61], v15 offset:0
v_mfma_f32_16x16x32_bf16 a[20:23], v[76:79], v[44:47], 0
ds_read_b64_tr_b16 v[62:63], v15 offset:2048
v_mfma_f32_16x16x32_bf16 a[24:27], v[80:83], v[44:47], 0
buffer_load_dwordx4 v5, s[8:11], s28 offen offset:1024 lds
v_mfma_f32_16x16x32_bf16 a[28:31], v[84:87], v[44:47], 0
s_waitcnt lgkmcnt(6)
v_mfma_f32_16x16x32_bf16 a[32:35], v[72:75], v[48:51], 0
ds_read_b64_tr_b16 v[64:65], v16 offset:0
v_mfma_f32_16x16x32_bf16 a[36:39], v[76:79], v[48:51], 0
v_mfma_f32_16x16x32_bf16 a[40:43], v[80:83], v[48:51], 0
ds_read_b64_tr_b16 v[66:67], v16 offset:2048
v_mfma_f32_16x16x32_bf16 a[44:47], v[84:87], v[48:51], 0
s_waitcnt lgkmcnt(6)
v_mfma_f32_16x16x32_bf16 a[48:51], v[72:75], v[52:55], 0
ds_read_b64_tr_b16 v[68:69], v17 offset:0
v_mfma_f32_16x16x32_bf16 a[52:55], v[76:79], v[52:55], 0
ds_read_b64_tr_b16 v[70:71], v17 offset:2048
v_mfma_f32_16x16x32_bf16 a[56:59], v[80:83], v[52:55], 0
buffer_load_dwordx4 v6, s[8:11], s28 offen offset:2048 lds
v_mfma_f32_16x16x32_bf16 a[60:63], v[84:87], v[52:55], 0
s_waitcnt lgkmcnt(6)
v_mfma_f32_16x16x32_bf16 a[64:67], v[72:75], v[56:59], 0
ds_read_b64_tr_b16 v[40:41], v10 offset:256
ds_read_b64_tr_b16 v[42:43], v10 offset:2304
v_mfma_f32_16x16x32_bf16 a[68:71], v[76:79], v[56:59], 0
buffer_load_dwordx4 v7, s[8:11], s28 offen offset:3072 lds
v_mfma_f32_16x16x32_bf16 a[72:75], v[80:83], v[56:59], 0
s_and_b32 s20, s6, 7
v_mfma_f32_16x16x32_bf16 a[76:79], v[84:87], v[56:59], 0
s_lshr_b32 s21, s6, 3
s_waitcnt lgkmcnt(6)
v_mfma_f32_16x16x32_bf16 a[80:83], v[72:75], v[60:63], 0
ds_read_b64_tr_b16 v[44:45], v11 offset:256
v_mfma_f32_16x16x32_bf16 a[84:87], v[76:79], v[60:63], 0
ds_read_b64_tr_b16 v[46:47], v11 offset:2304
v_mfma_f32_16x16x32_bf16 a[88:91], v[80:83], v[60:63], 0
s_lshr_b32 s22, s21, 3
v_mfma_f32_16x16x32_bf16 a[92:95], v[84:87], v[60:63], 0
s_waitcnt lgkmcnt(6)
v_mfma_f32_16x16x32_bf16 a[96:99], v[72:75], v[64:67], 0
ds_read_b64_tr_b16 v[48:49], v12 offset:256
ds_read_b64_tr_b16 v[50:51], v12 offset:2304
v_mfma_f32_16x16x32_bf16 a[100:103], v[76:79], v[64:67], 0
s_mov_b32 s29, 33024
s_add_u32 m0, s35, 0
v_mfma_f32_16x16x32_bf16 a[104:107], v[80:83], v[64:67], 0
buffer_load_dwordx4 v8, s[12:15], s29 offen lds
s_lshl_b32 s45, s20, 2
v_mfma_f32_16x16x32_bf16 a[108:111], v[84:87], v[64:67], 0
s_add_u32 s45, s45, s22
s_waitcnt lgkmcnt(6)
v_mfma_f32_16x16x32_bf16 a[112:115], v[72:75], v[68:71], 0
ds_read_b64_tr_b16 v[52:53], v13 offset:256
v_mfma_f32_16x16x32_bf16 a[116:119], v[76:79], v[68:71], 0
ds_read_b64_tr_b16 v[54:55], v13 offset:2304
v_mfma_f32_16x16x32_bf16 a[120:123], v[80:83], v[68:71], 0
buffer_load_dwordx4 v9, s[12:15], s29 offen offset:1024 lds
v_mfma_f32_16x16x32_bf16 a[124:127], v[84:87], v[68:71], 0
s_and_b32 s44, s21, 7
s_waitcnt lgkmcnt(6)
v_mfma_f32_16x16x32_bf16 a[128:131], v[72:75], v[40:43], 0
ds_read_b64_tr_b16 v[56:57], v14 offset:256
v_mfma_f32_16x16x32_bf16 a[132:135], v[76:79], v[40:43], 0
ds_read_b64_tr_b16 v[58:59], v14 offset:2304
v_mfma_f32_16x16x32_bf16 a[136:139], v[80:83], v[40:43], 0
s_and_b32 s22, s45, 7
v_mfma_f32_16x16x32_bf16 a[140:143], v[84:87], v[40:43], 0
s_and_b32 s23, s44, 4
s_waitcnt lgkmcnt(6)
v_mfma_f32_16x16x32_bf16 a[144:147], v[72:75], v[44:47], 0
ds_read_b64_tr_b16 v[60:61], v15 offset:256
v_mfma_f32_16x16x32_bf16 a[148:151], v[76:79], v[44:47], 0
ds_read_b64_tr_b16 v[62:63], v15 offset:2304
v_mfma_f32_16x16x32_bf16 a[152:155], v[80:83], v[44:47], 0
buffer_load_dwordx4 v37, s[12:15], s29 offen offset:2048 lds
v_mfma_f32_16x16x32_bf16 a[156:159], v[84:87], v[44:47], 0
s_xor_b32 s22, s22, s23
s_waitcnt lgkmcnt(6)
v_mfma_f32_16x16x32_bf16 a[160:163], v[72:75], v[48:51], 0
ds_read_b64_tr_b16 v[64:65], v16 offset:256
ds_read_b64_tr_b16 v[66:67], v16 offset:2304
v_mfma_f32_16x16x32_bf16 a[164:167], v[76:79], v[48:51], 0
buffer_load_dwordx4 v38, s[12:15], s29 offen offset:3072 lds
v_mfma_f32_16x16x32_bf16 a[168:171], v[80:83], v[48:51], 0
s_cmp_eq_u32 s22, 3
s_cselect_b32 s74, 1, 0
v_mfma_f32_16x16x32_bf16 a[172:175], v[84:87], v[48:51], 0
s_and_b32 s75, s7, 1
s_waitcnt lgkmcnt(6)
v_mfma_f32_16x16x32_bf16 a[176:179], v[72:75], v[52:55], 0
ds_read_b64_tr_b16 v[68:69], v17 offset:256
v_mfma_f32_16x16x32_bf16 a[180:183], v[76:79], v[52:55], 0
ds_read_b64_tr_b16 v[70:71], v17 offset:2304
v_mfma_f32_16x16x32_bf16 a[184:187], v[80:83], v[52:55], 0
s_sub_u32 s22, 15, s44
v_mfma_f32_16x16x32_bf16 a[188:191], v[84:87], v[52:55], 0
s_waitcnt vmcnt(24)
s_barrier
s_waitcnt lgkmcnt(6)
v_mfma_f32_16x16x32_bf16 a[192:195], v[72:75], v[56:59], 0
ds_read_b64_tr_b16 v[40:41], v10 offset:16384
v_mfma_f32_16x16x32_bf16 a[196:199], v[76:79], v[56:59], 0
ds_read_b64_tr_b16 v[42:43], v10 offset:18432
v_mfma_f32_16x16x32_bf16 a[200:203], v[80:83], v[56:59], 0
s_cmp_lt_u32 s7, 2
v_mfma_f32_16x16x32_bf16 a[204:207], v[84:87], v[56:59], 0
s_cselect_b32 s22, s44, s22
s_waitcnt lgkmcnt(6)
v_mfma_f32_16x16x32_bf16 a[208:211], v[72:75], v[60:63], 0
ds_read_b64_tr_b16 v[44:45], v11 offset:16384
v_mfma_f32_16x16x32_bf16 a[212:215], v[76:79], v[60:63], 0
ds_read_b64_tr_b16 v[46:47], v11 offset:18432
v_mfma_f32_16x16x32_bf16 a[216:219], v[80:83], v[60:63], 0
s_lshl_b32 s22, s22, 3
v_mfma_f32_16x16x32_bf16 a[220:223], v[84:87], v[60:63], 0
s_lshl_b32 s23, s75, 0
s_waitcnt lgkmcnt(6)
v_mfma_f32_16x16x32_bf16 a[224:227], v[72:75], v[64:67], 0
ds_read_b64_tr_b16 v[48:49], v12 offset:16384
v_mfma_f32_16x16x32_bf16 a[228:231], v[76:79], v[64:67], 0
ds_read_b64_tr_b16 v[50:51], v12 offset:18432
v_mfma_f32_16x16x32_bf16 a[232:235], v[80:83], v[64:67], 0
s_add_u32 s50, s22, s23
v_mfma_f32_16x16x32_bf16 a[236:239], v[84:87], v[64:67], 0
s_waitcnt lgkmcnt(6)
v_mfma_f32_16x16x32_bf16 a[240:243], v[72:75], v[68:71], 0
ds_read_b64_tr_b16 v[52:53], v13 offset:16384
v_mfma_f32_16x16x32_bf16 a[244:247], v[76:79], v[68:71], 0
ds_read_b64_tr_b16 v[54:55], v13 offset:18432
v_mfma_f32_16x16x32_bf16 a[248:251], v[80:83], v[68:71], 0
v_lshrrev_b32 v131, 4, v1
v_mfma_f32_16x16x32_bf16 a[252:255], v[84:87], v[68:71], 0
s_lshl_b32 s24, s75, 2
s_waitcnt lgkmcnt(6)
v_mfma_f32_16x16x32_bf16 a[0:3], v[88:91], v[40:43], a[0:3]
ds_read_b64_tr_b16 v[56:57], v14 offset:16384
ds_read_b64_tr_b16 v[58:59], v14 offset:18432
v_mfma_f32_16x16x32_bf16 a[4:7], v[92:95], v[40:43], a[4:7]
s_add_u32 s28, s37, 1048576
v_mfma_f32_16x16x32_bf16 a[8:11], v[96:99], v[40:43], a[8:11]
s_add_u32 m0, s34, 65536
s_nop 0
buffer_load_dwordx4 v4, s[8:11], s28 offen lds
v_mfma_f32_16x16x32_bf16 a[12:15], v[100:103], v[40:43], a[12:15]
v_or_b32 v128, s24, v131
s_waitcnt lgkmcnt(6)
v_mfma_f32_16x16x32_bf16 a[16:19], v[88:91], v[44:47], a[16:19]
ds_read_b64_tr_b16 v[60:61], v15 offset:16384
ds_read_b64_tr_b16 v[62:63], v15 offset:18432
v_mfma_f32_16x16x32_bf16 a[20:23], v[92:95], v[44:47], a[20:23]
buffer_load_dwordx4 v5, s[8:11], s28 offen offset:1024 lds
v_mfma_f32_16x16x32_bf16 a[24:27], v[96:99], v[44:47], a[24:27]
v_lshlrev_b32 v128, 1, v128
v_mfma_f32_16x16x32_bf16 a[28:31], v[100:103], v[44:47], a[28:31]
v_and_b32 v129, 15, v1
s_waitcnt lgkmcnt(6)
v_mfma_f32_16x16x32_bf16 a[32:35], v[88:91], v[48:51], a[32:35]
ds_read_b64_tr_b16 v[64:65], v16 offset:16384
v_mfma_f32_16x16x32_bf16 a[36:39], v[92:95], v[48:51], a[36:39]
ds_read_b64_tr_b16 v[66:67], v16 offset:18432
v_mfma_f32_16x16x32_bf16 a[40:43], v[96:99], v[48:51], a[40:43]
v_xor_b32 v129, v129, v128
v_mfma_f32_16x16x32_bf16 a[44:47], v[100:103], v[48:51], a[44:47]
s_waitcnt lgkmcnt(6)
v_mfma_f32_16x16x32_bf16 a[48:51], v[88:91], v[52:55], a[48:51]
ds_read_b64_tr_b16 v[68:69], v17 offset:16384
ds_read_b64_tr_b16 v[70:71], v17 offset:18432
v_mfma_f32_16x16x32_bf16 a[52:55], v[92:95], v[52:55], a[52:55]
s_waitcnt vmcnt(14)
v_mfma_f32_16x16x32_bf16 a[56:59], v[96:99], v[52:55], a[56:59]
buffer_load_dwordx4 v6, s[8:11], s28 offen offset:2048 lds
v_mfma_f32_16x16x32_bf16 a[60:63], v[100:103], v[52:55], a[60:63]
v_lshlrev_b32 v129, 4, v129
s_waitcnt lgkmcnt(6)
v_mfma_f32_16x16x32_bf16 a[64:67], v[88:91], v[56:59], a[64:67]
ds_read_b64_tr_b16 v[40:41], v10 offset:16640
ds_read_b64_tr_b16 v[42:43], v10 offset:18688
v_mfma_f32_16x16x32_bf16 a[68:71], v[92:95], v[56:59], a[68:71]
ds_read_b128 v[104:107], v26 offset:8192
v_mfma_f32_16x16x32_bf16 a[72:75], v[96:99], v[56:59], a[72:75]
buffer_load_dwordx4 v7, s[8:11], s28 offen offset:3072 lds
v_lshlrev_b32 v130, 13, v131
v_mfma_f32_16x16x32_bf16 a[76:79], v[100:103], v[56:59], a[76:79]
v_add_u32 v29, v129, v130
s_waitcnt lgkmcnt(7)
v_mfma_f32_16x16x32_bf16 a[80:83], v[88:91], v[60:63], a[80:83]
ds_read_b64_tr_b16 v[44:45], v11 offset:16640
v_mfma_f32_16x16x32_bf16 a[84:87], v[92:95], v[60:63], a[84:87]
ds_read_b64_tr_b16 v[46:47], v11 offset:18688
v_mfma_f32_16x16x32_bf16 a[88:91], v[96:99], v[60:63], a[88:91]
ds_read_b128 v[108:111], v26 offset:10240
v_mfma_f32_16x16x32_bf16 a[92:95], v[100:103], v[60:63], a[92:95]
v_and_b32 v132, 15, v1
s_waitcnt lgkmcnt(8)
v_mfma_f32_16x16x32_bf16 a[96:99], v[88:91], v[64:67], a[96:99]
ds_read_b64_tr_b16 v[48:49], v12 offset:16640
ds_read_b64_tr_b16 v[50:51], v12 offset:18688
v_mfma_f32_16x16x32_bf16 a[100:103], v[92:95], v[64:67], a[100:103]
ds_read_b128 v[112:115], v26 offset:12288
s_mov_b32 s29, 384
v_mfma_f32_16x16x32_bf16 a[104:107], v[96:99], v[64:67], a[104:107]
s_add_u32 m0, s35, 4096
s_nop 0
buffer_load_dwordx4 v8, s[12:15], s29 offen lds
v_mfma_f32_16x16x32_bf16 a[108:111], v[100:103], v[64:67], a[108:111]
v_lshrrev_b32 v132, 2, v132
v_and_b32 v133, 3, v1
s_waitcnt lgkmcnt(9)
v_mfma_f32_16x16x32_bf16 a[112:115], v[88:91], v[68:71], a[112:115]
ds_read_b64_tr_b16 v[52:53], v13 offset:16640
ds_read_b64_tr_b16 v[54:55], v13 offset:18688
v_mfma_f32_16x16x32_bf16 a[116:119], v[92:95], v[68:71], a[116:119]
ds_read_b128 v[116:119], v26 offset:14336
v_mfma_f32_16x16x32_bf16 a[120:123], v[96:99], v[68:71], a[120:123]
buffer_load_dwordx4 v9, s[12:15], s29 offen offset:1024 lds
v_mfma_f32_16x16x32_bf16 a[124:127], v[100:103], v[68:71], a[124:127]
v_and_b32 v134, 1, v131
s_waitcnt lgkmcnt(10)
v_mfma_f32_16x16x32_bf16 a[128:131], v[88:91], v[40:43], a[128:131]
ds_read_b64_tr_b16 v[56:57], v14 offset:16640
ds_read_b64_tr_b16 v[58:59], v14 offset:18688
v_mfma_f32_16x16x32_bf16 a[132:135], v[92:95], v[40:43], a[132:135]
ds_read_b128 v[72:75], v27 offset:8192
v_mfma_f32_16x16x32_bf16 a[136:139], v[96:99], v[40:43], a[136:139]
v_lshlrev_b32 v134, 2, v134
v_mfma_f32_16x16x32_bf16 a[140:143], v[100:103], v[40:43], a[140:143]
v_or_b32 v134, v134, v132
s_waitcnt lgkmcnt(10)
v_mfma_f32_16x16x32_bf16 a[144:147], v[88:91], v[44:47], a[144:147]
ds_read_b64_tr_b16 v[60:61], v15 offset:16640
ds_read_b64_tr_b16 v[62:63], v15 offset:18688
v_mfma_f32_16x16x32_bf16 a[148:151], v[92:95], v[44:47], a[148:151]
ds_read_b128 v[76:79], v27 offset:10240
v_mfma_f32_16x16x32_bf16 a[152:155], v[96:99], v[44:47], a[152:155]
buffer_load_dwordx4 v37, s[12:15], s29 offen offset:2048 lds
v_mfma_f32_16x16x32_bf16 a[156:159], v[100:103], v[44:47], a[156:159]
v_lshlrev_b32 v128, 4, v131
s_waitcnt lgkmcnt(10)
v_mfma_f32_16x16x32_bf16 a[160:163], v[88:91], v[48:51], a[160:163]
ds_read_b64_tr_b16 v[64:65], v16 offset:16640
ds_read_b64_tr_b16 v[66:67], v16 offset:18688
v_mfma_f32_16x16x32_bf16 a[164:167], v[92:95], v[48:51], a[164:167]
ds_read_b128 v[80:83], v27 offset:12288
v_mfma_f32_16x16x32_bf16 a[168:171], v[96:99], v[48:51], a[168:171]
buffer_load_dwordx4 v38, s[12:15], s29 offen offset:3072 lds
v_add_u32 v128, v128, v132
v_mfma_f32_16x16x32_bf16 a[172:175], v[100:103], v[48:51], a[172:175]
v_lshlrev_b32 v128, 8, v128
s_waitcnt lgkmcnt(10)
v_mfma_f32_16x16x32_bf16 a[176:179], v[88:91], v[52:55], a[176:179]
ds_read_b64_tr_b16 v[68:69], v17 offset:16640
v_mfma_f32_16x16x32_bf16 a[180:183], v[92:95], v[52:55], a[180:183]
ds_read_b64_tr_b16 v[70:71], v17 offset:18688
v_mfma_f32_16x16x32_bf16 a[184:187], v[96:99], v[52:55], a[184:187]
ds_read_b128 v[84:87], v27 offset:14336
v_mfma_f32_16x16x32_bf16 a[188:191], v[100:103], v[52:55], a[188:191]
v_lshlrev_b32 v129, 3, v133
s_barrier
s_waitcnt lgkmcnt(10)
v_mfma_f32_16x16x32_bf16 a[192:195], v[88:91], v[56:59], a[192:195]
ds_read_b64_tr_b16 v[40:41], v10 offset:32768
v_mfma_f32_16x16x32_bf16 a[196:199], v[92:95], v[56:59], a[196:199]
ds_read_b64_tr_b16 v[42:43], v10 offset:34816
v_mfma_f32_16x16x32_bf16 a[200:203], v[96:99], v[56:59], a[200:203]
v_add_u32 v128, v128, v129
v_mfma_f32_16x16x32_bf16 a[204:207], v[100:103], v[56:59], a[204:207]
s_waitcnt lgkmcnt(9)
v_mfma_f32_16x16x32_bf16 a[208:211], v[88:91], v[60:63], a[208:211]
ds_read_b64_tr_b16 v[44:45], v11 offset:32768
v_mfma_f32_16x16x32_bf16 a[212:215], v[92:95], v[60:63], a[212:215]
ds_read_b64_tr_b16 v[46:47], v11 offset:34816
v_mfma_f32_16x16x32_bf16 a[216:219], v[96:99], v[60:63], a[216:219]
v_add_u32 v128, 16384, v128
v_mfma_f32_16x16x32_bf16 a[220:223], v[100:103], v[60:63], a[220:223]
v_lshlrev_b32 v129, 5, v134
s_waitcnt lgkmcnt(8)
v_mfma_f32_16x16x32_bf16 a[224:227], v[88:91], v[64:67], a[224:227]
ds_read_b64_tr_b16 v[48:49], v12 offset:32768
v_mfma_f32_16x16x32_bf16 a[228:231], v[92:95], v[64:67], a[228:231]
ds_read_b64_tr_b16 v[50:51], v12 offset:34816
v_mfma_f32_16x16x32_bf16 a[232:235], v[96:99], v[64:67], a[232:235]
v_add_u32 v31, v128, v129
v_mfma_f32_16x16x32_bf16 a[236:239], v[100:103], v[64:67], a[236:239]
s_waitcnt lgkmcnt(7)
v_mfma_f32_16x16x32_bf16 a[240:243], v[88:91], v[68:71], a[240:243]
ds_read_b64_tr_b16 v[52:53], v13 offset:32768
v_mfma_f32_16x16x32_bf16 a[244:247], v[92:95], v[68:71], a[244:247]
ds_read_b64_tr_b16 v[54:55], v13 offset:34816
v_mfma_f32_16x16x32_bf16 a[248:251], v[96:99], v[68:71], a[248:251]
v_lshrrev_b32 v128, 3, v1
v_mfma_f32_16x16x32_bf16 a[252:255], v[100:103], v[68:71], a[252:255]
v_lshrrev_b32 v129, 1, v128
s_waitcnt lgkmcnt(6)
v_mfma_f32_16x16x32_bf16 a[0:3], v[104:107], v[40:43], a[0:3]
ds_read_b64_tr_b16 v[56:57], v14 offset:32768
ds_read_b64_tr_b16 v[58:59], v14 offset:34816
v_mfma_f32_16x16x32_bf16 a[4:7], v[108:111], v[40:43], a[4:7]
s_add_u32 s28, s37, 1310720
v_mfma_f32_16x16x32_bf16 a[8:11], v[112:115], v[40:43], a[8:11]
s_add_u32 m0, s34, 0
s_nop 0
buffer_load_dwordx4 v4, s[8:11], s28 offen lds
v_mfma_f32_16x16x32_bf16 a[12:15], v[116:119], v[40:43], a[12:15]
v_and_b32 v129, 1, v129
s_waitcnt lgkmcnt(6)
v_mfma_f32_16x16x32_bf16 a[16:19], v[104:107], v[44:47], a[16:19]
ds_read_b64_tr_b16 v[60:61], v15 offset:32768
ds_read_b64_tr_b16 v[62:63], v15 offset:34816
v_mfma_f32_16x16x32_bf16 a[20:23], v[108:111], v[44:47], a[20:23]
buffer_load_dwordx4 v5, s[8:11], s28 offen offset:1024 lds
v_mfma_f32_16x16x32_bf16 a[24:27], v[112:115], v[44:47], a[24:27]
s_lshl_b32 s26, s75, 1
v_mfma_f32_16x16x32_bf16 a[28:31], v[116:119], v[44:47], a[28:31]
v_or_b32 v129, s26, v129
s_waitcnt lgkmcnt(6)
v_mfma_f32_16x16x32_bf16 a[32:35], v[104:107], v[48:51], a[32:35]
ds_read_b64_tr_b16 v[64:65], v16 offset:32768
v_mfma_f32_16x16x32_bf16 a[36:39], v[108:111], v[48:51], a[36:39]
ds_read_b64_tr_b16 v[66:67], v16 offset:34816
v_mfma_f32_16x16x32_bf16 a[40:43], v[112:115], v[48:51], a[40:43]
v_lshlrev_b32 v129, 1, v129
v_mfma_f32_16x16x32_bf16 a[44:47], v[116:119], v[48:51], a[44:47]
s_waitcnt lgkmcnt(6)
v_mfma_f32_16x16x32_bf16 a[48:51], v[104:107], v[52:55], a[48:51]
ds_read_b64_tr_b16 v[68:69], v17 offset:32768
v_mfma_f32_16x16x32_bf16 a[52:55], v[108:111], v[52:55], a[52:55]
ds_read_b64_tr_b16 v[70:71], v17 offset:34816
v_mfma_f32_16x16x32_bf16 a[56:59], v[112:115], v[52:55], a[56:59]
buffer_load_dwordx4 v6, s[8:11], s28 offen offset:2048 lds
v_mfma_f32_16x16x32_bf16 a[60:63], v[116:119], v[52:55], a[60:63]
v_and_b32 v130, 7, v1
s_waitcnt lgkmcnt(6)
v_mfma_f32_16x16x32_bf16 a[64:67], v[104:107], v[56:59], a[64:67]
ds_read_b64_tr_b16 v[40:41], v10 offset:33024
ds_read_b64_tr_b16 v[42:43], v10 offset:35072
v_mfma_f32_16x16x32_bf16 a[68:71], v[108:111], v[56:59], a[68:71]
buffer_load_dwordx4 v7, s[8:11], s28 offen offset:3072 lds
v_mfma_f32_16x16x32_bf16 a[72:75], v[112:115], v[56:59], a[72:75]
v_xor_b32 v130, v130, v129
v_mfma_f32_16x16x32_bf16 a[76:79], v[116:119], v[56:59], a[76:79]
v_lshlrev_b32 v130, 4, v130
s_waitcnt lgkmcnt(6)
v_mfma_f32_16x16x32_bf16 a[80:83], v[104:107], v[60:63], a[80:83]
ds_read_b64_tr_b16 v[44:45], v11 offset:33024
v_mfma_f32_16x16x32_bf16 a[84:87], v[108:111], v[60:63], a[84:87]
ds_read_b64_tr_b16 v[46:47], v11 offset:35072
v_mfma_f32_16x16x32_bf16 a[88:91], v[112:115], v[60:63], a[88:91]
v_mul_u32_u24 v128, 3584, v128
v_mfma_f32_16x16x32_bf16 a[92:95], v[116:119], v[60:63], a[92:95]
s_waitcnt lgkmcnt(6)
v_mfma_f32_16x16x32_bf16 a[96:99], v[104:107], v[64:67], a[96:99]
ds_read_b64_tr_b16 v[48:49], v12 offset:33024
ds_read_b64_tr_b16 v[50:51], v12 offset:35072
v_mfma_f32_16x16x32_bf16 a[100:103], v[108:111], v[64:67], a[100:103]
s_mov_b32 s29, 33152
s_add_u32 m0, s35, 8192
v_mfma_f32_16x16x32_bf16 a[104:107], v[112:115], v[64:67], a[104:107]
buffer_load_dwordx4 v8, s[12:15], s29 offen lds
v_add_u32 v30, v128, v130
v_mfma_f32_16x16x32_bf16 a[108:111], v[116:119], v[64:67], a[108:111]
v_and_b32 v134, 1, v131
s_waitcnt lgkmcnt(6)
v_mfma_f32_16x16x32_bf16 a[112:115], v[104:107], v[68:71], a[112:115]
ds_read_b64_tr_b16 v[52:53], v13 offset:33024
v_mfma_f32_16x16x32_bf16 a[116:119], v[108:111], v[68:71], a[116:119]
ds_read_b64_tr_b16 v[54:55], v13 offset:35072
v_mfma_f32_16x16x32_bf16 a[120:123], v[112:115], v[68:71], a[120:123]
buffer_load_dwordx4 v9, s[12:15], s29 offen offset:1024 lds
v_mfma_f32_16x16x32_bf16 a[124:127], v[116:119], v[68:71], a[124:127]
v_lshlrev_b32 v134, 1, v134
s_waitcnt lgkmcnt(6)
v_mfma_f32_16x16x32_bf16 a[128:131], v[104:107], v[40:43], a[128:131]
ds_read_b64_tr_b16 v[56:57], v14 offset:33024
v_mfma_f32_16x16x32_bf16 a[132:135], v[108:111], v[40:43], a[132:135]
ds_read_b64_tr_b16 v[58:59], v14 offset:35072
v_mfma_f32_16x16x32_bf16 a[136:139], v[112:115], v[40:43], a[136:139]
v_lshrrev_b32 v128, 1, v132
v_mfma_f32_16x16x32_bf16 a[140:143], v[116:119], v[40:43], a[140:143]
v_or_b32 v134, v134, v128
s_waitcnt lgkmcnt(6)
v_mfma_f32_16x16x32_bf16 a[144:147], v[104:107], v[44:47], a[144:147]
ds_read_b64_tr_b16 v[60:61], v15 offset:33024
v_mfma_f32_16x16x32_bf16 a[148:151], v[108:111], v[44:47], a[148:151]
ds_read_b64_tr_b16 v[62:63], v15 offset:35072
v_mfma_f32_16x16x32_bf16 a[152:155], v[112:115], v[44:47], a[152:155]
buffer_load_dwordx4 v37, s[12:15], s29 offen offset:2048 lds
v_mfma_f32_16x16x32_bf16 a[156:159], v[116:119], v[44:47], a[156:159]
v_lshlrev_b32 v128, 5, v131
s_waitcnt lgkmcnt(6)
v_mfma_f32_16x16x32_bf16 a[160:163], v[104:107], v[48:51], a[160:163]
ds_read_b64_tr_b16 v[64:65], v16 offset:33024
v_mfma_f32_16x16x32_bf16 a[164:167], v[108:111], v[48:51], a[164:167]
ds_read_b64_tr_b16 v[66:67], v16 offset:35072
v_mfma_f32_16x16x32_bf16 a[168:171], v[112:115], v[48:51], a[168:171]
buffer_load_dwordx4 v38, s[12:15], s29 offen offset:3072 lds
v_mfma_f32_16x16x32_bf16 a[172:175], v[116:119], v[48:51], a[172:175]
v_add_u32 v128, v128, v132
s_waitcnt lgkmcnt(6)
v_mfma_f32_16x16x32_bf16 a[176:179], v[104:107], v[52:55], a[176:179]
ds_read_b64_tr_b16 v[68:69], v17 offset:33024
v_mfma_f32_16x16x32_bf16 a[180:183], v[108:111], v[52:55], a[180:183]
ds_read_b64_tr_b16 v[70:71], v17 offset:35072
v_mfma_f32_16x16x32_bf16 a[184:187], v[112:115], v[52:55], a[184:187]
v_lshlrev_b32 v128, 7, v128
v_mfma_f32_16x16x32_bf16 a[188:191], v[116:119], v[52:55], a[188:191]
v_lshlrev_b32 v129, 3, v133
s_waitcnt vmcnt(20)
s_barrier
s_waitcnt lgkmcnt(6)
v_mfma_f32_16x16x32_bf16 a[192:195], v[104:107], v[56:59], a[192:195]
ds_read_b64_tr_b16 v[40:41], v10 offset:49152
v_mfma_f32_16x16x32_bf16 a[196:199], v[108:111], v[56:59], a[196:199]
ds_read_b64_tr_b16 v[42:43], v10 offset:51200
v_mfma_f32_16x16x32_bf16 a[200:203], v[112:115], v[56:59], a[200:203]
v_add_u32 v128, v128, v129
v_mfma_f32_16x16x32_bf16 a[204:207], v[116:119], v[56:59], a[204:207]
s_waitcnt lgkmcnt(6)
v_mfma_f32_16x16x32_bf16 a[208:211], v[104:107], v[60:63], a[208:211]
ds_read_b64_tr_b16 v[44:45], v11 offset:49152
v_mfma_f32_16x16x32_bf16 a[212:215], v[108:111], v[60:63], a[212:215]
ds_read_b64_tr_b16 v[46:47], v11 offset:51200
v_mfma_f32_16x16x32_bf16 a[216:219], v[112:115], v[60:63], a[216:219]
v_add_u32 v128, 18432, v128
v_mfma_f32_16x16x32_bf16 a[220:223], v[116:119], v[60:63], a[220:223]
v_lshlrev_b32 v129, 5, v134
s_waitcnt lgkmcnt(6)
v_mfma_f32_16x16x32_bf16 a[224:227], v[104:107], v[64:67], a[224:227]
ds_read_b64_tr_b16 v[48:49], v12 offset:49152
v_mfma_f32_16x16x32_bf16 a[228:231], v[108:111], v[64:67], a[228:231]
ds_read_b64_tr_b16 v[50:51], v12 offset:51200
v_mfma_f32_16x16x32_bf16 a[232:235], v[112:115], v[64:67], a[232:235]
v_add_u32 v32, v128, v129
v_mfma_f32_16x16x32_bf16 a[236:239], v[116:119], v[64:67], a[236:239]
s_waitcnt lgkmcnt(6)
v_mfma_f32_16x16x32_bf16 a[240:243], v[104:107], v[68:71], a[240:243]
ds_read_b64_tr_b16 v[52:53], v13 offset:49152
ds_read_b64_tr_b16 v[54:55], v13 offset:51200
v_mfma_f32_16x16x32_bf16 a[244:247], v[108:111], v[68:71], a[244:247]
s_lshl_b32 s22, s45, 8
v_mfma_f32_16x16x32_bf16 a[248:251], v[112:115], v[68:71], a[248:251]
s_add_u32 s46, s56, s22
v_mfma_f32_16x16x32_bf16 a[252:255], v[116:119], v[68:71], a[252:255]
s_addc_u32 s47, s57, 0
s_waitcnt lgkmcnt(6)
v_mfma_f32_16x16x32_bf16 a[0:3], v[72:75], v[40:43], a[0:3]
ds_read_b64_tr_b16 v[56:57], v14 offset:49152
ds_read_b64_tr_b16 v[58:59], v14 offset:51200
v_mfma_f32_16x16x32_bf16 a[4:7], v[76:79], v[40:43], a[4:7]
s_add_u32 s28, s37, 1572864
v_mfma_f32_16x16x32_bf16 a[8:11], v[80:83], v[40:43], a[8:11]
s_add_u32 m0, s34, 16384
s_nop 0
buffer_load_dwordx4 v4, s[8:11], s28 offen lds
v_mfma_f32_16x16x32_bf16 a[12:15], v[84:87], v[40:43], a[12:15]
s_sub_u32 s23, 4194304, s22
s_waitcnt lgkmcnt(6)
v_mfma_f32_16x16x32_bf16 a[16:19], v[72:75], v[44:47], a[16:19]
ds_read_b64_tr_b16 v[60:61], v15 offset:49152
ds_read_b64_tr_b16 v[62:63], v15 offset:51200
v_mfma_f32_16x16x32_bf16 a[20:23], v[76:79], v[44:47], a[20:23]
buffer_load_dwordx4 v5, s[8:11], s28 offen offset:1024 lds
v_mfma_f32_16x16x32_bf16 a[24:27], v[80:83], v[44:47], a[24:27]
s_mov_b32 s76, s46
v_mfma_f32_16x16x32_bf16 a[28:31], v[84:87], v[44:47], a[28:31]
s_and_b32 s77, s47, 65535
s_waitcnt lgkmcnt(6)
v_mfma_f32_16x16x32_bf16 a[32:35], v[72:75], v[48:51], a[32:35]
ds_read_b64_tr_b16 v[64:65], v16 offset:49152
v_mfma_f32_16x16x32_bf16 a[36:39], v[76:79], v[48:51], a[36:39]
)ASM"
R"ASM(ds_read_b64_tr_b16 v[66:67], v16 offset:51200
v_mfma_f32_16x16x32_bf16 a[40:43], v[80:83], v[48:51], a[40:43]
s_mov_b32 s78, s23
v_mfma_f32_16x16x32_bf16 a[44:47], v[84:87], v[48:51], a[44:47]
s_waitcnt lgkmcnt(6)
v_mfma_f32_16x16x32_bf16 a[48:51], v[72:75], v[52:55], a[48:51]
ds_read_b64_tr_b16 v[68:69], v17 offset:49152
ds_read_b64_tr_b16 v[70:71], v17 offset:51200
v_mfma_f32_16x16x32_bf16 a[52:55], v[76:79], v[52:55], a[52:55]
s_waitcnt vmcnt(18)
v_mfma_f32_16x16x32_bf16 a[56:59], v[80:83], v[52:55], a[56:59]
buffer_load_dwordx4 v6, s[8:11], s28 offen offset:2048 lds
v_mfma_f32_16x16x32_bf16 a[60:63], v[84:87], v[52:55], a[60:63]
s_mov_b32 s79, 131072
s_waitcnt lgkmcnt(6)
v_mfma_f32_16x16x32_bf16 a[64:67], v[72:75], v[56:59], a[64:67]
ds_read_b64_tr_b16 v[40:41], v10 offset:49408
ds_read_b64_tr_b16 v[42:43], v10 offset:51456
v_mfma_f32_16x16x32_bf16 a[68:71], v[76:79], v[56:59], a[68:71]
ds_read_b128 v[88:91], v26 offset:16384
v_mfma_f32_16x16x32_bf16 a[72:75], v[80:83], v[56:59], a[72:75]
buffer_load_dwordx4 v7, s[8:11], s28 offen offset:3072 lds
s_and_b32 s24, s44, 4
v_mfma_f32_16x16x32_bf16 a[76:79], v[84:87], v[56:59], a[76:79]
s_xor_b32 s24, s24, 4
s_waitcnt lgkmcnt(7)
v_mfma_f32_16x16x32_bf16 a[80:83], v[72:75], v[60:63], a[80:83]
ds_read_b64_tr_b16 v[44:45], v11 offset:49408
v_mfma_f32_16x16x32_bf16 a[84:87], v[76:79], v[60:63], a[84:87]
ds_read_b64_tr_b16 v[46:47], v11 offset:51456
v_mfma_f32_16x16x32_bf16 a[88:91], v[80:83], v[60:63], a[88:91]
ds_read_b128 v[92:95], v26 offset:18432
v_mfma_f32_16x16x32_bf16 a[92:95], v[84:87], v[60:63], a[92:95]
s_add_u32 s24, s45, s24
s_waitcnt lgkmcnt(8)
v_mfma_f32_16x16x32_bf16 a[96:99], v[72:75], v[64:67], a[96:99]
ds_read_b64_tr_b16 v[48:49], v12 offset:49408
ds_read_b64_tr_b16 v[50:51], v12 offset:51456
v_mfma_f32_16x16x32_bf16 a[100:103], v[76:79], v[64:67], a[100:103]
ds_read_b128 v[96:99], v26 offset:0
s_mov_b32 s29, 512
v_mfma_f32_16x16x32_bf16 a[104:107], v[80:83], v[64:67], a[104:107]
s_add_u32 m0, s35, 12288
s_nop 0
buffer_load_dwordx4 v8, s[12:15], s29 offen lds
v_mfma_f32_16x16x32_bf16 a[108:111], v[84:87], v[64:67], a[108:111]
s_lshr_b32 s24, s24, 3
s_sub_u32 s24, s45, s24
s_waitcnt lgkmcnt(9)
v_mfma_f32_16x16x32_bf16 a[112:115], v[72:75], v[68:71], a[112:115]
ds_read_b64_tr_b16 v[52:53], v13 offset:49408
ds_read_b64_tr_b16 v[54:55], v13 offset:51456
v_mfma_f32_16x16x32_bf16 a[116:119], v[76:79], v[68:71], a[116:119]
ds_read_b128 v[100:103], v26 offset:2048
v_mfma_f32_16x16x32_bf16 a[120:123], v[80:83], v[68:71], a[120:123]
buffer_load_dwordx4 v9, s[12:15], s29 offen offset:1024 lds
v_mfma_f32_16x16x32_bf16 a[124:127], v[84:87], v[68:71], a[124:127]
s_min_u32 s24, s24, 27
s_waitcnt lgkmcnt(10)
v_mfma_f32_16x16x32_bf16 a[128:131], v[72:75], v[40:43], a[128:131]
ds_read_b64_tr_b16 v[56:57], v14 offset:49408
ds_read_b64_tr_b16 v[58:59], v14 offset:51456
v_mfma_f32_16x16x32_bf16 a[132:135], v[76:79], v[40:43], a[132:135]
ds_read_b128 v[104:107], v27 offset:16384
v_mfma_f32_16x16x32_bf16 a[136:139], v[80:83], v[40:43], a[136:139]
s_lshl_b32 s22, s24, 7
v_mfma_f32_16x16x32_bf16 a[140:143], v[84:87], v[40:43], a[140:143]
s_sub_u32 s23, 1835008, s22
s_waitcnt lgkmcnt(10)
v_mfma_f32_16x16x32_bf16 a[144:147], v[72:75], v[44:47], a[144:147]
ds_read_b64_tr_b16 v[60:61], v15 offset:49408
ds_read_b64_tr_b16 v[62:63], v15 offset:51456
v_mfma_f32_16x16x32_bf16 a[148:151], v[76:79], v[44:47], a[148:151]
ds_read_b128 v[108:111], v27 offset:18432
v_mfma_f32_16x16x32_bf16 a[152:155], v[80:83], v[44:47], a[152:155]
buffer_load_dwordx4 v37, s[12:15], s29 offen offset:2048 lds
s_add_u32 s46, s64, s22
v_mfma_f32_16x16x32_bf16 a[156:159], v[84:87], v[44:47], a[156:159]
s_addc_u32 s47, s65, 0
s_waitcnt lgkmcnt(10)
v_mfma_f32_16x16x32_bf16 a[160:163], v[72:75], v[48:51], a[160:163]
ds_read_b64_tr_b16 v[64:65], v16 offset:49408
ds_read_b64_tr_b16 v[66:67], v16 offset:51456
v_mfma_f32_16x16x32_bf16 a[164:167], v[76:79], v[48:51], a[164:167]
ds_read_b128 v[112:115], v27 offset:0
v_mfma_f32_16x16x32_bf16 a[168:171], v[80:83], v[48:51], a[168:171]
buffer_load_dwordx4 v38, s[12:15], s29 offen offset:3072 lds
s_sub_u32 s46, s46, 2048
v_mfma_f32_16x16x32_bf16 a[172:175], v[84:87], v[48:51], a[172:175]
s_subb_u32 s47, s47, 0
s_waitcnt lgkmcnt(10)
v_mfma_f32_16x16x32_bf16 a[176:179], v[72:75], v[52:55], a[176:179]
ds_read_b64_tr_b16 v[68:69], v17 offset:49408
ds_read_b64_tr_b16 v[70:71], v17 offset:51456
v_mfma_f32_16x16x32_bf16 a[180:183], v[76:79], v[52:55], a[180:183]
ds_read_b128 v[116:119], v27 offset:2048
v_mfma_f32_16x16x32_bf16 a[184:187], v[80:83], v[52:55], a[184:187]
s_add_u32 s25, s23, 2048
v_mfma_f32_16x16x32_bf16 a[188:191], v[84:87], v[52:55], a[188:191]
s_mov_b32 s80, s46
s_waitcnt vmcnt(20)
s_barrier
s_waitcnt lgkmcnt(10)
v_mfma_f32_16x16x32_bf16 a[192:195], v[72:75], v[56:59], a[192:195]
ds_read_b64_tr_b16 v[40:41], v18 offset:0
v_mfma_f32_16x16x32_bf16 a[196:199], v[76:79], v[56:59], a[196:199]
ds_read_b64_tr_b16 v[42:43], v18 offset:2048
v_mfma_f32_16x16x32_bf16 a[200:203], v[80:83], v[56:59], a[200:203]
s_and_b32 s81, s47, 65535
v_mfma_f32_16x16x32_bf16 a[204:207], v[84:87], v[56:59], a[204:207]
s_waitcnt lgkmcnt(9)
v_mfma_f32_16x16x32_bf16 a[208:211], v[72:75], v[60:63], a[208:211]
ds_read_b64_tr_b16 v[44:45], v19 offset:0
v_mfma_f32_16x16x32_bf16 a[212:215], v[76:79], v[60:63], a[212:215]
ds_read_b64_tr_b16 v[46:47], v19 offset:2048
v_mfma_f32_16x16x32_bf16 a[216:219], v[80:83], v[60:63], a[216:219]
s_mov_b32 s82, s25
v_mfma_f32_16x16x32_bf16 a[220:223], v[84:87], v[60:63], a[220:223]
s_mov_b32 s83, 131072
s_waitcnt lgkmcnt(8)
v_mfma_f32_16x16x32_bf16 a[224:227], v[72:75], v[64:67], a[224:227]
ds_read_b64_tr_b16 v[48:49], v20 offset:0
v_mfma_f32_16x16x32_bf16 a[228:231], v[76:79], v[64:67], a[228:231]
ds_read_b64_tr_b16 v[50:51], v20 offset:2048
v_mfma_f32_16x16x32_bf16 a[232:235], v[80:83], v[64:67], a[232:235]
s_add_u32 s46, s66, s22
v_mfma_f32_16x16x32_bf16 a[236:239], v[84:87], v[64:67], a[236:239]
s_addc_u32 s47, s67, 0
s_waitcnt lgkmcnt(7)
v_mfma_f32_16x16x32_bf16 a[240:243], v[72:75], v[68:71], a[240:243]
ds_read_b64_tr_b16 v[52:53], v21 offset:0
ds_read_b64_tr_b16 v[54:55], v21 offset:2048
v_mfma_f32_16x16x32_bf16 a[244:247], v[76:79], v[68:71], a[244:247]
s_sub_u32 s46, s46, 3072
v_mfma_f32_16x16x32_bf16 a[248:251], v[80:83], v[68:71], a[248:251]
s_subb_u32 s47, s47, 0
v_mfma_f32_16x16x32_bf16 a[252:255], v[84:87], v[68:71], a[252:255]
s_add_u32 s25, s23, 3072
s_waitcnt lgkmcnt(6)
v_mfma_f32_16x16x32_bf16 a[0:3], v[88:91], v[40:43], a[0:3]
ds_read_b64_tr_b16 v[56:57], v22 offset:0
ds_read_b64_tr_b16 v[58:59], v22 offset:2048
v_mfma_f32_16x16x32_bf16 a[4:7], v[92:95], v[40:43], a[4:7]
s_add_u32 s28, s37, 1835008
v_mfma_f32_16x16x32_bf16 a[8:11], v[96:99], v[40:43], a[8:11]
s_add_u32 m0, s34, 32768
s_nop 0
buffer_load_dwordx4 v4, s[8:11], s28 offen lds
v_mfma_f32_16x16x32_bf16 a[12:15], v[100:103], v[40:43], a[12:15]
s_mov_b32 s84, s46
s_waitcnt lgkmcnt(6)
v_mfma_f32_16x16x32_bf16 a[16:19], v[88:91], v[44:47], a[16:19]
ds_read_b64_tr_b16 v[60:61], v23 offset:0
v_mfma_f32_16x16x32_bf16 a[20:23], v[92:95], v[44:47], a[20:23]
ds_read_b64_tr_b16 v[62:63], v23 offset:2048
v_mfma_f32_16x16x32_bf16 a[24:27], v[96:99], v[44:47], a[24:27]
buffer_load_dwordx4 v5, s[8:11], s28 offen offset:1024 lds
v_mfma_f32_16x16x32_bf16 a[28:31], v[100:103], v[44:47], a[28:31]
s_and_b32 s85, s47, 65535
s_waitcnt lgkmcnt(6)
v_mfma_f32_16x16x32_bf16 a[32:35], v[88:91], v[48:51], a[32:35]
ds_read_b64_tr_b16 v[64:65], v24 offset:0
v_mfma_f32_16x16x32_bf16 a[36:39], v[92:95], v[48:51], a[36:39]
ds_read_b64_tr_b16 v[66:67], v24 offset:2048
v_mfma_f32_16x16x32_bf16 a[40:43], v[96:99], v[48:51], a[40:43]
s_mov_b32 s86, s25
v_mfma_f32_16x16x32_bf16 a[44:47], v[100:103], v[48:51], a[44:47]
s_mov_b32 s87, 131072
s_waitcnt lgkmcnt(6)
v_mfma_f32_16x16x32_bf16 a[48:51], v[88:91], v[52:55], a[48:51]
ds_read_b64_tr_b16 v[68:69], v25 offset:0
v_mfma_f32_16x16x32_bf16 a[52:55], v[92:95], v[52:55], a[52:55]
ds_read_b64_tr_b16 v[70:71], v25 offset:2048
v_mfma_f32_16x16x32_bf16 a[56:59], v[96:99], v[52:55], a[56:59]
buffer_load_dwordx4 v6, s[8:11], s28 offen offset:2048 lds
v_mfma_f32_16x16x32_bf16 a[60:63], v[100:103], v[52:55], a[60:63]
s_lshl_b32 s22, s50, 14
s_waitcnt lgkmcnt(6)
v_mfma_f32_16x16x32_bf16 a[64:67], v[88:91], v[56:59], a[64:67]
ds_read_b64_tr_b16 v[40:41], v18 offset:256
ds_read_b64_tr_b16 v[42:43], v18 offset:2304
v_mfma_f32_16x16x32_bf16 a[68:71], v[92:95], v[56:59], a[68:71]
buffer_load_dwordx4 v7, s[8:11], s28 offen offset:3072 lds
v_mfma_f32_16x16x32_bf16 a[72:75], v[96:99], v[56:59], a[72:75]
s_add_u32 s22, s22, 1048576
s_add_u32 s46, s48, s22
v_mfma_f32_16x16x32_bf16 a[76:79], v[100:103], v[56:59], a[76:79]
s_addc_u32 s47, s49, 0
s_waitcnt lgkmcnt(6)
v_mfma_f32_16x16x32_bf16 a[80:83], v[88:91], v[60:63], a[80:83]
ds_read_b64_tr_b16 v[44:45], v19 offset:256
v_mfma_f32_16x16x32_bf16 a[84:87], v[92:95], v[60:63], a[84:87]
ds_read_b64_tr_b16 v[46:47], v19 offset:2304
v_mfma_f32_16x16x32_bf16 a[88:91], v[96:99], v[60:63], a[88:91]
s_mov_b32 s52, s46
v_mfma_f32_16x16x32_bf16 a[92:95], v[100:103], v[60:63], a[92:95]
s_waitcnt lgkmcnt(6)
v_mfma_f32_16x16x32_bf16 a[96:99], v[88:91], v[64:67], a[96:99]
ds_read_b64_tr_b16 v[48:49], v20 offset:256
ds_read_b64_tr_b16 v[50:51], v20 offset:2304
v_mfma_f32_16x16x32_bf16 a[100:103], v[92:95], v[64:67], a[100:103]
s_mov_b32 s29, 33280
s_add_u32 m0, s35, 16384
v_mfma_f32_16x16x32_bf16 a[104:107], v[96:99], v[64:67], a[104:107]
buffer_load_dwordx4 v8, s[12:15], s29 offen lds
s_and_b32 s53, s47, 65535
v_mfma_f32_16x16x32_bf16 a[108:111], v[100:103], v[64:67], a[108:111]
s_mov_b32 s54, 114688
s_waitcnt lgkmcnt(6)
v_mfma_f32_16x16x32_bf16 a[112:115], v[88:91], v[68:71], a[112:115]
ds_read_b64_tr_b16 v[52:53], v21 offset:256
v_mfma_f32_16x16x32_bf16 a[116:119], v[92:95], v[68:71], a[116:119]
ds_read_b64_tr_b16 v[54:55], v21 offset:2304
v_mfma_f32_16x16x32_bf16 a[120:123], v[96:99], v[68:71], a[120:123]
buffer_load_dwordx4 v9, s[12:15], s29 offen offset:1024 lds
v_mfma_f32_16x16x32_bf16 a[124:127], v[100:103], v[68:71], a[124:127]
s_mov_b32 s55, 131072
s_waitcnt lgkmcnt(6)
v_mfma_f32_16x16x32_bf16 a[128:131], v[88:91], v[40:43], a[128:131]
ds_read_b64_tr_b16 v[56:57], v22 offset:256
v_mfma_f32_16x16x32_bf16 a[132:135], v[92:95], v[40:43], a[132:135]
ds_read_b64_tr_b16 v[58:59], v22 offset:2304
v_mfma_f32_16x16x32_bf16 a[136:139], v[96:99], v[40:43], a[136:139]
s_mul_i32 s22, s50, 184
v_mfma_f32_16x16x32_bf16 a[140:143], v[100:103], v[40:43], a[140:143]
s_lshl_b32 s23, s24, 1
s_waitcnt lgkmcnt(6)
v_mfma_f32_16x16x32_bf16 a[144:147], v[88:91], v[44:47], a[144:147]
ds_read_b64_tr_b16 v[60:61], v23 offset:256
v_mfma_f32_16x16x32_bf16 a[148:151], v[92:95], v[44:47], a[148:151]
ds_read_b64_tr_b16 v[62:63], v23 offset:2304
v_mfma_f32_16x16x32_bf16 a[152:155], v[96:99], v[44:47], a[152:155]
buffer_load_dwordx4 v37, s[12:15], s29 offen offset:2048 lds
v_mfma_f32_16x16x32_bf16 a[156:159], v[100:103], v[44:47], a[156:159]
s_add_u32 s22, s22, s23
s_waitcnt lgkmcnt(6)
v_mfma_f32_16x16x32_bf16 a[160:163], v[88:91], v[48:51], a[160:163]
ds_read_b64_tr_b16 v[64:65], v24 offset:256
v_mfma_f32_16x16x32_bf16 a[164:167], v[92:95], v[48:51], a[164:167]
ds_read_b64_tr_b16 v[66:67], v24 offset:2304
v_mfma_f32_16x16x32_bf16 a[168:171], v[96:99], v[48:51], a[168:171]
buffer_load_dwordx4 v38, s[12:15], s29 offen offset:3072 lds
v_mfma_f32_16x16x32_bf16 a[172:175], v[100:103], v[48:51], a[172:175]
s_add_u32 s22, s22, 128
s_waitcnt lgkmcnt(6)
v_mfma_f32_16x16x32_bf16 a[176:179], v[88:91], v[52:55], a[176:179]
ds_read_b64_tr_b16 v[68:69], v25 offset:256
v_mfma_f32_16x16x32_bf16 a[180:183], v[92:95], v[52:55], a[180:183]
ds_read_b64_tr_b16 v[70:71], v25 offset:2304
v_mfma_f32_16x16x32_bf16 a[184:187], v[96:99], v[52:55], a[184:187]
s_lshl_b32 s22, s22, 10
v_mfma_f32_16x16x32_bf16 a[188:191], v[100:103], v[52:55], a[188:191]
s_add_u32 s22, s22, 38797312
s_waitcnt vmcnt(20)
s_barrier
s_waitcnt lgkmcnt(6)
v_mfma_f32_16x16x32_bf16 a[192:195], v[88:91], v[56:59], a[192:195]
ds_read_b64_tr_b16 v[40:41], v10 offset:0
v_mfma_f32_16x16x32_bf16 a[196:199], v[92:95], v[56:59], a[196:199]
ds_read_b64_tr_b16 v[42:43], v10 offset:2048
v_mfma_f32_16x16x32_bf16 a[200:203], v[96:99], v[56:59], a[200:203]
s_add_u32 s46, s48, s22
v_mfma_f32_16x16x32_bf16 a[204:207], v[100:103], v[56:59], a[204:207]
s_addc_u32 s47, s49, 0
s_waitcnt lgkmcnt(6)
v_mfma_f32_16x16x32_bf16 a[208:211], v[88:91], v[60:63], a[208:211]
ds_read_b64_tr_b16 v[44:45], v11 offset:0
v_mfma_f32_16x16x32_bf16 a[212:215], v[92:95], v[60:63], a[212:215]
ds_read_b64_tr_b16 v[46:47], v11 offset:2048
v_mfma_f32_16x16x32_bf16 a[216:219], v[96:99], v[60:63], a[216:219]
s_mov_b32 s88, s46
v_mfma_f32_16x16x32_bf16 a[220:223], v[100:103], v[60:63], a[220:223]
s_and_b32 s89, s47, 65535
s_waitcnt lgkmcnt(6)
v_mfma_f32_16x16x32_bf16 a[224:227], v[88:91], v[64:67], a[224:227]
ds_read_b64_tr_b16 v[48:49], v12 offset:0
v_mfma_f32_16x16x32_bf16 a[228:231], v[92:95], v[64:67], a[228:231]
ds_read_b64_tr_b16 v[50:51], v12 offset:2048
v_mfma_f32_16x16x32_bf16 a[232:235], v[96:99], v[64:67], a[232:235]
s_mov_b32 s90, 1318912
v_mfma_f32_16x16x32_bf16 a[236:239], v[100:103], v[64:67], a[236:239]
s_waitcnt lgkmcnt(6)
v_mfma_f32_16x16x32_bf16 a[240:243], v[88:91], v[68:71], a[240:243]
ds_read_b64_tr_b16 v[52:53], v13 offset:0
v_mfma_f32_16x16x32_bf16 a[244:247], v[92:95], v[68:71], a[244:247]
ds_read_b64_tr_b16 v[54:55], v13 offset:2048
v_mfma_f32_16x16x32_bf16 a[248:251], v[96:99], v[68:71], a[248:251]
s_mov_b32 s91, 131072
v_mfma_f32_16x16x32_bf16 a[252:255], v[100:103], v[68:71], a[252:255]
s_lshl_b32 s70, s7, 12
s_waitcnt lgkmcnt(6)
v_mfma_f32_16x16x32_bf16 a[0:3], v[104:107], v[40:43], a[0:3]
ds_read_b64_tr_b16 v[56:57], v14 offset:0
ds_read_b64_tr_b16 v[58:59], v14 offset:2048
v_mfma_f32_16x16x32_bf16 a[4:7], v[108:111], v[40:43], a[4:7]
s_add_u32 s28, s37, 2097152
v_mfma_f32_16x16x32_bf16 a[8:11], v[112:115], v[40:43], a[8:11]
s_add_u32 m0, s34, 49152
s_nop 0
buffer_load_dwordx4 v4, s[8:11], s28 offen lds
v_mfma_f32_16x16x32_bf16 a[12:15], v[116:119], v[40:43], a[12:15]
s_add_u32 s70, s70, 16384
s_waitcnt lgkmcnt(6)
v_mfma_f32_16x16x32_bf16 a[16:19], v[104:107], v[44:47], a[16:19]
ds_read_b64_tr_b16 v[60:61], v15 offset:0
v_mfma_f32_16x16x32_bf16 a[20:23], v[108:111], v[44:47], a[20:23]
ds_read_b64_tr_b16 v[62:63], v15 offset:2048
v_mfma_f32_16x16x32_bf16 a[24:27], v[112:115], v[44:47], a[24:27]
buffer_load_dwordx4 v5, s[8:11], s28 offen offset:1024 lds
v_mfma_f32_16x16x32_bf16 a[28:31], v[116:119], v[44:47], a[28:31]
s_lshl_b32 s71, s7, 16
s_waitcnt lgkmcnt(6)
v_mfma_f32_16x16x32_bf16 a[32:35], v[104:107], v[48:51], a[32:35]
ds_read_b64_tr_b16 v[64:65], v16 offset:0
v_mfma_f32_16x16x32_bf16 a[36:39], v[108:111], v[48:51], a[36:39]
ds_read_b64_tr_b16 v[66:67], v16 offset:2048
v_mfma_f32_16x16x32_bf16 a[40:43], v[112:115], v[48:51], a[40:43]
s_mul_i32 s73, s7, 28672
v_mfma_f32_16x16x32_bf16 a[44:47], v[116:119], v[48:51], a[44:47]
s_lshr_b32 s22, s6, 3
s_waitcnt lgkmcnt(6)
v_mfma_f32_16x16x32_bf16 a[48:51], v[104:107], v[52:55], a[48:51]
ds_read_b64_tr_b16 v[68:69], v17 offset:0
ds_read_b64_tr_b16 v[70:71], v17 offset:2048
v_mfma_f32_16x16x32_bf16 a[52:55], v[108:111], v[52:55], a[52:55]
s_waitcnt vmcnt(18)
v_mfma_f32_16x16x32_bf16 a[56:59], v[112:115], v[52:55], a[56:59]
buffer_load_dwordx4 v6, s[8:11], s28 offen offset:2048 lds
v_mfma_f32_16x16x32_bf16 a[60:63], v[116:119], v[52:55], a[60:63]
s_lshl_b32 s22, s22, 2
s_waitcnt lgkmcnt(6)
v_mfma_f32_16x16x32_bf16 a[64:67], v[104:107], v[56:59], a[64:67]
ds_read_b64_tr_b16 v[40:41], v10 offset:256
ds_read_b64_tr_b16 v[42:43], v10 offset:2304
v_mfma_f32_16x16x32_bf16 a[68:71], v[108:111], v[56:59], a[68:71]
ds_read_b128 v[72:75], v26 offset:4096
v_mfma_f32_16x16x32_bf16 a[72:75], v[112:115], v[56:59], a[72:75]
buffer_load_dwordx4 v7, s[8:11], s28 offen offset:3072 lds
s_add_u32 s22, s22, s7
v_mfma_f32_16x16x32_bf16 a[76:79], v[116:119], v[56:59], a[76:79]
s_and_b32 s23, s6, 7
s_waitcnt lgkmcnt(7)
v_mfma_f32_16x16x32_bf16 a[80:83], v[104:107], v[60:63], a[80:83]
ds_read_b64_tr_b16 v[44:45], v11 offset:256
v_mfma_f32_16x16x32_bf16 a[84:87], v[108:111], v[60:63], a[84:87]
ds_read_b64_tr_b16 v[46:47], v11 offset:2304
v_mfma_f32_16x16x32_bf16 a[88:91], v[112:115], v[60:63], a[88:91]
ds_read_b128 v[76:79], v26 offset:6144
v_mfma_f32_16x16x32_bf16 a[92:95], v[116:119], v[60:63], a[92:95]
v_lshrrev_b32 v121, 3, v1
s_waitcnt lgkmcnt(8)
v_mfma_f32_16x16x32_bf16 a[96:99], v[104:107], v[64:67], a[96:99]
ds_read_b64_tr_b16 v[48:49], v12 offset:256
ds_read_b64_tr_b16 v[50:51], v12 offset:2304
v_mfma_f32_16x16x32_bf16 a[100:103], v[108:111], v[64:67], a[100:103]
ds_read_b128 v[80:83], v26 offset:8192
s_mov_b32 s29, 640
v_mfma_f32_16x16x32_bf16 a[104:107], v[112:115], v[64:67], a[104:107]
s_add_u32 m0, s35, 0
s_nop 0
buffer_load_dwordx4 v8, s[12:15], s29 offen lds
v_mfma_f32_16x16x32_bf16 a[108:111], v[116:119], v[64:67], a[108:111]
v_and_b32 v121, 1, v121
v_lshlrev_b32 v121, 13, v121
s_waitcnt lgkmcnt(9)
v_mfma_f32_16x16x32_bf16 a[112:115], v[104:107], v[68:71], a[112:115]
ds_read_b64_tr_b16 v[52:53], v13 offset:256
ds_read_b64_tr_b16 v[54:55], v13 offset:2304
v_mfma_f32_16x16x32_bf16 a[116:119], v[108:111], v[68:71], a[116:119]
ds_read_b128 v[84:87], v26 offset:10240
v_mfma_f32_16x16x32_bf16 a[120:123], v[112:115], v[68:71], a[120:123]
buffer_load_dwordx4 v9, s[12:15], s29 offen offset:1024 lds
v_mfma_f32_16x16x32_bf16 a[124:127], v[116:119], v[68:71], a[124:127]
v_and_b32 v128, 7, v1
s_waitcnt lgkmcnt(10)
v_mfma_f32_16x16x32_bf16 a[128:131], v[104:107], v[40:43], a[128:131]
ds_read_b64_tr_b16 v[56:57], v14 offset:256
v_mfma_f32_16x16x32_bf16 a[132:135], v[108:111], v[40:43], a[132:135]
ds_read_b64_tr_b16 v[58:59], v14 offset:2304
v_mfma_f32_16x16x32_bf16 a[136:139], v[112:115], v[40:43], a[136:139]
ds_read_b128 v[88:91], v27 offset:4096
v_mfma_f32_16x16x32_bf16 a[140:143], v[116:119], v[40:43], a[140:143]
v_lshlrev_b32 v128, 7, v128
s_waitcnt lgkmcnt(10)
v_mfma_f32_16x16x32_bf16 a[144:147], v[104:107], v[44:47], a[144:147]
ds_read_b64_tr_b16 v[60:61], v15 offset:256
ds_read_b64_tr_b16 v[62:63], v15 offset:2304
v_mfma_f32_16x16x32_bf16 a[148:151], v[108:111], v[44:47], a[148:151]
ds_read_b128 v[92:95], v27 offset:6144
v_mfma_f32_16x16x32_bf16 a[152:155], v[112:115], v[44:47], a[152:155]
buffer_load_dwordx4 v37, s[12:15], s29 offen offset:2048 lds
v_add_u32 v121, v121, v128
v_mfma_f32_16x16x32_bf16 a[156:159], v[116:119], v[44:47], a[156:159]
s_lshl_b32 s24, s23, 10
s_waitcnt lgkmcnt(10)
v_mfma_f32_16x16x32_bf16 a[160:163], v[104:107], v[48:51], a[160:163]
ds_read_b64_tr_b16 v[64:65], v16 offset:256
ds_read_b64_tr_b16 v[66:67], v16 offset:2304
v_mfma_f32_16x16x32_bf16 a[164:167], v[108:111], v[48:51], a[164:167]
ds_read_b128 v[96:99], v27 offset:8192
v_mfma_f32_16x16x32_bf16 a[168:171], v[112:115], v[48:51], a[168:171]
buffer_load_dwordx4 v38, s[12:15], s29 offen offset:3072 lds
v_mfma_f32_16x16x32_bf16 a[172:175], v[116:119], v[48:51], a[172:175]
s_lshl_b32 s25, s22, 15
s_waitcnt lgkmcnt(10)
v_mfma_f32_16x16x32_bf16 a[176:179], v[104:107], v[52:55], a[176:179]
ds_read_b64_tr_b16 v[68:69], v17 offset:256
ds_read_b64_tr_b16 v[70:71], v17 offset:2304
v_mfma_f32_16x16x32_bf16 a[180:183], v[108:111], v[52:55], a[180:183]
ds_read_b128 v[100:103], v27 offset:10240
v_mfma_f32_16x16x32_bf16 a[184:187], v[112:115], v[52:55], a[184:187]
s_add_u32 s24, s24, s25
v_mfma_f32_16x16x32_bf16 a[188:191], v[116:119], v[52:55], a[188:191]
v_add_u32 v121, s24, v121
s_waitcnt vmcnt(20)
s_barrier
s_waitcnt lgkmcnt(10)
v_mfma_f32_16x16x32_bf16 a[192:195], v[104:107], v[56:59], a[192:195]
ds_read_b64_tr_b16 v[40:41], v10 offset:16384
v_mfma_f32_16x16x32_bf16 a[196:199], v[108:111], v[56:59], a[196:199]
ds_read_b64_tr_b16 v[42:43], v10 offset:18432
v_mfma_f32_16x16x32_bf16 a[200:203], v[112:115], v[56:59], a[200:203]
v_add_u32 v124, 16384, v121
v_mfma_f32_16x16x32_bf16 a[204:207], v[116:119], v[56:59], a[204:207]
s_waitcnt lgkmcnt(9)
v_mfma_f32_16x16x32_bf16 a[208:211], v[104:107], v[60:63], a[208:211]
ds_read_b64_tr_b16 v[44:45], v11 offset:16384
v_mfma_f32_16x16x32_bf16 a[212:215], v[108:111], v[60:63], a[212:215]
ds_read_b64_tr_b16 v[46:47], v11 offset:18432
v_mfma_f32_16x16x32_bf16 a[216:219], v[112:115], v[60:63], a[216:219]
s_lshl_b32 s24, s23, 2
v_mfma_f32_16x16x32_bf16 a[220:223], v[116:119], v[60:63], a[220:223]
s_add_u32 s25, s24, 4
s_waitcnt lgkmcnt(8)
v_mfma_f32_16x16x32_bf16 a[224:227], v[104:107], v[64:67], a[224:227]
ds_read_b64_tr_b16 v[48:49], v12 offset:16384
v_mfma_f32_16x16x32_bf16 a[228:231], v[108:111], v[64:67], a[228:231]
ds_read_b64_tr_b16 v[50:51], v12 offset:18432
v_mfma_f32_16x16x32_bf16 a[232:235], v[112:115], v[64:67], a[232:235]
s_lshr_b32 s25, s25, 3
v_mfma_f32_16x16x32_bf16 a[236:239], v[116:119], v[64:67], a[236:239]
s_waitcnt lgkmcnt(7)
v_mfma_f32_16x16x32_bf16 a[240:243], v[104:107], v[68:71], a[240:243]
ds_read_b64_tr_b16 v[52:53], v13 offset:16384
v_mfma_f32_16x16x32_bf16 a[244:247], v[108:111], v[68:71], a[244:247]
ds_read_b64_tr_b16 v[54:55], v13 offset:18432
v_mfma_f32_16x16x32_bf16 a[248:251], v[112:115], v[68:71], a[248:251]
s_sub_u32 s24, s24, s25
v_mfma_f32_16x16x32_bf16 a[252:255], v[116:119], v[68:71], a[252:255]
v_and_b32 v128, 3, v1
s_waitcnt lgkmcnt(6)
v_mfma_f32_16x16x32_bf16 a[0:3], v[72:75], v[40:43], a[0:3]
ds_read_b64_tr_b16 v[56:57], v14 offset:16384
ds_read_b64_tr_b16 v[58:59], v14 offset:18432
v_mfma_f32_16x16x32_bf16 a[4:7], v[76:79], v[40:43], a[4:7]
s_add_u32 s28, s37, 2359296
v_mfma_f32_16x16x32_bf16 a[8:11], v[80:83], v[40:43], a[8:11]
s_add_u32 m0, s34, 65536
s_nop 0
buffer_load_dwordx4 v4, s[8:11], s28 offen lds
v_mfma_f32_16x16x32_bf16 a[12:15], v[84:87], v[40:43], a[12:15]
v_add_u32 v128, s24, v128
s_waitcnt lgkmcnt(6)
v_mfma_f32_16x16x32_bf16 a[16:19], v[72:75], v[44:47], a[16:19]
ds_read_b64_tr_b16 v[60:61], v15 offset:16384
v_mfma_f32_16x16x32_bf16 a[20:23], v[76:79], v[44:47], a[20:23]
ds_read_b64_tr_b16 v[62:63], v15 offset:18432
v_mfma_f32_16x16x32_bf16 a[24:27], v[80:83], v[44:47], a[24:27]
buffer_load_dwordx4 v5, s[8:11], s28 offen offset:1024 lds
v_mfma_f32_16x16x32_bf16 a[28:31], v[84:87], v[44:47], a[28:31]
v_min_u32 v128, 27, v128
s_waitcnt lgkmcnt(6)
v_mfma_f32_16x16x32_bf16 a[32:35], v[72:75], v[48:51], a[32:35]
ds_read_b64_tr_b16 v[64:65], v16 offset:16384
v_mfma_f32_16x16x32_bf16 a[36:39], v[76:79], v[48:51], a[36:39]
ds_read_b64_tr_b16 v[66:67], v16 offset:18432
v_mfma_f32_16x16x32_bf16 a[40:43], v[80:83], v[48:51], a[40:43]
v_lshlrev_b32 v128, 7, v128
v_mfma_f32_16x16x32_bf16 a[44:47], v[84:87], v[48:51], a[44:47]
v_lshrrev_b32 v125, 2, v1
s_waitcnt lgkmcnt(6)
v_mfma_f32_16x16x32_bf16 a[48:51], v[72:75], v[52:55], a[48:51]
ds_read_b64_tr_b16 v[68:69], v17 offset:16384
v_mfma_f32_16x16x32_bf16 a[52:55], v[76:79], v[52:55], a[52:55]
ds_read_b64_tr_b16 v[70:71], v17 offset:18432
v_mfma_f32_16x16x32_bf16 a[56:59], v[80:83], v[52:55], a[56:59]
buffer_load_dwordx4 v6, s[8:11], s28 offen offset:2048 lds
v_mfma_f32_16x16x32_bf16 a[60:63], v[84:87], v[52:55], a[60:63]
v_and_b32 v125, 3, v125
s_waitcnt lgkmcnt(6)
v_mfma_f32_16x16x32_bf16 a[64:67], v[72:75], v[56:59], a[64:67]
ds_read_b64_tr_b16 v[40:41], v10 offset:16640
ds_read_b64_tr_b16 v[42:43], v10 offset:18688
v_mfma_f32_16x16x32_bf16 a[68:71], v[76:79], v[56:59], a[68:71]
buffer_load_dwordx4 v7, s[8:11], s28 offen offset:3072 lds
v_mfma_f32_16x16x32_bf16 a[72:75], v[80:83], v[56:59], a[72:75]
v_mul_u32_u24 v125, 3584, v125
v_mfma_f32_16x16x32_bf16 a[76:79], v[84:87], v[56:59], a[76:79]
v_add_u32 v125, v125, v128
s_waitcnt lgkmcnt(6)
v_mfma_f32_16x16x32_bf16 a[80:83], v[72:75], v[60:63], a[80:83]
ds_read_b64_tr_b16 v[44:45], v11 offset:16640
v_mfma_f32_16x16x32_bf16 a[84:87], v[76:79], v[60:63], a[84:87]
ds_read_b64_tr_b16 v[46:47], v11 offset:18688
v_mfma_f32_16x16x32_bf16 a[88:91], v[80:83], v[60:63], a[88:91]
s_mul_i32 s25, s22, 14336
v_mfma_f32_16x16x32_bf16 a[92:95], v[84:87], v[60:63], a[92:95]
s_waitcnt lgkmcnt(6)
v_mfma_f32_16x16x32_bf16 a[96:99], v[72:75], v[64:67], a[96:99]
ds_read_b64_tr_b16 v[48:49], v12 offset:16640
ds_read_b64_tr_b16 v[50:51], v12 offset:18688
v_mfma_f32_16x16x32_bf16 a[100:103], v[76:79], v[64:67], a[100:103]
s_mov_b32 s29, 33408
s_add_u32 m0, s35, 4096
v_mfma_f32_16x16x32_bf16 a[104:107], v[80:83], v[64:67], a[104:107]
buffer_load_dwordx4 v8, s[12:15], s29 offen lds
v_add_u32 v125, s25, v125
v_mfma_f32_16x16x32_bf16 a[108:111], v[84:87], v[64:67], a[108:111]
v_and_b32 v120, 31, v1
s_waitcnt lgkmcnt(6)
v_mfma_f32_16x16x32_bf16 a[112:115], v[72:75], v[68:71], a[112:115]
ds_read_b64_tr_b16 v[52:53], v13 offset:16640
v_mfma_f32_16x16x32_bf16 a[116:119], v[76:79], v[68:71], a[116:119]
ds_read_b64_tr_b16 v[54:55], v13 offset:18688
v_mfma_f32_16x16x32_bf16 a[120:123], v[80:83], v[68:71], a[120:123]
buffer_load_dwordx4 v9, s[12:15], s29 offen offset:1024 lds
v_mfma_f32_16x16x32_bf16 a[124:127], v[84:87], v[68:71], a[124:127]
v_lshlrev_b32 v120, 6, v120
s_waitcnt lgkmcnt(6)
v_mfma_f32_16x16x32_bf16 a[128:131], v[72:75], v[40:43], a[128:131]
ds_read_b64_tr_b16 v[56:57], v14 offset:16640
v_mfma_f32_16x16x32_bf16 a[132:135], v[76:79], v[40:43], a[132:135]
ds_read_b64_tr_b16 v[58:59], v14 offset:18688
v_mfma_f32_16x16x32_bf16 a[136:139], v[80:83], v[40:43], a[136:139]
v_add_u32 v120, 16384, v120
v_mfma_f32_16x16x32_bf16 a[140:143], v[84:87], v[40:43], a[140:143]
s_waitcnt lgkmcnt(6)
v_mfma_f32_16x16x32_bf16 a[144:147], v[72:75], v[44:47], a[144:147]
ds_read_b64_tr_b16 v[60:61], v15 offset:16640
ds_read_b64_tr_b16 v[62:63], v15 offset:18688
v_mfma_f32_16x16x32_bf16 a[148:151], v[76:79], v[44:47], a[148:151]
buffer_load_dwordx4 v37, s[12:15], s29 offen offset:2048 lds
s_getpc_b64 s[46:47]
v_mfma_f32_16x16x32_bf16 a[152:155], v[80:83], v[44:47], a[152:155]
L_CP_AV:
s_add_u32 s46, s46, L_A_END-L_CP_AV
v_mfma_f32_16x16x32_bf16 a[156:159], v[84:87], v[44:47], a[156:159]
s_addc_u32 s47, s47, 0
s_lshr_b32 s22, s6, 3
s_waitcnt lgkmcnt(6)
v_mfma_f32_16x16x32_bf16 a[160:163], v[72:75], v[48:51], a[160:163]
ds_read_b64_tr_b16 v[64:65], v16 offset:16640
v_mfma_f32_16x16x32_bf16 a[164:167], v[76:79], v[48:51], a[164:167]
ds_read_b64_tr_b16 v[66:67], v16 offset:18688
v_mfma_f32_16x16x32_bf16 a[168:171], v[80:83], v[48:51], a[168:171]
buffer_load_dwordx4 v38, s[12:15], s29 offen offset:3072 lds
v_mfma_f32_16x16x32_bf16 a[172:175], v[84:87], v[48:51], a[172:175]
s_lshl_b32 s22, s22, 2
s_waitcnt lgkmcnt(6)
v_mfma_f32_16x16x32_bf16 a[176:179], v[72:75], v[52:55], a[176:179]
ds_read_b64_tr_b16 v[68:69], v17 offset:16640
v_mfma_f32_16x16x32_bf16 a[180:183], v[76:79], v[52:55], a[180:183]
ds_read_b64_tr_b16 v[70:71], v17 offset:18688
v_mfma_f32_16x16x32_bf16 a[184:187], v[80:83], v[52:55], a[184:187]
s_add_u32 s22, s22, s7
v_mfma_f32_16x16x32_bf16 a[188:191], v[84:87], v[52:55], a[188:191]
s_mul_i32 s22, s22, 256
s_waitcnt vmcnt(20)
s_barrier
s_waitcnt lgkmcnt(6)
v_mfma_f32_16x16x32_bf16 a[192:195], v[72:75], v[56:59], a[192:195]
ds_read_b64_tr_b16 v[40:41], v10 offset:32768
v_mfma_f32_16x16x32_bf16 a[196:199], v[76:79], v[56:59], a[196:199]
ds_read_b64_tr_b16 v[42:43], v10 offset:34816
v_mfma_f32_16x16x32_bf16 a[200:203], v[80:83], v[56:59], a[200:203]
v_min_u32 v126, 1, v1
v_mfma_f32_16x16x32_bf16 a[204:207], v[84:87], v[56:59], a[204:207]
s_waitcnt lgkmcnt(6)
v_mfma_f32_16x16x32_bf16 a[208:211], v[72:75], v[60:63], a[208:211]
ds_read_b64_tr_b16 v[44:45], v11 offset:32768
v_mfma_f32_16x16x32_bf16 a[212:215], v[76:79], v[60:63], a[212:215]
ds_read_b64_tr_b16 v[46:47], v11 offset:34816
v_mfma_f32_16x16x32_bf16 a[216:219], v[80:83], v[60:63], a[216:219]
v_lshlrev_b32 v126, 7, v126
v_mfma_f32_16x16x32_bf16 a[220:223], v[84:87], v[60:63], a[220:223]
v_add_u32 v126, s22, v126
s_waitcnt lgkmcnt(6)
v_mfma_f32_16x16x32_bf16 a[224:227], v[72:75], v[64:67], a[224:227]
ds_read_b64_tr_b16 v[48:49], v12 offset:32768
v_mfma_f32_16x16x32_bf16 a[228:231], v[76:79], v[64:67], a[228:231]
ds_read_b64_tr_b16 v[50:51], v12 offset:34816
v_mfma_f32_16x16x32_bf16 a[232:235], v[80:83], v[64:67], a[232:235]
v_min_u32 v126, L_CODE_END-L_A_END-4, v126
v_mfma_f32_16x16x32_bf16 a[236:239], v[84:87], v[64:67], a[236:239]
s_waitcnt lgkmcnt(6)
v_mfma_f32_16x16x32_bf16 a[240:243], v[72:75], v[68:71], a[240:243]
ds_read_b64_tr_b16 v[52:53], v13 offset:32768
v_mfma_f32_16x16x32_bf16 a[244:247], v[76:79], v[68:71], a[244:247]
ds_read_b64_tr_b16 v[54:55], v13 offset:34816
v_mfma_f32_16x16x32_bf16 a[248:251], v[80:83], v[68:71], a[248:251]
global_load_dword v127, v126, s[46:47]
v_mfma_f32_16x16x32_bf16 a[252:255], v[84:87], v[68:71], a[252:255]
s_waitcnt lgkmcnt(6)
v_mfma_f32_16x16x32_bf16 a[0:3], v[88:91], v[40:43], a[0:3]
ds_read_b64_tr_b16 v[56:57], v14 offset:32768
ds_read_b64_tr_b16 v[58:59], v14 offset:34816
v_mfma_f32_16x16x32_bf16 a[4:7], v[92:95], v[40:43], a[4:7]
s_add_u32 s28, s37, 2621440
v_mfma_f32_16x16x32_bf16 a[8:11], v[96:99], v[40:43], a[8:11]
s_add_u32 m0, s34, 0
v_mfma_f32_16x16x32_bf16 a[12:15], v[100:103], v[40:43], a[12:15]
buffer_load_dwordx4 v4, s[8:11], s28 offen lds
s_waitcnt lgkmcnt(6)
v_mfma_f32_16x16x32_bf16 a[16:19], v[88:91], v[44:47], a[16:19]
ds_read_b64_tr_b16 v[60:61], v15 offset:32768
v_mfma_f32_16x16x32_bf16 a[20:23], v[92:95], v[44:47], a[20:23]
ds_read_b64_tr_b16 v[62:63], v15 offset:34816
v_mfma_f32_16x16x32_bf16 a[24:27], v[96:99], v[44:47], a[24:27]
buffer_load_dwordx4 v5, s[8:11], s28 offen offset:1024 lds
v_mfma_f32_16x16x32_bf16 a[28:31], v[100:103], v[44:47], a[28:31]
s_waitcnt lgkmcnt(6)
v_mfma_f32_16x16x32_bf16 a[32:35], v[88:91], v[48:51], a[32:35]
ds_read_b64_tr_b16 v[64:65], v16 offset:32768
v_mfma_f32_16x16x32_bf16 a[36:39], v[92:95], v[48:51], a[36:39]
v_mfma_f32_16x16x32_bf16 a[40:43], v[96:99], v[48:51], a[40:43]
ds_read_b64_tr_b16 v[66:67], v16 offset:34816
v_mfma_f32_16x16x32_bf16 a[44:47], v[100:103], v[48:51], a[44:47]
s_waitcnt lgkmcnt(6)
v_mfma_f32_16x16x32_bf16 a[48:51], v[88:91], v[52:55], a[48:51]
ds_read_b64_tr_b16 v[68:69], v17 offset:32768
v_mfma_f32_16x16x32_bf16 a[52:55], v[92:95], v[52:55], a[52:55]
)ASM"
R"ASM(ds_read_b64_tr_b16 v[70:71], v17 offset:34816
v_mfma_f32_16x16x32_bf16 a[56:59], v[96:99], v[52:55], a[56:59]
s_waitcnt vmcnt(19)
v_mfma_f32_16x16x32_bf16 a[60:63], v[100:103], v[52:55], a[60:63]
buffer_load_dwordx4 v6, s[8:11], s28 offen offset:2048 lds
s_waitcnt lgkmcnt(6)
v_mfma_f32_16x16x32_bf16 a[64:67], v[88:91], v[56:59], a[64:67]
ds_read_b64_tr_b16 v[40:41], v10 offset:33024
v_mfma_f32_16x16x32_bf16 a[68:71], v[92:95], v[56:59], a[68:71]
ds_read_b64_tr_b16 v[42:43], v10 offset:35072
v_mfma_f32_16x16x32_bf16 a[72:75], v[96:99], v[56:59], a[72:75]
ds_read_b128 v[104:107], v26 offset:12288
v_mfma_f32_16x16x32_bf16 a[76:79], v[100:103], v[56:59], a[76:79]
buffer_load_dwordx4 v7, s[8:11], s28 offen offset:3072 lds
s_waitcnt lgkmcnt(7)
v_mfma_f32_16x16x32_bf16 a[80:83], v[88:91], v[60:63], a[80:83]
ds_read_b64_tr_b16 v[44:45], v11 offset:33024
v_mfma_f32_16x16x32_bf16 a[84:87], v[92:95], v[60:63], a[84:87]
ds_read_b64_tr_b16 v[46:47], v11 offset:35072
v_mfma_f32_16x16x32_bf16 a[88:91], v[96:99], v[60:63], a[88:91]
ds_read_b128 v[108:111], v26 offset:14336
v_mfma_f32_16x16x32_bf16 a[92:95], v[100:103], v[60:63], a[92:95]
s_waitcnt lgkmcnt(8)
v_mfma_f32_16x16x32_bf16 a[96:99], v[88:91], v[64:67], a[96:99]
ds_read_b64_tr_b16 v[48:49], v12 offset:33024
ds_read_b64_tr_b16 v[50:51], v12 offset:35072
v_mfma_f32_16x16x32_bf16 a[100:103], v[92:95], v[64:67], a[100:103]
ds_read_b128 v[112:115], v26 offset:16384
v_mfma_f32_16x16x32_bf16 a[104:107], v[96:99], v[64:67], a[104:107]
s_mov_b32 s29, 768
s_add_u32 m0, s35, 8192
v_mfma_f32_16x16x32_bf16 a[108:111], v[100:103], v[64:67], a[108:111]
buffer_load_dwordx4 v8, s[12:15], s29 offen lds
s_waitcnt lgkmcnt(9)
v_mfma_f32_16x16x32_bf16 a[112:115], v[88:91], v[68:71], a[112:115]
ds_read_b64_tr_b16 v[52:53], v13 offset:33024
v_mfma_f32_16x16x32_bf16 a[116:119], v[92:95], v[68:71], a[116:119]
ds_read_b64_tr_b16 v[54:55], v13 offset:35072
v_mfma_f32_16x16x32_bf16 a[120:123], v[96:99], v[68:71], a[120:123]
ds_read_b128 v[116:119], v26 offset:18432
v_mfma_f32_16x16x32_bf16 a[124:127], v[100:103], v[68:71], a[124:127]
buffer_load_dwordx4 v9, s[12:15], s29 offen offset:1024 lds
s_waitcnt lgkmcnt(10)
v_mfma_f32_16x16x32_bf16 a[128:131], v[88:91], v[40:43], a[128:131]
ds_read_b64_tr_b16 v[56:57], v14 offset:33024
v_mfma_f32_16x16x32_bf16 a[132:135], v[92:95], v[40:43], a[132:135]
ds_read_b64_tr_b16 v[58:59], v14 offset:35072
v_mfma_f32_16x16x32_bf16 a[136:139], v[96:99], v[40:43], a[136:139]
ds_read_b128 v[72:75], v27 offset:12288
v_mfma_f32_16x16x32_bf16 a[140:143], v[100:103], v[40:43], a[140:143]
s_waitcnt lgkmcnt(10)
v_mfma_f32_16x16x32_bf16 a[144:147], v[88:91], v[44:47], a[144:147]
ds_read_b64_tr_b16 v[60:61], v15 offset:33024
v_mfma_f32_16x16x32_bf16 a[148:151], v[92:95], v[44:47], a[148:151]
ds_read_b64_tr_b16 v[62:63], v15 offset:35072
v_mfma_f32_16x16x32_bf16 a[152:155], v[96:99], v[44:47], a[152:155]
ds_read_b128 v[76:79], v27 offset:14336
v_mfma_f32_16x16x32_bf16 a[156:159], v[100:103], v[44:47], a[156:159]
buffer_load_dwordx4 v37, s[12:15], s29 offen offset:2048 lds
s_waitcnt lgkmcnt(10)
v_mfma_f32_16x16x32_bf16 a[160:163], v[88:91], v[48:51], a[160:163]
ds_read_b64_tr_b16 v[64:65], v16 offset:33024
v_mfma_f32_16x16x32_bf16 a[164:167], v[92:95], v[48:51], a[164:167]
ds_read_b64_tr_b16 v[66:67], v16 offset:35072
v_mfma_f32_16x16x32_bf16 a[168:171], v[96:99], v[48:51], a[168:171]
ds_read_b128 v[80:83], v27 offset:16384
v_mfma_f32_16x16x32_bf16 a[172:175], v[100:103], v[48:51], a[172:175]
buffer_load_dwordx4 v38, s[12:15], s29 offen offset:3072 lds
s_waitcnt lgkmcnt(10)
v_mfma_f32_16x16x32_bf16 a[176:179], v[88:91], v[52:55], a[176:179]
ds_read_b64_tr_b16 v[68:69], v17 offset:33024
v_mfma_f32_16x16x32_bf16 a[180:183], v[92:95], v[52:55], a[180:183]
ds_read_b64_tr_b16 v[70:71], v17 offset:35072
v_mfma_f32_16x16x32_bf16 a[184:187], v[96:99], v[52:55], a[184:187]
ds_read_b128 v[84:87], v27 offset:18432
v_mfma_f32_16x16x32_bf16 a[188:191], v[100:103], v[52:55], a[188:191]
s_waitcnt vmcnt(21)
s_barrier
s_waitcnt lgkmcnt(10)
v_mfma_f32_16x16x32_bf16 a[192:195], v[88:91], v[56:59], a[192:195]
ds_read_b64_tr_b16 v[40:41], v10 offset:49152
v_mfma_f32_16x16x32_bf16 a[196:199], v[92:95], v[56:59], a[196:199]
v_mfma_f32_16x16x32_bf16 a[200:203], v[96:99], v[56:59], a[200:203]
ds_read_b64_tr_b16 v[42:43], v10 offset:51200
v_mfma_f32_16x16x32_bf16 a[204:207], v[100:103], v[56:59], a[204:207]
s_waitcnt lgkmcnt(9)
v_mfma_f32_16x16x32_bf16 a[208:211], v[88:91], v[60:63], a[208:211]
ds_read_b64_tr_b16 v[44:45], v11 offset:49152
v_mfma_f32_16x16x32_bf16 a[212:215], v[92:95], v[60:63], a[212:215]
ds_read_b64_tr_b16 v[46:47], v11 offset:51200
v_mfma_f32_16x16x32_bf16 a[216:219], v[96:99], v[60:63], a[216:219]
global_load_dword v127, v121, s[56:57]
v_mfma_f32_16x16x32_bf16 a[220:223], v[100:103], v[60:63], a[220:223]
s_waitcnt lgkmcnt(8)
v_mfma_f32_16x16x32_bf16 a[224:227], v[88:91], v[64:67], a[224:227]
ds_read_b64_tr_b16 v[48:49], v12 offset:49152
v_mfma_f32_16x16x32_bf16 a[228:231], v[92:95], v[64:67], a[228:231]
v_mfma_f32_16x16x32_bf16 a[232:235], v[96:99], v[64:67], a[232:235]
ds_read_b64_tr_b16 v[50:51], v12 offset:51200
v_mfma_f32_16x16x32_bf16 a[236:239], v[100:103], v[64:67], a[236:239]
s_waitcnt lgkmcnt(7)
v_mfma_f32_16x16x32_bf16 a[240:243], v[88:91], v[68:71], a[240:243]
ds_read_b64_tr_b16 v[52:53], v13 offset:49152
v_mfma_f32_16x16x32_bf16 a[244:247], v[92:95], v[68:71], a[244:247]
v_mfma_f32_16x16x32_bf16 a[248:251], v[96:99], v[68:71], a[248:251]
ds_read_b64_tr_b16 v[54:55], v13 offset:51200
v_mfma_f32_16x16x32_bf16 a[252:255], v[100:103], v[68:71], a[252:255]
s_waitcnt lgkmcnt(6)
v_mfma_f32_16x16x32_bf16 a[0:3], v[104:107], v[40:43], a[0:3]
ds_read_b64_tr_b16 v[56:57], v14 offset:49152
ds_read_b64_tr_b16 v[58:59], v14 offset:51200
v_mfma_f32_16x16x32_bf16 a[4:7], v[108:111], v[40:43], a[4:7]
s_add_u32 s28, s37, 2883584
v_mfma_f32_16x16x32_bf16 a[8:11], v[112:115], v[40:43], a[8:11]
s_add_u32 m0, s34, 16384
v_mfma_f32_16x16x32_bf16 a[12:15], v[116:119], v[40:43], a[12:15]
buffer_load_dwordx4 v4, s[8:11], s28 offen lds
s_waitcnt lgkmcnt(6)
v_mfma_f32_16x16x32_bf16 a[16:19], v[104:107], v[44:47], a[16:19]
ds_read_b64_tr_b16 v[60:61], v15 offset:49152
v_mfma_f32_16x16x32_bf16 a[20:23], v[108:111], v[44:47], a[20:23]
ds_read_b64_tr_b16 v[62:63], v15 offset:51200
v_mfma_f32_16x16x32_bf16 a[24:27], v[112:115], v[44:47], a[24:27]
buffer_load_dwordx4 v5, s[8:11], s28 offen offset:1024 lds
v_mfma_f32_16x16x32_bf16 a[28:31], v[116:119], v[44:47], a[28:31]
s_waitcnt lgkmcnt(6)
v_mfma_f32_16x16x32_bf16 a[32:35], v[104:107], v[48:51], a[32:35]
ds_read_b64_tr_b16 v[64:65], v16 offset:49152
v_mfma_f32_16x16x32_bf16 a[36:39], v[108:111], v[48:51], a[36:39]
v_mfma_f32_16x16x32_bf16 a[40:43], v[112:115], v[48:51], a[40:43]
ds_read_b64_tr_b16 v[66:67], v16 offset:51200
v_mfma_f32_16x16x32_bf16 a[44:47], v[116:119], v[48:51], a[44:47]
s_waitcnt lgkmcnt(6)
v_mfma_f32_16x16x32_bf16 a[48:51], v[104:107], v[52:55], a[48:51]
ds_read_b64_tr_b16 v[68:69], v17 offset:49152
v_mfma_f32_16x16x32_bf16 a[52:55], v[108:111], v[52:55], a[52:55]
ds_read_b64_tr_b16 v[70:71], v17 offset:51200
v_mfma_f32_16x16x32_bf16 a[56:59], v[112:115], v[52:55], a[56:59]
buffer_load_dwordx4 v6, s[8:11], s28 offen offset:2048 lds
v_mfma_f32_16x16x32_bf16 a[60:63], v[116:119], v[52:55], a[60:63]
s_waitcnt lgkmcnt(6)
v_mfma_f32_16x16x32_bf16 a[64:67], v[104:107], v[56:59], a[64:67]
ds_read_b64_tr_b16 v[40:41], v10 offset:49408
v_mfma_f32_16x16x32_bf16 a[68:71], v[108:111], v[56:59], a[68:71]
ds_read_b64_tr_b16 v[42:43], v10 offset:51456
v_mfma_f32_16x16x32_bf16 a[72:75], v[112:115], v[56:59], a[72:75]
buffer_load_dwordx4 v7, s[8:11], s28 offen offset:3072 lds
v_mfma_f32_16x16x32_bf16 a[76:79], v[116:119], v[56:59], a[76:79]
s_waitcnt lgkmcnt(6)
v_mfma_f32_16x16x32_bf16 a[80:83], v[104:107], v[60:63], a[80:83]
ds_read_b64_tr_b16 v[44:45], v11 offset:49408
v_mfma_f32_16x16x32_bf16 a[84:87], v[108:111], v[60:63], a[84:87]
v_mfma_f32_16x16x32_bf16 a[88:91], v[112:115], v[60:63], a[88:91]
ds_read_b64_tr_b16 v[46:47], v11 offset:51456
v_mfma_f32_16x16x32_bf16 a[92:95], v[116:119], v[60:63], a[92:95]
s_waitcnt lgkmcnt(6)
v_mfma_f32_16x16x32_bf16 a[96:99], v[104:107], v[64:67], a[96:99]
ds_read_b64_tr_b16 v[48:49], v12 offset:49408
ds_read_b64_tr_b16 v[50:51], v12 offset:51456
v_mfma_f32_16x16x32_bf16 a[100:103], v[108:111], v[64:67], a[100:103]
s_mov_b32 s29, 33536
v_mfma_f32_16x16x32_bf16 a[104:107], v[112:115], v[64:67], a[104:107]
s_add_u32 m0, s35, 12288
v_mfma_f32_16x16x32_bf16 a[108:111], v[116:119], v[64:67], a[108:111]
buffer_load_dwordx4 v8, s[12:15], s29 offen lds
s_waitcnt lgkmcnt(6)
v_mfma_f32_16x16x32_bf16 a[112:115], v[104:107], v[68:71], a[112:115]
ds_read_b64_tr_b16 v[52:53], v13 offset:49408
v_mfma_f32_16x16x32_bf16 a[116:119], v[108:111], v[68:71], a[116:119]
ds_read_b64_tr_b16 v[54:55], v13 offset:51456
v_mfma_f32_16x16x32_bf16 a[120:123], v[112:115], v[68:71], a[120:123]
buffer_load_dwordx4 v9, s[12:15], s29 offen offset:1024 lds
v_mfma_f32_16x16x32_bf16 a[124:127], v[116:119], v[68:71], a[124:127]
s_waitcnt lgkmcnt(6)
v_mfma_f32_16x16x32_bf16 a[128:131], v[104:107], v[40:43], a[128:131]
ds_read_b64_tr_b16 v[56:57], v14 offset:49408
v_mfma_f32_16x16x32_bf16 a[132:135], v[108:111], v[40:43], a[132:135]
v_mfma_f32_16x16x32_bf16 a[136:139], v[112:115], v[40:43], a[136:139]
ds_read_b64_tr_b16 v[58:59], v14 offset:51456
v_mfma_f32_16x16x32_bf16 a[140:143], v[116:119], v[40:43], a[140:143]
s_waitcnt lgkmcnt(6)
v_mfma_f32_16x16x32_bf16 a[144:147], v[104:107], v[44:47], a[144:147]
ds_read_b64_tr_b16 v[60:61], v15 offset:49408
v_mfma_f32_16x16x32_bf16 a[148:151], v[108:111], v[44:47], a[148:151]
ds_read_b64_tr_b16 v[62:63], v15 offset:51456
v_mfma_f32_16x16x32_bf16 a[152:155], v[112:115], v[44:47], a[152:155]
buffer_load_dwordx4 v37, s[12:15], s29 offen offset:2048 lds
v_mfma_f32_16x16x32_bf16 a[156:159], v[116:119], v[44:47], a[156:159]
s_waitcnt lgkmcnt(6)
v_mfma_f32_16x16x32_bf16 a[160:163], v[104:107], v[48:51], a[160:163]
ds_read_b64_tr_b16 v[64:65], v16 offset:49408
v_mfma_f32_16x16x32_bf16 a[164:167], v[108:111], v[48:51], a[164:167]
ds_read_b64_tr_b16 v[66:67], v16 offset:51456
v_mfma_f32_16x16x32_bf16 a[168:171], v[112:115], v[48:51], a[168:171]
buffer_load_dwordx4 v38, s[12:15], s29 offen offset:3072 lds
v_mfma_f32_16x16x32_bf16 a[172:175], v[116:119], v[48:51], a[172:175]
s_waitcnt lgkmcnt(6)
v_mfma_f32_16x16x32_bf16 a[176:179], v[104:107], v[52:55], a[176:179]
ds_read_b64_tr_b16 v[68:69], v17 offset:49408
v_mfma_f32_16x16x32_bf16 a[180:183], v[108:111], v[52:55], a[180:183]
v_mfma_f32_16x16x32_bf16 a[184:187], v[112:115], v[52:55], a[184:187]
ds_read_b64_tr_b16 v[70:71], v17 offset:51456
v_mfma_f32_16x16x32_bf16 a[188:191], v[116:119], v[52:55], a[188:191]
s_waitcnt vmcnt(22)
s_barrier
s_waitcnt lgkmcnt(6)
v_mfma_f32_16x16x32_bf16 a[192:195], v[104:107], v[56:59], a[192:195]
ds_read_b64_tr_b16 v[40:41], v18 offset:0
v_mfma_f32_16x16x32_bf16 a[196:199], v[108:111], v[56:59], a[196:199]
v_mfma_f32_16x16x32_bf16 a[200:203], v[112:115], v[56:59], a[200:203]
ds_read_b64_tr_b16 v[42:43], v18 offset:2048
v_mfma_f32_16x16x32_bf16 a[204:207], v[116:119], v[56:59], a[204:207]
s_waitcnt lgkmcnt(6)
v_mfma_f32_16x16x32_bf16 a[208:211], v[104:107], v[60:63], a[208:211]
ds_read_b64_tr_b16 v[44:45], v19 offset:0
v_mfma_f32_16x16x32_bf16 a[212:215], v[108:111], v[60:63], a[212:215]
ds_read_b64_tr_b16 v[46:47], v19 offset:2048
v_mfma_f32_16x16x32_bf16 a[216:219], v[112:115], v[60:63], a[216:219]
global_load_dword v127, v125, s[64:65]
v_mfma_f32_16x16x32_bf16 a[220:223], v[116:119], v[60:63], a[220:223]
s_waitcnt lgkmcnt(6)
v_mfma_f32_16x16x32_bf16 a[224:227], v[104:107], v[64:67], a[224:227]
ds_read_b64_tr_b16 v[48:49], v20 offset:0
v_mfma_f32_16x16x32_bf16 a[228:231], v[108:111], v[64:67], a[228:231]
v_mfma_f32_16x16x32_bf16 a[232:235], v[112:115], v[64:67], a[232:235]
ds_read_b64_tr_b16 v[50:51], v20 offset:2048
v_mfma_f32_16x16x32_bf16 a[236:239], v[116:119], v[64:67], a[236:239]
s_waitcnt lgkmcnt(6)
v_mfma_f32_16x16x32_bf16 a[240:243], v[104:107], v[68:71], a[240:243]
ds_read_b64_tr_b16 v[52:53], v21 offset:0
v_mfma_f32_16x16x32_bf16 a[244:247], v[108:111], v[68:71], a[244:247]
v_mfma_f32_16x16x32_bf16 a[248:251], v[112:115], v[68:71], a[248:251]
ds_read_b64_tr_b16 v[54:55], v21 offset:2048
v_mfma_f32_16x16x32_bf16 a[252:255], v[116:119], v[68:71], a[252:255]
s_waitcnt lgkmcnt(6)
v_mfma_f32_16x16x32_bf16 a[0:3], v[72:75], v[40:43], a[0:3]
ds_read_b64_tr_b16 v[56:57], v22 offset:0
ds_read_b64_tr_b16 v[58:59], v22 offset:2048
v_mfma_f32_16x16x32_bf16 a[4:7], v[76:79], v[40:43], a[4:7]
s_add_u32 s28, s37, 3145728
v_mfma_f32_16x16x32_bf16 a[8:11], v[80:83], v[40:43], a[8:11]
s_add_u32 m0, s34, 32768
v_mfma_f32_16x16x32_bf16 a[12:15], v[84:87], v[40:43], a[12:15]
buffer_load_dwordx4 v4, s[8:11], s28 offen lds
s_waitcnt lgkmcnt(6)
v_mfma_f32_16x16x32_bf16 a[16:19], v[72:75], v[44:47], a[16:19]
ds_read_b64_tr_b16 v[60:61], v23 offset:0
v_mfma_f32_16x16x32_bf16 a[20:23], v[76:79], v[44:47], a[20:23]
ds_read_b64_tr_b16 v[62:63], v23 offset:2048
v_mfma_f32_16x16x32_bf16 a[24:27], v[80:83], v[44:47], a[24:27]
buffer_load_dwordx4 v5, s[8:11], s28 offen offset:1024 lds
v_mfma_f32_16x16x32_bf16 a[28:31], v[84:87], v[44:47], a[28:31]
s_waitcnt lgkmcnt(6)
v_mfma_f32_16x16x32_bf16 a[32:35], v[72:75], v[48:51], a[32:35]
ds_read_b64_tr_b16 v[64:65], v24 offset:0
v_mfma_f32_16x16x32_bf16 a[36:39], v[76:79], v[48:51], a[36:39]
v_mfma_f32_16x16x32_bf16 a[40:43], v[80:83], v[48:51], a[40:43]
ds_read_b64_tr_b16 v[66:67], v24 offset:2048
v_mfma_f32_16x16x32_bf16 a[44:47], v[84:87], v[48:51], a[44:47]
s_waitcnt lgkmcnt(6)
v_mfma_f32_16x16x32_bf16 a[48:51], v[72:75], v[52:55], a[48:51]
ds_read_b64_tr_b16 v[68:69], v25 offset:0
v_mfma_f32_16x16x32_bf16 a[52:55], v[76:79], v[52:55], a[52:55]
ds_read_b64_tr_b16 v[70:71], v25 offset:2048
v_mfma_f32_16x16x32_bf16 a[56:59], v[80:83], v[52:55], a[56:59]
s_waitcnt vmcnt(21)
v_mfma_f32_16x16x32_bf16 a[60:63], v[84:87], v[52:55], a[60:63]
buffer_load_dwordx4 v6, s[8:11], s28 offen offset:2048 lds
s_waitcnt lgkmcnt(6)
v_mfma_f32_16x16x32_bf16 a[64:67], v[72:75], v[56:59], a[64:67]
ds_read_b64_tr_b16 v[40:41], v18 offset:256
v_mfma_f32_16x16x32_bf16 a[68:71], v[76:79], v[56:59], a[68:71]
ds_read_b64_tr_b16 v[42:43], v18 offset:2304
v_mfma_f32_16x16x32_bf16 a[72:75], v[80:83], v[56:59], a[72:75]
ds_read_b128 v[88:91], v26 offset:0
v_mfma_f32_16x16x32_bf16 a[76:79], v[84:87], v[56:59], a[76:79]
buffer_load_dwordx4 v7, s[8:11], s28 offen offset:3072 lds
s_waitcnt lgkmcnt(7)
v_mfma_f32_16x16x32_bf16 a[80:83], v[72:75], v[60:63], a[80:83]
ds_read_b64_tr_b16 v[44:45], v19 offset:256
v_mfma_f32_16x16x32_bf16 a[84:87], v[76:79], v[60:63], a[84:87]
ds_read_b64_tr_b16 v[46:47], v19 offset:2304
v_mfma_f32_16x16x32_bf16 a[88:91], v[80:83], v[60:63], a[88:91]
ds_read_b128 v[92:95], v26 offset:2048
v_mfma_f32_16x16x32_bf16 a[92:95], v[84:87], v[60:63], a[92:95]
s_waitcnt lgkmcnt(8)
v_mfma_f32_16x16x32_bf16 a[96:99], v[72:75], v[64:67], a[96:99]
ds_read_b64_tr_b16 v[48:49], v20 offset:256
ds_read_b64_tr_b16 v[50:51], v20 offset:2304
v_mfma_f32_16x16x32_bf16 a[100:103], v[76:79], v[64:67], a[100:103]
ds_read_b128 v[96:99], v26 offset:4096
v_mfma_f32_16x16x32_bf16 a[104:107], v[80:83], v[64:67], a[104:107]
s_mov_b32 s29, 896
s_add_u32 m0, s35, 16384
v_mfma_f32_16x16x32_bf16 a[108:111], v[84:87], v[64:67], a[108:111]
buffer_load_dwordx4 v8, s[12:15], s29 offen lds
s_waitcnt lgkmcnt(9)
v_mfma_f32_16x16x32_bf16 a[112:115], v[72:75], v[68:71], a[112:115]
ds_read_b64_tr_b16 v[52:53], v21 offset:256
v_mfma_f32_16x16x32_bf16 a[116:119], v[76:79], v[68:71], a[116:119]
ds_read_b64_tr_b16 v[54:55], v21 offset:2304
v_mfma_f32_16x16x32_bf16 a[120:123], v[80:83], v[68:71], a[120:123]
ds_read_b128 v[100:103], v26 offset:6144
v_mfma_f32_16x16x32_bf16 a[124:127], v[84:87], v[68:71], a[124:127]
buffer_load_dwordx4 v9, s[12:15], s29 offen offset:1024 lds
s_waitcnt lgkmcnt(10)
v_mfma_f32_16x16x32_bf16 a[128:131], v[72:75], v[40:43], a[128:131]
ds_read_b64_tr_b16 v[56:57], v22 offset:256
v_mfma_f32_16x16x32_bf16 a[132:135], v[76:79], v[40:43], a[132:135]
ds_read_b64_tr_b16 v[58:59], v22 offset:2304
v_mfma_f32_16x16x32_bf16 a[136:139], v[80:83], v[40:43], a[136:139]
ds_read_b128 v[104:107], v27 offset:0
v_mfma_f32_16x16x32_bf16 a[140:143], v[84:87], v[40:43], a[140:143]
s_waitcnt lgkmcnt(10)
v_mfma_f32_16x16x32_bf16 a[144:147], v[72:75], v[44:47], a[144:147]
ds_read_b64_tr_b16 v[60:61], v23 offset:256
v_mfma_f32_16x16x32_bf16 a[148:151], v[76:79], v[44:47], a[148:151]
ds_read_b64_tr_b16 v[62:63], v23 offset:2304
v_mfma_f32_16x16x32_bf16 a[152:155], v[80:83], v[44:47], a[152:155]
ds_read_b128 v[108:111], v27 offset:2048
v_mfma_f32_16x16x32_bf16 a[156:159], v[84:87], v[44:47], a[156:159]
buffer_load_dwordx4 v37, s[12:15], s29 offen offset:2048 lds
s_waitcnt lgkmcnt(10)
v_mfma_f32_16x16x32_bf16 a[160:163], v[72:75], v[48:51], a[160:163]
ds_read_b64_tr_b16 v[64:65], v24 offset:256
v_mfma_f32_16x16x32_bf16 a[164:167], v[76:79], v[48:51], a[164:167]
ds_read_b64_tr_b16 v[66:67], v24 offset:2304
v_mfma_f32_16x16x32_bf16 a[168:171], v[80:83], v[48:51], a[168:171]
ds_read_b128 v[112:115], v27 offset:4096
v_mfma_f32_16x16x32_bf16 a[172:175], v[84:87], v[48:51], a[172:175]
buffer_load_dwordx4 v38, s[12:15], s29 offen offset:3072 lds
s_waitcnt lgkmcnt(10)
v_mfma_f32_16x16x32_bf16 a[176:179], v[72:75], v[52:55], a[176:179]
ds_read_b64_tr_b16 v[68:69], v25 offset:256
v_mfma_f32_16x16x32_bf16 a[180:183], v[76:79], v[52:55], a[180:183]
ds_read_b64_tr_b16 v[70:71], v25 offset:2304
v_mfma_f32_16x16x32_bf16 a[184:187], v[80:83], v[52:55], a[184:187]
ds_read_b128 v[116:119], v27 offset:6144
v_mfma_f32_16x16x32_bf16 a[188:191], v[84:87], v[52:55], a[188:191]
s_waitcnt vmcnt(22)
s_barrier
s_waitcnt lgkmcnt(10)
v_mfma_f32_16x16x32_bf16 a[192:195], v[72:75], v[56:59], a[192:195]
ds_read_b64_tr_b16 v[40:41], v10 offset:0
v_mfma_f32_16x16x32_bf16 a[196:199], v[76:79], v[56:59], a[196:199]
v_mfma_f32_16x16x32_bf16 a[200:203], v[80:83], v[56:59], a[200:203]
ds_read_b64_tr_b16 v[42:43], v10 offset:2048
v_mfma_f32_16x16x32_bf16 a[204:207], v[84:87], v[56:59], a[204:207]
s_waitcnt lgkmcnt(9)
v_mfma_f32_16x16x32_bf16 a[208:211], v[72:75], v[60:63], a[208:211]
ds_read_b64_tr_b16 v[44:45], v11 offset:0
v_mfma_f32_16x16x32_bf16 a[212:215], v[76:79], v[60:63], a[212:215]
ds_read_b64_tr_b16 v[46:47], v11 offset:2048
v_mfma_f32_16x16x32_bf16 a[216:219], v[80:83], v[60:63], a[216:219]
global_load_dword v127, v124, s[56:57]
v_mfma_f32_16x16x32_bf16 a[220:223], v[84:87], v[60:63], a[220:223]
s_waitcnt lgkmcnt(8)
v_mfma_f32_16x16x32_bf16 a[224:227], v[72:75], v[64:67], a[224:227]
ds_read_b64_tr_b16 v[48:49], v12 offset:0
v_mfma_f32_16x16x32_bf16 a[228:231], v[76:79], v[64:67], a[228:231]
v_mfma_f32_16x16x32_bf16 a[232:235], v[80:83], v[64:67], a[232:235]
ds_read_b64_tr_b16 v[50:51], v12 offset:2048
v_mfma_f32_16x16x32_bf16 a[236:239], v[84:87], v[64:67], a[236:239]
s_waitcnt lgkmcnt(7)
v_mfma_f32_16x16x32_bf16 a[240:243], v[72:75], v[68:71], a[240:243]
ds_read_b64_tr_b16 v[52:53], v13 offset:0
v_mfma_f32_16x16x32_bf16 a[244:247], v[76:79], v[68:71], a[244:247]
v_mfma_f32_16x16x32_bf16 a[248:251], v[80:83], v[68:71], a[248:251]
ds_read_b64_tr_b16 v[54:55], v13 offset:2048
v_mfma_f32_16x16x32_bf16 a[252:255], v[84:87], v[68:71], a[252:255]
s_waitcnt lgkmcnt(6)
v_mfma_f32_16x16x32_bf16 a[0:3], v[88:91], v[40:43], a[0:3]
ds_read_b64_tr_b16 v[56:57], v14 offset:0
ds_read_b64_tr_b16 v[58:59], v14 offset:2048
v_mfma_f32_16x16x32_bf16 a[4:7], v[92:95], v[40:43], a[4:7]
s_add_u32 s28, s37, 3407872
v_mfma_f32_16x16x32_bf16 a[8:11], v[96:99], v[40:43], a[8:11]
s_add_u32 m0, s34, 49152
v_mfma_f32_16x16x32_bf16 a[12:15], v[100:103], v[40:43], a[12:15]
buffer_load_dwordx4 v4, s[8:11], s28 offen lds
s_waitcnt lgkmcnt(6)
v_mfma_f32_16x16x32_bf16 a[16:19], v[88:91], v[44:47], a[16:19]
ds_read_b64_tr_b16 v[60:61], v15 offset:0
v_mfma_f32_16x16x32_bf16 a[20:23], v[92:95], v[44:47], a[20:23]
ds_read_b64_tr_b16 v[62:63], v15 offset:2048
v_mfma_f32_16x16x32_bf16 a[24:27], v[96:99], v[44:47], a[24:27]
buffer_load_dwordx4 v5, s[8:11], s28 offen offset:1024 lds
v_mfma_f32_16x16x32_bf16 a[28:31], v[100:103], v[44:47], a[28:31]
s_waitcnt lgkmcnt(6)
v_mfma_f32_16x16x32_bf16 a[32:35], v[88:91], v[48:51], a[32:35]
ds_read_b64_tr_b16 v[64:65], v16 offset:0
v_mfma_f32_16x16x32_bf16 a[36:39], v[92:95], v[48:51], a[36:39]
v_mfma_f32_16x16x32_bf16 a[40:43], v[96:99], v[48:51], a[40:43]
ds_read_b64_tr_b16 v[66:67], v16 offset:2048
v_mfma_f32_16x16x32_bf16 a[44:47], v[100:103], v[48:51], a[44:47]
s_waitcnt lgkmcnt(6)
v_mfma_f32_16x16x32_bf16 a[48:51], v[88:91], v[52:55], a[48:51]
ds_read_b64_tr_b16 v[68:69], v17 offset:0
v_mfma_f32_16x16x32_bf16 a[52:55], v[92:95], v[52:55], a[52:55]
ds_read_b64_tr_b16 v[70:71], v17 offset:2048
v_mfma_f32_16x16x32_bf16 a[56:59], v[96:99], v[52:55], a[56:59]
buffer_load_dwordx4 v6, s[8:11], s28 offen offset:2048 lds
v_mfma_f32_16x16x32_bf16 a[60:63], v[100:103], v[52:55], a[60:63]
s_waitcnt lgkmcnt(6)
v_mfma_f32_16x16x32_bf16 a[64:67], v[88:91], v[56:59], a[64:67]
ds_read_b64_tr_b16 v[40:41], v10 offset:256
v_mfma_f32_16x16x32_bf16 a[68:71], v[92:95], v[56:59], a[68:71]
ds_read_b64_tr_b16 v[42:43], v10 offset:2304
v_mfma_f32_16x16x32_bf16 a[72:75], v[96:99], v[56:59], a[72:75]
buffer_load_dwordx4 v7, s[8:11], s28 offen offset:3072 lds
v_mfma_f32_16x16x32_bf16 a[76:79], v[100:103], v[56:59], a[76:79]
s_waitcnt lgkmcnt(6)
v_mfma_f32_16x16x32_bf16 a[80:83], v[88:91], v[60:63], a[80:83]
ds_read_b64_tr_b16 v[44:45], v11 offset:256
v_mfma_f32_16x16x32_bf16 a[84:87], v[92:95], v[60:63], a[84:87]
v_mfma_f32_16x16x32_bf16 a[88:91], v[96:99], v[60:63], a[88:91]
ds_read_b64_tr_b16 v[46:47], v11 offset:2304
v_mfma_f32_16x16x32_bf16 a[92:95], v[100:103], v[60:63], a[92:95]
s_waitcnt lgkmcnt(6)
v_mfma_f32_16x16x32_bf16 a[96:99], v[88:91], v[64:67], a[96:99]
ds_read_b64_tr_b16 v[48:49], v12 offset:256
ds_read_b64_tr_b16 v[50:51], v12 offset:2304
v_mfma_f32_16x16x32_bf16 a[100:103], v[92:95], v[64:67], a[100:103]
s_mov_b32 s29, 33664
v_mfma_f32_16x16x32_bf16 a[104:107], v[96:99], v[64:67], a[104:107]
s_add_u32 m0, s35, 0
v_mfma_f32_16x16x32_bf16 a[108:111], v[100:103], v[64:67], a[108:111]
buffer_load_dwordx4 v8, s[12:15], s29 offen lds
s_waitcnt lgkmcnt(6)
v_mfma_f32_16x16x32_bf16 a[112:115], v[88:91], v[68:71], a[112:115]
ds_read_b64_tr_b16 v[52:53], v13 offset:256
v_mfma_f32_16x16x32_bf16 a[116:119], v[92:95], v[68:71], a[116:119]
ds_read_b64_tr_b16 v[54:55], v13 offset:2304
v_mfma_f32_16x16x32_bf16 a[120:123], v[96:99], v[68:71], a[120:123]
buffer_load_dwordx4 v9, s[12:15], s29 offen offset:1024 lds
v_mfma_f32_16x16x32_bf16 a[124:127], v[100:103], v[68:71], a[124:127]
s_waitcnt lgkmcnt(6)
v_mfma_f32_16x16x32_bf16 a[128:131], v[88:91], v[40:43], a[128:131]
ds_read_b64_tr_b16 v[56:57], v14 offset:256
v_mfma_f32_16x16x32_bf16 a[132:135], v[92:95], v[40:43], a[132:135]
v_mfma_f32_16x16x32_bf16 a[136:139], v[96:99], v[40:43], a[136:139]
ds_read_b64_tr_b16 v[58:59], v14 offset:2304
v_mfma_f32_16x16x32_bf16 a[140:143], v[100:103], v[40:43], a[140:143]
s_waitcnt lgkmcnt(6)
v_mfma_f32_16x16x32_bf16 a[144:147], v[88:91], v[44:47], a[144:147]
ds_read_b64_tr_b16 v[60:61], v15 offset:256
v_mfma_f32_16x16x32_bf16 a[148:151], v[92:95], v[44:47], a[148:151]
ds_read_b64_tr_b16 v[62:63], v15 offset:2304
v_mfma_f32_16x16x32_bf16 a[152:155], v[96:99], v[44:47], a[152:155]
buffer_load_dwordx4 v37, s[12:15], s29 offen offset:2048 lds
v_mfma_f32_16x16x32_bf16 a[156:159], v[100:103], v[44:47], a[156:159]
s_waitcnt lgkmcnt(6)
v_mfma_f32_16x16x32_bf16 a[160:163], v[88:91], v[48:51], a[160:163]
ds_read_b64_tr_b16 v[64:65], v16 offset:256
v_mfma_f32_16x16x32_bf16 a[164:167], v[92:95], v[48:51], a[164:167]
ds_read_b64_tr_b16 v[66:67], v16 offset:2304
v_mfma_f32_16x16x32_bf16 a[168:171], v[96:99], v[48:51], a[168:171]
buffer_load_dwordx4 v38, s[12:15], s29 offen offset:3072 lds
v_mfma_f32_16x16x32_bf16 a[172:175], v[100:103], v[48:51], a[172:175]
s_waitcnt lgkmcnt(6)
v_mfma_f32_16x16x32_bf16 a[176:179], v[88:91], v[52:55], a[176:179]
ds_read_b64_tr_b16 v[68:69], v17 offset:256
v_mfma_f32_16x16x32_bf16 a[180:183], v[92:95], v[52:55], a[180:183]
v_mfma_f32_16x16x32_bf16 a[184:187], v[96:99], v[52:55], a[184:187]
ds_read_b64_tr_b16 v[70:71], v17 offset:2304
v_mfma_f32_16x16x32_bf16 a[188:191], v[100:103], v[52:55], a[188:191]
s_waitcnt vmcnt(22)
s_barrier
s_waitcnt lgkmcnt(6)
v_mfma_f32_16x16x32_bf16 a[192:195], v[88:91], v[56:59], a[192:195]
ds_read_b64_tr_b16 v[40:41], v10 offset:16384
v_mfma_f32_16x16x32_bf16 a[196:199], v[92:95], v[56:59], a[196:199]
v_mfma_f32_16x16x32_bf16 a[200:203], v[96:99], v[56:59], a[200:203]
ds_read_b64_tr_b16 v[42:43], v10 offset:18432
v_mfma_f32_16x16x32_bf16 a[204:207], v[100:103], v[56:59], a[204:207]
s_waitcnt lgkmcnt(6)
v_mfma_f32_16x16x32_bf16 a[208:211], v[88:91], v[60:63], a[208:211]
ds_read_b64_tr_b16 v[44:45], v11 offset:16384
v_mfma_f32_16x16x32_bf16 a[212:215], v[92:95], v[60:63], a[212:215]
ds_read_b64_tr_b16 v[46:47], v11 offset:18432
v_mfma_f32_16x16x32_bf16 a[216:219], v[96:99], v[60:63], a[216:219]
global_load_dword v127, v125, s[66:67]
v_mfma_f32_16x16x32_bf16 a[220:223], v[100:103], v[60:63], a[220:223]
s_waitcnt lgkmcnt(6)
v_mfma_f32_16x16x32_bf16 a[224:227], v[88:91], v[64:67], a[224:227]
ds_read_b64_tr_b16 v[48:49], v12 offset:16384
v_mfma_f32_16x16x32_bf16 a[228:231], v[92:95], v[64:67], a[228:231]
v_mfma_f32_16x16x32_bf16 a[232:235], v[96:99], v[64:67], a[232:235]
ds_read_b64_tr_b16 v[50:51], v12 offset:18432
v_mfma_f32_16x16x32_bf16 a[236:239], v[100:103], v[64:67], a[236:239]
s_waitcnt lgkmcnt(6)
v_mfma_f32_16x16x32_bf16 a[240:243], v[88:91], v[68:71], a[240:243]
ds_read_b64_tr_b16 v[52:53], v13 offset:16384
v_mfma_f32_16x16x32_bf16 a[244:247], v[92:95], v[68:71], a[244:247]
v_mfma_f32_16x16x32_bf16 a[248:251], v[96:99], v[68:71], a[248:251]
ds_read_b64_tr_b16 v[54:55], v13 offset:18432
v_mfma_f32_16x16x32_bf16 a[252:255], v[100:103], v[68:71], a[252:255]
s_waitcnt lgkmcnt(6)
v_mfma_f32_16x16x32_bf16 a[0:3], v[104:107], v[40:43], a[0:3]
ds_read_b64_tr_b16 v[56:57], v14 offset:16384
ds_read_b64_tr_b16 v[58:59], v14 offset:18432
v_mfma_f32_16x16x32_bf16 a[4:7], v[108:111], v[40:43], a[4:7]
s_add_u32 s28, s37, 3670016
v_mfma_f32_16x16x32_bf16 a[8:11], v[112:115], v[40:43], a[8:11]
s_add_u32 m0, s34, 65536
v_mfma_f32_16x16x32_bf16 a[12:15], v[116:119], v[40:43], a[12:15]
buffer_load_dwordx4 v4, s[8:11], s28 offen lds
s_waitcnt lgkmcnt(6)
v_mfma_f32_16x16x32_bf16 a[16:19], v[104:107], v[44:47], a[16:19]
ds_read_b64_tr_b16 v[60:61], v15 offset:16384
v_mfma_f32_16x16x32_bf16 a[20:23], v[108:111], v[44:47], a[20:23]
v_mfma_f32_16x16x32_bf16 a[24:27], v[112:115], v[44:47], a[24:27]
ds_read_b64_tr_b16 v[62:63], v15 offset:18432
v_mfma_f32_16x16x32_bf16 a[28:31], v[116:119], v[44:47], a[28:31]
s_waitcnt lgkmcnt(6)
v_mfma_f32_16x16x32_bf16 a[32:35], v[104:107], v[48:51], a[32:35]
ds_read_b64_tr_b16 v[64:65], v16 offset:16384
v_mfma_f32_16x16x32_bf16 a[36:39], v[108:111], v[48:51], a[36:39]
v_mfma_f32_16x16x32_bf16 a[40:43], v[112:115], v[48:51], a[40:43]
ds_read_b64_tr_b16 v[66:67], v16 offset:18432
v_mfma_f32_16x16x32_bf16 a[44:47], v[116:119], v[48:51], a[44:47]
s_waitcnt lgkmcnt(6)
v_mfma_f32_16x16x32_bf16 a[48:51], v[104:107], v[52:55], a[48:51]
ds_read_b64_tr_b16 v[68:69], v17 offset:16384
v_mfma_f32_16x16x32_bf16 a[52:55], v[108:111], v[52:55], a[52:55]
ds_read_b64_tr_b16 v[70:71], v17 offset:18432
v_mfma_f32_16x16x32_bf16 a[56:59], v[112:115], v[52:55], a[56:59]
s_waitcnt vmcnt(20)
v_mfma_f32_16x16x32_bf16 a[60:63], v[116:119], v[52:55], a[60:63]
buffer_load_dwordx4 v5, s[8:11], s28 offen offset:1024 lds
s_waitcnt lgkmcnt(6)
v_mfma_f32_16x16x32_bf16 a[64:67], v[104:107], v[56:59], a[64:67]
ds_read_b64_tr_b16 v[40:41], v10 offset:16640
v_mfma_f32_16x16x32_bf16 a[68:71], v[108:111], v[56:59], a[68:71]
ds_read_b64_tr_b16 v[42:43], v10 offset:18688
v_mfma_f32_16x16x32_bf16 a[72:75], v[112:115], v[56:59], a[72:75]
ds_read_b128 v[72:75], v26 offset:8192
v_mfma_f32_16x16x32_bf16 a[76:79], v[116:119], v[56:59], a[76:79]
s_waitcnt lgkmcnt(7)
v_mfma_f32_16x16x32_bf16 a[80:83], v[104:107], v[60:63], a[80:83]
ds_read_b64_tr_b16 v[44:45], v11 offset:16640
v_mfma_f32_16x16x32_bf16 a[84:87], v[108:111], v[60:63], a[84:87]
ds_read_b64_tr_b16 v[46:47], v11 offset:18688
v_mfma_f32_16x16x32_bf16 a[88:91], v[112:115], v[60:63], a[88:91]
ds_read_b128 v[76:79], v26 offset:10240
v_mfma_f32_16x16x32_bf16 a[92:95], v[116:119], v[60:63], a[92:95]
s_waitcnt lgkmcnt(8)
v_mfma_f32_16x16x32_bf16 a[96:99], v[104:107], v[64:67], a[96:99]
ds_read_b64_tr_b16 v[48:49], v12 offset:16640
v_mfma_f32_16x16x32_bf16 a[100:103], v[108:111], v[64:67], a[100:103]
ds_read_b64_tr_b16 v[50:51], v12 offset:18688
v_mfma_f32_16x16x32_bf16 a[104:107], v[112:115], v[64:67], a[104:107]
ds_read_b128 v[80:83], v26 offset:12288
v_mfma_f32_16x16x32_bf16 a[108:111], v[116:119], v[64:67], a[108:111]
buffer_load_dwordx4 v6, s[8:11], s28 offen offset:2048 lds
s_waitcnt lgkmcnt(9)
v_mfma_f32_16x16x32_bf16 a[112:115], v[104:107], v[68:71], a[112:115]
ds_read_b64_tr_b16 v[52:53], v13 offset:16640
v_mfma_f32_16x16x32_bf16 a[116:119], v[108:111], v[68:71], a[116:119]
ds_read_b64_tr_b16 v[54:55], v13 offset:18688
v_mfma_f32_16x16x32_bf16 a[120:123], v[112:115], v[68:71], a[120:123]
ds_read_b128 v[84:87], v26 offset:14336
v_mfma_f32_16x16x32_bf16 a[124:127], v[116:119], v[68:71], a[124:127]
s_waitcnt lgkmcnt(10)
v_mfma_f32_16x16x32_bf16 a[128:131], v[104:107], v[40:43], a[128:131]
ds_read_b64_tr_b16 v[56:57], v14 offset:16640
v_mfma_f32_16x16x32_bf16 a[132:135], v[108:111], v[40:43], a[132:135]
ds_read_b64_tr_b16 v[58:59], v14 offset:18688
v_mfma_f32_16x16x32_bf16 a[136:139], v[112:115], v[40:43], a[136:139]
ds_read_b128 v[88:91], v27 offset:8192
v_mfma_f32_16x16x32_bf16 a[140:143], v[116:119], v[40:43], a[140:143]
s_waitcnt lgkmcnt(10)
v_mfma_f32_16x16x32_bf16 a[144:147], v[104:107], v[44:47], a[144:147]
ds_read_b64_tr_b16 v[60:61], v15 offset:16640
v_mfma_f32_16x16x32_bf16 a[148:151], v[108:111], v[44:47], a[148:151]
)ASM"
R"ASM(ds_read_b64_tr_b16 v[62:63], v15 offset:18688
v_mfma_f32_16x16x32_bf16 a[152:155], v[112:115], v[44:47], a[152:155]
ds_read_b128 v[92:95], v27 offset:10240
v_mfma_f32_16x16x32_bf16 a[156:159], v[116:119], v[44:47], a[156:159]
buffer_load_dwordx4 v7, s[8:11], s28 offen offset:3072 lds
s_waitcnt lgkmcnt(10)
v_mfma_f32_16x16x32_bf16 a[160:163], v[104:107], v[48:51], a[160:163]
ds_read_b64_tr_b16 v[64:65], v16 offset:16640
v_mfma_f32_16x16x32_bf16 a[164:167], v[108:111], v[48:51], a[164:167]
ds_read_b64_tr_b16 v[66:67], v16 offset:18688
v_mfma_f32_16x16x32_bf16 a[168:171], v[112:115], v[48:51], a[168:171]
ds_read_b128 v[96:99], v27 offset:12288
v_mfma_f32_16x16x32_bf16 a[172:175], v[116:119], v[48:51], a[172:175]
s_waitcnt lgkmcnt(10)
v_mfma_f32_16x16x32_bf16 a[176:179], v[104:107], v[52:55], a[176:179]
ds_read_b64_tr_b16 v[68:69], v17 offset:16640
v_mfma_f32_16x16x32_bf16 a[180:183], v[108:111], v[52:55], a[180:183]
ds_read_b64_tr_b16 v[70:71], v17 offset:18688
v_mfma_f32_16x16x32_bf16 a[184:187], v[112:115], v[52:55], a[184:187]
ds_read_b128 v[100:103], v27 offset:14336
v_mfma_f32_16x16x32_bf16 a[188:191], v[116:119], v[52:55], a[188:191]
s_waitcnt vmcnt(18)
s_barrier
s_waitcnt lgkmcnt(10)
v_mfma_f32_16x16x32_bf16 a[192:195], v[104:107], v[56:59], a[192:195]
ds_read_b64_tr_b16 v[40:41], v10 offset:32768
v_mfma_f32_16x16x32_bf16 a[196:199], v[108:111], v[56:59], a[196:199]
v_mfma_f32_16x16x32_bf16 a[200:203], v[112:115], v[56:59], a[200:203]
ds_read_b64_tr_b16 v[42:43], v10 offset:34816
v_mfma_f32_16x16x32_bf16 a[204:207], v[116:119], v[56:59], a[204:207]
s_waitcnt lgkmcnt(9)
v_mfma_f32_16x16x32_bf16 a[208:211], v[104:107], v[60:63], a[208:211]
ds_read_b64_tr_b16 v[44:45], v11 offset:32768
v_mfma_f32_16x16x32_bf16 a[212:215], v[108:111], v[60:63], a[212:215]
ds_read_b64_tr_b16 v[46:47], v11 offset:34816
v_mfma_f32_16x16x32_bf16 a[216:219], v[112:115], v[60:63], a[216:219]
global_load_dwordx2 v[122:123], v120, s[48:49] sc1
v_mfma_f32_16x16x32_bf16 a[220:223], v[116:119], v[60:63], a[220:223]
s_waitcnt lgkmcnt(8)
v_mfma_f32_16x16x32_bf16 a[224:227], v[104:107], v[64:67], a[224:227]
ds_read_b64_tr_b16 v[48:49], v12 offset:32768
v_mfma_f32_16x16x32_bf16 a[228:231], v[108:111], v[64:67], a[228:231]
v_mfma_f32_16x16x32_bf16 a[232:235], v[112:115], v[64:67], a[232:235]
ds_read_b64_tr_b16 v[50:51], v12 offset:34816
v_mfma_f32_16x16x32_bf16 a[236:239], v[116:119], v[64:67], a[236:239]
s_waitcnt lgkmcnt(7)
v_mfma_f32_16x16x32_bf16 a[240:243], v[104:107], v[68:71], a[240:243]
ds_read_b64_tr_b16 v[52:53], v13 offset:32768
v_mfma_f32_16x16x32_bf16 a[244:247], v[108:111], v[68:71], a[244:247]
v_mfma_f32_16x16x32_bf16 a[248:251], v[112:115], v[68:71], a[248:251]
ds_read_b64_tr_b16 v[54:55], v13 offset:34816
v_mfma_f32_16x16x32_bf16 a[252:255], v[116:119], v[68:71], a[252:255]
s_waitcnt lgkmcnt(6)
v_mfma_f32_16x16x32_bf16 a[0:3], v[72:75], v[40:43], a[0:3]
ds_read_b64_tr_b16 v[56:57], v14 offset:32768
ds_read_b64_tr_b16 v[58:59], v14 offset:34816
v_mfma_f32_16x16x32_bf16 a[4:7], v[76:79], v[40:43], a[4:7]
s_add_u32 s28, s37, 3932160
v_mfma_f32_16x16x32_bf16 a[8:11], v[80:83], v[40:43], a[8:11]
s_add_u32 m0, s34, 0
v_mfma_f32_16x16x32_bf16 a[12:15], v[84:87], v[40:43], a[12:15]
buffer_load_dwordx4 v4, s[8:11], s28 offen lds
s_waitcnt lgkmcnt(6)
v_mfma_f32_16x16x32_bf16 a[16:19], v[72:75], v[44:47], a[16:19]
ds_read_b64_tr_b16 v[60:61], v15 offset:32768
v_mfma_f32_16x16x32_bf16 a[20:23], v[76:79], v[44:47], a[20:23]
v_mfma_f32_16x16x32_bf16 a[24:27], v[80:83], v[44:47], a[24:27]
ds_read_b64_tr_b16 v[62:63], v15 offset:34816
v_mfma_f32_16x16x32_bf16 a[28:31], v[84:87], v[44:47], a[28:31]
s_waitcnt lgkmcnt(6)
v_mfma_f32_16x16x32_bf16 a[32:35], v[72:75], v[48:51], a[32:35]
ds_read_b64_tr_b16 v[64:65], v16 offset:32768
v_mfma_f32_16x16x32_bf16 a[36:39], v[76:79], v[48:51], a[36:39]
v_mfma_f32_16x16x32_bf16 a[40:43], v[80:83], v[48:51], a[40:43]
ds_read_b64_tr_b16 v[66:67], v16 offset:34816
v_mfma_f32_16x16x32_bf16 a[44:47], v[84:87], v[48:51], a[44:47]
s_waitcnt lgkmcnt(6)
v_mfma_f32_16x16x32_bf16 a[48:51], v[72:75], v[52:55], a[48:51]
ds_read_b64_tr_b16 v[68:69], v17 offset:32768
v_mfma_f32_16x16x32_bf16 a[52:55], v[76:79], v[52:55], a[52:55]
ds_read_b64_tr_b16 v[70:71], v17 offset:34816
v_mfma_f32_16x16x32_bf16 a[56:59], v[80:83], v[52:55], a[56:59]
buffer_load_dwordx4 v5, s[8:11], s28 offen offset:1024 lds
v_mfma_f32_16x16x32_bf16 a[60:63], v[84:87], v[52:55], a[60:63]
s_waitcnt lgkmcnt(6)
v_mfma_f32_16x16x32_bf16 a[64:67], v[72:75], v[56:59], a[64:67]
ds_read_b64_tr_b16 v[40:41], v10 offset:33024
v_mfma_f32_16x16x32_bf16 a[68:71], v[76:79], v[56:59], a[68:71]
v_mfma_f32_16x16x32_bf16 a[72:75], v[80:83], v[56:59], a[72:75]
ds_read_b64_tr_b16 v[42:43], v10 offset:35072
v_mfma_f32_16x16x32_bf16 a[76:79], v[84:87], v[56:59], a[76:79]
s_waitcnt lgkmcnt(6)
v_mfma_f32_16x16x32_bf16 a[80:83], v[72:75], v[60:63], a[80:83]
ds_read_b64_tr_b16 v[44:45], v11 offset:33024
v_mfma_f32_16x16x32_bf16 a[84:87], v[76:79], v[60:63], a[84:87]
v_mfma_f32_16x16x32_bf16 a[88:91], v[80:83], v[60:63], a[88:91]
ds_read_b64_tr_b16 v[46:47], v11 offset:35072
v_mfma_f32_16x16x32_bf16 a[92:95], v[84:87], v[60:63], a[92:95]
s_waitcnt lgkmcnt(6)
v_mfma_f32_16x16x32_bf16 a[96:99], v[72:75], v[64:67], a[96:99]
ds_read_b64_tr_b16 v[48:49], v12 offset:33024
v_mfma_f32_16x16x32_bf16 a[100:103], v[76:79], v[64:67], a[100:103]
ds_read_b64_tr_b16 v[50:51], v12 offset:35072
v_mfma_f32_16x16x32_bf16 a[104:107], v[80:83], v[64:67], a[104:107]
buffer_load_dwordx4 v6, s[8:11], s28 offen offset:2048 lds
v_mfma_f32_16x16x32_bf16 a[108:111], v[84:87], v[64:67], a[108:111]
s_waitcnt lgkmcnt(6)
v_mfma_f32_16x16x32_bf16 a[112:115], v[72:75], v[68:71], a[112:115]
ds_read_b64_tr_b16 v[52:53], v13 offset:33024
v_mfma_f32_16x16x32_bf16 a[116:119], v[76:79], v[68:71], a[116:119]
v_mfma_f32_16x16x32_bf16 a[120:123], v[80:83], v[68:71], a[120:123]
ds_read_b64_tr_b16 v[54:55], v13 offset:35072
v_mfma_f32_16x16x32_bf16 a[124:127], v[84:87], v[68:71], a[124:127]
s_waitcnt lgkmcnt(6)
v_mfma_f32_16x16x32_bf16 a[128:131], v[72:75], v[40:43], a[128:131]
ds_read_b64_tr_b16 v[56:57], v14 offset:33024
v_mfma_f32_16x16x32_bf16 a[132:135], v[76:79], v[40:43], a[132:135]
v_mfma_f32_16x16x32_bf16 a[136:139], v[80:83], v[40:43], a[136:139]
ds_read_b64_tr_b16 v[58:59], v14 offset:35072
v_mfma_f32_16x16x32_bf16 a[140:143], v[84:87], v[40:43], a[140:143]
s_waitcnt lgkmcnt(6)
v_mfma_f32_16x16x32_bf16 a[144:147], v[72:75], v[44:47], a[144:147]
ds_read_b64_tr_b16 v[60:61], v15 offset:33024
v_mfma_f32_16x16x32_bf16 a[148:151], v[76:79], v[44:47], a[148:151]
ds_read_b64_tr_b16 v[62:63], v15 offset:35072
v_mfma_f32_16x16x32_bf16 a[152:155], v[80:83], v[44:47], a[152:155]
buffer_load_dwordx4 v7, s[8:11], s28 offen offset:3072 lds
v_mfma_f32_16x16x32_bf16 a[156:159], v[84:87], v[44:47], a[156:159]
s_waitcnt lgkmcnt(6)
v_mfma_f32_16x16x32_bf16 a[160:163], v[72:75], v[48:51], a[160:163]
ds_read_b64_tr_b16 v[64:65], v16 offset:33024
v_mfma_f32_16x16x32_bf16 a[164:167], v[76:79], v[48:51], a[164:167]
v_mfma_f32_16x16x32_bf16 a[168:171], v[80:83], v[48:51], a[168:171]
ds_read_b64_tr_b16 v[66:67], v16 offset:35072
v_mfma_f32_16x16x32_bf16 a[172:175], v[84:87], v[48:51], a[172:175]
s_waitcnt lgkmcnt(6)
v_mfma_f32_16x16x32_bf16 a[176:179], v[72:75], v[52:55], a[176:179]
ds_read_b64_tr_b16 v[68:69], v17 offset:33024
v_mfma_f32_16x16x32_bf16 a[180:183], v[76:79], v[52:55], a[180:183]
v_mfma_f32_16x16x32_bf16 a[184:187], v[80:83], v[52:55], a[184:187]
ds_read_b64_tr_b16 v[70:71], v17 offset:35072
v_mfma_f32_16x16x32_bf16 a[188:191], v[84:87], v[52:55], a[188:191]
s_waitcnt vmcnt(14)
s_barrier
s_waitcnt lgkmcnt(6)
v_mfma_f32_16x16x32_bf16 a[192:195], v[72:75], v[56:59], a[192:195]
ds_read_b64_tr_b16 v[40:41], v10 offset:49152
v_mfma_f32_16x16x32_bf16 a[196:199], v[76:79], v[56:59], a[196:199]
v_mfma_f32_16x16x32_bf16 a[200:203], v[80:83], v[56:59], a[200:203]
ds_read_b64_tr_b16 v[42:43], v10 offset:51200
v_mfma_f32_16x16x32_bf16 a[204:207], v[84:87], v[56:59], a[204:207]
s_waitcnt lgkmcnt(6)
v_mfma_f32_16x16x32_bf16 a[208:211], v[72:75], v[60:63], a[208:211]
ds_read_b64_tr_b16 v[44:45], v11 offset:49152
v_mfma_f32_16x16x32_bf16 a[212:215], v[76:79], v[60:63], a[212:215]
v_mfma_f32_16x16x32_bf16 a[216:219], v[80:83], v[60:63], a[216:219]
ds_read_b64_tr_b16 v[46:47], v11 offset:51200
v_mfma_f32_16x16x32_bf16 a[220:223], v[84:87], v[60:63], a[220:223]
s_waitcnt lgkmcnt(6)
v_mfma_f32_16x16x32_bf16 a[224:227], v[72:75], v[64:67], a[224:227]
ds_read_b64_tr_b16 v[48:49], v12 offset:49152
v_mfma_f32_16x16x32_bf16 a[228:231], v[76:79], v[64:67], a[228:231]
v_mfma_f32_16x16x32_bf16 a[232:235], v[80:83], v[64:67], a[232:235]
ds_read_b64_tr_b16 v[50:51], v12 offset:51200
v_mfma_f32_16x16x32_bf16 a[236:239], v[84:87], v[64:67], a[236:239]
s_waitcnt lgkmcnt(6)
v_mfma_f32_16x16x32_bf16 a[240:243], v[72:75], v[68:71], a[240:243]
ds_read_b64_tr_b16 v[52:53], v13 offset:49152
v_mfma_f32_16x16x32_bf16 a[244:247], v[76:79], v[68:71], a[244:247]
v_mfma_f32_16x16x32_bf16 a[248:251], v[80:83], v[68:71], a[248:251]
ds_read_b64_tr_b16 v[54:55], v13 offset:51200
v_mfma_f32_16x16x32_bf16 a[252:255], v[84:87], v[68:71], a[252:255]
s_waitcnt lgkmcnt(6)
v_mfma_f32_16x16x32_bf16 a[0:3], v[88:91], v[40:43], a[0:3]
ds_read_b64_tr_b16 v[56:57], v14 offset:49152
ds_read_b64_tr_b16 v[58:59], v14 offset:51200
v_mfma_f32_16x16x32_bf16 a[4:7], v[92:95], v[40:43], a[4:7]
s_add_u32 s28, s71, 0
v_mfma_f32_16x16x32_bf16 a[8:11], v[96:99], v[40:43], a[8:11]
s_add_u32 m0, s70, 0
v_mfma_f32_16x16x32_bf16 a[12:15], v[100:103], v[40:43], a[12:15]
buffer_load_dwordx4 v29, s[76:79], s28 offen lds
s_waitcnt lgkmcnt(6)
v_mfma_f32_16x16x32_bf16 a[16:19], v[88:91], v[44:47], a[16:19]
ds_read_b64_tr_b16 v[60:61], v15 offset:49152
v_mfma_f32_16x16x32_bf16 a[20:23], v[92:95], v[44:47], a[20:23]
v_mfma_f32_16x16x32_bf16 a[24:27], v[96:99], v[44:47], a[24:27]
ds_read_b64_tr_b16 v[62:63], v15 offset:51200
v_mfma_f32_16x16x32_bf16 a[28:31], v[100:103], v[44:47], a[28:31]
s_waitcnt lgkmcnt(6)
v_mfma_f32_16x16x32_bf16 a[32:35], v[88:91], v[48:51], a[32:35]
ds_read_b64_tr_b16 v[64:65], v16 offset:49152
v_mfma_f32_16x16x32_bf16 a[36:39], v[92:95], v[48:51], a[36:39]
v_mfma_f32_16x16x32_bf16 a[40:43], v[96:99], v[48:51], a[40:43]
ds_read_b64_tr_b16 v[66:67], v16 offset:51200
v_mfma_f32_16x16x32_bf16 a[44:47], v[100:103], v[48:51], a[44:47]
s_waitcnt lgkmcnt(6)
v_mfma_f32_16x16x32_bf16 a[48:51], v[88:91], v[52:55], a[48:51]
ds_read_b64_tr_b16 v[68:69], v17 offset:49152
ds_read_b64_tr_b16 v[70:71], v17 offset:51200
v_mfma_f32_16x16x32_bf16 a[52:55], v[92:95], v[52:55], a[52:55]
s_waitcnt vmcnt(11)
v_mfma_f32_16x16x32_bf16 a[56:59], v[96:99], v[52:55], a[56:59]
s_add_u32 s28, s28, 31744
v_mfma_f32_16x16x32_bf16 a[60:63], v[100:103], v[52:55], a[60:63]
buffer_load_dwordx4 v29, s[76:79], s28 offen offset:1024 lds
s_waitcnt lgkmcnt(6)
v_mfma_f32_16x16x32_bf16 a[64:67], v[88:91], v[56:59], a[64:67]
ds_read_b64_tr_b16 v[40:41], v10 offset:49408
v_mfma_f32_16x16x32_bf16 a[68:71], v[92:95], v[56:59], a[68:71]
ds_read_b64_tr_b16 v[42:43], v10 offset:51456
v_mfma_f32_16x16x32_bf16 a[72:75], v[96:99], v[56:59], a[72:75]
ds_read_b128 v[104:107], v26 offset:16384
v_mfma_f32_16x16x32_bf16 a[76:79], v[100:103], v[56:59], a[76:79]
s_waitcnt lgkmcnt(7)
v_mfma_f32_16x16x32_bf16 a[80:83], v[88:91], v[60:63], a[80:83]
ds_read_b64_tr_b16 v[44:45], v11 offset:49408
v_mfma_f32_16x16x32_bf16 a[84:87], v[92:95], v[60:63], a[84:87]
ds_read_b64_tr_b16 v[46:47], v11 offset:51456
v_mfma_f32_16x16x32_bf16 a[88:91], v[96:99], v[60:63], a[88:91]
ds_read_b128 v[108:111], v26 offset:18432
v_mfma_f32_16x16x32_bf16 a[92:95], v[100:103], v[60:63], a[92:95]
s_waitcnt lgkmcnt(8)
v_mfma_f32_16x16x32_bf16 a[96:99], v[88:91], v[64:67], a[96:99]
ds_read_b64_tr_b16 v[48:49], v12 offset:49408
ds_read_b64_tr_b16 v[50:51], v12 offset:51456
v_mfma_f32_16x16x32_bf16 a[100:103], v[92:95], v[64:67], a[100:103]
ds_read_b128 v[112:115], v26 offset:0
v_mfma_f32_16x16x32_bf16 a[104:107], v[96:99], v[64:67], a[104:107]
s_add_u32 s28, s73, 0
v_mfma_f32_16x16x32_bf16 a[108:111], v[100:103], v[64:67], a[108:111]
buffer_load_dwordx4 v30, s[80:83], s28 offen offset:2048 lds
s_waitcnt lgkmcnt(9)
v_mfma_f32_16x16x32_bf16 a[112:115], v[88:91], v[68:71], a[112:115]
ds_read_b64_tr_b16 v[52:53], v13 offset:49408
v_mfma_f32_16x16x32_bf16 a[116:119], v[92:95], v[68:71], a[116:119]
ds_read_b64_tr_b16 v[54:55], v13 offset:51456
v_mfma_f32_16x16x32_bf16 a[120:123], v[96:99], v[68:71], a[120:123]
ds_read_b128 v[116:119], v26 offset:2048
v_mfma_f32_16x16x32_bf16 a[124:127], v[100:103], v[68:71], a[124:127]
s_waitcnt lgkmcnt(10)
v_mfma_f32_16x16x32_bf16 a[128:131], v[88:91], v[40:43], a[128:131]
ds_read_b64_tr_b16 v[56:57], v14 offset:49408
v_mfma_f32_16x16x32_bf16 a[132:135], v[92:95], v[40:43], a[132:135]
ds_read_b64_tr_b16 v[58:59], v14 offset:51456
v_mfma_f32_16x16x32_bf16 a[136:139], v[96:99], v[40:43], a[136:139]
ds_read_b128 v[72:75], v27 offset:16384
v_mfma_f32_16x16x32_bf16 a[140:143], v[100:103], v[40:43], a[140:143]
s_waitcnt lgkmcnt(10)
v_mfma_f32_16x16x32_bf16 a[144:147], v[88:91], v[44:47], a[144:147]
ds_read_b64_tr_b16 v[60:61], v15 offset:49408
v_mfma_f32_16x16x32_bf16 a[148:151], v[92:95], v[44:47], a[148:151]
ds_read_b64_tr_b16 v[62:63], v15 offset:51456
v_mfma_f32_16x16x32_bf16 a[152:155], v[96:99], v[44:47], a[152:155]
ds_read_b128 v[76:79], v27 offset:18432
v_mfma_f32_16x16x32_bf16 a[156:159], v[100:103], v[44:47], a[156:159]
buffer_load_dwordx4 v30, s[84:87], s28 offen offset:3072 lds
s_waitcnt lgkmcnt(10)
v_mfma_f32_16x16x32_bf16 a[160:163], v[88:91], v[48:51], a[160:163]
ds_read_b64_tr_b16 v[64:65], v16 offset:49408
v_mfma_f32_16x16x32_bf16 a[164:167], v[92:95], v[48:51], a[164:167]
ds_read_b64_tr_b16 v[66:67], v16 offset:51456
v_mfma_f32_16x16x32_bf16 a[168:171], v[96:99], v[48:51], a[168:171]
ds_read_b128 v[80:83], v27 offset:0
v_mfma_f32_16x16x32_bf16 a[172:175], v[100:103], v[48:51], a[172:175]
s_waitcnt lgkmcnt(10)
v_mfma_f32_16x16x32_bf16 a[176:179], v[88:91], v[52:55], a[176:179]
ds_read_b64_tr_b16 v[68:69], v17 offset:49408
v_mfma_f32_16x16x32_bf16 a[180:183], v[92:95], v[52:55], a[180:183]
ds_read_b64_tr_b16 v[70:71], v17 offset:51456
v_mfma_f32_16x16x32_bf16 a[184:187], v[96:99], v[52:55], a[184:187]
ds_read_b128 v[84:87], v27 offset:2048
v_mfma_f32_16x16x32_bf16 a[188:191], v[100:103], v[52:55], a[188:191]
s_waitcnt vmcnt(4)
s_barrier
s_waitcnt lgkmcnt(10)
v_mfma_f32_16x16x32_bf16 a[192:195], v[88:91], v[56:59], a[192:195]
ds_read_b64_tr_b16 v[40:41], v18 offset:0
v_mfma_f32_16x16x32_bf16 a[196:199], v[92:95], v[56:59], a[196:199]
v_mfma_f32_16x16x32_bf16 a[200:203], v[96:99], v[56:59], a[200:203]
ds_read_b64_tr_b16 v[42:43], v18 offset:2048
v_mfma_f32_16x16x32_bf16 a[204:207], v[100:103], v[56:59], a[204:207]
s_waitcnt lgkmcnt(9)
v_mfma_f32_16x16x32_bf16 a[208:211], v[88:91], v[60:63], a[208:211]
ds_read_b64_tr_b16 v[44:45], v10 offset:0
v_mfma_f32_16x16x32_bf16 a[212:215], v[92:95], v[60:63], a[212:215]
v_mfma_f32_16x16x32_bf16 a[216:219], v[96:99], v[60:63], a[216:219]
ds_read_b64_tr_b16 v[46:47], v10 offset:2048
v_mfma_f32_16x16x32_bf16 a[220:223], v[100:103], v[60:63], a[220:223]
s_waitcnt lgkmcnt(8)
v_mfma_f32_16x16x32_bf16 a[224:227], v[88:91], v[64:67], a[224:227]
ds_read_b64_tr_b16 v[48:49], v19 offset:0
v_mfma_f32_16x16x32_bf16 a[228:231], v[92:95], v[64:67], a[228:231]
v_mfma_f32_16x16x32_bf16 a[232:235], v[96:99], v[64:67], a[232:235]
ds_read_b64_tr_b16 v[50:51], v19 offset:2048
v_mfma_f32_16x16x32_bf16 a[236:239], v[100:103], v[64:67], a[236:239]
s_waitcnt lgkmcnt(7)
v_mfma_f32_16x16x32_bf16 a[240:243], v[88:91], v[68:71], a[240:243]
ds_read_b64_tr_b16 v[52:53], v11 offset:0
v_mfma_f32_16x16x32_bf16 a[244:247], v[92:95], v[68:71], a[244:247]
v_mfma_f32_16x16x32_bf16 a[248:251], v[96:99], v[68:71], a[248:251]
ds_read_b64_tr_b16 v[54:55], v11 offset:2048
v_mfma_f32_16x16x32_bf16 a[252:255], v[100:103], v[68:71], a[252:255]
s_waitcnt lgkmcnt(6)
v_mfma_f32_16x16x32_bf16 a[0:3], v[104:107], v[40:43], a[0:3]
ds_read_b64_tr_b16 v[56:57], v20 offset:0
v_mfma_f32_16x16x32_bf16 a[4:7], v[108:111], v[40:43], a[4:7]
v_mfma_f32_16x16x32_bf16 a[8:11], v[112:115], v[40:43], a[8:11]
ds_read_b64_tr_b16 v[58:59], v20 offset:2048
v_mfma_f32_16x16x32_bf16 a[12:15], v[116:119], v[40:43], a[12:15]
s_waitcnt lgkmcnt(6)
v_mfma_f32_16x16x32_bf16 a[0:3], v[72:75], v[44:47], a[0:3]
ds_read_b64_tr_b16 v[60:61], v12 offset:0
ds_read_b64_tr_b16 v[62:63], v12 offset:2048
v_mfma_f32_16x16x32_bf16 a[4:7], v[76:79], v[44:47], a[4:7]
s_add_u32 s28, s71, 262144
v_mfma_f32_16x16x32_bf16 a[8:11], v[80:83], v[44:47], a[8:11]
s_add_u32 m0, s70, 16384
v_mfma_f32_16x16x32_bf16 a[12:15], v[84:87], v[44:47], a[12:15]
buffer_load_dwordx4 v29, s[76:79], s28 offen lds
s_waitcnt lgkmcnt(6)
v_mfma_f32_16x16x32_bf16 a[16:19], v[104:107], v[48:51], a[16:19]
ds_read_b64_tr_b16 v[64:65], v21 offset:0
v_mfma_f32_16x16x32_bf16 a[20:23], v[108:111], v[48:51], a[20:23]
ds_read_b64_tr_b16 v[66:67], v21 offset:2048
v_mfma_f32_16x16x32_bf16 a[24:27], v[112:115], v[48:51], a[24:27]
s_add_u32 s28, s28, 31744
v_mfma_f32_16x16x32_bf16 a[28:31], v[116:119], v[48:51], a[28:31]
buffer_load_dwordx4 v29, s[76:79], s28 offen offset:1024 lds
s_waitcnt lgkmcnt(6)
v_mfma_f32_16x16x32_bf16 a[16:19], v[72:75], v[52:55], a[16:19]
ds_read_b64_tr_b16 v[68:69], v13 offset:0
ds_read_b64_tr_b16 v[70:71], v13 offset:2048
s_add_u32 s28, s73, 114688
buffer_load_dwordx4 v30, s[80:83], s28 offen offset:2048 lds
v_mfma_f32_16x16x32_bf16 a[20:23], v[76:79], v[52:55], a[20:23]
v_accvgpr_read_b32 v33, a0
v_accvgpr_read_b32 v34, a1
v_accvgpr_read_b32 v35, a2
v_accvgpr_read_b32 v36, a3
v_mfma_f32_16x16x32_bf16 a[24:27], v[80:83], v[52:55], a[24:27]
v_cvt_pk_bf16_f32 v128, v33, v34
v_cvt_pk_bf16_f32 v129, v35, v36
v_accvgpr_read_b32 v121, a4
v_accvgpr_read_b32 v124, a5
v_mfma_f32_16x16x32_bf16 a[28:31], v[84:87], v[52:55], a[28:31]
v_accvgpr_read_b32 v125, a6
v_accvgpr_read_b32 v126, a7
v_cvt_pk_bf16_f32 v130, v121, v124
v_cvt_pk_bf16_f32 v131, v125, v126
s_waitcnt lgkmcnt(6)
v_mfma_f32_16x16x32_bf16 a[32:35], v[104:107], v[56:59], a[32:35]
ds_read_b64_tr_b16 v[40:41], v22 offset:0
ds_read_b64_tr_b16 v[42:43], v22 offset:2048
buffer_load_dwordx4 v30, s[84:87], s28 offen offset:3072 lds
v_accvgpr_read_b32 v33, a8
v_mfma_f32_16x16x32_bf16 a[36:39], v[108:111], v[56:59], a[36:39]
v_accvgpr_read_b32 v34, a9
v_accvgpr_read_b32 v35, a10
v_accvgpr_read_b32 v36, a11
v_cvt_pk_bf16_f32 v160, v33, v34
v_mfma_f32_16x16x32_bf16 a[40:43], v[112:115], v[56:59], a[40:43]
v_cvt_pk_bf16_f32 v161, v35, v36
v_accvgpr_read_b32 v121, a12
v_accvgpr_read_b32 v124, a13
v_accvgpr_read_b32 v125, a14
v_mfma_f32_16x16x32_bf16 a[44:47], v[116:119], v[56:59], a[44:47]
v_accvgpr_read_b32 v126, a15
v_cvt_pk_bf16_f32 v162, v121, v124
v_cvt_pk_bf16_f32 v163, v125, v126
L_PW_LNV:
v_cmp_ne_u64 vcc, s[98:99], v[122:123]
s_nop 4
s_cbranch_vccz L_PK_LNV
s_mov_b32 s31, 1048576
L_PL_LNV:
s_sleep 2
global_load_dwordx2 v[122:123], v120, s[48:49] sc1
s_waitcnt vmcnt(0)
v_cmp_ne_u64 vcc, s[98:99], v[122:123]
s_sub_u32 s31, s31, 1
s_cbranch_scc1 L_PK_LNV
s_nop 4
s_cbranch_vccnz L_PL_LNV
L_PK_LNV:
s_waitcnt lgkmcnt(6)
v_mfma_f32_16x16x32_bf16 a[32:35], v[72:75], v[60:63], a[32:35]
ds_read_b64_tr_b16 v[44:45], v14 offset:0
ds_read_b64_tr_b16 v[46:47], v14 offset:2048
v_accvgpr_read_b32 v33, a16
v_accvgpr_read_b32 v34, a17
v_mfma_f32_16x16x32_bf16 a[36:39], v[76:79], v[60:63], a[36:39]
v_accvgpr_read_b32 v35, a18
v_accvgpr_read_b32 v36, a19
v_cvt_pk_bf16_f32 v132, v33, v34
v_mfma_f32_16x16x32_bf16 a[40:43], v[80:83], v[60:63], a[40:43]
v_cvt_pk_bf16_f32 v133, v35, v36
v_accvgpr_read_b32 v121, a20
v_accvgpr_read_b32 v124, a21
v_accvgpr_read_b32 v125, a22
v_mfma_f32_16x16x32_bf16 a[44:47], v[84:87], v[60:63], a[44:47]
v_accvgpr_read_b32 v126, a23
v_cvt_pk_bf16_f32 v134, v121, v124
v_cvt_pk_bf16_f32 v135, v125, v126
s_waitcnt lgkmcnt(6)
v_mfma_f32_16x16x32_bf16 a[48:51], v[104:107], v[64:67], a[48:51]
ds_read_b64_tr_b16 v[48:49], v23 offset:0
ds_read_b64_tr_b16 v[50:51], v23 offset:2048
s_mov_b32 s8, 0
buffer_load_dwordx4 v[88:91], v28, s[52:55], s8 offen offset:0 sc1
v_mfma_f32_16x16x32_bf16 a[52:55], v[108:111], v[64:67], a[52:55]
v_accvgpr_read_b32 v33, a24
v_accvgpr_read_b32 v34, a25
v_accvgpr_read_b32 v35, a26
v_accvgpr_read_b32 v36, a27
v_mfma_f32_16x16x32_bf16 a[56:59], v[112:115], v[64:67], a[56:59]
v_cvt_pk_bf16_f32 v164, v33, v34
v_cvt_pk_bf16_f32 v165, v35, v36
v_accvgpr_read_b32 v121, a28
v_accvgpr_read_b32 v124, a29
v_mfma_f32_16x16x32_bf16 a[60:63], v[116:119], v[64:67], a[60:63]
v_accvgpr_read_b32 v125, a30
v_accvgpr_read_b32 v126, a31
v_cvt_pk_bf16_f32 v166, v121, v124
v_cvt_pk_bf16_f32 v167, v125, v126
s_waitcnt lgkmcnt(6)
v_mfma_f32_16x16x32_bf16 a[48:51], v[72:75], v[68:71], a[48:51]
ds_read_b64_tr_b16 v[52:53], v15 offset:0
ds_read_b64_tr_b16 v[54:55], v15 offset:2048
s_mov_b32 s9, 32768
buffer_load_dwordx4 v[92:95], v28, s[52:55], s9 offen offset:0 sc1
v_mfma_f32_16x16x32_bf16 a[52:55], v[76:79], v[68:71], a[52:55]
v_accvgpr_read_b32 v33, a32
v_accvgpr_read_b32 v34, a33
v_accvgpr_read_b32 v35, a34
v_accvgpr_read_b32 v36, a35
v_mfma_f32_16x16x32_bf16 a[56:59], v[80:83], v[68:71], a[56:59]
v_cvt_pk_bf16_f32 v136, v33, v34
v_cvt_pk_bf16_f32 v137, v35, v36
v_accvgpr_read_b32 v121, a36
v_accvgpr_read_b32 v124, a37
v_mfma_f32_16x16x32_bf16 a[60:63], v[84:87], v[68:71], a[60:63]
v_accvgpr_read_b32 v125, a38
v_accvgpr_read_b32 v126, a39
v_cvt_pk_bf16_f32 v138, v121, v124
v_cvt_pk_bf16_f32 v139, v125, v126
s_waitcnt lgkmcnt(6)
v_mfma_f32_16x16x32_bf16 a[64:67], v[104:107], v[40:43], a[64:67]
ds_read_b64_tr_b16 v[56:57], v24 offset:0
ds_read_b64_tr_b16 v[58:59], v24 offset:2048
s_mov_b32 s10, 65536
buffer_load_dwordx4 v[96:99], v28, s[52:55], s10 offen offset:0 sc1
v_mfma_f32_16x16x32_bf16 a[68:71], v[108:111], v[40:43], a[68:71]
v_accvgpr_read_b32 v33, a40
v_accvgpr_read_b32 v34, a41
v_accvgpr_read_b32 v35, a42
v_accvgpr_read_b32 v36, a43
v_mfma_f32_16x16x32_bf16 a[72:75], v[112:115], v[40:43], a[72:75]
v_cvt_pk_bf16_f32 v168, v33, v34
v_cvt_pk_bf16_f32 v169, v35, v36
v_accvgpr_read_b32 v121, a44
v_accvgpr_read_b32 v124, a45
v_mfma_f32_16x16x32_bf16 a[76:79], v[116:119], v[40:43], a[76:79]
v_accvgpr_read_b32 v125, a46
v_accvgpr_read_b32 v126, a47
v_cvt_pk_bf16_f32 v170, v121, v124
v_cvt_pk_bf16_f32 v171, v125, v126
s_waitcnt lgkmcnt(6)
v_mfma_f32_16x16x32_bf16 a[64:67], v[72:75], v[44:47], a[64:67]
ds_read_b64_tr_b16 v[60:61], v16 offset:0
ds_read_b64_tr_b16 v[62:63], v16 offset:2048
s_mov_b32 s11, 98304
buffer_load_dwordx4 v[100:103], v28, s[52:55], s11 offen offset:0 sc1
v_mfma_f32_16x16x32_bf16 a[68:71], v[76:79], v[44:47], a[68:71]
v_accvgpr_read_b32 v33, a48
v_accvgpr_read_b32 v34, a49
v_accvgpr_read_b32 v35, a50
v_accvgpr_read_b32 v36, a51
v_mfma_f32_16x16x32_bf16 a[72:75], v[80:83], v[44:47], a[72:75]
v_cvt_pk_bf16_f32 v140, v33, v34
v_cvt_pk_bf16_f32 v141, v35, v36
v_accvgpr_read_b32 v121, a52
v_accvgpr_read_b32 v124, a53
v_mfma_f32_16x16x32_bf16 a[76:79], v[84:87], v[44:47], a[76:79]
v_accvgpr_read_b32 v125, a54
v_accvgpr_read_b32 v126, a55
v_cvt_pk_bf16_f32 v142, v121, v124
v_cvt_pk_bf16_f32 v143, v125, v126
s_waitcnt lgkmcnt(6)
v_mfma_f32_16x16x32_bf16 a[80:83], v[104:107], v[48:51], a[80:83]
ds_read_b64_tr_b16 v[64:65], v25 offset:0
ds_read_b64_tr_b16 v[66:67], v25 offset:2048
v_accvgpr_read_b32 v33, a56
v_accvgpr_read_b32 v34, a57
v_mfma_f32_16x16x32_bf16 a[84:87], v[108:111], v[48:51], a[84:87]
v_accvgpr_read_b32 v35, a58
v_accvgpr_read_b32 v36, a59
v_cvt_pk_bf16_f32 v172, v33, v34
v_mfma_f32_16x16x32_bf16 a[88:91], v[112:115], v[48:51], a[88:91]
v_cvt_pk_bf16_f32 v173, v35, v36
v_accvgpr_read_b32 v121, a60
v_accvgpr_read_b32 v124, a61
v_accvgpr_read_b32 v125, a62
v_mfma_f32_16x16x32_bf16 a[92:95], v[116:119], v[48:51], a[92:95]
v_accvgpr_read_b32 v126, a63
v_cvt_pk_bf16_f32 v174, v121, v124
v_cvt_pk_bf16_f32 v175, v125, v126
s_barrier
s_waitcnt lgkmcnt(6)
v_mfma_f32_16x16x32_bf16 a[80:83], v[72:75], v[52:55], a[80:83]
ds_read_b64_tr_b16 v[68:69], v17 offset:0
ds_read_b64_tr_b16 v[70:71], v17 offset:2048
v_accvgpr_read_b32 v33, a64
v_accvgpr_read_b32 v34, a65
v_mfma_f32_16x16x32_bf16 a[84:87], v[76:79], v[52:55], a[84:87]
v_accvgpr_read_b32 v35, a66
v_accvgpr_read_b32 v36, a67
v_cvt_pk_bf16_f32 v144, v33, v34
v_mfma_f32_16x16x32_bf16 a[88:91], v[80:83], v[52:55], a[88:91]
v_cvt_pk_bf16_f32 v145, v35, v36
v_accvgpr_read_b32 v121, a68
v_accvgpr_read_b32 v124, a69
v_accvgpr_read_b32 v125, a70
v_mfma_f32_16x16x32_bf16 a[92:95], v[84:87], v[52:55], a[92:95]
v_accvgpr_read_b32 v126, a71
v_cvt_pk_bf16_f32 v146, v121, v124
v_cvt_pk_bf16_f32 v147, v125, v126
s_waitcnt lgkmcnt(6)
v_mfma_f32_16x16x32_bf16 a[96:99], v[104:107], v[56:59], a[96:99]
ds_read_b64_tr_b16 v[40:41], v18 offset:256
ds_read_b64_tr_b16 v[42:43], v18 offset:2304
s_add_u32 s28, s71, 524288
s_add_u32 m0, s70, 32768
s_nop 0
buffer_load_dwordx4 v29, s[76:79], s28 offen lds
v_mfma_f32_16x16x32_bf16 a[100:103], v[108:111], v[56:59], a[100:103]
v_accvgpr_read_b32 v33, a72
v_accvgpr_read_b32 v34, a73
v_accvgpr_read_b32 v35, a74
v_accvgpr_read_b32 v36, a75
v_mfma_f32_16x16x32_bf16 a[104:107], v[112:115], v[56:59], a[104:107]
v_cvt_pk_bf16_f32 v176, v33, v34
v_cvt_pk_bf16_f32 v177, v35, v36
v_accvgpr_read_b32 v121, a76
v_accvgpr_read_b32 v124, a77
v_mfma_f32_16x16x32_bf16 a[108:111], v[116:119], v[56:59], a[108:111]
v_accvgpr_read_b32 v125, a78
v_accvgpr_read_b32 v126, a79
v_cvt_pk_bf16_f32 v178, v121, v124
v_cvt_pk_bf16_f32 v179, v125, v126
s_waitcnt lgkmcnt(6)
v_mfma_f32_16x16x32_bf16 a[96:99], v[72:75], v[60:63], a[96:99]
ds_read_b64_tr_b16 v[44:45], v10 offset:256
ds_read_b64_tr_b16 v[46:47], v10 offset:2304
s_add_u32 s28, s28, 31744
buffer_load_dwordx4 v29, s[76:79], s28 offen offset:1024 lds
v_mfma_f32_16x16x32_bf16 a[100:103], v[76:79], v[60:63], a[100:103]
v_accvgpr_read_b32 v33, a80
v_accvgpr_read_b32 v34, a81
v_accvgpr_read_b32 v35, a82
v_accvgpr_read_b32 v36, a83
v_mfma_f32_16x16x32_bf16 a[104:107], v[80:83], v[60:63], a[104:107]
v_cvt_pk_bf16_f32 v148, v33, v34
v_cvt_pk_bf16_f32 v149, v35, v36
v_accvgpr_read_b32 v121, a84
v_accvgpr_read_b32 v124, a85
v_mfma_f32_16x16x32_bf16 a[108:111], v[84:87], v[60:63], a[108:111]
v_accvgpr_read_b32 v125, a86
v_accvgpr_read_b32 v126, a87
v_cvt_pk_bf16_f32 v150, v121, v124
v_cvt_pk_bf16_f32 v151, v125, v126
s_waitcnt lgkmcnt(6)
v_mfma_f32_16x16x32_bf16 a[112:115], v[104:107], v[64:67], a[112:115]
ds_read_b64_tr_b16 v[48:49], v19 offset:256
ds_read_b64_tr_b16 v[50:51], v19 offset:2304
s_add_u32 s28, s73, 229376
buffer_load_dwordx4 v30, s[80:83], s28 offen offset:2048 lds
v_mfma_f32_16x16x32_bf16 a[116:119], v[108:111], v[64:67], a[116:119]
v_accvgpr_read_b32 v33, a88
v_accvgpr_read_b32 v34, a89
v_accvgpr_read_b32 v35, a90
v_accvgpr_read_b32 v36, a91
v_mfma_f32_16x16x32_bf16 a[120:123], v[112:115], v[64:67], a[120:123]
v_cvt_pk_bf16_f32 v180, v33, v34
v_cvt_pk_bf16_f32 v181, v35, v36
v_accvgpr_read_b32 v121, a92
v_accvgpr_read_b32 v124, a93
v_mfma_f32_16x16x32_bf16 a[124:127], v[116:119], v[64:67], a[124:127]
v_accvgpr_read_b32 v125, a94
v_accvgpr_read_b32 v126, a95
v_cvt_pk_bf16_f32 v182, v121, v124
v_cvt_pk_bf16_f32 v183, v125, v126
s_waitcnt lgkmcnt(6)
v_mfma_f32_16x16x32_bf16 a[112:115], v[72:75], v[68:71], a[112:115]
ds_read_b64_tr_b16 v[52:53], v11 offset:256
ds_read_b64_tr_b16 v[54:55], v11 offset:2304
buffer_load_dwordx4 v30, s[84:87], s28 offen offset:3072 lds
v_accvgpr_read_b32 v33, a96
v_mfma_f32_16x16x32_bf16 a[116:119], v[76:79], v[68:71], a[116:119]
v_accvgpr_read_b32 v34, a97
v_accvgpr_read_b32 v35, a98
v_accvgpr_read_b32 v36, a99
v_cvt_pk_bf16_f32 v152, v33, v34
v_mfma_f32_16x16x32_bf16 a[120:123], v[80:83], v[68:71], a[120:123]
v_cvt_pk_bf16_f32 v153, v35, v36
v_accvgpr_read_b32 v121, a100
v_accvgpr_read_b32 v124, a101
v_accvgpr_read_b32 v125, a102
v_mfma_f32_16x16x32_bf16 a[124:127], v[84:87], v[68:71], a[124:127]
v_accvgpr_read_b32 v126, a103
v_cvt_pk_bf16_f32 v154, v121, v124
v_cvt_pk_bf16_f32 v155, v125, v126
s_waitcnt lgkmcnt(6)
v_mfma_f32_16x16x32_bf16 a[128:131], v[104:107], v[40:43], a[128:131]
ds_read_b64_tr_b16 v[56:57], v20 offset:256
ds_read_b64_tr_b16 v[58:59], v20 offset:2304
v_accvgpr_read_b32 v33, a104
v_accvgpr_read_b32 v34, a105
v_mfma_f32_16x16x32_bf16 a[132:135], v[108:111], v[40:43], a[132:135]
v_accvgpr_read_b32 v35, a106
v_accvgpr_read_b32 v36, a107
v_cvt_pk_bf16_f32 v184, v33, v34
v_mfma_f32_16x16x32_bf16 a[136:139], v[112:115], v[40:43], a[136:139]
v_cvt_pk_bf16_f32 v185, v35, v36
v_accvgpr_read_b32 v121, a108
v_accvgpr_read_b32 v124, a109
v_accvgpr_read_b32 v125, a110
v_mfma_f32_16x16x32_bf16 a[140:143], v[116:119], v[40:43], a[140:143]
v_accvgpr_read_b32 v126, a111
v_cvt_pk_bf16_f32 v186, v121, v124
v_cvt_pk_bf16_f32 v187, v125, v126
s_waitcnt lgkmcnt(6)
v_mfma_f32_16x16x32_bf16 a[128:131], v[72:75], v[44:47], a[128:131]
ds_read_b64_tr_b16 v[60:61], v12 offset:256
ds_read_b64_tr_b16 v[62:63], v12 offset:2304
v_accvgpr_read_b32 v33, a112
v_accvgpr_read_b32 v34, a113
v_mfma_f32_16x16x32_bf16 a[132:135], v[76:79], v[44:47], a[132:135]
v_accvgpr_read_b32 v35, a114
v_accvgpr_read_b32 v36, a115
v_cvt_pk_bf16_f32 v156, v33, v34
v_mfma_f32_16x16x32_bf16 a[136:139], v[80:83], v[44:47], a[136:139]
v_cvt_pk_bf16_f32 v157, v35, v36
v_accvgpr_read_b32 v121, a116
v_accvgpr_read_b32 v124, a117
v_accvgpr_read_b32 v125, a118
v_mfma_f32_16x16x32_bf16 a[140:143], v[84:87], v[44:47], a[140:143]
v_accvgpr_read_b32 v126, a119
v_cvt_pk_bf16_f32 v158, v121, v124
v_cvt_pk_bf16_f32 v159, v125, v126
s_waitcnt lgkmcnt(6)
v_mfma_f32_16x16x32_bf16 a[144:147], v[104:107], v[48:51], a[144:147]
ds_read_b64_tr_b16 v[64:65], v21 offset:256
ds_read_b64_tr_b16 v[66:67], v21 offset:2304
s_mov_b32 s31, 0
buffer_store_dwordx4 v[128:131], v28, s[92:95], s31 offen offset:0 sc1
v_mfma_f32_16x16x32_bf16 a[148:151], v[108:111], v[48:51], a[148:151]
v_accvgpr_read_b32 v33, a120
v_accvgpr_read_b32 v34, a121
v_accvgpr_read_b32 v35, a122
v_accvgpr_read_b32 v36, a123
v_mfma_f32_16x16x32_bf16 a[152:155], v[112:115], v[48:51], a[152:155]
v_cvt_pk_bf16_f32 v188, v33, v34
)ASM"
R"ASM(v_cvt_pk_bf16_f32 v189, v35, v36
v_accvgpr_read_b32 v121, a124
v_accvgpr_read_b32 v124, a125
v_mfma_f32_16x16x32_bf16 a[156:159], v[116:119], v[48:51], a[156:159]
v_accvgpr_read_b32 v125, a126
v_accvgpr_read_b32 v126, a127
v_cvt_pk_bf16_f32 v190, v121, v124
v_cvt_pk_bf16_f32 v191, v125, v126
s_waitcnt lgkmcnt(6)
v_mfma_f32_16x16x32_bf16 a[144:147], v[72:75], v[52:55], a[144:147]
ds_read_b64_tr_b16 v[68:69], v13 offset:256
ds_read_b64_tr_b16 v[70:71], v13 offset:2304
buffer_store_dwordx4 v[132:135], v28, s[92:95], s31 offen offset:1024 sc1
v_accvgpr_read_b32 v33, a128
v_mfma_f32_16x16x32_bf16 a[148:151], v[76:79], v[52:55], a[148:151]
v_accvgpr_read_b32 v34, a129
v_accvgpr_read_b32 v35, a130
v_accvgpr_read_b32 v36, a131
v_cvt_pk_bf16_f32 v192, v33, v34
v_mfma_f32_16x16x32_bf16 a[152:155], v[80:83], v[52:55], a[152:155]
v_cvt_pk_bf16_f32 v193, v35, v36
v_accvgpr_read_b32 v121, a132
v_accvgpr_read_b32 v124, a133
v_accvgpr_read_b32 v125, a134
v_mfma_f32_16x16x32_bf16 a[156:159], v[84:87], v[52:55], a[156:159]
v_accvgpr_read_b32 v126, a135
v_cvt_pk_bf16_f32 v194, v121, v124
v_cvt_pk_bf16_f32 v195, v125, v126
s_waitcnt lgkmcnt(6)
v_mfma_f32_16x16x32_bf16 a[160:163], v[104:107], v[56:59], a[160:163]
ds_read_b64_tr_b16 v[40:41], v22 offset:256
ds_read_b64_tr_b16 v[42:43], v22 offset:2304
v_accvgpr_read_b32 v33, a136
v_accvgpr_read_b32 v34, a137
v_mfma_f32_16x16x32_bf16 a[164:167], v[108:111], v[56:59], a[164:167]
v_accvgpr_read_b32 v35, a138
v_accvgpr_read_b32 v36, a139
v_cvt_pk_bf16_f32 v224, v33, v34
v_mfma_f32_16x16x32_bf16 a[168:171], v[112:115], v[56:59], a[168:171]
v_cvt_pk_bf16_f32 v225, v35, v36
v_accvgpr_read_b32 v121, a140
v_accvgpr_read_b32 v124, a141
v_accvgpr_read_b32 v125, a142
v_mfma_f32_16x16x32_bf16 a[172:175], v[116:119], v[56:59], a[172:175]
v_accvgpr_read_b32 v126, a143
v_cvt_pk_bf16_f32 v226, v121, v124
v_cvt_pk_bf16_f32 v227, v125, v126
s_waitcnt lgkmcnt(6)
v_mfma_f32_16x16x32_bf16 a[160:163], v[72:75], v[60:63], a[160:163]
ds_read_b64_tr_b16 v[44:45], v14 offset:256
ds_read_b64_tr_b16 v[46:47], v14 offset:2304
buffer_store_dwordx4 v[136:139], v28, s[92:95], s31 offen offset:2048 sc1
v_accvgpr_read_b32 v33, a144
v_mfma_f32_16x16x32_bf16 a[164:167], v[76:79], v[60:63], a[164:167]
v_accvgpr_read_b32 v34, a145
v_accvgpr_read_b32 v35, a146
v_accvgpr_read_b32 v36, a147
v_cvt_pk_bf16_f32 v196, v33, v34
v_mfma_f32_16x16x32_bf16 a[168:171], v[80:83], v[60:63], a[168:171]
v_cvt_pk_bf16_f32 v197, v35, v36
v_accvgpr_read_b32 v121, a148
v_accvgpr_read_b32 v124, a149
v_accvgpr_read_b32 v125, a150
v_mfma_f32_16x16x32_bf16 a[172:175], v[84:87], v[60:63], a[172:175]
v_accvgpr_read_b32 v126, a151
v_cvt_pk_bf16_f32 v198, v121, v124
v_cvt_pk_bf16_f32 v199, v125, v126
s_waitcnt lgkmcnt(6)
v_mfma_f32_16x16x32_bf16 a[176:179], v[104:107], v[64:67], a[176:179]
ds_read_b64_tr_b16 v[48:49], v23 offset:256
ds_read_b64_tr_b16 v[50:51], v23 offset:2304
buffer_store_dwordx4 v[140:143], v28, s[92:95], s31 offen offset:3072 sc1
v_accvgpr_read_b32 v33, a152
v_mfma_f32_16x16x32_bf16 a[180:183], v[108:111], v[64:67], a[180:183]
v_accvgpr_read_b32 v34, a153
v_accvgpr_read_b32 v35, a154
v_accvgpr_read_b32 v36, a155
v_cvt_pk_bf16_f32 v228, v33, v34
v_mfma_f32_16x16x32_bf16 a[184:187], v[112:115], v[64:67], a[184:187]
v_cvt_pk_bf16_f32 v229, v35, v36
v_accvgpr_read_b32 v121, a156
v_accvgpr_read_b32 v124, a157
v_accvgpr_read_b32 v125, a158
v_mfma_f32_16x16x32_bf16 a[188:191], v[116:119], v[64:67], a[188:191]
v_accvgpr_read_b32 v126, a159
v_cvt_pk_bf16_f32 v230, v121, v124
v_cvt_pk_bf16_f32 v231, v125, v126
s_waitcnt lgkmcnt(6)
v_mfma_f32_16x16x32_bf16 a[176:179], v[72:75], v[68:71], a[176:179]
ds_read_b64_tr_b16 v[52:53], v15 offset:256
ds_read_b64_tr_b16 v[54:55], v15 offset:2304
v_accvgpr_read_b32 v33, a160
v_accvgpr_read_b32 v34, a161
v_mfma_f32_16x16x32_bf16 a[180:183], v[76:79], v[68:71], a[180:183]
v_accvgpr_read_b32 v35, a162
v_accvgpr_read_b32 v36, a163
v_cvt_pk_bf16_f32 v200, v33, v34
v_mfma_f32_16x16x32_bf16 a[184:187], v[80:83], v[68:71], a[184:187]
v_cvt_pk_bf16_f32 v201, v35, v36
v_accvgpr_read_b32 v121, a164
v_accvgpr_read_b32 v124, a165
v_accvgpr_read_b32 v125, a166
v_mfma_f32_16x16x32_bf16 a[188:191], v[84:87], v[68:71], a[188:191]
v_accvgpr_read_b32 v126, a167
v_cvt_pk_bf16_f32 v202, v121, v124
v_cvt_pk_bf16_f32 v203, v125, v126
s_waitcnt lgkmcnt(6)
v_mfma_f32_16x16x32_bf16 a[192:195], v[104:107], v[40:43], a[192:195]
ds_read_b64_tr_b16 v[56:57], v24 offset:256
ds_read_b64_tr_b16 v[58:59], v24 offset:2304
s_mov_b32 s31, 4096
buffer_store_dwordx4 v[144:147], v28, s[92:95], s31 offen offset:0 sc1
v_mfma_f32_16x16x32_bf16 a[196:199], v[108:111], v[40:43], a[196:199]
v_accvgpr_read_b32 v33, a168
v_accvgpr_read_b32 v34, a169
v_accvgpr_read_b32 v35, a170
v_accvgpr_read_b32 v36, a171
v_mfma_f32_16x16x32_bf16 a[200:203], v[112:115], v[40:43], a[200:203]
v_cvt_pk_bf16_f32 v232, v33, v34
v_cvt_pk_bf16_f32 v233, v35, v36
v_accvgpr_read_b32 v121, a172
v_accvgpr_read_b32 v124, a173
v_mfma_f32_16x16x32_bf16 a[204:207], v[116:119], v[40:43], a[204:207]
v_accvgpr_read_b32 v125, a174
v_accvgpr_read_b32 v126, a175
v_cvt_pk_bf16_f32 v234, v121, v124
v_cvt_pk_bf16_f32 v235, v125, v126
s_waitcnt lgkmcnt(6)
v_mfma_f32_16x16x32_bf16 a[192:195], v[72:75], v[44:47], a[192:195]
ds_read_b64_tr_b16 v[60:61], v16 offset:256
ds_read_b64_tr_b16 v[62:63], v16 offset:2304
v_accvgpr_read_b32 v33, a176
v_accvgpr_read_b32 v34, a177
v_mfma_f32_16x16x32_bf16 a[196:199], v[76:79], v[44:47], a[196:199]
v_accvgpr_read_b32 v35, a178
v_accvgpr_read_b32 v36, a179
v_cvt_pk_bf16_f32 v204, v33, v34
v_mfma_f32_16x16x32_bf16 a[200:203], v[80:83], v[44:47], a[200:203]
v_cvt_pk_bf16_f32 v205, v35, v36
v_accvgpr_read_b32 v121, a180
v_accvgpr_read_b32 v124, a181
v_accvgpr_read_b32 v125, a182
v_mfma_f32_16x16x32_bf16 a[204:207], v[84:87], v[44:47], a[204:207]
v_accvgpr_read_b32 v126, a183
v_cvt_pk_bf16_f32 v206, v121, v124
v_cvt_pk_bf16_f32 v207, v125, v126
s_waitcnt lgkmcnt(6)
v_mfma_f32_16x16x32_bf16 a[208:211], v[104:107], v[48:51], a[208:211]
ds_read_b64_tr_b16 v[64:65], v25 offset:256
ds_read_b64_tr_b16 v[66:67], v25 offset:2304
buffer_store_dwordx4 v[148:151], v28, s[92:95], s31 offen offset:1024 sc1
v_accvgpr_read_b32 v33, a184
v_mfma_f32_16x16x32_bf16 a[212:215], v[108:111], v[48:51], a[212:215]
v_accvgpr_read_b32 v34, a185
v_accvgpr_read_b32 v35, a186
v_accvgpr_read_b32 v36, a187
v_cvt_pk_bf16_f32 v236, v33, v34
v_mfma_f32_16x16x32_bf16 a[216:219], v[112:115], v[48:51], a[216:219]
v_cvt_pk_bf16_f32 v237, v35, v36
v_accvgpr_read_b32 v121, a188
v_accvgpr_read_b32 v124, a189
v_accvgpr_read_b32 v125, a190
v_mfma_f32_16x16x32_bf16 a[220:223], v[116:119], v[48:51], a[220:223]
v_accvgpr_read_b32 v126, a191
v_cvt_pk_bf16_f32 v238, v121, v124
v_cvt_pk_bf16_f32 v239, v125, v126
s_waitcnt lgkmcnt(6)
v_mfma_f32_16x16x32_bf16 a[208:211], v[72:75], v[52:55], a[208:211]
ds_read_b64_tr_b16 v[68:69], v17 offset:256
ds_read_b64_tr_b16 v[70:71], v17 offset:2304
buffer_store_dwordx4 v[152:155], v28, s[92:95], s31 offen offset:2048 sc1
v_accvgpr_read_b32 v33, a192
v_mfma_f32_16x16x32_bf16 a[212:215], v[76:79], v[52:55], a[212:215]
v_accvgpr_read_b32 v34, a193
v_accvgpr_read_b32 v35, a194
v_accvgpr_read_b32 v36, a195
v_cvt_pk_bf16_f32 v208, v33, v34
v_mfma_f32_16x16x32_bf16 a[216:219], v[80:83], v[52:55], a[216:219]
v_cvt_pk_bf16_f32 v209, v35, v36
v_accvgpr_read_b32 v121, a196
v_accvgpr_read_b32 v124, a197
v_accvgpr_read_b32 v125, a198
v_mfma_f32_16x16x32_bf16 a[220:223], v[84:87], v[52:55], a[220:223]
v_accvgpr_read_b32 v126, a199
v_cvt_pk_bf16_f32 v210, v121, v124
v_cvt_pk_bf16_f32 v211, v125, v126
s_waitcnt lgkmcnt(6)
v_mfma_f32_16x16x32_bf16 a[224:227], v[104:107], v[56:59], a[224:227]
v_accvgpr_read_b32 v33, a200
v_accvgpr_read_b32 v34, a201
v_accvgpr_read_b32 v35, a202
v_mfma_f32_16x16x32_bf16 a[228:231], v[108:111], v[56:59], a[228:231]
v_accvgpr_read_b32 v36, a203
v_cvt_pk_bf16_f32 v240, v33, v34
v_cvt_pk_bf16_f32 v241, v35, v36
v_mfma_f32_16x16x32_bf16 a[232:235], v[112:115], v[56:59], a[232:235]
v_accvgpr_read_b32 v121, a204
v_accvgpr_read_b32 v124, a205
v_accvgpr_read_b32 v125, a206
v_mfma_f32_16x16x32_bf16 a[236:239], v[116:119], v[56:59], a[236:239]
v_accvgpr_read_b32 v126, a207
v_cvt_pk_bf16_f32 v242, v121, v124
v_cvt_pk_bf16_f32 v243, v125, v126
s_waitcnt lgkmcnt(4)
v_mfma_f32_16x16x32_bf16 a[224:227], v[72:75], v[60:63], a[224:227]
buffer_store_dwordx4 v[156:159], v28, s[92:95], s31 offen offset:3072 sc1
v_accvgpr_read_b32 v33, a208
v_accvgpr_read_b32 v34, a209
v_accvgpr_read_b32 v35, a210
v_mfma_f32_16x16x32_bf16 a[228:231], v[76:79], v[60:63], a[228:231]
v_accvgpr_read_b32 v36, a211
v_cvt_pk_bf16_f32 v212, v33, v34
v_cvt_pk_bf16_f32 v213, v35, v36
v_mfma_f32_16x16x32_bf16 a[232:235], v[80:83], v[60:63], a[232:235]
v_accvgpr_read_b32 v121, a212
v_accvgpr_read_b32 v124, a213
v_accvgpr_read_b32 v125, a214
v_mfma_f32_16x16x32_bf16 a[236:239], v[84:87], v[60:63], a[236:239]
v_accvgpr_read_b32 v126, a215
v_cvt_pk_bf16_f32 v214, v121, v124
v_cvt_pk_bf16_f32 v215, v125, v126
s_waitcnt lgkmcnt(2)
v_mfma_f32_16x16x32_bf16 a[240:243], v[104:107], v[64:67], a[240:243]
v_accvgpr_read_b32 v33, a216
v_accvgpr_read_b32 v34, a217
v_accvgpr_read_b32 v35, a218
v_mfma_f32_16x16x32_bf16 a[244:247], v[108:111], v[64:67], a[244:247]
v_accvgpr_read_b32 v36, a219
v_cvt_pk_bf16_f32 v244, v33, v34
v_cvt_pk_bf16_f32 v245, v35, v36
v_mfma_f32_16x16x32_bf16 a[248:251], v[112:115], v[64:67], a[248:251]
v_accvgpr_read_b32 v121, a220
v_accvgpr_read_b32 v124, a221
v_accvgpr_read_b32 v125, a222
v_mfma_f32_16x16x32_bf16 a[252:255], v[116:119], v[64:67], a[252:255]
v_accvgpr_read_b32 v126, a223
v_cvt_pk_bf16_f32 v246, v121, v124
v_cvt_pk_bf16_f32 v247, v125, v126
s_waitcnt lgkmcnt(0)
v_mfma_f32_16x16x32_bf16 a[240:243], v[72:75], v[68:71], a[240:243]
v_accvgpr_read_b32 v33, a224
v_accvgpr_read_b32 v34, a225
v_accvgpr_read_b32 v35, a226
v_mfma_f32_16x16x32_bf16 a[244:247], v[76:79], v[68:71], a[244:247]
v_accvgpr_read_b32 v36, a227
v_cvt_pk_bf16_f32 v216, v33, v34
v_cvt_pk_bf16_f32 v217, v35, v36
v_mfma_f32_16x16x32_bf16 a[248:251], v[80:83], v[68:71], a[248:251]
v_accvgpr_read_b32 v121, a228
v_accvgpr_read_b32 v124, a229
v_accvgpr_read_b32 v125, a230
v_mfma_f32_16x16x32_bf16 a[252:255], v[84:87], v[68:71], a[252:255]
v_accvgpr_read_b32 v126, a231
v_cvt_pk_bf16_f32 v218, v121, v124
v_cvt_pk_bf16_f32 v219, v125, v126
s_nop 3
v_accvgpr_read_b32 v33, a232
v_accvgpr_read_b32 v34, a233
v_accvgpr_read_b32 v35, a234
v_accvgpr_read_b32 v36, a235
v_cvt_pk_bf16_f32 v248, v33, v34
v_cvt_pk_bf16_f32 v249, v35, v36
v_accvgpr_read_b32 v121, a236
v_accvgpr_read_b32 v124, a237
v_accvgpr_read_b32 v125, a238
v_accvgpr_read_b32 v126, a239
v_cvt_pk_bf16_f32 v250, v121, v124
v_cvt_pk_bf16_f32 v251, v125, v126
v_accvgpr_read_b32 v33, a240
v_accvgpr_read_b32 v34, a241
v_accvgpr_read_b32 v35, a242
v_accvgpr_read_b32 v36, a243
v_cvt_pk_bf16_f32 v220, v33, v34
v_cvt_pk_bf16_f32 v221, v35, v36
v_accvgpr_read_b32 v121, a244
v_accvgpr_read_b32 v124, a245
v_accvgpr_read_b32 v125, a246
v_accvgpr_read_b32 v126, a247
v_cvt_pk_bf16_f32 v222, v121, v124
v_cvt_pk_bf16_f32 v223, v125, v126
v_accvgpr_read_b32 v33, a248
v_accvgpr_read_b32 v34, a249
v_accvgpr_read_b32 v35, a250
v_accvgpr_read_b32 v36, a251
v_cvt_pk_bf16_f32 v252, v33, v34
v_cvt_pk_bf16_f32 v253, v35, v36
v_accvgpr_read_b32 v121, a252
v_accvgpr_read_b32 v124, a253
v_accvgpr_read_b32 v125, a254
v_accvgpr_read_b32 v126, a255
v_cvt_pk_bf16_f32 v254, v121, v124
v_cvt_pk_bf16_f32 v255, v125, v126
v_xor_b32 v10, 0, v31
v_xor_b32 v11, 32, v31
v_xor_b32 v12, 64, v31
v_xor_b32 v13, 96, v31
v_xor_b32 v14, 128, v31
v_xor_b32 v15, 160, v31
v_xor_b32 v16, 192, v31
v_xor_b32 v17, 224, v31
v_xor_b32 v18, 0, v32
v_xor_b32 v19, 32, v32
v_xor_b32 v20, 64, v32
v_xor_b32 v21, 96, v32
buffer_load_dwordx4 v[72:75], v28, s[52:55], s8 offen offset:1024 sc1
buffer_load_dwordx4 v[76:79], v28, s[52:55], s9 offen offset:1024 sc1
buffer_load_dwordx4 v[80:83], v28, s[52:55], s10 offen offset:1024 sc1
buffer_load_dwordx4 v[84:87], v28, s[52:55], s11 offen offset:1024 sc1
buffer_load_dwordx4 v[104:107], v28, s[52:55], s8 offen offset:2048 sc1
buffer_load_dwordx4 v[108:111], v28, s[52:55], s9 offen offset:2048 sc1
buffer_load_dwordx4 v[112:115], v28, s[52:55], s10 offen offset:2048 sc1
buffer_load_dwordx4 v[116:119], v28, s[52:55], s11 offen offset:2048 sc1
L_A_END:
s_cmp_eq_u32 s74, 1
s_cbranch_scc1 L_QONLY
s_waitcnt vmcnt(28)
s_barrier
ds_read_b64_tr_b16 v[40:41], v10 offset:0
ds_read_b64_tr_b16 v[42:43], v10 offset:1024
ds_read_b64_tr_b16 v[44:45], v11 offset:0
ds_read_b64_tr_b16 v[46:47], v11 offset:1024
ds_read_b64_tr_b16 v[48:49], v12 offset:0
ds_read_b64_tr_b16 v[50:51], v12 offset:1024
ds_read_b64_tr_b16 v[52:53], v13 offset:0
ds_read_b64_tr_b16 v[54:55], v13 offset:1024
s_waitcnt vmcnt(23) lgkmcnt(6)
v_mfma_f32_16x16x32_bf16 a[128:131], v[40:43], v[88:91], 0
ds_read_b64_tr_b16 v[56:57], v14 offset:0
ds_read_b64_tr_b16 v[58:59], v14 offset:1024
s_waitcnt vmcnt(22)
v_mfma_f32_16x16x32_bf16 a[132:135], v[40:43], v[92:95], 0
buffer_load_dwordx4 v[64:67], v28, s[52:55], s8 offen offset:3072 sc1
s_mov_b32 s31, 8192
s_waitcnt vmcnt(22)
v_mfma_f32_16x16x32_bf16 a[136:139], v[40:43], v[96:99], 0
buffer_store_dwordx4 v[160:163], v28, s[92:95], s31 offen offset:0 sc1
s_lshl_b32 s35, s44, 1
s_waitcnt vmcnt(22)
v_mfma_f32_16x16x32_bf16 a[140:143], v[40:43], v[100:103], 0
s_add_u32 s35, s35, 2
s_waitcnt lgkmcnt(6)
v_mfma_f32_16x16x32_bf16 a[144:147], v[44:47], v[88:91], 0
ds_read_b64_tr_b16 v[60:61], v15 offset:0
ds_read_b64_tr_b16 v[62:63], v15 offset:1024
v_mfma_f32_16x16x32_bf16 a[148:151], v[44:47], v[92:95], 0
buffer_load_dwordx4 v[68:71], v28, s[52:55], s9 offen offset:3072 sc1
v_mfma_f32_16x16x32_bf16 a[152:155], v[44:47], v[96:99], 0
s_cmp_lt_u32 s7, 2
v_mfma_f32_16x16x32_bf16 a[156:159], v[44:47], v[100:103], 0
s_cselect_b32 s36, 1, 0
s_waitcnt lgkmcnt(6)
v_mfma_f32_16x16x32_bf16 a[160:163], v[48:51], v[88:91], 0
ds_read_b64_tr_b16 v[40:41], v16 offset:0
v_mfma_f32_16x16x32_bf16 a[164:167], v[48:51], v[92:95], 0
ds_read_b64_tr_b16 v[42:43], v16 offset:1024
v_mfma_f32_16x16x32_bf16 a[168:171], v[48:51], v[96:99], 0
buffer_load_dwordx4 v[120:123], v28, s[52:55], s10 offen offset:3072 sc1
v_mfma_f32_16x16x32_bf16 a[172:175], v[48:51], v[100:103], 0
v_lshlrev_b32 v2, 4, v1
s_waitcnt lgkmcnt(6)
v_mfma_f32_16x16x32_bf16 a[176:179], v[52:55], v[88:91], 0
ds_read_b64_tr_b16 v[44:45], v17 offset:0
v_mfma_f32_16x16x32_bf16 a[180:183], v[52:55], v[92:95], 0
ds_read_b64_tr_b16 v[46:47], v17 offset:1024
v_mfma_f32_16x16x32_bf16 a[184:187], v[52:55], v[96:99], 0
buffer_load_dwordx4 v[124:127], v28, s[52:55], s11 offen offset:3072 sc1
v_mfma_f32_16x16x32_bf16 a[188:191], v[52:55], v[100:103], 0
v_add_u32 v9, 8192, v2
s_waitcnt lgkmcnt(6)
v_mfma_f32_16x16x32_bf16 a[192:195], v[56:59], v[88:91], 0
ds_read_b64_tr_b16 v[48:49], v18 offset:0
v_mfma_f32_16x16x32_bf16 a[196:199], v[56:59], v[92:95], 0
ds_read_b64_tr_b16 v[50:51], v18 offset:512
v_mfma_f32_16x16x32_bf16 a[200:203], v[56:59], v[96:99], 0
v_lshrrev_b32 v22, 4, v1
v_mfma_f32_16x16x32_bf16 a[204:207], v[56:59], v[100:103], 0
s_waitcnt lgkmcnt(6)
v_mfma_f32_16x16x32_bf16 a[208:211], v[60:63], v[88:91], 0
ds_read_b64_tr_b16 v[52:53], v19 offset:0
v_mfma_f32_16x16x32_bf16 a[212:215], v[60:63], v[92:95], 0
ds_read_b64_tr_b16 v[54:55], v19 offset:512
v_mfma_f32_16x16x32_bf16 a[216:219], v[60:63], v[96:99], 0
v_lshlrev_b32 v22, 2, v22
v_mfma_f32_16x16x32_bf16 a[220:223], v[60:63], v[100:103], 0
s_waitcnt lgkmcnt(6)
v_mfma_f32_16x16x32_bf16 a[224:227], v[40:43], v[88:91], 0
ds_read_b64_tr_b16 v[56:57], v20 offset:0
v_mfma_f32_16x16x32_bf16 a[228:231], v[40:43], v[92:95], 0
ds_read_b64_tr_b16 v[58:59], v20 offset:512
v_mfma_f32_16x16x32_bf16 a[232:235], v[40:43], v[96:99], 0
buffer_store_dwordx4 v[164:167], v28, s[92:95], s31 offen offset:1024 sc1
v_mfma_f32_16x16x32_bf16 a[236:239], v[40:43], v[100:103], 0
v_and_b32 v23, 15, v1
s_waitcnt lgkmcnt(6)
v_mfma_f32_16x16x32_bf16 a[240:243], v[44:47], v[88:91], 0
ds_read_b64_tr_b16 v[60:61], v21 offset:0
v_mfma_f32_16x16x32_bf16 a[244:247], v[44:47], v[92:95], 0
ds_read_b64_tr_b16 v[62:63], v21 offset:512
v_mfma_f32_16x16x32_bf16 a[248:251], v[44:47], v[96:99], 0
v_sub_u32 v6, v22, v23
v_mfma_f32_16x16x32_bf16 a[252:255], v[44:47], v[100:103], 0
s_waitcnt lgkmcnt(6)
v_mfma_f32_16x16x32_bf16 a[0:3], v[48:51], v[88:91], 0
ds_read_b64_tr_b16 v[40:41], v18 offset:1024
v_mfma_f32_16x16x32_bf16 a[4:7], v[48:51], v[92:95], 0
ds_read_b64_tr_b16 v[42:43], v18 offset:1536
v_mfma_f32_16x16x32_bf16 a[8:11], v[48:51], v[96:99], 0
v_mov_b32 v7, 0xff800000
v_mfma_f32_16x16x32_bf16 a[12:15], v[48:51], v[100:103], 0
s_waitcnt lgkmcnt(6)
v_mfma_f32_16x16x32_bf16 a[16:19], v[52:55], v[88:91], 0
ds_read_b64_tr_b16 v[44:45], v19 offset:1024
v_mfma_f32_16x16x32_bf16 a[20:23], v[52:55], v[92:95], 0
ds_read_b64_tr_b16 v[46:47], v19 offset:1536
v_mfma_f32_16x16x32_bf16 a[24:27], v[52:55], v[96:99], 0
s_lshr_b32 s24, s7, 1
v_mfma_f32_16x16x32_bf16 a[28:31], v[52:55], v[100:103], 0
s_waitcnt lgkmcnt(6)
v_mfma_f32_16x16x32_bf16 a[32:35], v[56:59], v[88:91], 0
ds_read_b64_tr_b16 v[48:49], v20 offset:1024
v_mfma_f32_16x16x32_bf16 a[36:39], v[56:59], v[92:95], 0
ds_read_b64_tr_b16 v[50:51], v20 offset:1536
v_mfma_f32_16x16x32_bf16 a[40:43], v[56:59], v[96:99], 0
s_lshl_b32 s24, s24, 16
v_mfma_f32_16x16x32_bf16 a[44:47], v[56:59], v[100:103], 0
s_waitcnt lgkmcnt(6)
v_mfma_f32_16x16x32_bf16 a[48:51], v[60:63], v[88:91], 0
ds_read_b64_tr_b16 v[52:53], v21 offset:1024
v_mfma_f32_16x16x32_bf16 a[52:55], v[60:63], v[92:95], 0
ds_read_b64_tr_b16 v[54:55], v21 offset:1536
v_mfma_f32_16x16x32_bf16 a[56:59], v[60:63], v[96:99], 0
v_add_u32 v3, s24, v2
v_mfma_f32_16x16x32_bf16 a[60:63], v[60:63], v[100:103], 0
s_barrier
s_waitcnt lgkmcnt(6)
v_mfma_f32_16x16x32_bf16 a[64:67], v[40:43], v[88:91], 0
ds_read_b64_tr_b16 v[56:57], v10 offset:16384
ds_read_b64_tr_b16 v[58:59], v10 offset:17408
v_mfma_f32_16x16x32_bf16 a[68:71], v[40:43], v[92:95], 0
s_add_u32 s28, s71, 786432
s_add_u32 m0, s70, 49152
v_mfma_f32_16x16x32_bf16 a[72:75], v[40:43], v[96:99], 0
buffer_load_dwordx4 v29, s[76:79], s28 offen lds
s_add_u32 s28, s28, 31744
v_mfma_f32_16x16x32_bf16 a[76:79], v[40:43], v[100:103], 0
buffer_load_dwordx4 v29, s[76:79], s28 offen offset:1024 lds
v_add_u32 v4, 49152, v3
s_waitcnt lgkmcnt(6)
v_mfma_f32_16x16x32_bf16 a[80:83], v[44:47], v[88:91], 0
ds_read_b64_tr_b16 v[60:61], v11 offset:16384
ds_read_b64_tr_b16 v[62:63], v11 offset:17408
v_mfma_f32_16x16x32_bf16 a[84:87], v[44:47], v[92:95], 0
s_add_u32 s28, s73, 344064
v_mfma_f32_16x16x32_bf16 a[88:91], v[44:47], v[96:99], 0
buffer_load_dwordx4 v30, s[80:83], s28 offen offset:2048 lds
buffer_store_dwordx4 v[168:171], v28, s[92:95], s31 offen offset:2048 sc1
v_mfma_f32_16x16x32_bf16 a[92:95], v[44:47], v[100:103], 0
s_lshl_b32 s25, s75, 12
s_waitcnt lgkmcnt(6)
v_mfma_f32_16x16x32_bf16 a[96:99], v[48:51], v[88:91], 0
ds_read_b64_tr_b16 v[40:41], v12 offset:16384
v_mfma_f32_16x16x32_bf16 a[100:103], v[48:51], v[92:95], 0
ds_read_b64_tr_b16 v[42:43], v12 offset:17408
v_mfma_f32_16x16x32_bf16 a[104:107], v[48:51], v[96:99], 0
buffer_load_dwordx4 v30, s[84:87], s28 offen offset:3072 lds
v_mfma_f32_16x16x32_bf16 a[108:111], v[48:51], v[100:103], 0
s_lshl_b32 s27, s44, 1
s_waitcnt lgkmcnt(6)
v_mfma_f32_16x16x32_bf16 a[112:115], v[52:55], v[88:91], 0
ds_read_b64_tr_b16 v[44:45], v13 offset:16384
v_mfma_f32_16x16x32_bf16 a[116:119], v[52:55], v[92:95], 0
ds_read_b64_tr_b16 v[46:47], v13 offset:17408
v_mfma_f32_16x16x32_bf16 a[120:123], v[52:55], v[96:99], 0
s_sub_u32 s22, 13, s27
v_mfma_f32_16x16x32_bf16 a[124:127], v[52:55], v[100:103], 0
s_waitcnt vmcnt(18) lgkmcnt(6)
v_mfma_f32_16x16x32_bf16 a[128:131], v[56:59], v[72:75], a[128:131]
ds_read_b64_tr_b16 v[48:49], v14 offset:16384
ds_read_b64_tr_b16 v[50:51], v14 offset:17408
s_waitcnt vmcnt(17)
v_mfma_f32_16x16x32_bf16 a[132:135], v[56:59], v[76:79], a[132:135]
s_mov_b32 s8, 4096
buffer_load_dwordx4 v[88:91], v28, s[52:55], s8 offen offset:0 sc1
s_waitcnt vmcnt(17)
v_mfma_f32_16x16x32_bf16 a[136:139], v[56:59], v[80:83], a[136:139]
s_mov_b32 s9, 36864
buffer_load_dwordx4 v[92:95], v28, s[52:55], s9 offen offset:0 sc1
s_waitcnt vmcnt(17)
v_mfma_f32_16x16x32_bf16 a[140:143], v[56:59], v[84:87], a[140:143]
s_cmp_lt_u32 s7, 2
s_cselect_b32 s22, s22, -2
s_waitcnt lgkmcnt(6)
v_mfma_f32_16x16x32_bf16 a[144:147], v[60:63], v[72:75], a[144:147]
ds_read_b64_tr_b16 v[52:53], v15 offset:16384
ds_read_b64_tr_b16 v[54:55], v15 offset:17408
v_mfma_f32_16x16x32_bf16 a[148:151], v[60:63], v[76:79], a[148:151]
s_mov_b32 s10, 69632
v_mfma_f32_16x16x32_bf16 a[152:155], v[60:63], v[80:83], a[152:155]
buffer_load_dwordx4 v[96:99], v28, s[52:55], s10 offen offset:0 sc1
v_mfma_f32_16x16x32_bf16 a[156:159], v[60:63], v[84:87], a[156:159]
s_lshl_b32 s23, s44, 2
s_waitcnt lgkmcnt(6)
v_mfma_f32_16x16x32_bf16 a[160:163], v[40:43], v[72:75], a[160:163]
ds_read_b64_tr_b16 v[56:57], v16 offset:16384
ds_read_b64_tr_b16 v[58:59], v16 offset:17408
v_mfma_f32_16x16x32_bf16 a[164:167], v[40:43], v[76:79], a[164:167]
s_mov_b32 s11, 102400
v_mfma_f32_16x16x32_bf16 a[168:171], v[40:43], v[80:83], a[168:171]
buffer_load_dwordx4 v[100:103], v28, s[52:55], s11 offen offset:0 sc1
v_mfma_f32_16x16x32_bf16 a[172:175], v[40:43], v[84:87], a[172:175]
s_sub_u32 s23, 30, s23
s_waitcnt lgkmcnt(6)
v_mfma_f32_16x16x32_bf16 a[176:179], v[44:47], v[72:75], a[176:179]
ds_read_b64_tr_b16 v[60:61], v17 offset:16384
v_mfma_f32_16x16x32_bf16 a[180:183], v[44:47], v[76:79], a[180:183]
ds_read_b64_tr_b16 v[62:63], v17 offset:17408
v_mfma_f32_16x16x32_bf16 a[184:187], v[44:47], v[80:83], a[184:187]
s_cmp_lt_u32 s7, 2
v_mfma_f32_16x16x32_bf16 a[188:191], v[44:47], v[84:87], a[188:191]
s_cselect_b32 s23, 0, s23
s_waitcnt lgkmcnt(6)
v_mfma_f32_16x16x32_bf16 a[192:195], v[48:51], v[72:75], a[192:195]
ds_read_b64_tr_b16 v[40:41], v18 offset:16384
v_mfma_f32_16x16x32_bf16 a[196:199], v[48:51], v[76:79], a[196:199]
ds_read_b64_tr_b16 v[42:43], v18 offset:16896
v_mfma_f32_16x16x32_bf16 a[200:203], v[48:51], v[80:83], a[200:203]
buffer_store_dwordx4 v[172:175], v28, s[92:95], s31 offen offset:3072 sc1
v_mfma_f32_16x16x32_bf16 a[204:207], v[48:51], v[84:87], a[204:207]
v_mov_b32 v22, s23
s_waitcnt lgkmcnt(6)
v_mfma_f32_16x16x32_bf16 a[208:211], v[52:55], v[72:75], a[208:211]
ds_read_b64_tr_b16 v[44:45], v19 offset:16384
v_mfma_f32_16x16x32_bf16 a[212:215], v[52:55], v[76:79], a[212:215]
ds_read_b64_tr_b16 v[46:47], v19 offset:16896
v_mfma_f32_16x16x32_bf16 a[216:219], v[52:55], v[80:83], a[216:219]
v_cmp_le_u32 vcc, s27, v1
v_mfma_f32_16x16x32_bf16 a[220:223], v[52:55], v[84:87], a[220:223]
s_nop 0
v_cndmask_b32 v22, 0, v22
s_waitcnt lgkmcnt(6)
v_mfma_f32_16x16x32_bf16 a[224:227], v[56:59], v[72:75], a[224:227]
ds_read_b64_tr_b16 v[48:49], v20 offset:16384
v_mfma_f32_16x16x32_bf16 a[228:231], v[56:59], v[76:79], a[228:231]
ds_read_b64_tr_b16 v[50:51], v20 offset:16896
v_mfma_f32_16x16x32_bf16 a[232:235], v[56:59], v[80:83], a[232:235]
v_mov_b32 v23, s22
v_mfma_f32_16x16x32_bf16 a[236:239], v[56:59], v[84:87], a[236:239]
s_waitcnt lgkmcnt(6)
v_mfma_f32_16x16x32_bf16 a[240:243], v[60:63], v[72:75], a[240:243]
ds_read_b64_tr_b16 v[52:53], v21 offset:16384
v_mfma_f32_16x16x32_bf16 a[244:247], v[60:63], v[76:79], a[244:247]
ds_read_b64_tr_b16 v[54:55], v21 offset:16896
v_mfma_f32_16x16x32_bf16 a[248:251], v[60:63], v[80:83], a[248:251]
v_cmp_le_u32 vcc, s35, v1
v_mfma_f32_16x16x32_bf16 a[252:255], v[60:63], v[84:87], a[252:255]
s_nop 0
v_cndmask_b32 v22, v22, v23
s_waitcnt lgkmcnt(6)
v_mfma_f32_16x16x32_bf16 a[0:3], v[40:43], v[72:75], a[0:3]
ds_read_b64_tr_b16 v[56:57], v18 offset:17408
v_mfma_f32_16x16x32_bf16 a[4:7], v[40:43], v[76:79], a[4:7]
ds_read_b64_tr_b16 v[58:59], v18 offset:17920
v_mfma_f32_16x16x32_bf16 a[8:11], v[40:43], v[80:83], a[8:11]
v_add_u32 v22, v22, v1
v_mfma_f32_16x16x32_bf16 a[12:15], v[40:43], v[84:87], a[12:15]
s_waitcnt lgkmcnt(6)
v_mfma_f32_16x16x32_bf16 a[16:19], v[44:47], v[72:75], a[16:19]
ds_read_b64_tr_b16 v[60:61], v19 offset:17408
v_mfma_f32_16x16x32_bf16 a[20:23], v[44:47], v[76:79], a[20:23]
ds_read_b64_tr_b16 v[62:63], v19 offset:17920
v_mfma_f32_16x16x32_bf16 a[24:27], v[44:47], v[80:83], a[24:27]
v_lshlrev_b32 v22, 14, v22
v_mfma_f32_16x16x32_bf16 a[28:31], v[44:47], v[84:87], a[28:31]
s_waitcnt lgkmcnt(6)
v_mfma_f32_16x16x32_bf16 a[32:35], v[48:51], v[72:75], a[32:35]
ds_read_b64_tr_b16 v[40:41], v20 offset:17408
ds_read_b64_tr_b16 v[42:43], v20 offset:17920
v_mfma_f32_16x16x32_bf16 a[36:39], v[48:51], v[76:79], a[36:39]
s_mov_b32 s31, 12288
v_mfma_f32_16x16x32_bf16 a[40:43], v[48:51], v[80:83], a[40:43]
buffer_store_dwordx4 v[176:179], v28, s[92:95], s31 offen offset:0 sc1
v_mfma_f32_16x16x32_bf16 a[44:47], v[48:51], v[84:87], a[44:47]
v_add_u32 v5, s25, v22
s_waitcnt lgkmcnt(6)
v_mfma_f32_16x16x32_bf16 a[48:51], v[52:55], v[72:75], a[48:51]
ds_read_b64_tr_b16 v[44:45], v21 offset:17408
v_mfma_f32_16x16x32_bf16 a[52:55], v[52:55], v[76:79], a[52:55]
ds_read_b64_tr_b16 v[46:47], v21 offset:17920
v_mfma_f32_16x16x32_bf16 a[56:59], v[52:55], v[80:83], a[56:59]
v_mov_b32 v22, 1073741824
v_mfma_f32_16x16x32_bf16 a[60:63], v[52:55], v[84:87], a[60:63]
s_barrier
s_waitcnt lgkmcnt(6)
v_mfma_f32_16x16x32_bf16 a[64:67], v[56:59], v[72:75], a[64:67]
ds_read_b64_tr_b16 v[48:49], v10 offset:32768
ds_read_b64_tr_b16 v[50:51], v10 offset:33792
s_add_u32 s28, s71, 1048576
v_mfma_f32_16x16x32_bf16 a[68:71], v[56:59], v[76:79], a[68:71]
s_add_u32 m0, s70, 0
s_nop 0
buffer_load_dwordx4 v29, s[76:79], s28 offen lds
v_mfma_f32_16x16x32_bf16 a[72:75], v[56:59], v[80:83], a[72:75]
s_add_u32 s28, s28, 31744
buffer_load_dwordx4 v29, s[76:79], s28 offen offset:1024 lds
v_mfma_f32_16x16x32_bf16 a[76:79], v[56:59], v[84:87], a[76:79]
v_cmp_lt_u32 vcc, 16, v1
s_nop 1
v_cndmask_b32 v5, v5, v22
s_waitcnt lgkmcnt(6)
v_mfma_f32_16x16x32_bf16 a[80:83], v[60:63], v[72:75], a[80:83]
ds_read_b64_tr_b16 v[52:53], v11 offset:32768
ds_read_b64_tr_b16 v[54:55], v11 offset:33792
v_mfma_f32_16x16x32_bf16 a[84:87], v[60:63], v[76:79], a[84:87]
s_add_u32 s28, s73, 458752
v_mfma_f32_16x16x32_bf16 a[88:91], v[60:63], v[80:83], a[88:91]
buffer_load_dwordx4 v30, s[80:83], s28 offen offset:2048 lds
v_mfma_f32_16x16x32_bf16 a[92:95], v[60:63], v[84:87], a[92:95]
s_lshl_b32 s22, 1, s35
s_waitcnt lgkmcnt(6)
v_mfma_f32_16x16x32_bf16 a[96:99], v[40:43], v[72:75], a[96:99]
ds_read_b64_tr_b16 v[56:57], v12 offset:32768
v_mfma_f32_16x16x32_bf16 a[100:103], v[40:43], v[76:79], a[100:103]
ds_read_b64_tr_b16 v[58:59], v12 offset:33792
v_mfma_f32_16x16x32_bf16 a[104:107], v[40:43], v[80:83], a[104:107]
buffer_load_dwordx4 v30, s[84:87], s28 offen offset:3072 lds
v_mfma_f32_16x16x32_bf16 a[108:111], v[40:43], v[84:87], a[108:111]
s_lshr_b32 s23, s22, 3
s_waitcnt lgkmcnt(6)
v_mfma_f32_16x16x32_bf16 a[112:115], v[44:47], v[72:75], a[112:115]
ds_read_b64_tr_b16 v[60:61], v13 offset:32768
v_mfma_f32_16x16x32_bf16 a[116:119], v[44:47], v[76:79], a[116:119]
ds_read_b64_tr_b16 v[62:63], v13 offset:33792
v_mfma_f32_16x16x32_bf16 a[120:123], v[44:47], v[80:83], a[120:123]
s_cmp_lt_u32 s7, 2
v_mfma_f32_16x16x32_bf16 a[124:127], v[44:47], v[84:87], a[124:127]
s_cselect_b32 s22, s22, 0
s_waitcnt vmcnt(24) lgkmcnt(6)
v_mfma_f32_16x16x32_bf16 a[128:131], v[48:51], v[104:107], a[128:131]
ds_read_b64_tr_b16 v[40:41], v14 offset:32768
ds_read_b64_tr_b16 v[42:43], v14 offset:33792
s_waitcnt vmcnt(23)
v_mfma_f32_16x16x32_bf16 a[132:135], v[48:51], v[108:111], a[132:135]
buffer_load_dwordx4 v[72:75], v28, s[52:55], s8 offen offset:1024 sc1
s_waitcnt vmcnt(23)
v_mfma_f32_16x16x32_bf16 a[136:139], v[48:51], v[112:115], a[136:139]
buffer_load_dwordx4 v[76:79], v28, s[52:55], s9 offen offset:1024 sc1
s_waitcnt vmcnt(23)
v_mfma_f32_16x16x32_bf16 a[140:143], v[48:51], v[116:119], a[140:143]
s_lshl_b32 s22, 1, s35
s_waitcnt lgkmcnt(6)
v_mfma_f32_16x16x32_bf16 a[144:147], v[52:55], v[104:107], a[144:147]
ds_read_b64_tr_b16 v[44:45], v15 offset:32768
ds_read_b64_tr_b16 v[46:47], v15 offset:33792
v_mfma_f32_16x16x32_bf16 a[148:151], v[52:55], v[108:111], a[148:151]
buffer_load_dwordx4 v[80:83], v28, s[52:55], s10 offen offset:1024 sc1
v_mfma_f32_16x16x32_bf16 a[152:155], v[52:55], v[112:115], a[152:155]
buffer_store_dwordx4 v[180:183], v28, s[92:95], s31 offen offset:1024 sc1
v_mfma_f32_16x16x32_bf16 a[156:159], v[52:55], v[116:119], a[156:159]
s_or_b32 s51, s22, 65536
s_waitcnt lgkmcnt(6)
v_mfma_f32_16x16x32_bf16 a[160:163], v[56:59], v[104:107], a[160:163]
ds_read_b64_tr_b16 v[48:49], v16 offset:32768
v_mfma_f32_16x16x32_bf16 a[164:167], v[56:59], v[108:111], a[164:167]
ds_read_b64_tr_b16 v[50:51], v16 offset:33792
v_mfma_f32_16x16x32_bf16 a[168:171], v[56:59], v[112:115], a[168:171]
buffer_load_dwordx4 v[84:87], v28, s[52:55], s11 offen offset:1024 sc1
v_mfma_f32_16x16x32_bf16 a[172:175], v[56:59], v[116:119], a[172:175]
s_or_b32 s60, s51, s23
s_waitcnt lgkmcnt(6)
v_mfma_f32_16x16x32_bf16 a[176:179], v[60:63], v[104:107], a[176:179]
ds_read_b64_tr_b16 v[52:53], v17 offset:32768
v_mfma_f32_16x16x32_bf16 a[180:183], v[60:63], v[108:111], a[180:183]
ds_read_b64_tr_b16 v[54:55], v17 offset:33792
v_mfma_f32_16x16x32_bf16 a[184:187], v[60:63], v[112:115], a[184:187]
s_lshl_b32 s22, s45, 19
v_mfma_f32_16x16x32_bf16 a[188:191], v[60:63], v[116:119], a[188:191]
s_waitcnt lgkmcnt(6)
v_mfma_f32_16x16x32_bf16 a[192:195], v[40:43], v[104:107], a[192:195]
ds_read_b64_tr_b16 v[56:57], v18 offset:32768
v_mfma_f32_16x16x32_bf16 a[196:199], v[40:43], v[108:111], a[196:199]
ds_read_b64_tr_b16 v[58:59], v18 offset:33280
v_mfma_f32_16x16x32_bf16 a[200:203], v[40:43], v[112:115], a[200:203]
s_add_u32 s23, s22, 5242880
v_mfma_f32_16x16x32_bf16 a[204:207], v[40:43], v[116:119], a[204:207]
s_waitcnt lgkmcnt(6)
v_mfma_f32_16x16x32_bf16 a[208:211], v[44:47], v[104:107], a[208:211]
ds_read_b64_tr_b16 v[60:61], v19 offset:32768
v_mfma_f32_16x16x32_bf16 a[212:215], v[44:47], v[108:111], a[212:215]
ds_read_b64_tr_b16 v[62:63], v19 offset:33280
v_mfma_f32_16x16x32_bf16 a[216:219], v[44:47], v[112:115], a[216:219]
s_add_u32 s46, s48, s23
v_mfma_f32_16x16x32_bf16 a[220:223], v[44:47], v[116:119], a[220:223]
)ASM"
R"ASM(s_addc_u32 s47, s49, 0
s_waitcnt lgkmcnt(6)
v_mfma_f32_16x16x32_bf16 a[224:227], v[48:51], v[104:107], a[224:227]
ds_read_b64_tr_b16 v[40:41], v20 offset:32768
v_mfma_f32_16x16x32_bf16 a[228:231], v[48:51], v[108:111], a[228:231]
ds_read_b64_tr_b16 v[42:43], v20 offset:33280
v_mfma_f32_16x16x32_bf16 a[232:235], v[48:51], v[112:115], a[232:235]
s_mov_b32 s12, s46
v_mfma_f32_16x16x32_bf16 a[236:239], v[48:51], v[116:119], a[236:239]
s_waitcnt lgkmcnt(6)
v_mfma_f32_16x16x32_bf16 a[240:243], v[52:55], v[104:107], a[240:243]
ds_read_b64_tr_b16 v[44:45], v21 offset:32768
v_mfma_f32_16x16x32_bf16 a[244:247], v[52:55], v[108:111], a[244:247]
ds_read_b64_tr_b16 v[46:47], v21 offset:33280
v_mfma_f32_16x16x32_bf16 a[248:251], v[52:55], v[112:115], a[248:251]
s_and_b32 s13, s47, 65535
v_mfma_f32_16x16x32_bf16 a[252:255], v[52:55], v[116:119], a[252:255]
s_waitcnt lgkmcnt(6)
v_mfma_f32_16x16x32_bf16 a[0:3], v[56:59], v[104:107], a[0:3]
ds_read_b64_tr_b16 v[48:49], v18 offset:33792
v_mfma_f32_16x16x32_bf16 a[4:7], v[56:59], v[108:111], a[4:7]
ds_read_b64_tr_b16 v[50:51], v18 offset:34304
v_mfma_f32_16x16x32_bf16 a[8:11], v[56:59], v[112:115], a[8:11]
buffer_store_dwordx4 v[184:187], v28, s[92:95], s31 offen offset:2048 sc1
v_mfma_f32_16x16x32_bf16 a[12:15], v[56:59], v[116:119], a[12:15]
s_mov_b32 s14, 524288
s_waitcnt lgkmcnt(6)
v_mfma_f32_16x16x32_bf16 a[16:19], v[60:63], v[104:107], a[16:19]
ds_read_b64_tr_b16 v[52:53], v19 offset:33792
v_mfma_f32_16x16x32_bf16 a[20:23], v[60:63], v[108:111], a[20:23]
ds_read_b64_tr_b16 v[54:55], v19 offset:34304
v_mfma_f32_16x16x32_bf16 a[24:27], v[60:63], v[112:115], a[24:27]
s_mov_b32 s15, 131072
v_mfma_f32_16x16x32_bf16 a[28:31], v[60:63], v[116:119], a[28:31]
s_waitcnt lgkmcnt(6)
v_mfma_f32_16x16x32_bf16 a[32:35], v[40:43], v[104:107], a[32:35]
ds_read_b64_tr_b16 v[56:57], v20 offset:33792
v_mfma_f32_16x16x32_bf16 a[36:39], v[40:43], v[108:111], a[36:39]
ds_read_b64_tr_b16 v[58:59], v20 offset:34304
v_mfma_f32_16x16x32_bf16 a[40:43], v[40:43], v[112:115], a[40:43]
s_add_u32 s23, s22, 22020096
v_mfma_f32_16x16x32_bf16 a[44:47], v[40:43], v[116:119], a[44:47]
s_waitcnt lgkmcnt(6)
v_mfma_f32_16x16x32_bf16 a[48:51], v[44:47], v[104:107], a[48:51]
ds_read_b64_tr_b16 v[60:61], v21 offset:33792
v_mfma_f32_16x16x32_bf16 a[52:55], v[44:47], v[108:111], a[52:55]
ds_read_b64_tr_b16 v[62:63], v21 offset:34304
v_mfma_f32_16x16x32_bf16 a[56:59], v[44:47], v[112:115], a[56:59]
s_add_u32 s46, s48, s23
v_mfma_f32_16x16x32_bf16 a[60:63], v[44:47], v[116:119], a[60:63]
s_addc_u32 s47, s49, 0
s_waitcnt vmcnt(16)
s_barrier
s_waitcnt lgkmcnt(6)
v_mfma_f32_16x16x32_bf16 a[64:67], v[48:51], v[104:107], a[64:67]
ds_read_b64_tr_b16 v[40:41], v10 offset:49152
ds_read_b64_tr_b16 v[42:43], v10 offset:50176
v_mfma_f32_16x16x32_bf16 a[68:71], v[48:51], v[108:111], a[68:71]
s_add_u32 s28, s71, 1310720
s_add_u32 m0, s70, 16384
v_mfma_f32_16x16x32_bf16 a[72:75], v[48:51], v[112:115], a[72:75]
buffer_load_dwordx4 v29, s[76:79], s28 offen lds
s_add_u32 s28, s28, 31744
v_mfma_f32_16x16x32_bf16 a[76:79], v[48:51], v[116:119], a[76:79]
buffer_load_dwordx4 v29, s[76:79], s28 offen offset:1024 lds
s_mov_b32 s16, s46
s_waitcnt lgkmcnt(6)
v_mfma_f32_16x16x32_bf16 a[80:83], v[52:55], v[104:107], a[80:83]
ds_read_b64_tr_b16 v[44:45], v11 offset:49152
ds_read_b64_tr_b16 v[46:47], v11 offset:50176
v_mfma_f32_16x16x32_bf16 a[84:87], v[52:55], v[108:111], a[84:87]
s_add_u32 s28, s73, 573440
v_mfma_f32_16x16x32_bf16 a[88:91], v[52:55], v[112:115], a[88:91]
buffer_load_dwordx4 v30, s[80:83], s28 offen offset:2048 lds
v_mfma_f32_16x16x32_bf16 a[92:95], v[52:55], v[116:119], a[92:95]
s_and_b32 s17, s47, 65535
s_waitcnt lgkmcnt(6)
v_mfma_f32_16x16x32_bf16 a[96:99], v[56:59], v[104:107], a[96:99]
ds_read_b64_tr_b16 v[48:49], v12 offset:49152
ds_read_b64_tr_b16 v[50:51], v12 offset:50176
v_mfma_f32_16x16x32_bf16 a[100:103], v[56:59], v[108:111], a[100:103]
buffer_load_dwordx4 v30, s[84:87], s28 offen offset:3072 lds
v_mfma_f32_16x16x32_bf16 a[104:107], v[56:59], v[112:115], a[104:107]
buffer_store_dwordx4 v[188:191], v28, s[92:95], s31 offen offset:3072 sc1
v_mfma_f32_16x16x32_bf16 a[108:111], v[56:59], v[116:119], a[108:111]
s_mov_b32 s18, 524288
s_waitcnt lgkmcnt(6)
v_mfma_f32_16x16x32_bf16 a[112:115], v[60:63], v[104:107], a[112:115]
ds_read_b64_tr_b16 v[52:53], v13 offset:49152
v_mfma_f32_16x16x32_bf16 a[116:119], v[60:63], v[108:111], a[116:119]
ds_read_b64_tr_b16 v[54:55], v13 offset:50176
v_mfma_f32_16x16x32_bf16 a[120:123], v[60:63], v[112:115], a[120:123]
s_mov_b32 s19, 131072
v_mfma_f32_16x16x32_bf16 a[124:127], v[60:63], v[116:119], a[124:127]
s_waitcnt lgkmcnt(6)
v_mfma_f32_16x16x32_bf16 a[128:131], v[40:43], v[64:67], a[128:131]
ds_read_b64_tr_b16 v[56:57], v14 offset:49152
ds_read_b64_tr_b16 v[58:59], v14 offset:50176
v_mfma_f32_16x16x32_bf16 a[132:135], v[40:43], v[68:71], a[132:135]
buffer_load_dwordx4 v[104:107], v28, s[52:55], s8 offen offset:2048 sc1
v_mfma_f32_16x16x32_bf16 a[136:139], v[40:43], v[120:123], a[136:139]
buffer_load_dwordx4 v[108:111], v28, s[52:55], s9 offen offset:2048 sc1
v_mfma_f32_16x16x32_bf16 a[140:143], v[40:43], v[124:127], a[140:143]
s_lshr_b32 s22, s50, 3
s_waitcnt lgkmcnt(6)
v_mfma_f32_16x16x32_bf16 a[144:147], v[44:47], v[64:67], a[144:147]
ds_read_b64_tr_b16 v[60:61], v15 offset:49152
v_mfma_f32_16x16x32_bf16 a[148:151], v[44:47], v[68:71], a[148:151]
ds_read_b64_tr_b16 v[62:63], v15 offset:50176
v_mfma_f32_16x16x32_bf16 a[152:155], v[44:47], v[120:123], a[152:155]
buffer_load_dwordx4 v[112:115], v28, s[52:55], s10 offen offset:2048 sc1
v_mfma_f32_16x16x32_bf16 a[156:159], v[44:47], v[124:127], a[156:159]
s_lshl_b32 s22, s22, 15
s_waitcnt lgkmcnt(6)
v_mfma_f32_16x16x32_bf16 a[160:163], v[48:51], v[64:67], a[160:163]
ds_read_b64_tr_b16 v[40:41], v16 offset:49152
v_mfma_f32_16x16x32_bf16 a[164:167], v[48:51], v[68:71], a[164:167]
ds_read_b64_tr_b16 v[42:43], v16 offset:50176
v_mfma_f32_16x16x32_bf16 a[168:171], v[48:51], v[120:123], a[168:171]
buffer_load_dwordx4 v[116:119], v28, s[52:55], s11 offen offset:2048 sc1
v_mfma_f32_16x16x32_bf16 a[172:175], v[48:51], v[124:127], a[172:175]
s_lshl_b32 s23, s75, 12
s_waitcnt lgkmcnt(6)
v_mfma_f32_16x16x32_bf16 a[176:179], v[52:55], v[64:67], a[176:179]
ds_read_b64_tr_b16 v[44:45], v17 offset:49152
v_mfma_f32_16x16x32_bf16 a[180:183], v[52:55], v[68:71], a[180:183]
ds_read_b64_tr_b16 v[46:47], v17 offset:50176
v_mfma_f32_16x16x32_bf16 a[184:187], v[52:55], v[120:123], a[184:187]
s_add_u32 s37, s22, s23
v_mfma_f32_16x16x32_bf16 a[188:191], v[52:55], v[124:127], a[188:191]
s_waitcnt lgkmcnt(6)
v_mfma_f32_16x16x32_bf16 a[192:195], v[56:59], v[64:67], a[192:195]
ds_read_b64_tr_b16 v[48:49], v18 offset:49152
v_mfma_f32_16x16x32_bf16 a[196:199], v[56:59], v[68:71], a[196:199]
ds_read_b64_tr_b16 v[50:51], v18 offset:49664
v_mfma_f32_16x16x32_bf16 a[200:203], v[56:59], v[120:123], a[200:203]
s_mul_i32 s22, s50, 184
v_mfma_f32_16x16x32_bf16 a[204:207], v[56:59], v[124:127], a[204:207]
s_waitcnt lgkmcnt(6)
v_mfma_f32_16x16x32_bf16 a[208:211], v[60:63], v[64:67], a[208:211]
ds_read_b64_tr_b16 v[52:53], v19 offset:49152
ds_read_b64_tr_b16 v[54:55], v19 offset:49664
v_mfma_f32_16x16x32_bf16 a[212:215], v[60:63], v[68:71], a[212:215]
s_mov_b32 s31, 524288
v_mfma_f32_16x16x32_bf16 a[216:219], v[60:63], v[120:123], a[216:219]
buffer_store_dwordx4 v[192:195], v28, s[92:95], s31 offen offset:0 sc1
v_mfma_f32_16x16x32_bf16 a[220:223], v[60:63], v[124:127], a[220:223]
s_lshl_b32 s23, s45, 2
s_waitcnt lgkmcnt(6)
v_mfma_f32_16x16x32_bf16 a[224:227], v[40:43], v[64:67], a[224:227]
ds_read_b64_tr_b16 v[56:57], v20 offset:49152
v_mfma_f32_16x16x32_bf16 a[228:231], v[40:43], v[68:71], a[228:231]
ds_read_b64_tr_b16 v[58:59], v20 offset:49664
v_mfma_f32_16x16x32_bf16 a[232:235], v[40:43], v[120:123], a[232:235]
s_add_u32 s22, s22, s23
v_mfma_f32_16x16x32_bf16 a[236:239], v[40:43], v[124:127], a[236:239]
s_waitcnt lgkmcnt(6)
v_mfma_f32_16x16x32_bf16 a[240:243], v[44:47], v[64:67], a[240:243]
ds_read_b64_tr_b16 v[60:61], v21 offset:49152
v_mfma_f32_16x16x32_bf16 a[244:247], v[44:47], v[68:71], a[244:247]
ds_read_b64_tr_b16 v[62:63], v21 offset:49664
v_mfma_f32_16x16x32_bf16 a[248:251], v[44:47], v[120:123], a[248:251]
s_lshl_b32 s22, s22, 10
v_mfma_f32_16x16x32_bf16 a[252:255], v[44:47], v[124:127], a[252:255]
s_waitcnt lgkmcnt(6)
v_mfma_f32_16x16x32_bf16 a[0:3], v[48:51], v[64:67], a[0:3]
ds_read_b64_tr_b16 v[40:41], v18 offset:50176
v_mfma_f32_16x16x32_bf16 a[4:7], v[48:51], v[68:71], a[4:7]
ds_read_b64_tr_b16 v[42:43], v18 offset:50688
v_mfma_f32_16x16x32_bf16 a[8:11], v[48:51], v[120:123], a[8:11]
s_add_u32 s22, s22, 38797312
v_mfma_f32_16x16x32_bf16 a[12:15], v[48:51], v[124:127], a[12:15]
s_waitcnt lgkmcnt(6)
v_mfma_f32_16x16x32_bf16 a[16:19], v[52:55], v[64:67], a[16:19]
ds_read_b64_tr_b16 v[44:45], v19 offset:50176
v_mfma_f32_16x16x32_bf16 a[20:23], v[52:55], v[68:71], a[20:23]
ds_read_b64_tr_b16 v[46:47], v19 offset:50688
v_mfma_f32_16x16x32_bf16 a[24:27], v[52:55], v[120:123], a[24:27]
s_add_u32 s46, s48, s22
v_mfma_f32_16x16x32_bf16 a[28:31], v[52:55], v[124:127], a[28:31]
s_addc_u32 s47, s49, 0
s_waitcnt lgkmcnt(6)
v_mfma_f32_16x16x32_bf16 a[32:35], v[56:59], v[64:67], a[32:35]
ds_read_b64_tr_b16 v[48:49], v20 offset:50176
v_mfma_f32_16x16x32_bf16 a[36:39], v[56:59], v[68:71], a[36:39]
ds_read_b64_tr_b16 v[50:51], v20 offset:50688
v_mfma_f32_16x16x32_bf16 a[40:43], v[56:59], v[120:123], a[40:43]
s_mov_b32 s56, s46
v_mfma_f32_16x16x32_bf16 a[44:47], v[56:59], v[124:127], a[44:47]
s_waitcnt lgkmcnt(6)
v_mfma_f32_16x16x32_bf16 a[48:51], v[60:63], v[64:67], a[48:51]
ds_read_b64_tr_b16 v[52:53], v21 offset:50176
v_mfma_f32_16x16x32_bf16 a[52:55], v[60:63], v[68:71], a[52:55]
ds_read_b64_tr_b16 v[54:55], v21 offset:50688
v_mfma_f32_16x16x32_bf16 a[56:59], v[60:63], v[120:123], a[56:59]
s_and_b32 s57, s47, 65535
v_mfma_f32_16x16x32_bf16 a[60:63], v[60:63], v[124:127], a[60:63]
s_waitcnt vmcnt(16)
s_barrier
s_waitcnt lgkmcnt(6)
v_mfma_f32_16x16x32_bf16 a[64:67], v[40:43], v[64:67], a[64:67]
ds_read_b64_tr_b16 v[56:57], v10 offset:0
ds_read_b64_tr_b16 v[58:59], v10 offset:1024
s_add_u32 s28, s71, 1572864
v_mfma_f32_16x16x32_bf16 a[68:71], v[40:43], v[68:71], a[68:71]
s_add_u32 m0, s70, 32768
s_nop 0
buffer_load_dwordx4 v29, s[76:79], s28 offen lds
v_mfma_f32_16x16x32_bf16 a[72:75], v[40:43], v[120:123], a[72:75]
s_add_u32 s28, s28, 31744
buffer_load_dwordx4 v29, s[76:79], s28 offen offset:1024 lds
v_mfma_f32_16x16x32_bf16 a[76:79], v[40:43], v[124:127], a[76:79]
buffer_store_dwordx4 v[196:199], v28, s[92:95], s31 offen offset:1024 sc1
s_mov_b32 s58, 1318912
s_waitcnt lgkmcnt(6)
v_mfma_f32_16x16x32_bf16 a[80:83], v[44:47], v[64:67], a[80:83]
ds_read_b64_tr_b16 v[60:61], v11 offset:0
ds_read_b64_tr_b16 v[62:63], v11 offset:1024
v_mfma_f32_16x16x32_bf16 a[84:87], v[44:47], v[68:71], a[84:87]
s_add_u32 s28, s73, 688128
v_mfma_f32_16x16x32_bf16 a[88:91], v[44:47], v[120:123], a[88:91]
buffer_load_dwordx4 v30, s[80:83], s28 offen offset:2048 lds
v_mfma_f32_16x16x32_bf16 a[92:95], v[44:47], v[124:127], a[92:95]
s_mov_b32 s59, 131072
s_waitcnt lgkmcnt(6)
v_mfma_f32_16x16x32_bf16 a[96:99], v[48:51], v[64:67], a[96:99]
ds_read_b64_tr_b16 v[40:41], v12 offset:0
ds_read_b64_tr_b16 v[42:43], v12 offset:1024
v_mfma_f32_16x16x32_bf16 a[100:103], v[48:51], v[68:71], a[100:103]
buffer_load_dwordx4 v30, s[84:87], s28 offen offset:3072 lds
s_getpc_b64 s[46:47]
v_mfma_f32_16x16x32_bf16 a[104:107], v[48:51], v[120:123], a[104:107]
L_CP_X2:
s_add_u32 s46, s46, L_XM-L_CP_X2
v_mfma_f32_16x16x32_bf16 a[108:111], v[48:51], v[124:127], a[108:111]
s_addc_u32 s47, s47, 0
s_waitcnt lgkmcnt(6)
v_mfma_f32_16x16x32_bf16 a[112:115], v[52:55], v[64:67], a[112:115]
ds_read_b64_tr_b16 v[44:45], v13 offset:0
v_mfma_f32_16x16x32_bf16 a[116:119], v[52:55], v[68:71], a[116:119]
ds_read_b64_tr_b16 v[46:47], v13 offset:1024
v_mfma_f32_16x16x32_bf16 a[120:123], v[52:55], v[120:123], a[120:123]
s_lshr_b32 s22, s6, 3
v_mfma_f32_16x16x32_bf16 a[124:127], v[52:55], v[124:127], a[124:127]
s_waitcnt lgkmcnt(6)
v_mfma_f32_16x16x32_bf16 a[128:131], v[56:59], v[88:91], a[128:131]
ds_read_b64_tr_b16 v[48:49], v14 offset:0
ds_read_b64_tr_b16 v[50:51], v14 offset:1024
v_mfma_f32_16x16x32_bf16 a[132:135], v[56:59], v[92:95], a[132:135]
buffer_load_dwordx4 v[64:67], v28, s[52:55], s8 offen offset:3072 sc1
v_mfma_f32_16x16x32_bf16 a[136:139], v[56:59], v[96:99], a[136:139]
buffer_load_dwordx4 v[68:71], v28, s[52:55], s9 offen offset:3072 sc1
v_mfma_f32_16x16x32_bf16 a[140:143], v[56:59], v[100:103], a[140:143]
s_lshl_b32 s22, s22, 2
s_waitcnt lgkmcnt(6)
v_mfma_f32_16x16x32_bf16 a[144:147], v[60:63], v[88:91], a[144:147]
ds_read_b64_tr_b16 v[52:53], v15 offset:0
v_mfma_f32_16x16x32_bf16 a[148:151], v[60:63], v[92:95], a[148:151]
ds_read_b64_tr_b16 v[54:55], v15 offset:1024
v_mfma_f32_16x16x32_bf16 a[152:155], v[60:63], v[96:99], a[152:155]
buffer_load_dwordx4 v[120:123], v28, s[52:55], s10 offen offset:3072 sc1
v_mfma_f32_16x16x32_bf16 a[156:159], v[60:63], v[100:103], a[156:159]
s_add_u32 s22, s22, s7
s_waitcnt lgkmcnt(6)
v_mfma_f32_16x16x32_bf16 a[160:163], v[40:43], v[88:91], a[160:163]
ds_read_b64_tr_b16 v[56:57], v16 offset:0
ds_read_b64_tr_b16 v[58:59], v16 offset:1024
v_mfma_f32_16x16x32_bf16 a[164:167], v[40:43], v[92:95], a[164:167]
buffer_load_dwordx4 v[124:127], v28, s[52:55], s11 offen offset:3072 sc1
v_mfma_f32_16x16x32_bf16 a[168:171], v[40:43], v[96:99], a[168:171]
buffer_store_dwordx4 v[200:203], v28, s[92:95], s31 offen offset:2048 sc1
s_mul_i32 s22, s22, 256
v_mfma_f32_16x16x32_bf16 a[172:175], v[40:43], v[100:103], a[172:175]
v_min_u32 v22, 1, v1
s_waitcnt lgkmcnt(6)
v_mfma_f32_16x16x32_bf16 a[176:179], v[44:47], v[88:91], a[176:179]
ds_read_b64_tr_b16 v[60:61], v17 offset:0
v_mfma_f32_16x16x32_bf16 a[180:183], v[44:47], v[92:95], a[180:183]
ds_read_b64_tr_b16 v[62:63], v17 offset:1024
v_mfma_f32_16x16x32_bf16 a[184:187], v[44:47], v[96:99], a[184:187]
v_lshlrev_b32 v22, 7, v22
v_mfma_f32_16x16x32_bf16 a[188:191], v[44:47], v[100:103], a[188:191]
s_waitcnt lgkmcnt(6)
v_mfma_f32_16x16x32_bf16 a[192:195], v[48:51], v[88:91], a[192:195]
ds_read_b64_tr_b16 v[40:41], v18 offset:0
v_mfma_f32_16x16x32_bf16 a[196:199], v[48:51], v[92:95], a[196:199]
ds_read_b64_tr_b16 v[42:43], v18 offset:512
v_mfma_f32_16x16x32_bf16 a[200:203], v[48:51], v[96:99], a[200:203]
v_add_u32 v22, s22, v22
v_mfma_f32_16x16x32_bf16 a[204:207], v[48:51], v[100:103], a[204:207]
s_waitcnt lgkmcnt(6)
v_mfma_f32_16x16x32_bf16 a[208:211], v[52:55], v[88:91], a[208:211]
ds_read_b64_tr_b16 v[44:45], v19 offset:0
v_mfma_f32_16x16x32_bf16 a[212:215], v[52:55], v[92:95], a[212:215]
ds_read_b64_tr_b16 v[46:47], v19 offset:512
v_mfma_f32_16x16x32_bf16 a[216:219], v[52:55], v[96:99], a[216:219]
v_min_u32 v22, L_CODE_END-L_XM-4, v22
v_mfma_f32_16x16x32_bf16 a[220:223], v[52:55], v[100:103], a[220:223]
s_waitcnt lgkmcnt(6)
v_mfma_f32_16x16x32_bf16 a[224:227], v[56:59], v[88:91], a[224:227]
ds_read_b64_tr_b16 v[48:49], v20 offset:0
v_mfma_f32_16x16x32_bf16 a[228:231], v[56:59], v[92:95], a[228:231]
ds_read_b64_tr_b16 v[50:51], v20 offset:512
v_mfma_f32_16x16x32_bf16 a[232:235], v[56:59], v[96:99], a[232:235]
global_load_dword v8, v22, s[46:47]
v_mfma_f32_16x16x32_bf16 a[236:239], v[56:59], v[100:103], a[236:239]
s_waitcnt lgkmcnt(6)
v_mfma_f32_16x16x32_bf16 a[240:243], v[60:63], v[88:91], a[240:243]
ds_read_b64_tr_b16 v[52:53], v21 offset:0
v_mfma_f32_16x16x32_bf16 a[244:247], v[60:63], v[92:95], a[244:247]
ds_read_b64_tr_b16 v[54:55], v21 offset:512
v_mfma_f32_16x16x32_bf16 a[248:251], v[60:63], v[96:99], a[248:251]
v_and_b32 v24, 7, v1
v_mfma_f32_16x16x32_bf16 a[252:255], v[60:63], v[100:103], a[252:255]
s_waitcnt lgkmcnt(6)
v_mfma_f32_16x16x32_bf16 a[0:3], v[40:43], v[88:91], a[0:3]
ds_read_b64_tr_b16 v[56:57], v18 offset:1024
v_mfma_f32_16x16x32_bf16 a[4:7], v[40:43], v[92:95], a[4:7]
ds_read_b64_tr_b16 v[58:59], v18 offset:1536
v_mfma_f32_16x16x32_bf16 a[8:11], v[40:43], v[96:99], a[8:11]
v_lshrrev_b32 v25, 3, v1
v_mfma_f32_16x16x32_bf16 a[12:15], v[40:43], v[100:103], a[12:15]
s_waitcnt lgkmcnt(6)
v_mfma_f32_16x16x32_bf16 a[16:19], v[44:47], v[88:91], a[16:19]
ds_read_b64_tr_b16 v[60:61], v19 offset:1024
v_mfma_f32_16x16x32_bf16 a[20:23], v[44:47], v[92:95], a[20:23]
ds_read_b64_tr_b16 v[62:63], v19 offset:1536
v_mfma_f32_16x16x32_bf16 a[24:27], v[44:47], v[96:99], a[24:27]
buffer_store_dwordx4 v[204:207], v28, s[92:95], s31 offen offset:3072 sc1
v_mfma_f32_16x16x32_bf16 a[28:31], v[44:47], v[100:103], a[28:31]
v_and_b32 v25, 1, v25
s_waitcnt lgkmcnt(6)
v_mfma_f32_16x16x32_bf16 a[32:35], v[48:51], v[88:91], a[32:35]
ds_read_b64_tr_b16 v[40:41], v20 offset:1024
v_mfma_f32_16x16x32_bf16 a[36:39], v[48:51], v[92:95], a[36:39]
ds_read_b64_tr_b16 v[42:43], v20 offset:1536
v_mfma_f32_16x16x32_bf16 a[40:43], v[48:51], v[96:99], a[40:43]
v_lshlrev_b32 v25, 4, v25
v_mfma_f32_16x16x32_bf16 a[44:47], v[48:51], v[100:103], a[44:47]
s_waitcnt lgkmcnt(6)
v_mfma_f32_16x16x32_bf16 a[48:51], v[52:55], v[88:91], a[48:51]
ds_read_b64_tr_b16 v[44:45], v21 offset:1024
v_mfma_f32_16x16x32_bf16 a[52:55], v[52:55], v[92:95], a[52:55]
ds_read_b64_tr_b16 v[46:47], v21 offset:1536
v_mfma_f32_16x16x32_bf16 a[56:59], v[52:55], v[96:99], a[56:59]
v_add_u32 v24, v24, v25
v_mfma_f32_16x16x32_bf16 a[60:63], v[52:55], v[100:103], a[60:63]
s_waitcnt vmcnt(18)
s_barrier
s_waitcnt lgkmcnt(6)
v_mfma_f32_16x16x32_bf16 a[64:67], v[56:59], v[88:91], a[64:67]
ds_read_b64_tr_b16 v[48:49], v10 offset:16384
ds_read_b64_tr_b16 v[50:51], v10 offset:17408
v_mfma_f32_16x16x32_bf16 a[68:71], v[56:59], v[92:95], a[68:71]
s_add_u32 s28, s71, 1835008
s_add_u32 m0, s70, 49152
v_mfma_f32_16x16x32_bf16 a[72:75], v[56:59], v[96:99], a[72:75]
buffer_load_dwordx4 v29, s[76:79], s28 offen lds
s_add_u32 s28, s28, 31744
v_mfma_f32_16x16x32_bf16 a[76:79], v[56:59], v[100:103], a[76:79]
buffer_load_dwordx4 v29, s[76:79], s28 offen offset:1024 lds
s_and_b32 s22, s45, 3
s_waitcnt lgkmcnt(6)
v_mfma_f32_16x16x32_bf16 a[80:83], v[60:63], v[88:91], a[80:83]
ds_read_b64_tr_b16 v[52:53], v11 offset:16384
ds_read_b64_tr_b16 v[54:55], v11 offset:17408
v_mfma_f32_16x16x32_bf16 a[84:87], v[60:63], v[92:95], a[84:87]
s_add_u32 s28, s73, 802816
v_mfma_f32_16x16x32_bf16 a[88:91], v[60:63], v[96:99], a[88:91]
buffer_load_dwordx4 v30, s[80:83], s28 offen offset:2048 lds
v_mfma_f32_16x16x32_bf16 a[92:95], v[60:63], v[100:103], a[92:95]
s_lshr_b32 s22, s22, 1
s_waitcnt lgkmcnt(6)
v_mfma_f32_16x16x32_bf16 a[96:99], v[40:43], v[88:91], a[96:99]
ds_read_b64_tr_b16 v[56:57], v12 offset:16384
v_mfma_f32_16x16x32_bf16 a[100:103], v[40:43], v[92:95], a[100:103]
ds_read_b64_tr_b16 v[58:59], v12 offset:17408
v_mfma_f32_16x16x32_bf16 a[104:107], v[40:43], v[96:99], a[104:107]
buffer_load_dwordx4 v30, s[84:87], s28 offen offset:3072 lds
v_mfma_f32_16x16x32_bf16 a[108:111], v[40:43], v[100:103], a[108:111]
s_lshl_b32 s22, s22, 3
s_waitcnt lgkmcnt(6)
v_mfma_f32_16x16x32_bf16 a[112:115], v[44:47], v[88:91], a[112:115]
ds_read_b64_tr_b16 v[60:61], v13 offset:16384
v_mfma_f32_16x16x32_bf16 a[116:119], v[44:47], v[92:95], a[116:119]
ds_read_b64_tr_b16 v[62:63], v13 offset:17408
v_mfma_f32_16x16x32_bf16 a[120:123], v[44:47], v[96:99], a[120:123]
v_add_u32 v24, s22, v24
v_mfma_f32_16x16x32_bf16 a[124:127], v[44:47], v[100:103], a[124:127]
s_waitcnt lgkmcnt(6)
v_mfma_f32_16x16x32_bf16 a[128:131], v[48:51], v[72:75], a[128:131]
ds_read_b64_tr_b16 v[40:41], v14 offset:16384
ds_read_b64_tr_b16 v[42:43], v14 offset:17408
s_mov_b32 s8, 8192
v_mfma_f32_16x16x32_bf16 a[132:135], v[48:51], v[76:79], a[132:135]
buffer_load_dwordx4 v[88:91], v28, s[52:55], s8 offen offset:0 sc1
s_mov_b32 s9, 40960
v_mfma_f32_16x16x32_bf16 a[136:139], v[48:51], v[80:83], a[136:139]
buffer_load_dwordx4 v[92:95], v28, s[52:55], s9 offen offset:0 sc1
s_mov_b32 s31, 528384
v_mfma_f32_16x16x32_bf16 a[140:143], v[48:51], v[84:87], a[140:143]
buffer_store_dwordx4 v[208:211], v28, s[92:95], s31 offen offset:0 sc1
v_lshlrev_b32 v24, 3, v24
s_waitcnt lgkmcnt(6)
v_mfma_f32_16x16x32_bf16 a[144:147], v[52:55], v[72:75], a[144:147]
ds_read_b64_tr_b16 v[44:45], v15 offset:16384
ds_read_b64_tr_b16 v[46:47], v15 offset:17408
v_mfma_f32_16x16x32_bf16 a[148:151], v[52:55], v[76:79], a[148:151]
s_mov_b32 s10, 73728
v_mfma_f32_16x16x32_bf16 a[152:155], v[52:55], v[80:83], a[152:155]
buffer_load_dwordx4 v[96:99], v28, s[52:55], s10 offen offset:0 sc1
v_mfma_f32_16x16x32_bf16 a[156:159], v[52:55], v[84:87], a[156:159]
s_and_b32 s22, s6, 7
s_waitcnt lgkmcnt(6)
v_mfma_f32_16x16x32_bf16 a[160:163], v[56:59], v[72:75], a[160:163]
ds_read_b64_tr_b16 v[48:49], v16 offset:16384
ds_read_b64_tr_b16 v[50:51], v16 offset:17408
v_mfma_f32_16x16x32_bf16 a[164:167], v[56:59], v[76:79], a[164:167]
s_mov_b32 s11, 106496
v_mfma_f32_16x16x32_bf16 a[168:171], v[56:59], v[80:83], a[168:171]
buffer_load_dwordx4 v[100:103], v28, s[52:55], s11 offen offset:0 sc1
v_mfma_f32_16x16x32_bf16 a[172:175], v[56:59], v[84:87], a[172:175]
v_add_u32 v24, s22, v24
s_waitcnt lgkmcnt(6)
v_mfma_f32_16x16x32_bf16 a[176:179], v[60:63], v[72:75], a[176:179]
ds_read_b64_tr_b16 v[52:53], v17 offset:16384
v_mfma_f32_16x16x32_bf16 a[180:183], v[60:63], v[76:79], a[180:183]
ds_read_b64_tr_b16 v[54:55], v17 offset:17408
v_mfma_f32_16x16x32_bf16 a[184:187], v[60:63], v[80:83], a[184:187]
v_lshlrev_b32 v24, 6, v24
v_mfma_f32_16x16x32_bf16 a[188:191], v[60:63], v[84:87], a[188:191]
s_waitcnt lgkmcnt(6)
v_mfma_f32_16x16x32_bf16 a[192:195], v[40:43], v[72:75], a[192:195]
ds_read_b64_tr_b16 v[56:57], v18 offset:16384
v_mfma_f32_16x16x32_bf16 a[196:199], v[40:43], v[76:79], a[196:199]
ds_read_b64_tr_b16 v[58:59], v18 offset:16896
v_mfma_f32_16x16x32_bf16 a[200:203], v[40:43], v[80:83], a[200:203]
v_add_u32 v24, 32768, v24
v_mfma_f32_16x16x32_bf16 a[204:207], v[40:43], v[84:87], a[204:207]
s_waitcnt lgkmcnt(6)
v_mfma_f32_16x16x32_bf16 a[208:211], v[44:47], v[72:75], a[208:211]
ds_read_b64_tr_b16 v[60:61], v19 offset:16384
v_mfma_f32_16x16x32_bf16 a[212:215], v[44:47], v[76:79], a[212:215]
ds_read_b64_tr_b16 v[62:63], v19 offset:16896
v_mfma_f32_16x16x32_bf16 a[216:219], v[44:47], v[80:83], a[216:219]
s_and_b32 s20, s6, 7
v_mfma_f32_16x16x32_bf16 a[220:223], v[44:47], v[84:87], a[220:223]
s_waitcnt lgkmcnt(6)
v_mfma_f32_16x16x32_bf16 a[224:227], v[48:51], v[72:75], a[224:227]
ds_read_b64_tr_b16 v[40:41], v20 offset:16384
v_mfma_f32_16x16x32_bf16 a[228:231], v[48:51], v[76:79], a[228:231]
ds_read_b64_tr_b16 v[42:43], v20 offset:16896
v_mfma_f32_16x16x32_bf16 a[232:235], v[48:51], v[80:83], a[232:235]
buffer_store_dwordx4 v[212:215], v28, s[92:95], s31 offen offset:1024 sc1
v_mfma_f32_16x16x32_bf16 a[236:239], v[48:51], v[84:87], a[236:239]
s_lshr_b32 s21, s6, 3
s_waitcnt lgkmcnt(6)
v_mfma_f32_16x16x32_bf16 a[240:243], v[52:55], v[72:75], a[240:243]
ds_read_b64_tr_b16 v[44:45], v21 offset:16384
v_mfma_f32_16x16x32_bf16 a[244:247], v[52:55], v[76:79], a[244:247]
ds_read_b64_tr_b16 v[46:47], v21 offset:16896
v_mfma_f32_16x16x32_bf16 a[248:251], v[52:55], v[80:83], a[248:251]
s_and_b32 s25, s20, 3
v_mfma_f32_16x16x32_bf16 a[252:255], v[52:55], v[84:87], a[252:255]
s_waitcnt lgkmcnt(6)
v_mfma_f32_16x16x32_bf16 a[0:3], v[56:59], v[72:75], a[0:3]
ds_read_b64_tr_b16 v[48:49], v18 offset:17408
v_mfma_f32_16x16x32_bf16 a[4:7], v[56:59], v[76:79], a[4:7]
ds_read_b64_tr_b16 v[50:51], v18 offset:17920
v_mfma_f32_16x16x32_bf16 a[8:11], v[56:59], v[80:83], a[8:11]
s_lshr_b32 s22, s20, 2
v_mfma_f32_16x16x32_bf16 a[12:15], v[56:59], v[84:87], a[12:15]
s_waitcnt lgkmcnt(6)
v_mfma_f32_16x16x32_bf16 a[16:19], v[60:63], v[72:75], a[16:19]
ds_read_b64_tr_b16 v[52:53], v19 offset:17408
v_mfma_f32_16x16x32_bf16 a[20:23], v[60:63], v[76:79], a[20:23]
ds_read_b64_tr_b16 v[54:55], v19 offset:17920
v_mfma_f32_16x16x32_bf16 a[24:27], v[60:63], v[80:83], a[24:27]
s_lshl_b32 s22, s22, 3
v_mfma_f32_16x16x32_bf16 a[28:31], v[60:63], v[84:87], a[28:31]
s_waitcnt lgkmcnt(6)
v_mfma_f32_16x16x32_bf16 a[32:35], v[40:43], v[72:75], a[32:35]
ds_read_b64_tr_b16 v[56:57], v20 offset:17408
v_mfma_f32_16x16x32_bf16 a[36:39], v[40:43], v[76:79], a[36:39]
ds_read_b64_tr_b16 v[58:59], v20 offset:17920
v_mfma_f32_16x16x32_bf16 a[40:43], v[40:43], v[80:83], a[40:43]
s_lshr_b32 s23, s21, 2
v_mfma_f32_16x16x32_bf16 a[44:47], v[40:43], v[84:87], a[44:47]
s_waitcnt lgkmcnt(6)
v_mfma_f32_16x16x32_bf16 a[48:51], v[44:47], v[72:75], a[48:51]
ds_read_b64_tr_b16 v[60:61], v21 offset:17408
v_mfma_f32_16x16x32_bf16 a[52:55], v[44:47], v[76:79], a[52:55]
ds_read_b64_tr_b16 v[62:63], v21 offset:17920
v_mfma_f32_16x16x32_bf16 a[56:59], v[44:47], v[80:83], a[56:59]
s_add_u32 s26, s22, s23
v_mfma_f32_16x16x32_bf16 a[60:63], v[44:47], v[84:87], a[60:63]
s_waitcnt vmcnt(17)
s_barrier
s_waitcnt lgkmcnt(6)
v_mfma_f32_16x16x32_bf16 a[64:67], v[48:51], v[72:75], a[64:67]
ds_read_b64_tr_b16 v[40:41], v10 offset:32768
ds_read_b64_tr_b16 v[42:43], v10 offset:33792
v_mfma_f32_16x16x32_bf16 a[68:71], v[48:51], v[76:79], a[68:71]
s_add_u32 s28, s71, 2097152
s_add_u32 m0, s70, 0
v_mfma_f32_16x16x32_bf16 a[72:75], v[48:51], v[80:83], a[72:75]
buffer_load_dwordx4 v29, s[76:79], s28 offen lds
s_add_u32 s28, s28, 31744
v_mfma_f32_16x16x32_bf16 a[76:79], v[48:51], v[84:87], a[76:79]
buffer_load_dwordx4 v29, s[76:79], s28 offen offset:1024 lds
s_and_b32 s27, s21, 3
s_waitcnt lgkmcnt(6)
v_mfma_f32_16x16x32_bf16 a[80:83], v[52:55], v[72:75], a[80:83]
ds_read_b64_tr_b16 v[44:45], v11 offset:32768
ds_read_b64_tr_b16 v[46:47], v11 offset:33792
v_mfma_f32_16x16x32_bf16 a[84:87], v[52:55], v[76:79], a[84:87]
s_add_u32 s28, s73, 917504
v_mfma_f32_16x16x32_bf16 a[88:91], v[52:55], v[80:83], a[88:91]
buffer_load_dwordx4 v30, s[80:83], s28 offen offset:2048 lds
buffer_store_dwordx4 v[216:219], v28, s[92:95], s31 offen offset:2048 sc1
v_mfma_f32_16x16x32_bf16 a[92:95], v[52:55], v[84:87], a[92:95]
v_lshrrev_b32 v26, 4, v1
s_waitcnt lgkmcnt(6)
v_mfma_f32_16x16x32_bf16 a[96:99], v[56:59], v[72:75], a[96:99]
ds_read_b64_tr_b16 v[48:49], v12 offset:32768
v_mfma_f32_16x16x32_bf16 a[100:103], v[56:59], v[76:79], a[100:103]
ds_read_b64_tr_b16 v[50:51], v12 offset:33792
v_mfma_f32_16x16x32_bf16 a[104:107], v[56:59], v[80:83], a[104:107]
buffer_load_dwordx4 v30, s[84:87], s28 offen offset:3072 lds
v_mfma_f32_16x16x32_bf16 a[108:111], v[56:59], v[84:87], a[108:111]
s_lshl_b32 s24, s7, 14
s_waitcnt lgkmcnt(6)
v_mfma_f32_16x16x32_bf16 a[112:115], v[60:63], v[72:75], a[112:115]
ds_read_b64_tr_b16 v[52:53], v13 offset:32768
v_mfma_f32_16x16x32_bf16 a[116:119], v[60:63], v[76:79], a[116:119]
ds_read_b64_tr_b16 v[54:55], v13 offset:33792
v_mfma_f32_16x16x32_bf16 a[120:123], v[60:63], v[80:83], a[120:123]
s_add_u32 s23, s25, 1
v_mfma_f32_16x16x32_bf16 a[124:127], v[60:63], v[84:87], a[124:127]
s_waitcnt lgkmcnt(6)
v_mfma_f32_16x16x32_bf16 a[128:131], v[40:43], v[104:107], a[128:131]
ds_read_b64_tr_b16 v[56:57], v14 offset:32768
ds_read_b64_tr_b16 v[58:59], v14 offset:33792
v_mfma_f32_16x16x32_bf16 a[132:135], v[40:43], v[108:111], a[132:135]
buffer_load_dwordx4 v[72:75], v28, s[52:55], s8 offen offset:1024 sc1
v_mfma_f32_16x16x32_bf16 a[136:139], v[40:43], v[112:115], a[136:139]
buffer_load_dwordx4 v[76:79], v28, s[52:55], s9 offen offset:1024 sc1
v_mfma_f32_16x16x32_bf16 a[140:143], v[40:43], v[116:119], a[140:143]
s_lshl_b32 s23, s23, 2
s_waitcnt lgkmcnt(6)
v_mfma_f32_16x16x32_bf16 a[144:147], v[44:47], v[104:107], a[144:147]
ds_read_b64_tr_b16 v[60:61], v15 offset:32768
v_mfma_f32_16x16x32_bf16 a[148:151], v[44:47], v[108:111], a[148:151]
ds_read_b64_tr_b16 v[62:63], v15 offset:33792
v_mfma_f32_16x16x32_bf16 a[152:155], v[44:47], v[112:115], a[152:155]
buffer_load_dwordx4 v[80:83], v28, s[52:55], s10 offen offset:1024 sc1
v_mfma_f32_16x16x32_bf16 a[156:159], v[44:47], v[116:119], a[156:159]
v_add_u32 v22, 0, v26
s_waitcnt lgkmcnt(6)
v_mfma_f32_16x16x32_bf16 a[160:163], v[48:51], v[104:107], a[160:163]
ds_read_b64_tr_b16 v[40:41], v16 offset:32768
v_mfma_f32_16x16x32_bf16 a[164:167], v[48:51], v[108:111], a[164:167]
ds_read_b64_tr_b16 v[42:43], v16 offset:33792
v_mfma_f32_16x16x32_bf16 a[168:171], v[48:51], v[112:115], a[168:171]
buffer_load_dwordx4 v[84:87], v28, s[52:55], s11 offen offset:1024 sc1
v_mfma_f32_16x16x32_bf16 a[172:175], v[48:51], v[116:119], a[172:175]
v_lshlrev_b32 v22, 1, v22
s_waitcnt lgkmcnt(6)
v_mfma_f32_16x16x32_bf16 a[176:179], v[52:55], v[104:107], a[176:179]
ds_read_b64_tr_b16 v[44:45], v17 offset:32768
v_mfma_f32_16x16x32_bf16 a[180:183], v[52:55], v[108:111], a[180:183]
ds_read_b64_tr_b16 v[46:47], v17 offset:33792
v_mfma_f32_16x16x32_bf16 a[184:187], v[52:55], v[112:115], a[184:187]
v_and_b32 v23, 15, v1
v_mfma_f32_16x16x32_bf16 a[188:191], v[52:55], v[116:119], a[188:191]
s_waitcnt lgkmcnt(6)
v_mfma_f32_16x16x32_bf16 a[192:195], v[56:59], v[104:107], a[192:195]
ds_read_b64_tr_b16 v[48:49], v18 offset:32768
v_mfma_f32_16x16x32_bf16 a[196:199], v[56:59], v[108:111], a[196:199]
ds_read_b64_tr_b16 v[50:51], v18 offset:33280
v_mfma_f32_16x16x32_bf16 a[200:203], v[56:59], v[112:115], a[200:203]
buffer_store_dwordx4 v[220:223], v28, s[92:95], s31 offen offset:3072 sc1
v_mfma_f32_16x16x32_bf16 a[204:207], v[56:59], v[116:119], a[204:207]
v_xor_b32 v23, v23, v22
s_waitcnt lgkmcnt(6)
v_mfma_f32_16x16x32_bf16 a[208:211], v[60:63], v[104:107], a[208:211]
ds_read_b64_tr_b16 v[52:53], v19 offset:32768
v_mfma_f32_16x16x32_bf16 a[212:215], v[60:63], v[108:111], a[212:215]
ds_read_b64_tr_b16 v[54:55], v19 offset:33280
v_mfma_f32_16x16x32_bf16 a[216:219], v[60:63], v[112:115], a[216:219]
v_add_u32 v23, s23, v23
v_mfma_f32_16x16x32_bf16 a[220:223], v[60:63], v[116:119], a[220:223]
s_waitcnt lgkmcnt(6)
v_mfma_f32_16x16x32_bf16 a[224:227], v[40:43], v[104:107], a[224:227]
ds_read_b64_tr_b16 v[56:57], v20 offset:32768
v_mfma_f32_16x16x32_bf16 a[228:231], v[40:43], v[108:111], a[228:231]
ds_read_b64_tr_b16 v[58:59], v20 offset:33280
v_mfma_f32_16x16x32_bf16 a[232:235], v[40:43], v[112:115], a[232:235]
v_and_b32 v23, 15, v23
v_mfma_f32_16x16x32_bf16 a[236:239], v[40:43], v[116:119], a[236:239]
s_waitcnt lgkmcnt(6)
v_mfma_f32_16x16x32_bf16 a[240:243], v[44:47], v[104:107], a[240:243]
)ASM"
R"ASM(ds_read_b64_tr_b16 v[60:61], v21 offset:32768
v_mfma_f32_16x16x32_bf16 a[244:247], v[44:47], v[108:111], a[244:247]
ds_read_b64_tr_b16 v[62:63], v21 offset:33280
v_mfma_f32_16x16x32_bf16 a[248:251], v[44:47], v[112:115], a[248:251]
v_lshlrev_b32 v23, 4, v23
v_mfma_f32_16x16x32_bf16 a[252:255], v[44:47], v[116:119], a[252:255]
s_waitcnt lgkmcnt(6)
v_mfma_f32_16x16x32_bf16 a[0:3], v[48:51], v[104:107], a[0:3]
ds_read_b64_tr_b16 v[40:41], v18 offset:33792
v_mfma_f32_16x16x32_bf16 a[4:7], v[48:51], v[108:111], a[4:7]
ds_read_b64_tr_b16 v[42:43], v18 offset:34304
v_mfma_f32_16x16x32_bf16 a[8:11], v[48:51], v[112:115], a[8:11]
v_lshlrev_b32 v31, 10, v26
v_mfma_f32_16x16x32_bf16 a[12:15], v[48:51], v[116:119], a[12:15]
s_waitcnt lgkmcnt(6)
v_mfma_f32_16x16x32_bf16 a[16:19], v[52:55], v[104:107], a[16:19]
ds_read_b64_tr_b16 v[44:45], v19 offset:33792
v_mfma_f32_16x16x32_bf16 a[20:23], v[52:55], v[108:111], a[20:23]
ds_read_b64_tr_b16 v[46:47], v19 offset:34304
v_mfma_f32_16x16x32_bf16 a[24:27], v[52:55], v[112:115], a[24:27]
v_add_u32 v23, v23, v31
v_mfma_f32_16x16x32_bf16 a[28:31], v[52:55], v[116:119], a[28:31]
s_waitcnt lgkmcnt(6)
v_mfma_f32_16x16x32_bf16 a[32:35], v[56:59], v[104:107], a[32:35]
ds_read_b64_tr_b16 v[48:49], v20 offset:33792
ds_read_b64_tr_b16 v[50:51], v20 offset:34304
v_mfma_f32_16x16x32_bf16 a[36:39], v[56:59], v[108:111], a[36:39]
s_mov_b32 s31, 532480
v_mfma_f32_16x16x32_bf16 a[40:43], v[56:59], v[112:115], a[40:43]
buffer_store_dwordx4 v[224:227], v28, s[92:95], s31 offen offset:0 sc1
v_mfma_f32_16x16x32_bf16 a[44:47], v[56:59], v[116:119], a[44:47]
v_add_u32 v23, 0, v23
s_waitcnt lgkmcnt(6)
v_mfma_f32_16x16x32_bf16 a[48:51], v[60:63], v[104:107], a[48:51]
ds_read_b64_tr_b16 v[52:53], v21 offset:33792
v_mfma_f32_16x16x32_bf16 a[52:55], v[60:63], v[108:111], a[52:55]
ds_read_b64_tr_b16 v[54:55], v21 offset:34304
v_mfma_f32_16x16x32_bf16 a[56:59], v[60:63], v[112:115], a[56:59]
v_add_u32 v32, s24, v23
v_mfma_f32_16x16x32_bf16 a[60:63], v[60:63], v[116:119], a[60:63]
s_waitcnt vmcnt(17)
s_barrier
s_waitcnt lgkmcnt(6)
v_mfma_f32_16x16x32_bf16 a[64:67], v[40:43], v[104:107], a[64:67]
ds_read_b64_tr_b16 v[56:57], v10 offset:49152
ds_read_b64_tr_b16 v[58:59], v10 offset:50176
v_mfma_f32_16x16x32_bf16 a[68:71], v[40:43], v[108:111], a[68:71]
s_add_u32 s28, s71, 2359296
s_add_u32 m0, s70, 16384
v_mfma_f32_16x16x32_bf16 a[72:75], v[40:43], v[112:115], a[72:75]
buffer_load_dwordx4 v29, s[76:79], s28 offen lds
s_add_u32 s28, s28, 31744
v_mfma_f32_16x16x32_bf16 a[76:79], v[40:43], v[116:119], a[76:79]
buffer_load_dwordx4 v29, s[76:79], s28 offen offset:1024 lds
v_add_u32 v22, 4, v26
s_waitcnt lgkmcnt(6)
v_mfma_f32_16x16x32_bf16 a[80:83], v[44:47], v[104:107], a[80:83]
ds_read_b64_tr_b16 v[60:61], v11 offset:49152
ds_read_b64_tr_b16 v[62:63], v11 offset:50176
v_mfma_f32_16x16x32_bf16 a[84:87], v[44:47], v[108:111], a[84:87]
s_add_u32 s28, s73, 1032192
v_mfma_f32_16x16x32_bf16 a[88:91], v[44:47], v[112:115], a[88:91]
buffer_load_dwordx4 v30, s[80:83], s28 offen offset:2048 lds
v_mfma_f32_16x16x32_bf16 a[92:95], v[44:47], v[116:119], a[92:95]
v_lshlrev_b32 v22, 1, v22
s_waitcnt lgkmcnt(6)
v_mfma_f32_16x16x32_bf16 a[96:99], v[48:51], v[104:107], a[96:99]
ds_read_b64_tr_b16 v[40:41], v12 offset:49152
v_mfma_f32_16x16x32_bf16 a[100:103], v[48:51], v[108:111], a[100:103]
ds_read_b64_tr_b16 v[42:43], v12 offset:50176
v_mfma_f32_16x16x32_bf16 a[104:107], v[48:51], v[112:115], a[104:107]
buffer_load_dwordx4 v30, s[84:87], s28 offen offset:3072 lds
v_mfma_f32_16x16x32_bf16 a[108:111], v[48:51], v[116:119], a[108:111]
v_and_b32 v23, 15, v1
s_waitcnt lgkmcnt(6)
v_mfma_f32_16x16x32_bf16 a[112:115], v[52:55], v[104:107], a[112:115]
ds_read_b64_tr_b16 v[44:45], v13 offset:49152
v_mfma_f32_16x16x32_bf16 a[116:119], v[52:55], v[108:111], a[116:119]
ds_read_b64_tr_b16 v[46:47], v13 offset:50176
v_mfma_f32_16x16x32_bf16 a[120:123], v[52:55], v[112:115], a[120:123]
v_xor_b32 v23, v23, v22
v_mfma_f32_16x16x32_bf16 a[124:127], v[52:55], v[116:119], a[124:127]
s_waitcnt lgkmcnt(6)
v_mfma_f32_16x16x32_bf16 a[128:131], v[56:59], v[64:67], a[128:131]
ds_read_b64_tr_b16 v[48:49], v14 offset:49152
ds_read_b64_tr_b16 v[50:51], v14 offset:50176
v_mfma_f32_16x16x32_bf16 a[132:135], v[56:59], v[68:71], a[132:135]
buffer_load_dwordx4 v[104:107], v28, s[52:55], s8 offen offset:2048 sc1
v_mfma_f32_16x16x32_bf16 a[136:139], v[56:59], v[120:123], a[136:139]
buffer_load_dwordx4 v[108:111], v28, s[52:55], s9 offen offset:2048 sc1
v_mfma_f32_16x16x32_bf16 a[140:143], v[56:59], v[124:127], a[140:143]
v_add_u32 v23, s23, v23
s_waitcnt lgkmcnt(6)
v_mfma_f32_16x16x32_bf16 a[144:147], v[60:63], v[64:67], a[144:147]
ds_read_b64_tr_b16 v[52:53], v15 offset:49152
ds_read_b64_tr_b16 v[54:55], v15 offset:50176
v_mfma_f32_16x16x32_bf16 a[148:151], v[60:63], v[68:71], a[148:151]
buffer_load_dwordx4 v[112:115], v28, s[52:55], s10 offen offset:2048 sc1
v_mfma_f32_16x16x32_bf16 a[152:155], v[60:63], v[120:123], a[152:155]
buffer_store_dwordx4 v[228:231], v28, s[92:95], s31 offen offset:1024 sc1
v_mfma_f32_16x16x32_bf16 a[156:159], v[60:63], v[124:127], a[156:159]
v_and_b32 v23, 15, v23
s_waitcnt lgkmcnt(6)
v_mfma_f32_16x16x32_bf16 a[160:163], v[40:43], v[64:67], a[160:163]
ds_read_b64_tr_b16 v[56:57], v16 offset:49152
v_mfma_f32_16x16x32_bf16 a[164:167], v[40:43], v[68:71], a[164:167]
ds_read_b64_tr_b16 v[58:59], v16 offset:50176
v_mfma_f32_16x16x32_bf16 a[168:171], v[40:43], v[120:123], a[168:171]
buffer_load_dwordx4 v[116:119], v28, s[52:55], s11 offen offset:2048 sc1
v_mfma_f32_16x16x32_bf16 a[172:175], v[40:43], v[124:127], a[172:175]
v_lshlrev_b32 v23, 4, v23
s_waitcnt lgkmcnt(6)
v_mfma_f32_16x16x32_bf16 a[176:179], v[44:47], v[64:67], a[176:179]
ds_read_b64_tr_b16 v[60:61], v17 offset:49152
v_mfma_f32_16x16x32_bf16 a[180:183], v[44:47], v[68:71], a[180:183]
ds_read_b64_tr_b16 v[62:63], v17 offset:50176
v_mfma_f32_16x16x32_bf16 a[184:187], v[44:47], v[120:123], a[184:187]
v_lshlrev_b32 v31, 10, v26
v_mfma_f32_16x16x32_bf16 a[188:191], v[44:47], v[124:127], a[188:191]
s_waitcnt lgkmcnt(6)
v_mfma_f32_16x16x32_bf16 a[192:195], v[48:51], v[64:67], a[192:195]
ds_read_b64_tr_b16 v[40:41], v18 offset:49152
v_mfma_f32_16x16x32_bf16 a[196:199], v[48:51], v[68:71], a[196:199]
ds_read_b64_tr_b16 v[42:43], v18 offset:49664
v_mfma_f32_16x16x32_bf16 a[200:203], v[48:51], v[120:123], a[200:203]
v_add_u32 v23, v23, v31
v_mfma_f32_16x16x32_bf16 a[204:207], v[48:51], v[124:127], a[204:207]
s_waitcnt lgkmcnt(6)
v_mfma_f32_16x16x32_bf16 a[208:211], v[52:55], v[64:67], a[208:211]
ds_read_b64_tr_b16 v[44:45], v19 offset:49152
v_mfma_f32_16x16x32_bf16 a[212:215], v[52:55], v[68:71], a[212:215]
ds_read_b64_tr_b16 v[46:47], v19 offset:49664
v_mfma_f32_16x16x32_bf16 a[216:219], v[52:55], v[120:123], a[216:219]
v_add_u32 v23, 3072, v23
v_mfma_f32_16x16x32_bf16 a[220:223], v[52:55], v[124:127], a[220:223]
s_waitcnt lgkmcnt(6)
v_mfma_f32_16x16x32_bf16 a[224:227], v[56:59], v[64:67], a[224:227]
ds_read_b64_tr_b16 v[48:49], v20 offset:49152
v_mfma_f32_16x16x32_bf16 a[228:231], v[56:59], v[68:71], a[228:231]
ds_read_b64_tr_b16 v[50:51], v20 offset:49664
v_mfma_f32_16x16x32_bf16 a[232:235], v[56:59], v[120:123], a[232:235]
v_add_u32 v33, s24, v23
v_mfma_f32_16x16x32_bf16 a[236:239], v[56:59], v[124:127], a[236:239]
s_waitcnt lgkmcnt(6)
v_mfma_f32_16x16x32_bf16 a[240:243], v[60:63], v[64:67], a[240:243]
ds_read_b64_tr_b16 v[52:53], v21 offset:49152
v_mfma_f32_16x16x32_bf16 a[244:247], v[60:63], v[68:71], a[244:247]
ds_read_b64_tr_b16 v[54:55], v21 offset:49664
v_mfma_f32_16x16x32_bf16 a[248:251], v[60:63], v[120:123], a[248:251]
v_add_u32 v22, 0, v26
v_mfma_f32_16x16x32_bf16 a[252:255], v[60:63], v[124:127], a[252:255]
s_waitcnt lgkmcnt(6)
v_mfma_f32_16x16x32_bf16 a[0:3], v[40:43], v[64:67], a[0:3]
ds_read_b64_tr_b16 v[56:57], v18 offset:50176
v_mfma_f32_16x16x32_bf16 a[4:7], v[40:43], v[68:71], a[4:7]
ds_read_b64_tr_b16 v[58:59], v18 offset:50688
v_mfma_f32_16x16x32_bf16 a[8:11], v[40:43], v[120:123], a[8:11]
buffer_store_dwordx4 v[232:235], v28, s[92:95], s31 offen offset:2048 sc1
v_mfma_f32_16x16x32_bf16 a[12:15], v[40:43], v[124:127], a[12:15]
v_lshlrev_b32 v22, 1, v22
s_waitcnt lgkmcnt(6)
v_mfma_f32_16x16x32_bf16 a[16:19], v[44:47], v[64:67], a[16:19]
ds_read_b64_tr_b16 v[60:61], v19 offset:50176
v_mfma_f32_16x16x32_bf16 a[20:23], v[44:47], v[68:71], a[20:23]
ds_read_b64_tr_b16 v[62:63], v19 offset:50688
v_mfma_f32_16x16x32_bf16 a[24:27], v[44:47], v[120:123], a[24:27]
v_and_b32 v23, 15, v1
v_mfma_f32_16x16x32_bf16 a[28:31], v[44:47], v[124:127], a[28:31]
s_waitcnt lgkmcnt(6)
v_mfma_f32_16x16x32_bf16 a[32:35], v[48:51], v[64:67], a[32:35]
ds_read_b64_tr_b16 v[40:41], v20 offset:50176
v_mfma_f32_16x16x32_bf16 a[36:39], v[48:51], v[68:71], a[36:39]
ds_read_b64_tr_b16 v[42:43], v20 offset:50688
v_mfma_f32_16x16x32_bf16 a[40:43], v[48:51], v[120:123], a[40:43]
v_xor_b32 v23, v23, v22
v_mfma_f32_16x16x32_bf16 a[44:47], v[48:51], v[124:127], a[44:47]
s_waitcnt lgkmcnt(6)
v_mfma_f32_16x16x32_bf16 a[48:51], v[52:55], v[64:67], a[48:51]
ds_read_b64_tr_b16 v[44:45], v21 offset:50176
v_mfma_f32_16x16x32_bf16 a[52:55], v[52:55], v[68:71], a[52:55]
ds_read_b64_tr_b16 v[46:47], v21 offset:50688
v_mfma_f32_16x16x32_bf16 a[56:59], v[52:55], v[120:123], a[56:59]
v_add_u32 v23, s23, v23
v_mfma_f32_16x16x32_bf16 a[60:63], v[52:55], v[124:127], a[60:63]
s_waitcnt vmcnt(16)
s_barrier
s_waitcnt lgkmcnt(6)
v_mfma_f32_16x16x32_bf16 a[64:67], v[56:59], v[64:67], a[64:67]
ds_read_b64_tr_b16 v[48:49], v10 offset:0
ds_read_b64_tr_b16 v[50:51], v10 offset:1024
v_mfma_f32_16x16x32_bf16 a[68:71], v[56:59], v[68:71], a[68:71]
s_add_u32 s28, s71, 2621440
s_add_u32 m0, s70, 32768
v_mfma_f32_16x16x32_bf16 a[72:75], v[56:59], v[120:123], a[72:75]
buffer_load_dwordx4 v29, s[76:79], s28 offen lds
s_add_u32 s28, s28, 31744
v_mfma_f32_16x16x32_bf16 a[76:79], v[56:59], v[124:127], a[76:79]
buffer_load_dwordx4 v29, s[76:79], s28 offen offset:1024 lds
v_and_b32 v23, 15, v23
s_waitcnt lgkmcnt(6)
v_mfma_f32_16x16x32_bf16 a[80:83], v[60:63], v[64:67], a[80:83]
ds_read_b64_tr_b16 v[52:53], v11 offset:0
ds_read_b64_tr_b16 v[54:55], v11 offset:1024
v_mfma_f32_16x16x32_bf16 a[84:87], v[60:63], v[68:71], a[84:87]
s_add_u32 s28, s73, 1146880
v_mfma_f32_16x16x32_bf16 a[88:91], v[60:63], v[120:123], a[88:91]
buffer_load_dwordx4 v30, s[80:83], s28 offen offset:2048 lds
v_mfma_f32_16x16x32_bf16 a[92:95], v[60:63], v[124:127], a[92:95]
v_lshlrev_b32 v23, 4, v23
s_waitcnt lgkmcnt(6)
v_mfma_f32_16x16x32_bf16 a[96:99], v[40:43], v[64:67], a[96:99]
ds_read_b64_tr_b16 v[56:57], v12 offset:0
ds_read_b64_tr_b16 v[58:59], v12 offset:1024
v_mfma_f32_16x16x32_bf16 a[100:103], v[40:43], v[68:71], a[100:103]
buffer_load_dwordx4 v30, s[84:87], s28 offen offset:3072 lds
v_mfma_f32_16x16x32_bf16 a[104:107], v[40:43], v[120:123], a[104:107]
buffer_store_dwordx4 v[236:239], v28, s[92:95], s31 offen offset:3072 sc1
v_mfma_f32_16x16x32_bf16 a[108:111], v[40:43], v[124:127], a[108:111]
v_lshlrev_b32 v31, 10, v26
s_waitcnt lgkmcnt(6)
v_mfma_f32_16x16x32_bf16 a[112:115], v[44:47], v[64:67], a[112:115]
ds_read_b64_tr_b16 v[60:61], v13 offset:0
v_mfma_f32_16x16x32_bf16 a[116:119], v[44:47], v[68:71], a[116:119]
ds_read_b64_tr_b16 v[62:63], v13 offset:1024
v_mfma_f32_16x16x32_bf16 a[120:123], v[44:47], v[120:123], a[120:123]
v_add_u32 v23, v23, v31
v_mfma_f32_16x16x32_bf16 a[124:127], v[44:47], v[124:127], a[124:127]
s_waitcnt lgkmcnt(6)
v_mfma_f32_16x16x32_bf16 a[128:131], v[48:51], v[88:91], a[128:131]
ds_read_b64_tr_b16 v[40:41], v14 offset:0
ds_read_b64_tr_b16 v[42:43], v14 offset:1024
v_mfma_f32_16x16x32_bf16 a[132:135], v[48:51], v[92:95], a[132:135]
buffer_load_dwordx4 v[64:67], v28, s[52:55], s8 offen offset:3072 sc1
v_mfma_f32_16x16x32_bf16 a[136:139], v[48:51], v[96:99], a[136:139]
buffer_load_dwordx4 v[68:71], v28, s[52:55], s9 offen offset:3072 sc1
v_mfma_f32_16x16x32_bf16 a[140:143], v[48:51], v[100:103], a[140:143]
v_add_u32 v23, 6144, v23
s_waitcnt lgkmcnt(6)
v_mfma_f32_16x16x32_bf16 a[144:147], v[52:55], v[88:91], a[144:147]
ds_read_b64_tr_b16 v[44:45], v15 offset:0
v_mfma_f32_16x16x32_bf16 a[148:151], v[52:55], v[92:95], a[148:151]
ds_read_b64_tr_b16 v[46:47], v15 offset:1024
v_mfma_f32_16x16x32_bf16 a[152:155], v[52:55], v[96:99], a[152:155]
buffer_load_dwordx4 v[120:123], v28, s[52:55], s10 offen offset:3072 sc1
v_mfma_f32_16x16x32_bf16 a[156:159], v[52:55], v[100:103], a[156:159]
v_add_u32 v34, s24, v23
s_waitcnt lgkmcnt(6)
v_mfma_f32_16x16x32_bf16 a[160:163], v[56:59], v[88:91], a[160:163]
ds_read_b64_tr_b16 v[48:49], v16 offset:0
v_mfma_f32_16x16x32_bf16 a[164:167], v[56:59], v[92:95], a[164:167]
ds_read_b64_tr_b16 v[50:51], v16 offset:1024
v_mfma_f32_16x16x32_bf16 a[168:171], v[56:59], v[96:99], a[168:171]
buffer_load_dwordx4 v[124:127], v28, s[52:55], s11 offen offset:3072 sc1
v_mfma_f32_16x16x32_bf16 a[172:175], v[56:59], v[100:103], a[172:175]
v_add_u32 v22, 4, v26
s_waitcnt lgkmcnt(6)
v_mfma_f32_16x16x32_bf16 a[176:179], v[60:63], v[88:91], a[176:179]
ds_read_b64_tr_b16 v[52:53], v17 offset:0
v_mfma_f32_16x16x32_bf16 a[180:183], v[60:63], v[92:95], a[180:183]
ds_read_b64_tr_b16 v[54:55], v17 offset:1024
v_mfma_f32_16x16x32_bf16 a[184:187], v[60:63], v[96:99], a[184:187]
v_lshlrev_b32 v22, 1, v22
v_mfma_f32_16x16x32_bf16 a[188:191], v[60:63], v[100:103], a[188:191]
s_waitcnt lgkmcnt(6)
v_mfma_f32_16x16x32_bf16 a[192:195], v[40:43], v[88:91], a[192:195]
ds_read_b64_tr_b16 v[56:57], v18 offset:0
v_mfma_f32_16x16x32_bf16 a[196:199], v[40:43], v[92:95], a[196:199]
ds_read_b64_tr_b16 v[58:59], v18 offset:512
v_mfma_f32_16x16x32_bf16 a[200:203], v[40:43], v[96:99], a[200:203]
v_and_b32 v23, 15, v1
v_mfma_f32_16x16x32_bf16 a[204:207], v[40:43], v[100:103], a[204:207]
s_waitcnt lgkmcnt(6)
v_mfma_f32_16x16x32_bf16 a[208:211], v[44:47], v[88:91], a[208:211]
ds_read_b64_tr_b16 v[60:61], v19 offset:0
ds_read_b64_tr_b16 v[62:63], v19 offset:512
v_mfma_f32_16x16x32_bf16 a[212:215], v[44:47], v[92:95], a[212:215]
s_mov_b32 s31, 536576
v_mfma_f32_16x16x32_bf16 a[216:219], v[44:47], v[96:99], a[216:219]
buffer_store_dwordx4 v[240:243], v28, s[92:95], s31 offen offset:0 sc1
v_xor_b32 v23, v23, v22
v_mfma_f32_16x16x32_bf16 a[220:223], v[44:47], v[100:103], a[220:223]
v_add_u32 v23, s23, v23
s_waitcnt lgkmcnt(6)
v_mfma_f32_16x16x32_bf16 a[224:227], v[48:51], v[88:91], a[224:227]
ds_read_b64_tr_b16 v[40:41], v20 offset:0
v_mfma_f32_16x16x32_bf16 a[228:231], v[48:51], v[92:95], a[228:231]
ds_read_b64_tr_b16 v[42:43], v20 offset:512
v_mfma_f32_16x16x32_bf16 a[232:235], v[48:51], v[96:99], a[232:235]
v_and_b32 v23, 15, v23
v_mfma_f32_16x16x32_bf16 a[236:239], v[48:51], v[100:103], a[236:239]
s_waitcnt lgkmcnt(6)
v_mfma_f32_16x16x32_bf16 a[240:243], v[52:55], v[88:91], a[240:243]
ds_read_b64_tr_b16 v[44:45], v21 offset:0
v_mfma_f32_16x16x32_bf16 a[244:247], v[52:55], v[92:95], a[244:247]
ds_read_b64_tr_b16 v[46:47], v21 offset:512
v_mfma_f32_16x16x32_bf16 a[248:251], v[52:55], v[96:99], a[248:251]
v_lshlrev_b32 v23, 4, v23
v_mfma_f32_16x16x32_bf16 a[252:255], v[52:55], v[100:103], a[252:255]
s_waitcnt lgkmcnt(6)
v_mfma_f32_16x16x32_bf16 a[0:3], v[56:59], v[88:91], a[0:3]
ds_read_b64_tr_b16 v[48:49], v18 offset:1024
v_mfma_f32_16x16x32_bf16 a[4:7], v[56:59], v[92:95], a[4:7]
ds_read_b64_tr_b16 v[50:51], v18 offset:1536
v_mfma_f32_16x16x32_bf16 a[8:11], v[56:59], v[96:99], a[8:11]
v_lshlrev_b32 v31, 10, v26
v_mfma_f32_16x16x32_bf16 a[12:15], v[56:59], v[100:103], a[12:15]
s_waitcnt lgkmcnt(6)
v_mfma_f32_16x16x32_bf16 a[16:19], v[60:63], v[88:91], a[16:19]
ds_read_b64_tr_b16 v[52:53], v19 offset:1024
v_mfma_f32_16x16x32_bf16 a[20:23], v[60:63], v[92:95], a[20:23]
ds_read_b64_tr_b16 v[54:55], v19 offset:1536
v_mfma_f32_16x16x32_bf16 a[24:27], v[60:63], v[96:99], a[24:27]
v_add_u32 v23, v23, v31
v_mfma_f32_16x16x32_bf16 a[28:31], v[60:63], v[100:103], a[28:31]
s_waitcnt lgkmcnt(6)
v_mfma_f32_16x16x32_bf16 a[32:35], v[40:43], v[88:91], a[32:35]
ds_read_b64_tr_b16 v[56:57], v20 offset:1024
v_mfma_f32_16x16x32_bf16 a[36:39], v[40:43], v[92:95], a[36:39]
ds_read_b64_tr_b16 v[58:59], v20 offset:1536
v_mfma_f32_16x16x32_bf16 a[40:43], v[40:43], v[96:99], a[40:43]
v_add_u32 v23, 9216, v23
v_mfma_f32_16x16x32_bf16 a[44:47], v[40:43], v[100:103], a[44:47]
s_waitcnt lgkmcnt(6)
v_mfma_f32_16x16x32_bf16 a[48:51], v[44:47], v[88:91], a[48:51]
ds_read_b64_tr_b16 v[60:61], v21 offset:1024
v_mfma_f32_16x16x32_bf16 a[52:55], v[44:47], v[92:95], a[52:55]
ds_read_b64_tr_b16 v[62:63], v21 offset:1536
v_mfma_f32_16x16x32_bf16 a[56:59], v[44:47], v[96:99], a[56:59]
v_add_u32 v35, s24, v23
v_mfma_f32_16x16x32_bf16 a[60:63], v[44:47], v[100:103], a[60:63]
s_waitcnt vmcnt(16)
s_barrier
s_waitcnt lgkmcnt(6)
v_mfma_f32_16x16x32_bf16 a[64:67], v[48:51], v[88:91], a[64:67]
ds_read_b64_tr_b16 v[40:41], v10 offset:16384
ds_read_b64_tr_b16 v[42:43], v10 offset:17408
s_add_u32 s28, s71, 2883584
v_mfma_f32_16x16x32_bf16 a[68:71], v[48:51], v[92:95], a[68:71]
s_add_u32 m0, s70, 49152
s_nop 0
buffer_load_dwordx4 v29, s[76:79], s28 offen lds
v_mfma_f32_16x16x32_bf16 a[72:75], v[48:51], v[96:99], a[72:75]
s_add_u32 s28, s28, 31744
buffer_load_dwordx4 v29, s[76:79], s28 offen offset:1024 lds
v_mfma_f32_16x16x32_bf16 a[76:79], v[48:51], v[100:103], a[76:79]
buffer_store_dwordx4 v[244:247], v28, s[92:95], s31 offen offset:1024 sc1
v_and_b32 v27, 15, v1
s_waitcnt lgkmcnt(6)
v_mfma_f32_16x16x32_bf16 a[80:83], v[52:55], v[88:91], a[80:83]
ds_read_b64_tr_b16 v[44:45], v11 offset:16384
ds_read_b64_tr_b16 v[46:47], v11 offset:17408
v_mfma_f32_16x16x32_bf16 a[84:87], v[52:55], v[92:95], a[84:87]
s_add_u32 s28, s73, 1261568
v_mfma_f32_16x16x32_bf16 a[88:91], v[52:55], v[96:99], a[88:91]
buffer_load_dwordx4 v30, s[80:83], s28 offen offset:2048 lds
v_mfma_f32_16x16x32_bf16 a[92:95], v[52:55], v[100:103], a[92:95]
v_lshrrev_b32 v27, 2, v27
s_waitcnt lgkmcnt(6)
v_mfma_f32_16x16x32_bf16 a[96:99], v[56:59], v[88:91], a[96:99]
ds_read_b64_tr_b16 v[48:49], v12 offset:16384
v_mfma_f32_16x16x32_bf16 a[100:103], v[56:59], v[92:95], a[100:103]
ds_read_b64_tr_b16 v[50:51], v12 offset:17408
v_mfma_f32_16x16x32_bf16 a[104:107], v[56:59], v[96:99], a[104:107]
buffer_load_dwordx4 v30, s[84:87], s28 offen offset:3072 lds
v_mfma_f32_16x16x32_bf16 a[108:111], v[56:59], v[100:103], a[108:111]
v_lshlrev_b32 v22, 2, v26
s_waitcnt lgkmcnt(6)
v_mfma_f32_16x16x32_bf16 a[112:115], v[60:63], v[88:91], a[112:115]
ds_read_b64_tr_b16 v[52:53], v13 offset:16384
v_mfma_f32_16x16x32_bf16 a[116:119], v[60:63], v[92:95], a[116:119]
ds_read_b64_tr_b16 v[54:55], v13 offset:17408
v_mfma_f32_16x16x32_bf16 a[120:123], v[60:63], v[96:99], a[120:123]
v_add_u32 v22, v22, v27
v_mfma_f32_16x16x32_bf16 a[124:127], v[60:63], v[100:103], a[124:127]
s_waitcnt lgkmcnt(6)
v_mfma_f32_16x16x32_bf16 a[128:131], v[40:43], v[72:75], a[128:131]
ds_read_b64_tr_b16 v[56:57], v14 offset:16384
ds_read_b64_tr_b16 v[58:59], v14 offset:17408
v_mfma_f32_16x16x32_bf16 a[132:135], v[40:43], v[76:79], a[132:135]
s_mov_b32 s8, 12288
buffer_load_dwordx4 v[88:91], v28, s[52:55], s8 offen offset:0 sc1
v_mfma_f32_16x16x32_bf16 a[136:139], v[40:43], v[80:83], a[136:139]
s_mov_b32 s9, 45056
buffer_load_dwordx4 v[92:95], v28, s[52:55], s9 offen offset:0 sc1
v_mfma_f32_16x16x32_bf16 a[140:143], v[40:43], v[84:87], a[140:143]
v_and_b32 v25, 7, v22
s_waitcnt lgkmcnt(6)
v_mfma_f32_16x16x32_bf16 a[144:147], v[44:47], v[72:75], a[144:147]
ds_read_b64_tr_b16 v[60:61], v15 offset:16384
ds_read_b64_tr_b16 v[62:63], v15 offset:17408
v_mfma_f32_16x16x32_bf16 a[148:151], v[44:47], v[76:79], a[148:151]
s_mov_b32 s10, 77824
v_mfma_f32_16x16x32_bf16 a[152:155], v[44:47], v[80:83], a[152:155]
buffer_load_dwordx4 v[96:99], v28, s[52:55], s10 offen offset:0 sc1
v_mfma_f32_16x16x32_bf16 a[156:159], v[44:47], v[84:87], a[156:159]
v_lshlrev_b32 v22, 8, v22
s_waitcnt lgkmcnt(6)
v_mfma_f32_16x16x32_bf16 a[160:163], v[48:51], v[72:75], a[160:163]
ds_read_b64_tr_b16 v[40:41], v16 offset:16384
ds_read_b64_tr_b16 v[42:43], v16 offset:17408
v_mfma_f32_16x16x32_bf16 a[164:167], v[48:51], v[76:79], a[164:167]
s_mov_b32 s11, 110592
v_mfma_f32_16x16x32_bf16 a[168:171], v[48:51], v[80:83], a[168:171]
buffer_load_dwordx4 v[100:103], v28, s[52:55], s11 offen offset:0 sc1
buffer_store_dwordx4 v[248:251], v28, s[92:95], s31 offen offset:2048 sc1
v_mfma_f32_16x16x32_bf16 a[172:175], v[48:51], v[84:87], a[172:175]
v_and_b32 v23, 3, v1
s_waitcnt lgkmcnt(6)
v_mfma_f32_16x16x32_bf16 a[176:179], v[52:55], v[72:75], a[176:179]
ds_read_b64_tr_b16 v[44:45], v17 offset:16384
v_mfma_f32_16x16x32_bf16 a[180:183], v[52:55], v[76:79], a[180:183]
ds_read_b64_tr_b16 v[46:47], v17 offset:17408
v_mfma_f32_16x16x32_bf16 a[184:187], v[52:55], v[80:83], a[184:187]
v_lshlrev_b32 v23, 3, v23
v_mfma_f32_16x16x32_bf16 a[188:191], v[52:55], v[84:87], a[188:191]
s_waitcnt lgkmcnt(6)
v_mfma_f32_16x16x32_bf16 a[192:195], v[56:59], v[72:75], a[192:195]
ds_read_b64_tr_b16 v[48:49], v18 offset:16384
v_mfma_f32_16x16x32_bf16 a[196:199], v[56:59], v[76:79], a[196:199]
ds_read_b64_tr_b16 v[50:51], v18 offset:16896
v_mfma_f32_16x16x32_bf16 a[200:203], v[56:59], v[80:83], a[200:203]
v_add_u32 v22, v22, v23
v_mfma_f32_16x16x32_bf16 a[204:207], v[56:59], v[84:87], a[204:207]
s_waitcnt lgkmcnt(6)
v_mfma_f32_16x16x32_bf16 a[208:211], v[60:63], v[72:75], a[208:211]
ds_read_b64_tr_b16 v[52:53], v19 offset:16384
v_mfma_f32_16x16x32_bf16 a[212:215], v[60:63], v[76:79], a[212:215]
ds_read_b64_tr_b16 v[54:55], v19 offset:16896
v_mfma_f32_16x16x32_bf16 a[216:219], v[60:63], v[80:83], a[216:219]
v_lshlrev_b32 v23, 5, v25
v_mfma_f32_16x16x32_bf16 a[220:223], v[60:63], v[84:87], a[220:223]
s_waitcnt lgkmcnt(6)
v_mfma_f32_16x16x32_bf16 a[224:227], v[40:43], v[72:75], a[224:227]
ds_read_b64_tr_b16 v[56:57], v20 offset:16384
v_mfma_f32_16x16x32_bf16 a[228:231], v[40:43], v[76:79], a[228:231]
ds_read_b64_tr_b16 v[58:59], v20 offset:16896
v_mfma_f32_16x16x32_bf16 a[232:235], v[40:43], v[80:83], a[232:235]
v_add_u32 v36, v22, v23
v_mfma_f32_16x16x32_bf16 a[236:239], v[40:43], v[84:87], a[236:239]
s_waitcnt lgkmcnt(6)
v_mfma_f32_16x16x32_bf16 a[240:243], v[44:47], v[72:75], a[240:243]
ds_read_b64_tr_b16 v[60:61], v21 offset:16384
v_mfma_f32_16x16x32_bf16 a[244:247], v[44:47], v[76:79], a[244:247]
ds_read_b64_tr_b16 v[62:63], v21 offset:16896
v_mfma_f32_16x16x32_bf16 a[248:251], v[44:47], v[80:83], a[248:251]
v_and_b32 v22, 7, v1
v_mfma_f32_16x16x32_bf16 a[252:255], v[44:47], v[84:87], a[252:255]
s_waitcnt lgkmcnt(6)
v_mfma_f32_16x16x32_bf16 a[0:3], v[48:51], v[72:75], a[0:3]
ds_read_b64_tr_b16 v[40:41], v18 offset:17408
v_mfma_f32_16x16x32_bf16 a[4:7], v[48:51], v[76:79], a[4:7]
ds_read_b64_tr_b16 v[42:43], v18 offset:17920
v_mfma_f32_16x16x32_bf16 a[8:11], v[48:51], v[80:83], a[8:11]
v_min_u32 v23, 6, v22
v_mfma_f32_16x16x32_bf16 a[12:15], v[48:51], v[84:87], a[12:15]
s_waitcnt lgkmcnt(6)
v_mfma_f32_16x16x32_bf16 a[16:19], v[52:55], v[72:75], a[16:19]
ds_read_b64_tr_b16 v[44:45], v19 offset:17408
v_mfma_f32_16x16x32_bf16 a[20:23], v[52:55], v[76:79], a[20:23]
ds_read_b64_tr_b16 v[46:47], v19 offset:17920
v_mfma_f32_16x16x32_bf16 a[24:27], v[52:55], v[80:83], a[24:27]
buffer_store_dwordx4 v[252:255], v28, s[92:95], s31 offen offset:3072 sc1
v_mfma_f32_16x16x32_bf16 a[28:31], v[52:55], v[84:87], a[28:31]
s_mul_i32 s22, s25, 7
s_waitcnt lgkmcnt(6)
v_mfma_f32_16x16x32_bf16 a[32:35], v[56:59], v[72:75], a[32:35]
ds_read_b64_tr_b16 v[48:49], v20 offset:17408
v_mfma_f32_16x16x32_bf16 a[36:39], v[56:59], v[76:79], a[36:39]
ds_read_b64_tr_b16 v[50:51], v20 offset:17920
v_mfma_f32_16x16x32_bf16 a[40:43], v[56:59], v[80:83], a[40:43]
v_add_u32 v23, s22, v23
v_mfma_f32_16x16x32_bf16 a[44:47], v[56:59], v[84:87], a[44:47]
s_waitcnt lgkmcnt(6)
v_mfma_f32_16x16x32_bf16 a[48:51], v[60:63], v[72:75], a[48:51]
ds_read_b64_tr_b16 v[52:53], v21 offset:17408
v_mfma_f32_16x16x32_bf16 a[52:55], v[60:63], v[76:79], a[52:55]
ds_read_b64_tr_b16 v[54:55], v21 offset:17920
v_mfma_f32_16x16x32_bf16 a[56:59], v[60:63], v[80:83], a[56:59]
s_sub_u32 s22, 15, s26
v_mfma_f32_16x16x32_bf16 a[60:63], v[60:63], v[84:87], a[60:63]
s_waitcnt vmcnt(17)
s_barrier
s_waitcnt lgkmcnt(6)
v_mfma_f32_16x16x32_bf16 a[64:67], v[40:43], v[72:75], a[64:67]
ds_read_b64_tr_b16 v[56:57], v10 offset:32768
ds_read_b64_tr_b16 v[58:59], v10 offset:33792
v_mfma_f32_16x16x32_bf16 a[68:71], v[40:43], v[76:79], a[68:71]
s_add_u32 s28, s71, 3145728
s_add_u32 m0, s70, 0
v_mfma_f32_16x16x32_bf16 a[72:75], v[40:43], v[80:83], a[72:75]
buffer_load_dwordx4 v29, s[76:79], s28 offen lds
s_add_u32 s28, s28, 31744
v_mfma_f32_16x16x32_bf16 a[76:79], v[40:43], v[84:87], a[76:79]
buffer_load_dwordx4 v29, s[76:79], s28 offen offset:1024 lds
s_min_u32 s22, s22, s26
s_waitcnt lgkmcnt(6)
v_mfma_f32_16x16x32_bf16 a[80:83], v[44:47], v[72:75], a[80:83]
ds_read_b64_tr_b16 v[60:61], v11 offset:32768
ds_read_b64_tr_b16 v[62:63], v11 offset:33792
v_mfma_f32_16x16x32_bf16 a[84:87], v[44:47], v[76:79], a[84:87]
s_add_u32 s28, s73, 1376256
v_mfma_f32_16x16x32_bf16 a[88:91], v[44:47], v[80:83], a[88:91]
buffer_load_dwordx4 v30, s[80:83], s28 offen offset:2048 lds
v_mfma_f32_16x16x32_bf16 a[92:95], v[44:47], v[84:87], a[92:95]
s_and_b32 s23, s22, 4
s_waitcnt lgkmcnt(6)
v_mfma_f32_16x16x32_bf16 a[96:99], v[48:51], v[72:75], a[96:99]
ds_read_b64_tr_b16 v[40:41], v12 offset:32768
v_mfma_f32_16x16x32_bf16 a[100:103], v[48:51], v[76:79], a[100:103]
ds_read_b64_tr_b16 v[42:43], v12 offset:33792
v_mfma_f32_16x16x32_bf16 a[104:107], v[48:51], v[80:83], a[104:107]
buffer_load_dwordx4 v30, s[84:87], s28 offen offset:3072 lds
v_mfma_f32_16x16x32_bf16 a[108:111], v[48:51], v[84:87], a[108:111]
s_xor_b32 s23, s23, 4
s_waitcnt lgkmcnt(6)
v_mfma_f32_16x16x32_bf16 a[112:115], v[52:55], v[72:75], a[112:115]
ds_read_b64_tr_b16 v[44:45], v13 offset:32768
v_mfma_f32_16x16x32_bf16 a[116:119], v[52:55], v[76:79], a[116:119]
ds_read_b64_tr_b16 v[46:47], v13 offset:33792
v_mfma_f32_16x16x32_bf16 a[120:123], v[52:55], v[80:83], a[120:123]
v_add_u32 v37, s23, v23
v_mfma_f32_16x16x32_bf16 a[124:127], v[52:55], v[84:87], a[124:127]
s_waitcnt lgkmcnt(6)
v_mfma_f32_16x16x32_bf16 a[128:131], v[56:59], v[104:107], a[128:131]
ds_read_b64_tr_b16 v[48:49], v14 offset:32768
ds_read_b64_tr_b16 v[50:51], v14 offset:33792
v_mfma_f32_16x16x32_bf16 a[132:135], v[56:59], v[108:111], a[132:135]
buffer_load_dwordx4 v[128:131], v28, s[52:55], s8 offen offset:1024 sc1
v_mfma_f32_16x16x32_bf16 a[136:139], v[56:59], v[112:115], a[136:139]
buffer_load_dwordx4 v[132:135], v28, s[52:55], s9 offen offset:1024 sc1
v_mfma_f32_16x16x32_bf16 a[140:143], v[56:59], v[116:119], a[140:143]
v_mul_u32_u24 v37, 37, v37
s_waitcnt lgkmcnt(6)
v_mfma_f32_16x16x32_bf16 a[144:147], v[60:63], v[104:107], a[144:147]
ds_read_b64_tr_b16 v[52:53], v15 offset:32768
v_mfma_f32_16x16x32_bf16 a[148:151], v[60:63], v[108:111], a[148:151]
ds_read_b64_tr_b16 v[54:55], v15 offset:33792
v_mfma_f32_16x16x32_bf16 a[152:155], v[60:63], v[112:115], a[152:155]
buffer_load_dwordx4 v[136:139], v28, s[52:55], s10 offen offset:1024 sc1
v_mfma_f32_16x16x32_bf16 a[156:159], v[60:63], v[116:119], a[156:159]
v_lshrrev_b32 v37, 8, v37
s_waitcnt lgkmcnt(6)
v_mfma_f32_16x16x32_bf16 a[160:163], v[40:43], v[104:107], a[160:163]
ds_read_b64_tr_b16 v[56:57], v16 offset:32768
v_mfma_f32_16x16x32_bf16 a[164:167], v[40:43], v[108:111], a[164:167]
ds_read_b64_tr_b16 v[58:59], v16 offset:33792
v_mfma_f32_16x16x32_bf16 a[168:171], v[40:43], v[112:115], a[168:171]
buffer_load_dwordx4 v[140:143], v28, s[52:55], s11 offen offset:1024 sc1
v_mfma_f32_16x16x32_bf16 a[172:175], v[40:43], v[116:119], a[172:175]
v_add_u32 v23, v23, v37
s_waitcnt lgkmcnt(6)
v_mfma_f32_16x16x32_bf16 a[176:179], v[44:47], v[104:107], a[176:179]
ds_read_b64_tr_b16 v[60:61], v17 offset:32768
v_mfma_f32_16x16x32_bf16 a[180:183], v[44:47], v[108:111], a[180:183]
ds_read_b64_tr_b16 v[62:63], v17 offset:33792
v_mfma_f32_16x16x32_bf16 a[184:187], v[44:47], v[112:115], a[184:187]
v_and_b32 v37, 3, v23
v_mfma_f32_16x16x32_bf16 a[188:191], v[44:47], v[116:119], a[188:191]
s_waitcnt lgkmcnt(6)
v_mfma_f32_16x16x32_bf16 a[192:195], v[48:51], v[104:107], a[192:195]
ds_read_b64_tr_b16 v[40:41], v18 offset:32768
v_mfma_f32_16x16x32_bf16 a[196:199], v[48:51], v[108:111], a[196:199]
ds_read_b64_tr_b16 v[42:43], v18 offset:33280
v_mfma_f32_16x16x32_bf16 a[200:203], v[48:51], v[112:115], a[200:203]
v_lshlrev_b32 v37, 3, v37
v_mfma_f32_16x16x32_bf16 a[204:207], v[48:51], v[116:119], a[204:207]
s_waitcnt lgkmcnt(6)
v_mfma_f32_16x16x32_bf16 a[208:211], v[52:55], v[104:107], a[208:211]
ds_read_b64_tr_b16 v[44:45], v19 offset:32768
v_mfma_f32_16x16x32_bf16 a[212:215], v[52:55], v[108:111], a[212:215]
ds_read_b64_tr_b16 v[46:47], v19 offset:33280
v_mfma_f32_16x16x32_bf16 a[216:219], v[52:55], v[112:115], a[216:219]
v_add_u32 v37, s22, v37
v_mfma_f32_16x16x32_bf16 a[220:223], v[52:55], v[116:119], a[220:223]
s_waitcnt lgkmcnt(6)
v_mfma_f32_16x16x32_bf16 a[224:227], v[56:59], v[104:107], a[224:227]
ds_read_b64_tr_b16 v[48:49], v20 offset:32768
v_mfma_f32_16x16x32_bf16 a[228:231], v[56:59], v[108:111], a[228:231]
ds_read_b64_tr_b16 v[50:51], v20 offset:33280
v_mfma_f32_16x16x32_bf16 a[232:235], v[56:59], v[112:115], a[232:235]
v_lshlrev_b32 v37, 3, v37
v_mfma_f32_16x16x32_bf16 a[236:239], v[56:59], v[116:119], a[236:239]
s_waitcnt lgkmcnt(6)
v_mfma_f32_16x16x32_bf16 a[240:243], v[60:63], v[104:107], a[240:243]
ds_read_b64_tr_b16 v[52:53], v21 offset:32768
v_mfma_f32_16x16x32_bf16 a[244:247], v[60:63], v[108:111], a[244:247]
ds_read_b64_tr_b16 v[54:55], v21 offset:33280
v_mfma_f32_16x16x32_bf16 a[248:251], v[60:63], v[112:115], a[248:251]
v_lshrrev_b32 v23, 2, v23
v_mfma_f32_16x16x32_bf16 a[252:255], v[60:63], v[116:119], a[252:255]
s_waitcnt lgkmcnt(6)
v_mfma_f32_16x16x32_bf16 a[0:3], v[40:43], v[104:107], a[0:3]
ds_read_b64_tr_b16 v[56:57], v18 offset:33792
v_mfma_f32_16x16x32_bf16 a[4:7], v[40:43], v[108:111], a[4:7]
ds_read_b64_tr_b16 v[58:59], v18 offset:34304
v_mfma_f32_16x16x32_bf16 a[8:11], v[40:43], v[112:115], a[8:11]
v_add_u32 v23, v23, v37
v_mfma_f32_16x16x32_bf16 a[12:15], v[40:43], v[116:119], a[12:15]
s_waitcnt lgkmcnt(6)
v_mfma_f32_16x16x32_bf16 a[16:19], v[44:47], v[104:107], a[16:19]
)ASM"
R"ASM(ds_read_b64_tr_b16 v[60:61], v19 offset:33792
v_mfma_f32_16x16x32_bf16 a[20:23], v[44:47], v[108:111], a[20:23]
ds_read_b64_tr_b16 v[62:63], v19 offset:34304
v_mfma_f32_16x16x32_bf16 a[24:27], v[44:47], v[112:115], a[24:27]
v_lshlrev_b32 v23, 6, v23
v_mfma_f32_16x16x32_bf16 a[28:31], v[44:47], v[116:119], a[28:31]
s_waitcnt lgkmcnt(6)
v_mfma_f32_16x16x32_bf16 a[32:35], v[48:51], v[104:107], a[32:35]
ds_read_b64_tr_b16 v[40:41], v20 offset:33792
v_mfma_f32_16x16x32_bf16 a[36:39], v[48:51], v[108:111], a[36:39]
ds_read_b64_tr_b16 v[42:43], v20 offset:34304
v_mfma_f32_16x16x32_bf16 a[40:43], v[48:51], v[112:115], a[40:43]
v_add_u32 v37, 49152, v23
v_mfma_f32_16x16x32_bf16 a[44:47], v[48:51], v[116:119], a[44:47]
s_waitcnt lgkmcnt(6)
v_mfma_f32_16x16x32_bf16 a[48:51], v[52:55], v[104:107], a[48:51]
ds_read_b64_tr_b16 v[44:45], v21 offset:33792
v_mfma_f32_16x16x32_bf16 a[52:55], v[52:55], v[108:111], a[52:55]
ds_read_b64_tr_b16 v[46:47], v21 offset:34304
v_mfma_f32_16x16x32_bf16 a[56:59], v[52:55], v[112:115], a[56:59]
s_lshl_b32 s23, s25, 3
v_mfma_f32_16x16x32_bf16 a[60:63], v[52:55], v[116:119], a[60:63]
s_waitcnt vmcnt(14)
s_barrier
s_waitcnt lgkmcnt(6)
v_mfma_f32_16x16x32_bf16 a[64:67], v[56:59], v[104:107], a[64:67]
ds_read_b64_tr_b16 v[48:49], v10 offset:49152
ds_read_b64_tr_b16 v[50:51], v10 offset:50176
v_mfma_f32_16x16x32_bf16 a[68:71], v[56:59], v[108:111], a[68:71]
s_add_u32 s28, s71, 3407872
s_add_u32 m0, s70, 16384
v_mfma_f32_16x16x32_bf16 a[72:75], v[56:59], v[112:115], a[72:75]
buffer_load_dwordx4 v29, s[76:79], s28 offen lds
s_add_u32 s28, s28, 31744
v_mfma_f32_16x16x32_bf16 a[76:79], v[56:59], v[116:119], a[76:79]
buffer_load_dwordx4 v29, s[76:79], s28 offen offset:1024 lds
v_add_u32 v22, s23, v22
s_waitcnt lgkmcnt(6)
v_mfma_f32_16x16x32_bf16 a[80:83], v[60:63], v[104:107], a[80:83]
ds_read_b64_tr_b16 v[52:53], v11 offset:49152
ds_read_b64_tr_b16 v[54:55], v11 offset:50176
v_mfma_f32_16x16x32_bf16 a[84:87], v[60:63], v[108:111], a[84:87]
s_add_u32 s28, s73, 1490944
v_mfma_f32_16x16x32_bf16 a[88:91], v[60:63], v[112:115], a[88:91]
buffer_load_dwordx4 v30, s[80:83], s28 offen offset:2048 lds
v_mfma_f32_16x16x32_bf16 a[92:95], v[60:63], v[116:119], a[92:95]
v_and_b32 v23, 3, v22
s_waitcnt lgkmcnt(6)
v_mfma_f32_16x16x32_bf16 a[96:99], v[40:43], v[104:107], a[96:99]
ds_read_b64_tr_b16 v[56:57], v12 offset:49152
v_mfma_f32_16x16x32_bf16 a[100:103], v[40:43], v[108:111], a[100:103]
ds_read_b64_tr_b16 v[58:59], v12 offset:50176
v_mfma_f32_16x16x32_bf16 a[104:107], v[40:43], v[112:115], a[104:107]
buffer_load_dwordx4 v30, s[84:87], s28 offen offset:3072 lds
v_mfma_f32_16x16x32_bf16 a[108:111], v[40:43], v[116:119], a[108:111]
v_lshlrev_b32 v23, 3, v23
s_waitcnt lgkmcnt(6)
v_mfma_f32_16x16x32_bf16 a[112:115], v[44:47], v[104:107], a[112:115]
ds_read_b64_tr_b16 v[60:61], v13 offset:49152
v_mfma_f32_16x16x32_bf16 a[116:119], v[44:47], v[108:111], a[116:119]
ds_read_b64_tr_b16 v[62:63], v13 offset:50176
v_mfma_f32_16x16x32_bf16 a[120:123], v[44:47], v[112:115], a[120:123]
v_add_u32 v23, s22, v23
v_mfma_f32_16x16x32_bf16 a[124:127], v[44:47], v[116:119], a[124:127]
s_waitcnt vmcnt(12)
s_waitcnt lgkmcnt(6)
v_mfma_f32_16x16x32_bf16 a[128:131], v[48:51], v[64:67], a[128:131]
ds_read_b64_tr_b16 v[40:41], v14 offset:49152
ds_read_b64_tr_b16 v[42:43], v14 offset:50176
v_mfma_f32_16x16x32_bf16 a[132:135], v[48:51], v[68:71], a[132:135]
buffer_load_dwordx4 v[144:147], v28, s[52:55], s8 offen offset:2048 sc1
v_mfma_f32_16x16x32_bf16 a[136:139], v[48:51], v[120:123], a[136:139]
buffer_load_dwordx4 v[148:151], v28, s[52:55], s9 offen offset:2048 sc1
v_mfma_f32_16x16x32_bf16 a[140:143], v[48:51], v[124:127], a[140:143]
v_lshlrev_b32 v23, 3, v23
s_waitcnt lgkmcnt(6)
v_mfma_f32_16x16x32_bf16 a[144:147], v[52:55], v[64:67], a[144:147]
ds_read_b64_tr_b16 v[44:45], v15 offset:49152
v_mfma_f32_16x16x32_bf16 a[148:151], v[52:55], v[68:71], a[148:151]
ds_read_b64_tr_b16 v[46:47], v15 offset:50176
v_mfma_f32_16x16x32_bf16 a[152:155], v[52:55], v[120:123], a[152:155]
buffer_load_dwordx4 v[152:155], v28, s[52:55], s10 offen offset:2048 sc1
v_mfma_f32_16x16x32_bf16 a[156:159], v[52:55], v[124:127], a[156:159]
v_lshrrev_b32 v22, 2, v22
s_waitcnt lgkmcnt(6)
v_mfma_f32_16x16x32_bf16 a[160:163], v[56:59], v[64:67], a[160:163]
ds_read_b64_tr_b16 v[48:49], v16 offset:49152
v_mfma_f32_16x16x32_bf16 a[164:167], v[56:59], v[68:71], a[164:167]
ds_read_b64_tr_b16 v[50:51], v16 offset:50176
v_mfma_f32_16x16x32_bf16 a[168:171], v[56:59], v[120:123], a[168:171]
buffer_load_dwordx4 v[156:159], v28, s[52:55], s11 offen offset:2048 sc1
v_mfma_f32_16x16x32_bf16 a[172:175], v[56:59], v[124:127], a[172:175]
v_add_u32 v23, v23, v22
s_waitcnt lgkmcnt(6)
v_mfma_f32_16x16x32_bf16 a[176:179], v[60:63], v[64:67], a[176:179]
ds_read_b64_tr_b16 v[52:53], v17 offset:49152
v_mfma_f32_16x16x32_bf16 a[180:183], v[60:63], v[68:71], a[180:183]
ds_read_b64_tr_b16 v[54:55], v17 offset:50176
v_mfma_f32_16x16x32_bf16 a[184:187], v[60:63], v[120:123], a[184:187]
v_lshlrev_b32 v23, 6, v23
v_mfma_f32_16x16x32_bf16 a[188:191], v[60:63], v[124:127], a[188:191]
s_waitcnt lgkmcnt(6)
v_mfma_f32_16x16x32_bf16 a[192:195], v[40:43], v[64:67], a[192:195]
ds_read_b64_tr_b16 v[56:57], v18 offset:49152
v_mfma_f32_16x16x32_bf16 a[196:199], v[40:43], v[68:71], a[196:199]
ds_read_b64_tr_b16 v[58:59], v18 offset:49664
v_mfma_f32_16x16x32_bf16 a[200:203], v[40:43], v[120:123], a[200:203]
v_add_u32 v38, 65536, v23
v_mfma_f32_16x16x32_bf16 a[204:207], v[40:43], v[124:127], a[204:207]
s_waitcnt lgkmcnt(6)
v_mfma_f32_16x16x32_bf16 a[208:211], v[44:47], v[64:67], a[208:211]
ds_read_b64_tr_b16 v[60:61], v19 offset:49152
v_mfma_f32_16x16x32_bf16 a[212:215], v[44:47], v[68:71], a[212:215]
ds_read_b64_tr_b16 v[62:63], v19 offset:49664
v_mfma_f32_16x16x32_bf16 a[216:219], v[44:47], v[120:123], a[216:219]
v_and_b32 v22, 3, v1
v_mfma_f32_16x16x32_bf16 a[220:223], v[44:47], v[124:127], a[220:223]
s_waitcnt lgkmcnt(6)
v_mfma_f32_16x16x32_bf16 a[224:227], v[48:51], v[64:67], a[224:227]
ds_read_b64_tr_b16 v[40:41], v20 offset:49152
v_mfma_f32_16x16x32_bf16 a[228:231], v[48:51], v[68:71], a[228:231]
ds_read_b64_tr_b16 v[42:43], v20 offset:49664
v_mfma_f32_16x16x32_bf16 a[232:235], v[48:51], v[120:123], a[232:235]
s_and_b32 s22, s6, 0xfffffffc
v_mfma_f32_16x16x32_bf16 a[236:239], v[48:51], v[124:127], a[236:239]
s_waitcnt lgkmcnt(6)
v_mfma_f32_16x16x32_bf16 a[240:243], v[52:55], v[64:67], a[240:243]
ds_read_b64_tr_b16 v[44:45], v21 offset:49152
v_mfma_f32_16x16x32_bf16 a[244:247], v[52:55], v[68:71], a[244:247]
ds_read_b64_tr_b16 v[46:47], v21 offset:49664
v_mfma_f32_16x16x32_bf16 a[248:251], v[52:55], v[120:123], a[248:251]
v_or_b32 v22, s22, v22
v_mfma_f32_16x16x32_bf16 a[252:255], v[52:55], v[124:127], a[252:255]
s_waitcnt lgkmcnt(6)
v_mfma_f32_16x16x32_bf16 a[0:3], v[56:59], v[64:67], a[0:3]
ds_read_b64_tr_b16 v[48:49], v18 offset:50176
v_mfma_f32_16x16x32_bf16 a[4:7], v[56:59], v[68:71], a[4:7]
ds_read_b64_tr_b16 v[50:51], v18 offset:50688
v_mfma_f32_16x16x32_bf16 a[8:11], v[56:59], v[120:123], a[8:11]
v_lshlrev_b32 v22, 6, v22
v_mfma_f32_16x16x32_bf16 a[12:15], v[56:59], v[124:127], a[12:15]
s_waitcnt lgkmcnt(6)
v_mfma_f32_16x16x32_bf16 a[16:19], v[60:63], v[64:67], a[16:19]
ds_read_b64_tr_b16 v[52:53], v19 offset:50176
v_mfma_f32_16x16x32_bf16 a[20:23], v[60:63], v[68:71], a[20:23]
ds_read_b64_tr_b16 v[54:55], v19 offset:50688
v_mfma_f32_16x16x32_bf16 a[24:27], v[60:63], v[120:123], a[24:27]
v_add_u32 v22, 49152, v22
v_mfma_f32_16x16x32_bf16 a[28:31], v[60:63], v[124:127], a[28:31]
s_waitcnt lgkmcnt(6)
v_mfma_f32_16x16x32_bf16 a[32:35], v[40:43], v[64:67], a[32:35]
ds_read_b64_tr_b16 v[56:57], v20 offset:50176
v_mfma_f32_16x16x32_bf16 a[36:39], v[40:43], v[68:71], a[36:39]
ds_read_b64_tr_b16 v[58:59], v20 offset:50688
v_mfma_f32_16x16x32_bf16 a[40:43], v[40:43], v[120:123], a[40:43]
v_cmp_gt_u32 vcc, 8, v1
v_mfma_f32_16x16x32_bf16 a[44:47], v[40:43], v[124:127], a[44:47]
s_nop 0
v_cndmask_b32 v38, v22, v38
s_waitcnt lgkmcnt(6)
v_mfma_f32_16x16x32_bf16 a[48:51], v[44:47], v[64:67], a[48:51]
ds_read_b64_tr_b16 v[60:61], v21 offset:50176
v_mfma_f32_16x16x32_bf16 a[52:55], v[44:47], v[68:71], a[52:55]
ds_read_b64_tr_b16 v[62:63], v21 offset:50688
v_mfma_f32_16x16x32_bf16 a[56:59], v[44:47], v[120:123], a[56:59]
v_lshrrev_b32 v22, 2, v1
v_mfma_f32_16x16x32_bf16 a[60:63], v[44:47], v[124:127], a[60:63]
s_waitcnt vmcnt(12)
s_barrier
s_waitcnt lgkmcnt(6)
v_mfma_f32_16x16x32_bf16 a[64:67], v[48:51], v[64:67], a[64:67]
ds_read_b64_tr_b16 v[40:41], v10 offset:0
ds_read_b64_tr_b16 v[42:43], v10 offset:1024
v_mfma_f32_16x16x32_bf16 a[68:71], v[48:51], v[68:71], a[68:71]
s_add_u32 s28, s71, 3670016
s_add_u32 m0, s70, 32768
v_mfma_f32_16x16x32_bf16 a[72:75], v[48:51], v[120:123], a[72:75]
buffer_load_dwordx4 v29, s[76:79], s28 offen lds
s_add_u32 s28, s28, 31744
v_mfma_f32_16x16x32_bf16 a[76:79], v[48:51], v[124:127], a[76:79]
buffer_load_dwordx4 v29, s[76:79], s28 offen offset:1024 lds
v_lshlrev_b32 v22, 10, v22
s_waitcnt lgkmcnt(6)
v_mfma_f32_16x16x32_bf16 a[80:83], v[52:55], v[64:67], a[80:83]
ds_read_b64_tr_b16 v[44:45], v11 offset:0
ds_read_b64_tr_b16 v[46:47], v11 offset:1024
v_mfma_f32_16x16x32_bf16 a[84:87], v[52:55], v[68:71], a[84:87]
s_add_u32 s28, s73, 1605632
v_mfma_f32_16x16x32_bf16 a[88:91], v[52:55], v[120:123], a[88:91]
buffer_load_dwordx4 v30, s[80:83], s28 offen offset:2048 lds
v_mfma_f32_16x16x32_bf16 a[92:95], v[52:55], v[124:127], a[92:95]
v_and_b32 v23, 3, v1
s_waitcnt lgkmcnt(6)
v_mfma_f32_16x16x32_bf16 a[96:99], v[56:59], v[64:67], a[96:99]
ds_read_b64_tr_b16 v[48:49], v12 offset:0
v_mfma_f32_16x16x32_bf16 a[100:103], v[56:59], v[68:71], a[100:103]
ds_read_b64_tr_b16 v[50:51], v12 offset:1024
v_mfma_f32_16x16x32_bf16 a[104:107], v[56:59], v[120:123], a[104:107]
buffer_load_dwordx4 v30, s[84:87], s28 offen offset:3072 lds
v_mfma_f32_16x16x32_bf16 a[108:111], v[56:59], v[124:127], a[108:111]
v_lshlrev_b32 v23, 4, v23
s_waitcnt lgkmcnt(6)
v_mfma_f32_16x16x32_bf16 a[112:115], v[60:63], v[64:67], a[112:115]
ds_read_b64_tr_b16 v[52:53], v13 offset:0
v_mfma_f32_16x16x32_bf16 a[116:119], v[60:63], v[68:71], a[116:119]
ds_read_b64_tr_b16 v[54:55], v13 offset:1024
v_mfma_f32_16x16x32_bf16 a[120:123], v[60:63], v[120:123], a[120:123]
v_add_u32 v22, v22, v23
v_mfma_f32_16x16x32_bf16 a[124:127], v[60:63], v[124:127], a[124:127]
s_cmp_eq_u32 s7, 0
s_cbranch_scc0 L_FS_A
s_lshl_b32 s31, s6, 6
s_add_u32 s31, s31, 32768
v_mov_b32 v197, s31
v_mov_b32 v198, s98
v_mov_b32 v199, s99
s_mov_b64 exec, 1
global_store_dwordx2 v197, v[198:199], s[48:49] sc1
s_mov_b64 exec, -1
L_FS_A:
s_waitcnt lgkmcnt(6)
s_nop 2
v_mfma_f32_16x16x32_bf16 a[128:131], v[40:43], v[88:91], a[128:131]
ds_read_b64_tr_b16 v[56:57], v14 offset:0
ds_read_b64_tr_b16 v[58:59], v14 offset:1024
v_mfma_f32_16x16x32_bf16 a[132:135], v[40:43], v[92:95], a[132:135]
buffer_load_dwordx4 v[160:163], v28, s[52:55], s8 offen offset:3072 sc1
v_mfma_f32_16x16x32_bf16 a[136:139], v[40:43], v[96:99], a[136:139]
buffer_load_dwordx4 v[164:167], v28, s[52:55], s9 offen offset:3072 sc1
v_mfma_f32_16x16x32_bf16 a[140:143], v[40:43], v[100:103], a[140:143]
s_lshl_b32 s22, s26, 17
s_waitcnt lgkmcnt(6)
v_mfma_f32_16x16x32_bf16 a[144:147], v[44:47], v[88:91], a[144:147]
ds_read_b64_tr_b16 v[60:61], v15 offset:0
v_mfma_f32_16x16x32_bf16 a[148:151], v[44:47], v[92:95], a[148:151]
ds_read_b64_tr_b16 v[62:63], v15 offset:1024
v_mfma_f32_16x16x32_bf16 a[152:155], v[44:47], v[96:99], a[152:155]
buffer_load_dwordx4 v[168:171], v28, s[52:55], s10 offen offset:3072 sc1
v_mfma_f32_16x16x32_bf16 a[156:159], v[44:47], v[100:103], a[156:159]
s_lshl_b32 s23, s7, 15
s_waitcnt lgkmcnt(6)
v_mfma_f32_16x16x32_bf16 a[160:163], v[48:51], v[88:91], a[160:163]
ds_read_b64_tr_b16 v[40:41], v16 offset:0
v_mfma_f32_16x16x32_bf16 a[164:167], v[48:51], v[92:95], a[164:167]
ds_read_b64_tr_b16 v[42:43], v16 offset:1024
v_mfma_f32_16x16x32_bf16 a[168:171], v[48:51], v[96:99], a[168:171]
buffer_load_dwordx4 v[172:175], v28, s[52:55], s11 offen offset:3072 sc1
v_mfma_f32_16x16x32_bf16 a[172:175], v[48:51], v[100:103], a[172:175]
s_add_u32 s22, s22, s23
s_waitcnt lgkmcnt(6)
v_mfma_f32_16x16x32_bf16 a[176:179], v[52:55], v[88:91], a[176:179]
ds_read_b64_tr_b16 v[44:45], v17 offset:0
v_mfma_f32_16x16x32_bf16 a[180:183], v[52:55], v[92:95], a[180:183]
ds_read_b64_tr_b16 v[46:47], v17 offset:1024
v_mfma_f32_16x16x32_bf16 a[184:187], v[52:55], v[96:99], a[184:187]
s_lshl_b32 s23, s27, 8
v_mfma_f32_16x16x32_bf16 a[188:191], v[52:55], v[100:103], a[188:191]
s_waitcnt lgkmcnt(6)
v_mfma_f32_16x16x32_bf16 a[192:195], v[56:59], v[88:91], a[192:195]
ds_read_b64_tr_b16 v[48:49], v18 offset:0
v_mfma_f32_16x16x32_bf16 a[196:199], v[56:59], v[92:95], a[196:199]
ds_read_b64_tr_b16 v[50:51], v18 offset:512
v_mfma_f32_16x16x32_bf16 a[200:203], v[56:59], v[96:99], a[200:203]
s_add_u32 s22, s22, s23
v_mfma_f32_16x16x32_bf16 a[204:207], v[56:59], v[100:103], a[204:207]
s_waitcnt lgkmcnt(6)
v_mfma_f32_16x16x32_bf16 a[208:211], v[60:63], v[88:91], a[208:211]
ds_read_b64_tr_b16 v[52:53], v19 offset:0
v_mfma_f32_16x16x32_bf16 a[212:215], v[60:63], v[92:95], a[212:215]
ds_read_b64_tr_b16 v[54:55], v19 offset:512
v_mfma_f32_16x16x32_bf16 a[216:219], v[60:63], v[96:99], a[216:219]
s_lshl_b32 s23, s25, 6
v_mfma_f32_16x16x32_bf16 a[220:223], v[60:63], v[100:103], a[220:223]
s_waitcnt lgkmcnt(6)
v_mfma_f32_16x16x32_bf16 a[224:227], v[40:43], v[88:91], a[224:227]
ds_read_b64_tr_b16 v[56:57], v20 offset:0
v_mfma_f32_16x16x32_bf16 a[228:231], v[40:43], v[92:95], a[228:231]
ds_read_b64_tr_b16 v[58:59], v20 offset:512
v_mfma_f32_16x16x32_bf16 a[232:235], v[40:43], v[96:99], a[232:235]
s_add_u32 s22, s22, s23
v_mfma_f32_16x16x32_bf16 a[236:239], v[40:43], v[100:103], a[236:239]
s_waitcnt lgkmcnt(6)
v_mfma_f32_16x16x32_bf16 a[240:243], v[44:47], v[88:91], a[240:243]
ds_read_b64_tr_b16 v[60:61], v21 offset:0
v_mfma_f32_16x16x32_bf16 a[244:247], v[44:47], v[92:95], a[244:247]
ds_read_b64_tr_b16 v[62:63], v21 offset:512
v_mfma_f32_16x16x32_bf16 a[248:251], v[44:47], v[96:99], a[248:251]
v_add_u32 v39, s22, v22
v_mfma_f32_16x16x32_bf16 a[252:255], v[44:47], v[100:103], a[252:255]
s_waitcnt lgkmcnt(6)
v_mfma_f32_16x16x32_bf16 a[0:3], v[48:51], v[88:91], a[0:3]
ds_read_b64_tr_b16 v[40:41], v18 offset:1024
v_mfma_f32_16x16x32_bf16 a[4:7], v[48:51], v[92:95], a[4:7]
v_mfma_f32_16x16x32_bf16 a[8:11], v[48:51], v[96:99], a[8:11]
ds_read_b64_tr_b16 v[42:43], v18 offset:1536
v_mfma_f32_16x16x32_bf16 a[12:15], v[48:51], v[100:103], a[12:15]
s_waitcnt lgkmcnt(6)
v_mfma_f32_16x16x32_bf16 a[16:19], v[52:55], v[88:91], a[16:19]
ds_read_b64_tr_b16 v[44:45], v19 offset:1024
v_mfma_f32_16x16x32_bf16 a[20:23], v[52:55], v[92:95], a[20:23]
v_mfma_f32_16x16x32_bf16 a[24:27], v[52:55], v[96:99], a[24:27]
ds_read_b64_tr_b16 v[46:47], v19 offset:1536
v_mfma_f32_16x16x32_bf16 a[28:31], v[52:55], v[100:103], a[28:31]
s_waitcnt lgkmcnt(6)
v_mfma_f32_16x16x32_bf16 a[32:35], v[56:59], v[88:91], a[32:35]
ds_read_b64_tr_b16 v[48:49], v20 offset:1024
v_mfma_f32_16x16x32_bf16 a[36:39], v[56:59], v[92:95], a[36:39]
v_mfma_f32_16x16x32_bf16 a[40:43], v[56:59], v[96:99], a[40:43]
ds_read_b64_tr_b16 v[50:51], v20 offset:1536
v_mfma_f32_16x16x32_bf16 a[44:47], v[56:59], v[100:103], a[44:47]
s_waitcnt lgkmcnt(6)
v_mfma_f32_16x16x32_bf16 a[48:51], v[60:63], v[88:91], a[48:51]
ds_read_b64_tr_b16 v[52:53], v21 offset:1024
v_mfma_f32_16x16x32_bf16 a[52:55], v[60:63], v[92:95], a[52:55]
v_mfma_f32_16x16x32_bf16 a[56:59], v[60:63], v[96:99], a[56:59]
ds_read_b64_tr_b16 v[54:55], v21 offset:1536
v_mfma_f32_16x16x32_bf16 a[60:63], v[60:63], v[100:103], a[60:63]
s_waitcnt vmcnt(12)
s_barrier
s_waitcnt lgkmcnt(6)
v_mfma_f32_16x16x32_bf16 a[64:67], v[40:43], v[88:91], a[64:67]
ds_read_b64_tr_b16 v[56:57], v10 offset:16384
ds_read_b64_tr_b16 v[58:59], v10 offset:17408
v_mfma_f32_16x16x32_bf16 a[68:71], v[40:43], v[92:95], a[68:71]
s_add_u32 s28, s71, 3932160
s_add_u32 m0, s70, 49152
v_mfma_f32_16x16x32_bf16 a[72:75], v[40:43], v[96:99], a[72:75]
buffer_load_dwordx4 v29, s[76:79], s28 offen lds
s_add_u32 s28, s28, 31744
v_mfma_f32_16x16x32_bf16 a[76:79], v[40:43], v[100:103], a[76:79]
buffer_load_dwordx4 v29, s[76:79], s28 offen offset:1024 lds
s_waitcnt lgkmcnt(6)
v_mfma_f32_16x16x32_bf16 a[80:83], v[44:47], v[88:91], a[80:83]
ds_read_b64_tr_b16 v[60:61], v11 offset:16384
v_mfma_f32_16x16x32_bf16 a[84:87], v[44:47], v[92:95], a[84:87]
ds_read_b64_tr_b16 v[62:63], v11 offset:17408
v_mfma_f32_16x16x32_bf16 a[88:91], v[44:47], v[96:99], a[88:91]
s_add_u32 s28, s73, 1720320
v_mfma_f32_16x16x32_bf16 a[92:95], v[44:47], v[100:103], a[92:95]
buffer_load_dwordx4 v30, s[80:83], s28 offen offset:2048 lds
s_waitcnt lgkmcnt(6)
v_mfma_f32_16x16x32_bf16 a[96:99], v[48:51], v[88:91], a[96:99]
ds_read_b64_tr_b16 v[40:41], v12 offset:16384
v_mfma_f32_16x16x32_bf16 a[100:103], v[48:51], v[92:95], a[100:103]
ds_read_b64_tr_b16 v[42:43], v12 offset:17408
v_mfma_f32_16x16x32_bf16 a[104:107], v[48:51], v[96:99], a[104:107]
buffer_load_dwordx4 v30, s[84:87], s28 offen offset:3072 lds
v_mfma_f32_16x16x32_bf16 a[108:111], v[48:51], v[100:103], a[108:111]
s_waitcnt lgkmcnt(6)
v_mfma_f32_16x16x32_bf16 a[112:115], v[52:55], v[88:91], a[112:115]
ds_read_b64_tr_b16 v[44:45], v13 offset:16384
v_mfma_f32_16x16x32_bf16 a[116:119], v[52:55], v[92:95], a[116:119]
v_mfma_f32_16x16x32_bf16 a[120:123], v[52:55], v[96:99], a[120:123]
ds_read_b64_tr_b16 v[46:47], v13 offset:17408
v_mfma_f32_16x16x32_bf16 a[124:127], v[52:55], v[100:103], a[124:127]
s_waitcnt lgkmcnt(6)
v_mfma_f32_16x16x32_bf16 a[128:131], v[56:59], v[128:131], a[128:131]
ds_read_b64_tr_b16 v[48:49], v14 offset:16384
v_mfma_f32_16x16x32_bf16 a[132:135], v[56:59], v[132:135], a[132:135]
ds_read_b64_tr_b16 v[50:51], v14 offset:17408
v_mfma_f32_16x16x32_bf16 a[136:139], v[56:59], v[136:139], a[136:139]
s_and_b32 s20, s6, 7
v_mfma_f32_16x16x32_bf16 a[140:143], v[56:59], v[140:143], a[140:143]
s_lshr_b32 s21, s6, 3
s_waitcnt lgkmcnt(6)
v_mfma_f32_16x16x32_bf16 a[144:147], v[60:63], v[128:131], a[144:147]
ds_read_b64_tr_b16 v[52:53], v15 offset:16384
v_mfma_f32_16x16x32_bf16 a[148:151], v[60:63], v[132:135], a[148:151]
ds_read_b64_tr_b16 v[54:55], v15 offset:17408
v_mfma_f32_16x16x32_bf16 a[152:155], v[60:63], v[136:139], a[152:155]
s_and_b32 s25, s20, 3
v_mfma_f32_16x16x32_bf16 a[156:159], v[60:63], v[140:143], a[156:159]
s_lshr_b32 s22, s20, 2
s_waitcnt lgkmcnt(6)
v_mfma_f32_16x16x32_bf16 a[160:163], v[40:43], v[128:131], a[160:163]
ds_read_b64_tr_b16 v[56:57], v16 offset:16384
v_mfma_f32_16x16x32_bf16 a[164:167], v[40:43], v[132:135], a[164:167]
ds_read_b64_tr_b16 v[58:59], v16 offset:17408
v_mfma_f32_16x16x32_bf16 a[168:171], v[40:43], v[136:139], a[168:171]
s_lshl_b32 s22, s22, 3
v_mfma_f32_16x16x32_bf16 a[172:175], v[40:43], v[140:143], a[172:175]
s_lshr_b32 s23, s21, 2
s_waitcnt lgkmcnt(6)
v_mfma_f32_16x16x32_bf16 a[176:179], v[44:47], v[128:131], a[176:179]
ds_read_b64_tr_b16 v[60:61], v17 offset:16384
v_mfma_f32_16x16x32_bf16 a[180:183], v[44:47], v[132:135], a[180:183]
ds_read_b64_tr_b16 v[62:63], v17 offset:17408
v_mfma_f32_16x16x32_bf16 a[184:187], v[44:47], v[136:139], a[184:187]
s_add_u32 s26, s22, s23
v_mfma_f32_16x16x32_bf16 a[188:191], v[44:47], v[140:143], a[188:191]
s_and_b32 s27, s21, 3
s_waitcnt vmcnt(8)
s_barrier
s_waitcnt lgkmcnt(6)
v_mfma_f32_16x16x32_bf16 a[192:195], v[48:51], v[128:131], a[192:195]
ds_read_b64_tr_b16 v[40:41], v10 offset:32768
v_mfma_f32_16x16x32_bf16 a[196:199], v[48:51], v[132:135], a[196:199]
ds_read_b64_tr_b16 v[42:43], v10 offset:33792
v_mfma_f32_16x16x32_bf16 a[200:203], v[48:51], v[136:139], a[200:203]
s_lshl_b32 s24, s26, 2
v_mfma_f32_16x16x32_bf16 a[204:207], v[48:51], v[140:143], a[204:207]
s_add_u32 s24, s24, s27
s_waitcnt lgkmcnt(6)
v_mfma_f32_16x16x32_bf16 a[208:211], v[52:55], v[128:131], a[208:211]
ds_read_b64_tr_b16 v[44:45], v11 offset:32768
v_mfma_f32_16x16x32_bf16 a[212:215], v[52:55], v[132:135], a[212:215]
ds_read_b64_tr_b16 v[46:47], v11 offset:33792
v_mfma_f32_16x16x32_bf16 a[216:219], v[52:55], v[136:139], a[216:219]
s_lshl_b32 s24, s24, 17
v_mfma_f32_16x16x32_bf16 a[220:223], v[52:55], v[140:143], a[220:223]
s_lshl_b32 s23, s7, 11
s_waitcnt lgkmcnt(6)
v_mfma_f32_16x16x32_bf16 a[224:227], v[56:59], v[128:131], a[224:227]
ds_read_b64_tr_b16 v[48:49], v12 offset:32768
v_mfma_f32_16x16x32_bf16 a[228:231], v[56:59], v[132:135], a[228:231]
ds_read_b64_tr_b16 v[50:51], v12 offset:33792
v_mfma_f32_16x16x32_bf16 a[232:235], v[56:59], v[136:139], a[232:235]
s_add_u32 s24, s24, s23
v_mfma_f32_16x16x32_bf16 a[236:239], v[56:59], v[140:143], a[236:239]
s_add_u32 s24, s24, 62914560
s_waitcnt lgkmcnt(6)
v_mfma_f32_16x16x32_bf16 a[240:243], v[60:63], v[128:131], a[240:243]
ds_read_b64_tr_b16 v[52:53], v13 offset:32768
v_mfma_f32_16x16x32_bf16 a[244:247], v[60:63], v[132:135], a[244:247]
ds_read_b64_tr_b16 v[54:55], v13 offset:33792
v_mfma_f32_16x16x32_bf16 a[248:251], v[60:63], v[136:139], a[248:251]
s_add_u32 s92, s48, s24
v_mfma_f32_16x16x32_bf16 a[252:255], v[60:63], v[140:143], a[252:255]
s_addc_u32 s93, s49, 0
s_waitcnt lgkmcnt(6)
v_mfma_f32_16x16x32_bf16 a[128:131], v[40:43], v[144:147], a[128:131]
ds_read_b64_tr_b16 v[56:57], v14 offset:32768
v_mfma_f32_16x16x32_bf16 a[132:135], v[40:43], v[148:151], a[132:135]
ds_read_b64_tr_b16 v[58:59], v14 offset:33792
v_mfma_f32_16x16x32_bf16 a[136:139], v[40:43], v[152:155], a[136:139]
s_lshl_b32 s22, s25, 15
v_mfma_f32_16x16x32_bf16 a[140:143], v[40:43], v[156:159], a[140:143]
v_mov_b32 v192, -1
s_waitcnt lgkmcnt(6)
v_mfma_f32_16x16x32_bf16 a[144:147], v[44:47], v[144:147], a[144:147]
ds_read_b64_tr_b16 v[60:61], v15 offset:32768
v_mfma_f32_16x16x32_bf16 a[148:151], v[44:47], v[148:151], a[148:151]
ds_read_b64_tr_b16 v[62:63], v15 offset:33792
v_mfma_f32_16x16x32_bf16 a[152:155], v[44:47], v[152:155], a[152:155]
v_mov_b32 v193, -1
v_mfma_f32_16x16x32_bf16 a[156:159], v[44:47], v[156:159], a[156:159]
v_mov_b32 v194, -1
s_waitcnt lgkmcnt(6)
v_mfma_f32_16x16x32_bf16 a[160:163], v[48:51], v[144:147], a[160:163]
ds_read_b64_tr_b16 v[40:41], v16 offset:32768
v_mfma_f32_16x16x32_bf16 a[164:167], v[48:51], v[148:151], a[164:167]
ds_read_b64_tr_b16 v[42:43], v16 offset:33792
v_mfma_f32_16x16x32_bf16 a[168:171], v[48:51], v[152:155], a[168:171]
v_mov_b32 v195, -1
v_mfma_f32_16x16x32_bf16 a[172:175], v[48:51], v[156:159], a[172:175]
s_add_u32 s23, s25, 1
s_waitcnt lgkmcnt(6)
v_mfma_f32_16x16x32_bf16 a[176:179], v[52:55], v[144:147], a[176:179]
ds_read_b64_tr_b16 v[44:45], v17 offset:32768
v_mfma_f32_16x16x32_bf16 a[180:183], v[52:55], v[148:151], a[180:183]
ds_read_b64_tr_b16 v[46:47], v17 offset:33792
v_mfma_f32_16x16x32_bf16 a[184:187], v[52:55], v[152:155], a[184:187]
s_and_b32 s23, s23, 3
v_mfma_f32_16x16x32_bf16 a[188:191], v[52:55], v[156:159], a[188:191]
s_lshl_b32 s23, s23, 13
s_waitcnt vmcnt(0)
s_barrier
s_waitcnt lgkmcnt(6)
v_mfma_f32_16x16x32_bf16 a[192:195], v[56:59], v[144:147], a[192:195]
ds_read_b64_tr_b16 v[48:49], v10 offset:49152
v_mfma_f32_16x16x32_bf16 a[196:199], v[56:59], v[148:151], a[196:199]
ds_read_b64_tr_b16 v[50:51], v10 offset:50176
v_mfma_f32_16x16x32_bf16 a[200:203], v[56:59], v[152:155], a[200:203]
s_add_u32 s23, s23, s22
v_mfma_f32_16x16x32_bf16 a[204:207], v[56:59], v[156:159], a[204:207]
v_add_u32 v196, s23, v28
s_waitcnt lgkmcnt(6)
v_mfma_f32_16x16x32_bf16 a[208:211], v[60:63], v[144:147], a[208:211]
ds_read_b64_tr_b16 v[52:53], v11 offset:49152
v_mfma_f32_16x16x32_bf16 a[212:215], v[60:63], v[148:151], a[212:215]
ds_read_b64_tr_b16 v[54:55], v11 offset:50176
v_mfma_f32_16x16x32_bf16 a[216:219], v[60:63], v[152:155], a[216:219]
global_store_dwordx4 v196, v[192:195], s[92:93] offset:0 sc1
v_mfma_f32_16x16x32_bf16 a[220:223], v[60:63], v[156:159], a[220:223]
global_store_dwordx4 v196, v[192:195], s[92:93] offset:1024 sc1
s_waitcnt lgkmcnt(6)
v_mfma_f32_16x16x32_bf16 a[224:227], v[40:43], v[144:147], a[224:227]
ds_read_b64_tr_b16 v[56:57], v12 offset:49152
v_mfma_f32_16x16x32_bf16 a[228:231], v[40:43], v[148:151], a[228:231]
ds_read_b64_tr_b16 v[58:59], v12 offset:50176
v_mfma_f32_16x16x32_bf16 a[232:235], v[40:43], v[152:155], a[232:235]
s_add_u32 s23, s25, 2
v_mfma_f32_16x16x32_bf16 a[236:239], v[40:43], v[156:159], a[236:239]
s_waitcnt lgkmcnt(6)
v_mfma_f32_16x16x32_bf16 a[240:243], v[44:47], v[144:147], a[240:243]
ds_read_b64_tr_b16 v[60:61], v13 offset:49152
v_mfma_f32_16x16x32_bf16 a[244:247], v[44:47], v[148:151], a[244:247]
ds_read_b64_tr_b16 v[62:63], v13 offset:50176
v_mfma_f32_16x16x32_bf16 a[248:251], v[44:47], v[152:155], a[248:251]
s_and_b32 s23, s23, 3
v_mfma_f32_16x16x32_bf16 a[252:255], v[44:47], v[156:159], a[252:255]
s_lshl_b32 s23, s23, 13
s_waitcnt lgkmcnt(6)
v_mfma_f32_16x16x32_bf16 a[128:131], v[48:51], v[160:163], a[128:131]
ds_read_b64_tr_b16 v[40:41], v14 offset:49152
v_mfma_f32_16x16x32_bf16 a[132:135], v[48:51], v[164:167], a[132:135]
ds_read_b64_tr_b16 v[42:43], v14 offset:50176
v_mfma_f32_16x16x32_bf16 a[136:139], v[48:51], v[168:171], a[136:139]
s_add_u32 s23, s23, s22
v_mfma_f32_16x16x32_bf16 a[140:143], v[48:51], v[172:175], a[140:143]
v_add_u32 v196, s23, v28
s_waitcnt lgkmcnt(6)
v_mfma_f32_16x16x32_bf16 a[144:147], v[52:55], v[160:163], a[144:147]
ds_read_b64_tr_b16 v[44:45], v15 offset:49152
v_mfma_f32_16x16x32_bf16 a[148:151], v[52:55], v[164:167], a[148:151]
ds_read_b64_tr_b16 v[46:47], v15 offset:50176
v_mfma_f32_16x16x32_bf16 a[152:155], v[52:55], v[168:171], a[152:155]
global_store_dwordx4 v196, v[192:195], s[92:93] offset:0 sc1
v_mfma_f32_16x16x32_bf16 a[156:159], v[52:55], v[172:175], a[156:159]
global_store_dwordx4 v196, v[192:195], s[92:93] offset:1024 sc1
s_waitcnt lgkmcnt(6)
v_mfma_f32_16x16x32_bf16 a[160:163], v[56:59], v[160:163], a[160:163]
ds_read_b64_tr_b16 v[48:49], v16 offset:49152
v_mfma_f32_16x16x32_bf16 a[164:167], v[56:59], v[164:167], a[164:167]
ds_read_b64_tr_b16 v[50:51], v16 offset:50176
v_mfma_f32_16x16x32_bf16 a[168:171], v[56:59], v[168:171], a[168:171]
s_add_u32 s23, s25, 3
v_mfma_f32_16x16x32_bf16 a[172:175], v[56:59], v[172:175], a[172:175]
s_and_b32 s23, s23, 3
s_waitcnt lgkmcnt(6)
v_mfma_f32_16x16x32_bf16 a[176:179], v[60:63], v[160:163], a[176:179]
ds_read_b64_tr_b16 v[52:53], v17 offset:49152
v_mfma_f32_16x16x32_bf16 a[180:183], v[60:63], v[164:167], a[180:183]
ds_read_b64_tr_b16 v[54:55], v17 offset:50176
v_mfma_f32_16x16x32_bf16 a[184:187], v[60:63], v[168:171], a[184:187]
s_lshl_b32 s23, s23, 13
v_mfma_f32_16x16x32_bf16 a[188:191], v[60:63], v[172:175], a[188:191]
s_add_u32 s23, s23, s22
s_waitcnt lgkmcnt(6)
v_mfma_f32_16x16x32_bf16 a[192:195], v[40:43], v[160:163], a[192:195]
ds_read_b64_tr_b16 v[56:57], v18 offset:16384
v_mfma_f32_16x16x32_bf16 a[196:199], v[40:43], v[164:167], a[196:199]
ds_read_b64_tr_b16 v[58:59], v18 offset:16896
v_mfma_f32_16x16x32_bf16 a[200:203], v[40:43], v[168:171], a[200:203]
v_add_u32 v196, s23, v28
v_mfma_f32_16x16x32_bf16 a[204:207], v[40:43], v[172:175], a[204:207]
global_store_dwordx4 v196, v[192:195], s[92:93] offset:0 sc1
s_waitcnt lgkmcnt(6)
v_mfma_f32_16x16x32_bf16 a[208:211], v[44:47], v[160:163], a[208:211]
ds_read_b64_tr_b16 v[60:61], v19 offset:16384
v_mfma_f32_16x16x32_bf16 a[212:215], v[44:47], v[164:167], a[212:215]
ds_read_b64_tr_b16 v[62:63], v19 offset:16896
v_mfma_f32_16x16x32_bf16 a[216:219], v[44:47], v[168:171], a[216:219]
global_store_dwordx4 v196, v[192:195], s[92:93] offset:1024 sc1
v_mfma_f32_16x16x32_bf16 a[220:223], v[44:47], v[172:175], a[220:223]
s_waitcnt lgkmcnt(6)
v_mfma_f32_16x16x32_bf16 a[224:227], v[48:51], v[160:163], a[224:227]
ds_read_b64_tr_b16 v[40:41], v20 offset:16384
v_mfma_f32_16x16x32_bf16 a[228:231], v[48:51], v[164:167], a[228:231]
v_mfma_f32_16x16x32_bf16 a[232:235], v[48:51], v[168:171], a[232:235]
ds_read_b64_tr_b16 v[42:43], v20 offset:16896
v_mfma_f32_16x16x32_bf16 a[236:239], v[48:51], v[172:175], a[236:239]
s_waitcnt lgkmcnt(6)
v_mfma_f32_16x16x32_bf16 a[240:243], v[52:55], v[160:163], a[240:243]
ds_read_b64_tr_b16 v[44:45], v21 offset:16384
v_mfma_f32_16x16x32_bf16 a[244:247], v[52:55], v[164:167], a[244:247]
v_mfma_f32_16x16x32_bf16 a[248:251], v[52:55], v[168:171], a[248:251]
ds_read_b64_tr_b16 v[46:47], v21 offset:16896
v_mfma_f32_16x16x32_bf16 a[252:255], v[52:55], v[172:175], a[252:255]
s_waitcnt lgkmcnt(6)
v_mfma_f32_16x16x32_bf16 a[0:3], v[56:59], v[128:131], a[0:3]
ds_read_b64_tr_b16 v[48:49], v18 offset:17408
v_mfma_f32_16x16x32_bf16 a[4:7], v[56:59], v[132:135], a[4:7]
v_mfma_f32_16x16x32_bf16 a[8:11], v[56:59], v[136:139], a[8:11]
ds_read_b64_tr_b16 v[50:51], v18 offset:17920
v_mfma_f32_16x16x32_bf16 a[12:15], v[56:59], v[140:143], a[12:15]
s_waitcnt lgkmcnt(6)
v_mfma_f32_16x16x32_bf16 a[16:19], v[60:63], v[128:131], a[16:19]
ds_read_b64_tr_b16 v[52:53], v19 offset:17408
ds_read_b64_tr_b16 v[54:55], v19 offset:17920
s_mov_b32 s46, 0x3e0293ee
v_accvgpr_read_b32 v176, a128
v_mfma_f32_16x16x32_bf16 a[20:23], v[60:63], v[132:135], a[20:23]
v_accvgpr_read_b32 v177, a129
v_accvgpr_read_b32 v178, a130
v_accvgpr_read_b32 v179, a131
v_mul_f32 v176, s46, v176
v_mfma_f32_16x16x32_bf16 a[24:27], v[60:63], v[136:139], a[24:27]
v_mul_f32 v177, s46, v177
v_mul_f32 v178, s46, v178
v_mul_f32 v179, s46, v179
v_cvt_pk_bf16_f32 v64, v176, v177
v_mfma_f32_16x16x32_bf16 a[28:31], v[60:63], v[140:143], a[28:31]
v_cvt_pk_bf16_f32 v65, v178, v179
v_accvgpr_read_b32 v180, a144
v_accvgpr_read_b32 v181, a145
v_accvgpr_read_b32 v182, a146
s_waitcnt lgkmcnt(6)
v_mfma_f32_16x16x32_bf16 a[32:35], v[40:43], v[128:131], a[32:35]
ds_read_b64_tr_b16 v[56:57], v20 offset:17408
ds_read_b64_tr_b16 v[58:59], v20 offset:17920
v_accvgpr_read_b32 v183, a147
v_mul_f32 v180, s46, v180
v_mfma_f32_16x16x32_bf16 a[36:39], v[40:43], v[132:135], a[36:39]
v_mul_f32 v181, s46, v181
v_mul_f32 v182, s46, v182
v_mul_f32 v183, s46, v183
v_cvt_pk_bf16_f32 v66, v180, v181
v_mfma_f32_16x16x32_bf16 a[40:43], v[40:43], v[136:139], a[40:43]
v_cvt_pk_bf16_f32 v67, v182, v183
v_accvgpr_read_b32 v176, a160
v_accvgpr_read_b32 v177, a161
v_accvgpr_read_b32 v178, a162
v_mfma_f32_16x16x32_bf16 a[44:47], v[40:43], v[140:143], a[44:47]
v_accvgpr_read_b32 v179, a163
)ASM"
R"ASM(v_mul_f32 v176, s46, v176
v_mul_f32 v177, s46, v177
v_mul_f32 v178, s46, v178
s_waitcnt lgkmcnt(6)
v_mfma_f32_16x16x32_bf16 a[48:51], v[44:47], v[128:131], a[48:51]
ds_read_b64_tr_b16 v[60:61], v21 offset:17408
ds_read_b64_tr_b16 v[62:63], v21 offset:17920
v_mul_f32 v179, s46, v179
v_cvt_pk_bf16_f32 v68, v176, v177
v_mfma_f32_16x16x32_bf16 a[52:55], v[44:47], v[132:135], a[52:55]
v_cvt_pk_bf16_f32 v69, v178, v179
v_accvgpr_read_b32 v180, a176
v_accvgpr_read_b32 v181, a177
v_accvgpr_read_b32 v182, a178
v_mfma_f32_16x16x32_bf16 a[56:59], v[44:47], v[136:139], a[56:59]
v_accvgpr_read_b32 v183, a179
v_mul_f32 v180, s46, v180
v_mul_f32 v181, s46, v181
v_mul_f32 v182, s46, v182
v_mfma_f32_16x16x32_bf16 a[60:63], v[44:47], v[140:143], a[60:63]
v_mul_f32 v183, s46, v183
v_cvt_pk_bf16_f32 v70, v180, v181
v_cvt_pk_bf16_f32 v71, v182, v183
v_accvgpr_read_b32 v176, a192
s_waitcnt lgkmcnt(6)
v_mfma_f32_16x16x32_bf16 a[64:67], v[48:51], v[128:131], a[64:67]
ds_read_b64_tr_b16 v[40:41], v18 offset:32768
ds_read_b64_tr_b16 v[42:43], v18 offset:33280
v_accvgpr_read_b32 v177, a193
v_accvgpr_read_b32 v178, a194
v_mfma_f32_16x16x32_bf16 a[68:71], v[48:51], v[132:135], a[68:71]
v_accvgpr_read_b32 v179, a195
v_mul_f32 v176, s46, v176
v_mul_f32 v177, s46, v177
v_mul_f32 v178, s46, v178
v_mfma_f32_16x16x32_bf16 a[72:75], v[48:51], v[136:139], a[72:75]
v_mul_f32 v179, s46, v179
v_cvt_pk_bf16_f32 v72, v176, v177
v_cvt_pk_bf16_f32 v73, v178, v179
v_accvgpr_read_b32 v180, a208
v_mfma_f32_16x16x32_bf16 a[76:79], v[48:51], v[140:143], a[76:79]
v_accvgpr_read_b32 v181, a209
v_accvgpr_read_b32 v182, a210
v_accvgpr_read_b32 v183, a211
v_mul_f32 v180, s46, v180
s_waitcnt lgkmcnt(6)
v_mfma_f32_16x16x32_bf16 a[80:83], v[52:55], v[128:131], a[80:83]
ds_read_b64_tr_b16 v[44:45], v19 offset:32768
ds_read_b64_tr_b16 v[46:47], v19 offset:33280
v_mul_f32 v181, s46, v181
v_mul_f32 v182, s46, v182
v_mfma_f32_16x16x32_bf16 a[84:87], v[52:55], v[132:135], a[84:87]
v_mul_f32 v183, s46, v183
v_cvt_pk_bf16_f32 v74, v180, v181
v_cvt_pk_bf16_f32 v75, v182, v183
v_accvgpr_read_b32 v176, a224
v_mfma_f32_16x16x32_bf16 a[88:91], v[52:55], v[136:139], a[88:91]
v_accvgpr_read_b32 v177, a225
v_accvgpr_read_b32 v178, a226
v_accvgpr_read_b32 v179, a227
v_mul_f32 v176, s46, v176
v_mfma_f32_16x16x32_bf16 a[92:95], v[52:55], v[140:143], a[92:95]
v_mul_f32 v177, s46, v177
v_mul_f32 v178, s46, v178
v_mul_f32 v179, s46, v179
v_cvt_pk_bf16_f32 v76, v176, v177
s_waitcnt lgkmcnt(6)
v_mfma_f32_16x16x32_bf16 a[96:99], v[56:59], v[128:131], a[96:99]
ds_read_b64_tr_b16 v[48:49], v20 offset:32768
ds_read_b64_tr_b16 v[50:51], v20 offset:33280
v_cvt_pk_bf16_f32 v77, v178, v179
v_accvgpr_read_b32 v180, a240
v_mfma_f32_16x16x32_bf16 a[100:103], v[56:59], v[132:135], a[100:103]
v_accvgpr_read_b32 v181, a241
v_accvgpr_read_b32 v182, a242
v_accvgpr_read_b32 v183, a243
v_mul_f32 v180, s46, v180
v_mfma_f32_16x16x32_bf16 a[104:107], v[56:59], v[136:139], a[104:107]
v_mul_f32 v181, s46, v181
v_mul_f32 v182, s46, v182
v_mul_f32 v183, s46, v183
v_cvt_pk_bf16_f32 v78, v180, v181
v_mfma_f32_16x16x32_bf16 a[108:111], v[56:59], v[140:143], a[108:111]
v_cvt_pk_bf16_f32 v79, v182, v183
v_accvgpr_read_b32 v176, a132
v_accvgpr_read_b32 v177, a133
v_accvgpr_read_b32 v178, a134
s_waitcnt lgkmcnt(6)
v_mfma_f32_16x16x32_bf16 a[112:115], v[60:63], v[128:131], a[112:115]
ds_read_b64_tr_b16 v[52:53], v21 offset:32768
ds_read_b64_tr_b16 v[54:55], v21 offset:33280
v_accvgpr_read_b32 v179, a135
v_mul_f32 v176, s46, v176
v_mfma_f32_16x16x32_bf16 a[116:119], v[60:63], v[132:135], a[116:119]
v_mul_f32 v177, s46, v177
v_mul_f32 v178, s46, v178
v_mul_f32 v179, s46, v179
v_cvt_pk_bf16_f32 v80, v176, v177
v_mfma_f32_16x16x32_bf16 a[120:123], v[60:63], v[136:139], a[120:123]
v_cvt_pk_bf16_f32 v81, v178, v179
v_accvgpr_read_b32 v180, a148
v_accvgpr_read_b32 v181, a149
v_accvgpr_read_b32 v182, a150
v_mfma_f32_16x16x32_bf16 a[124:127], v[60:63], v[140:143], a[124:127]
v_accvgpr_read_b32 v183, a151
v_mul_f32 v180, s46, v180
v_mul_f32 v181, s46, v181
v_mul_f32 v182, s46, v182
s_waitcnt lgkmcnt(6)
v_mfma_f32_16x16x32_bf16 a[0:3], v[40:43], v[144:147], a[0:3]
ds_read_b64_tr_b16 v[56:57], v18 offset:33792
ds_read_b64_tr_b16 v[58:59], v18 offset:34304
v_mul_f32 v183, s46, v183
v_cvt_pk_bf16_f32 v82, v180, v181
v_mfma_f32_16x16x32_bf16 a[4:7], v[40:43], v[148:151], a[4:7]
v_cvt_pk_bf16_f32 v83, v182, v183
v_accvgpr_read_b32 v176, a164
v_accvgpr_read_b32 v177, a165
v_accvgpr_read_b32 v178, a166
v_mfma_f32_16x16x32_bf16 a[8:11], v[40:43], v[152:155], a[8:11]
v_accvgpr_read_b32 v179, a167
v_mul_f32 v176, s46, v176
v_mul_f32 v177, s46, v177
v_mul_f32 v178, s46, v178
v_mfma_f32_16x16x32_bf16 a[12:15], v[40:43], v[156:159], a[12:15]
v_mul_f32 v179, s46, v179
v_cvt_pk_bf16_f32 v84, v176, v177
v_cvt_pk_bf16_f32 v85, v178, v179
v_accvgpr_read_b32 v180, a180
s_waitcnt lgkmcnt(6)
v_mfma_f32_16x16x32_bf16 a[16:19], v[44:47], v[144:147], a[16:19]
ds_read_b64_tr_b16 v[60:61], v19 offset:33792
ds_read_b64_tr_b16 v[62:63], v19 offset:34304
v_accvgpr_read_b32 v181, a181
v_accvgpr_read_b32 v182, a182
v_mfma_f32_16x16x32_bf16 a[20:23], v[44:47], v[148:151], a[20:23]
v_accvgpr_read_b32 v183, a183
v_mul_f32 v180, s46, v180
v_mul_f32 v181, s46, v181
v_mul_f32 v182, s46, v182
v_mfma_f32_16x16x32_bf16 a[24:27], v[44:47], v[152:155], a[24:27]
v_mul_f32 v183, s46, v183
v_cvt_pk_bf16_f32 v86, v180, v181
v_cvt_pk_bf16_f32 v87, v182, v183
v_accvgpr_read_b32 v176, a196
v_mfma_f32_16x16x32_bf16 a[28:31], v[44:47], v[156:159], a[28:31]
v_accvgpr_read_b32 v177, a197
v_accvgpr_read_b32 v178, a198
v_accvgpr_read_b32 v179, a199
v_mul_f32 v176, s46, v176
s_waitcnt lgkmcnt(6)
v_mfma_f32_16x16x32_bf16 a[32:35], v[48:51], v[144:147], a[32:35]
ds_read_b64_tr_b16 v[40:41], v20 offset:33792
ds_read_b64_tr_b16 v[42:43], v20 offset:34304
v_mul_f32 v177, s46, v177
v_mul_f32 v178, s46, v178
v_mfma_f32_16x16x32_bf16 a[36:39], v[48:51], v[148:151], a[36:39]
v_mul_f32 v179, s46, v179
v_cvt_pk_bf16_f32 v88, v176, v177
v_cvt_pk_bf16_f32 v89, v178, v179
v_accvgpr_read_b32 v180, a212
v_mfma_f32_16x16x32_bf16 a[40:43], v[48:51], v[152:155], a[40:43]
v_accvgpr_read_b32 v181, a213
v_accvgpr_read_b32 v182, a214
v_accvgpr_read_b32 v183, a215
v_mul_f32 v180, s46, v180
v_mfma_f32_16x16x32_bf16 a[44:47], v[48:51], v[156:159], a[44:47]
v_mul_f32 v181, s46, v181
v_mul_f32 v182, s46, v182
v_mul_f32 v183, s46, v183
v_cvt_pk_bf16_f32 v90, v180, v181
s_waitcnt lgkmcnt(6)
v_mfma_f32_16x16x32_bf16 a[48:51], v[52:55], v[144:147], a[48:51]
ds_read_b64_tr_b16 v[44:45], v21 offset:33792
ds_read_b64_tr_b16 v[46:47], v21 offset:34304
v_cvt_pk_bf16_f32 v91, v182, v183
v_accvgpr_read_b32 v176, a228
v_mfma_f32_16x16x32_bf16 a[52:55], v[52:55], v[148:151], a[52:55]
v_accvgpr_read_b32 v177, a229
v_accvgpr_read_b32 v178, a230
v_accvgpr_read_b32 v179, a231
v_mul_f32 v176, s46, v176
v_mfma_f32_16x16x32_bf16 a[56:59], v[52:55], v[152:155], a[56:59]
v_mul_f32 v177, s46, v177
v_mul_f32 v178, s46, v178
v_mul_f32 v179, s46, v179
v_cvt_pk_bf16_f32 v92, v176, v177
v_mfma_f32_16x16x32_bf16 a[60:63], v[52:55], v[156:159], a[60:63]
v_cvt_pk_bf16_f32 v93, v178, v179
v_accvgpr_read_b32 v180, a244
v_accvgpr_read_b32 v181, a245
v_accvgpr_read_b32 v182, a246
s_waitcnt lgkmcnt(6)
v_mfma_f32_16x16x32_bf16 a[64:67], v[56:59], v[144:147], a[64:67]
ds_read_b64_tr_b16 v[48:49], v18 offset:49152
ds_read_b64_tr_b16 v[50:51], v18 offset:49664
v_accvgpr_read_b32 v183, a247
v_mul_f32 v180, s46, v180
v_mfma_f32_16x16x32_bf16 a[68:71], v[56:59], v[148:151], a[68:71]
v_mul_f32 v181, s46, v181
v_mul_f32 v182, s46, v182
v_mul_f32 v183, s46, v183
v_cvt_pk_bf16_f32 v94, v180, v181
v_mfma_f32_16x16x32_bf16 a[72:75], v[56:59], v[152:155], a[72:75]
v_cvt_pk_bf16_f32 v95, v182, v183
v_accvgpr_read_b32 v176, a136
v_accvgpr_read_b32 v177, a137
v_accvgpr_read_b32 v178, a138
v_mfma_f32_16x16x32_bf16 a[76:79], v[56:59], v[156:159], a[76:79]
v_accvgpr_read_b32 v179, a139
v_mul_f32 v176, s46, v176
v_mul_f32 v177, s46, v177
v_mul_f32 v178, s46, v178
s_waitcnt lgkmcnt(6)
v_mfma_f32_16x16x32_bf16 a[80:83], v[60:63], v[144:147], a[80:83]
ds_read_b64_tr_b16 v[52:53], v19 offset:49152
ds_read_b64_tr_b16 v[54:55], v19 offset:49664
v_mul_f32 v179, s46, v179
v_cvt_pk_bf16_f32 v96, v176, v177
v_mfma_f32_16x16x32_bf16 a[84:87], v[60:63], v[148:151], a[84:87]
v_cvt_pk_bf16_f32 v97, v178, v179
v_accvgpr_read_b32 v180, a152
v_accvgpr_read_b32 v181, a153
v_accvgpr_read_b32 v182, a154
v_mfma_f32_16x16x32_bf16 a[88:91], v[60:63], v[152:155], a[88:91]
v_accvgpr_read_b32 v183, a155
v_mul_f32 v180, s46, v180
v_mul_f32 v181, s46, v181
v_mul_f32 v182, s46, v182
v_mfma_f32_16x16x32_bf16 a[92:95], v[60:63], v[156:159], a[92:95]
v_mul_f32 v183, s46, v183
v_cvt_pk_bf16_f32 v98, v180, v181
v_cvt_pk_bf16_f32 v99, v182, v183
v_accvgpr_read_b32 v176, a168
s_waitcnt lgkmcnt(6)
v_mfma_f32_16x16x32_bf16 a[96:99], v[40:43], v[144:147], a[96:99]
ds_read_b64_tr_b16 v[56:57], v20 offset:49152
ds_read_b64_tr_b16 v[58:59], v20 offset:49664
v_accvgpr_read_b32 v177, a169
v_accvgpr_read_b32 v178, a170
v_mfma_f32_16x16x32_bf16 a[100:103], v[40:43], v[148:151], a[100:103]
v_accvgpr_read_b32 v179, a171
v_mul_f32 v176, s46, v176
v_mul_f32 v177, s46, v177
v_mul_f32 v178, s46, v178
v_mfma_f32_16x16x32_bf16 a[104:107], v[40:43], v[152:155], a[104:107]
v_mul_f32 v179, s46, v179
v_cvt_pk_bf16_f32 v100, v176, v177
v_cvt_pk_bf16_f32 v101, v178, v179
v_accvgpr_read_b32 v180, a184
v_mfma_f32_16x16x32_bf16 a[108:111], v[40:43], v[156:159], a[108:111]
v_accvgpr_read_b32 v181, a185
v_accvgpr_read_b32 v182, a186
v_accvgpr_read_b32 v183, a187
v_mul_f32 v180, s46, v180
s_waitcnt lgkmcnt(6)
v_mfma_f32_16x16x32_bf16 a[112:115], v[44:47], v[144:147], a[112:115]
ds_read_b64_tr_b16 v[60:61], v21 offset:49152
ds_read_b64_tr_b16 v[62:63], v21 offset:49664
v_mul_f32 v181, s46, v181
v_mul_f32 v182, s46, v182
v_mfma_f32_16x16x32_bf16 a[116:119], v[44:47], v[148:151], a[116:119]
v_mul_f32 v183, s46, v183
v_cvt_pk_bf16_f32 v102, v180, v181
v_cvt_pk_bf16_f32 v103, v182, v183
v_accvgpr_read_b32 v176, a200
v_mfma_f32_16x16x32_bf16 a[120:123], v[44:47], v[152:155], a[120:123]
v_accvgpr_read_b32 v177, a201
v_accvgpr_read_b32 v178, a202
v_accvgpr_read_b32 v179, a203
v_mul_f32 v176, s46, v176
v_mfma_f32_16x16x32_bf16 a[124:127], v[44:47], v[156:159], a[124:127]
v_mul_f32 v177, s46, v177
v_mul_f32 v178, s46, v178
v_mul_f32 v179, s46, v179
v_cvt_pk_bf16_f32 v104, v176, v177
s_waitcnt lgkmcnt(6)
v_mfma_f32_16x16x32_bf16 a[0:3], v[48:51], v[160:163], a[0:3]
ds_read_b64_tr_b16 v[40:41], v18 offset:50176
ds_read_b64_tr_b16 v[42:43], v18 offset:50688
v_cvt_pk_bf16_f32 v105, v178, v179
v_accvgpr_read_b32 v180, a216
v_mfma_f32_16x16x32_bf16 a[4:7], v[48:51], v[164:167], a[4:7]
v_accvgpr_read_b32 v181, a217
v_accvgpr_read_b32 v182, a218
v_accvgpr_read_b32 v183, a219
v_mul_f32 v180, s46, v180
v_mfma_f32_16x16x32_bf16 a[8:11], v[48:51], v[168:171], a[8:11]
v_mul_f32 v181, s46, v181
v_mul_f32 v182, s46, v182
v_mul_f32 v183, s46, v183
v_cvt_pk_bf16_f32 v106, v180, v181
v_mfma_f32_16x16x32_bf16 a[12:15], v[48:51], v[172:175], a[12:15]
v_cvt_pk_bf16_f32 v107, v182, v183
v_accvgpr_read_b32 v176, a232
v_accvgpr_read_b32 v177, a233
v_accvgpr_read_b32 v178, a234
s_waitcnt lgkmcnt(6)
v_mfma_f32_16x16x32_bf16 a[16:19], v[52:55], v[160:163], a[16:19]
ds_read_b64_tr_b16 v[44:45], v19 offset:50176
ds_read_b64_tr_b16 v[46:47], v19 offset:50688
v_accvgpr_read_b32 v179, a235
v_mul_f32 v176, s46, v176
v_mfma_f32_16x16x32_bf16 a[20:23], v[52:55], v[164:167], a[20:23]
v_mul_f32 v177, s46, v177
v_mul_f32 v178, s46, v178
v_mul_f32 v179, s46, v179
v_cvt_pk_bf16_f32 v108, v176, v177
v_mfma_f32_16x16x32_bf16 a[24:27], v[52:55], v[168:171], a[24:27]
v_cvt_pk_bf16_f32 v109, v178, v179
v_accvgpr_read_b32 v180, a248
v_accvgpr_read_b32 v181, a249
v_accvgpr_read_b32 v182, a250
v_mfma_f32_16x16x32_bf16 a[28:31], v[52:55], v[172:175], a[28:31]
v_accvgpr_read_b32 v183, a251
v_mul_f32 v180, s46, v180
v_mul_f32 v181, s46, v181
v_mul_f32 v182, s46, v182
s_waitcnt lgkmcnt(6)
v_mfma_f32_16x16x32_bf16 a[32:35], v[56:59], v[160:163], a[32:35]
ds_read_b64_tr_b16 v[48:49], v20 offset:50176
ds_read_b64_tr_b16 v[50:51], v20 offset:50688
v_mul_f32 v183, s46, v183
v_cvt_pk_bf16_f32 v110, v180, v181
v_mfma_f32_16x16x32_bf16 a[36:39], v[56:59], v[164:167], a[36:39]
v_cvt_pk_bf16_f32 v111, v182, v183
v_accvgpr_read_b32 v176, a140
v_accvgpr_read_b32 v177, a141
v_accvgpr_read_b32 v178, a142
v_mfma_f32_16x16x32_bf16 a[40:43], v[56:59], v[168:171], a[40:43]
v_accvgpr_read_b32 v179, a143
v_mul_f32 v176, s46, v176
v_mul_f32 v177, s46, v177
v_mul_f32 v178, s46, v178
v_mfma_f32_16x16x32_bf16 a[44:47], v[56:59], v[172:175], a[44:47]
v_mul_f32 v179, s46, v179
v_cvt_pk_bf16_f32 v112, v176, v177
v_cvt_pk_bf16_f32 v113, v178, v179
v_accvgpr_read_b32 v180, a156
s_waitcnt lgkmcnt(6)
v_mfma_f32_16x16x32_bf16 a[48:51], v[60:63], v[160:163], a[48:51]
ds_read_b64_tr_b16 v[52:53], v21 offset:50176
ds_read_b64_tr_b16 v[54:55], v21 offset:50688
v_accvgpr_read_b32 v181, a157
v_accvgpr_read_b32 v182, a158
v_mfma_f32_16x16x32_bf16 a[52:55], v[60:63], v[164:167], a[52:55]
v_accvgpr_read_b32 v183, a159
v_mul_f32 v180, s46, v180
v_mul_f32 v181, s46, v181
v_mul_f32 v182, s46, v182
v_mfma_f32_16x16x32_bf16 a[56:59], v[60:63], v[168:171], a[56:59]
v_mul_f32 v183, s46, v183
v_cvt_pk_bf16_f32 v114, v180, v181
v_cvt_pk_bf16_f32 v115, v182, v183
v_accvgpr_read_b32 v176, a172
v_mfma_f32_16x16x32_bf16 a[60:63], v[60:63], v[172:175], a[60:63]
v_accvgpr_read_b32 v177, a173
v_accvgpr_read_b32 v178, a174
v_accvgpr_read_b32 v179, a175
v_mul_f32 v176, s46, v176
s_waitcnt lgkmcnt(6)
v_mfma_f32_16x16x32_bf16 a[64:67], v[40:43], v[160:163], a[64:67]
v_mul_f32 v177, s46, v177
v_mul_f32 v178, s46, v178
v_mul_f32 v179, s46, v179
v_cvt_pk_bf16_f32 v116, v176, v177
v_mfma_f32_16x16x32_bf16 a[68:71], v[40:43], v[164:167], a[68:71]
v_cvt_pk_bf16_f32 v117, v178, v179
v_accvgpr_read_b32 v180, a188
v_accvgpr_read_b32 v181, a189
v_mfma_f32_16x16x32_bf16 a[72:75], v[40:43], v[168:171], a[72:75]
v_accvgpr_read_b32 v182, a190
v_accvgpr_read_b32 v183, a191
v_mul_f32 v180, s46, v180
v_mul_f32 v181, s46, v181
v_mfma_f32_16x16x32_bf16 a[76:79], v[40:43], v[172:175], a[76:79]
v_mul_f32 v182, s46, v182
v_mul_f32 v183, s46, v183
v_cvt_pk_bf16_f32 v118, v180, v181
s_waitcnt lgkmcnt(4)
v_mfma_f32_16x16x32_bf16 a[80:83], v[44:47], v[160:163], a[80:83]
v_cvt_pk_bf16_f32 v119, v182, v183
v_accvgpr_read_b32 v176, a204
v_accvgpr_read_b32 v177, a205
v_accvgpr_read_b32 v178, a206
v_mfma_f32_16x16x32_bf16 a[84:87], v[44:47], v[164:167], a[84:87]
v_accvgpr_read_b32 v179, a207
v_mul_f32 v176, s46, v176
v_mul_f32 v177, s46, v177
v_mfma_f32_16x16x32_bf16 a[88:91], v[44:47], v[168:171], a[88:91]
v_mul_f32 v178, s46, v178
v_mul_f32 v179, s46, v179
v_cvt_pk_bf16_f32 v120, v176, v177
v_cvt_pk_bf16_f32 v121, v178, v179
v_mfma_f32_16x16x32_bf16 a[92:95], v[44:47], v[172:175], a[92:95]
v_accvgpr_read_b32 v180, a220
v_accvgpr_read_b32 v181, a221
v_accvgpr_read_b32 v182, a222
s_waitcnt lgkmcnt(2)
v_mfma_f32_16x16x32_bf16 a[96:99], v[48:51], v[160:163], a[96:99]
v_accvgpr_read_b32 v183, a223
v_mul_f32 v180, s46, v180
v_mul_f32 v181, s46, v181
v_mul_f32 v182, s46, v182
v_mfma_f32_16x16x32_bf16 a[100:103], v[48:51], v[164:167], a[100:103]
v_mul_f32 v183, s46, v183
v_cvt_pk_bf16_f32 v122, v180, v181
v_cvt_pk_bf16_f32 v123, v182, v183
v_mfma_f32_16x16x32_bf16 a[104:107], v[48:51], v[168:171], a[104:107]
v_accvgpr_read_b32 v176, a236
v_accvgpr_read_b32 v177, a237
v_accvgpr_read_b32 v178, a238
v_accvgpr_read_b32 v179, a239
v_mfma_f32_16x16x32_bf16 a[108:111], v[48:51], v[172:175], a[108:111]
v_mul_f32 v176, s46, v176
v_mul_f32 v177, s46, v177
v_mul_f32 v178, s46, v178
s_waitcnt lgkmcnt(0)
v_mfma_f32_16x16x32_bf16 a[112:115], v[52:55], v[160:163], a[112:115]
v_mul_f32 v179, s46, v179
v_cvt_pk_bf16_f32 v124, v176, v177
v_cvt_pk_bf16_f32 v125, v178, v179
v_accvgpr_read_b32 v180, a252
v_mfma_f32_16x16x32_bf16 a[116:119], v[52:55], v[164:167], a[116:119]
v_accvgpr_read_b32 v181, a253
v_accvgpr_read_b32 v182, a254
v_accvgpr_read_b32 v183, a255
v_mfma_f32_16x16x32_bf16 a[120:123], v[52:55], v[168:171], a[120:123]
v_mul_f32 v180, s46, v180
v_mul_f32 v181, s46, v181
v_mul_f32 v182, s46, v182
v_mfma_f32_16x16x32_bf16 a[124:127], v[52:55], v[172:175], a[124:127]
v_mul_f32 v183, s46, v183
v_cvt_pk_bf16_f32 v126, v180, v181
v_cvt_pk_bf16_f32 v127, v182, v183
L_XM:
global_load_dwordx2 v[26:27], v24, s[48:49] sc1
s_waitcnt lgkmcnt(0)
s_barrier
s_load_dwordx2 s[76:77], s[4:5], 0x40
s_load_dwordx2 s[78:79], s[4:5], 0x28
s_load_dwordx2 s[80:81], s[4:5], 0x48
s_load_dwordx2 s[82:83], s[4:5], 0x0
v_mov_b32 v228, 0
v_mov_b32 v224, 0x3f803f80
v_mov_b32 v229, 0
v_mov_b32 v225, 0x3f803f80
v_mov_b32 v230, 0
v_mov_b32 v226, 0x3f803f80
v_mov_b32 v231, 0
v_mov_b32 v227, 0x3f803f80
s_mov_b32 s47, 0xbfb8aa3b
s_cmp_eq_u32 s74, 1
s_cbranch_scc0 L_C1
s_waitcnt vmcnt(0)
s_branch L_C1E
L_C1:
s_nop 9
v_accvgpr_read_b32 v40, a0
v_accvgpr_read_b32 v41, a1
v_accvgpr_read_b32 v42, a2
v_accvgpr_read_b32 v43, a3
v_mfma_f32_16x16x32_bf16 a[0:3], v[228:231], v[228:231], 0
v_mul_f32 v44, s47, v40
v_mul_f32 v45, s47, v41
v_mul_f32 v46, s47, v42
v_mul_f32 v47, s47, v43
v_exp_f32 v44, v44
v_exp_f32 v45, v45
v_exp_f32 v46, v46
v_exp_f32 v47, v47
v_add_f32 v44, 1.0, v44
v_add_f32 v45, 1.0, v45
v_add_f32 v46, 1.0, v46
v_add_f32 v47, 1.0, v47
v_rcp_f32 v44, v44
v_rcp_f32 v45, v45
v_rcp_f32 v46, v46
v_rcp_f32 v47, v47
v_mul_f32 v44, v40, v44
v_mul_f32 v45, v41, v45
v_mul_f32 v46, v42, v46
v_mul_f32 v47, v43, v47
v_accvgpr_read_b32 v40, a64
v_accvgpr_read_b32 v41, a65
v_accvgpr_read_b32 v42, a66
v_accvgpr_read_b32 v43, a67
v_mfma_f32_16x16x32_bf16 a[64:67], v[228:231], v[228:231], 0
v_mul_f32 v44, v40, v44
v_mul_f32 v45, v41, v45
v_mul_f32 v46, v42, v46
v_mul_f32 v47, v43, v47
v_cvt_pk_bf16_f32 v48, v44, v45
v_cvt_pk_bf16_f32 v49, v46, v47
v_accvgpr_read_b32 v40, a16
v_accvgpr_read_b32 v41, a17
v_accvgpr_read_b32 v42, a18
v_accvgpr_read_b32 v43, a19
v_mfma_f32_16x16x32_bf16 a[16:19], v[228:231], v[228:231], 0
v_mul_f32 v44, s47, v40
v_mul_f32 v45, s47, v41
v_mul_f32 v46, s47, v42
v_mul_f32 v47, s47, v43
v_exp_f32 v44, v44
v_exp_f32 v45, v45
v_exp_f32 v46, v46
v_exp_f32 v47, v47
v_add_f32 v44, 1.0, v44
v_add_f32 v45, 1.0, v45
v_add_f32 v46, 1.0, v46
v_add_f32 v47, 1.0, v47
v_rcp_f32 v44, v44
v_rcp_f32 v45, v45
v_rcp_f32 v46, v46
v_rcp_f32 v47, v47
v_mul_f32 v44, v40, v44
v_mul_f32 v45, v41, v45
v_mul_f32 v46, v42, v46
v_mul_f32 v47, v43, v47
v_accvgpr_read_b32 v40, a80
v_accvgpr_read_b32 v41, a81
v_accvgpr_read_b32 v42, a82
v_accvgpr_read_b32 v43, a83
v_mfma_f32_16x16x32_bf16 a[80:83], v[228:231], v[228:231], 0
v_mul_f32 v44, v40, v44
v_mul_f32 v45, v41, v45
v_mul_f32 v46, v42, v46
v_mul_f32 v47, v43, v47
v_cvt_pk_bf16_f32 v50, v44, v45
v_cvt_pk_bf16_f32 v51, v46, v47
s_mov_b32 s30, 0
buffer_store_dwordx4 v[48:51], v2, s[88:91], s30 offen offset:0 sc1
v_accvgpr_read_b32 v52, a32
v_accvgpr_read_b32 v53, a33
v_accvgpr_read_b32 v54, a34
v_accvgpr_read_b32 v55, a35
v_mfma_f32_16x16x32_bf16 a[32:35], v[228:231], v[228:231], 0
v_mul_f32 v56, s47, v52
v_mul_f32 v57, s47, v53
v_mul_f32 v58, s47, v54
v_mul_f32 v59, s47, v55
v_exp_f32 v56, v56
v_exp_f32 v57, v57
v_exp_f32 v58, v58
v_exp_f32 v59, v59
v_add_f32 v56, 1.0, v56
v_add_f32 v57, 1.0, v57
v_add_f32 v58, 1.0, v58
v_add_f32 v59, 1.0, v59
v_rcp_f32 v56, v56
v_rcp_f32 v57, v57
v_rcp_f32 v58, v58
v_rcp_f32 v59, v59
v_mul_f32 v56, v52, v56
v_mul_f32 v57, v53, v57
v_mul_f32 v58, v54, v58
v_mul_f32 v59, v55, v59
v_accvgpr_read_b32 v52, a96
v_accvgpr_read_b32 v53, a97
v_accvgpr_read_b32 v54, a98
v_accvgpr_read_b32 v55, a99
v_mfma_f32_16x16x32_bf16 a[96:99], v[228:231], v[228:231], 0
v_mul_f32 v56, v52, v56
v_mul_f32 v57, v53, v57
v_mul_f32 v58, v54, v58
v_mul_f32 v59, v55, v59
v_cvt_pk_bf16_f32 v60, v56, v57
v_cvt_pk_bf16_f32 v61, v58, v59
v_accvgpr_read_b32 v52, a48
v_accvgpr_read_b32 v53, a49
v_accvgpr_read_b32 v54, a50
v_accvgpr_read_b32 v55, a51
v_mfma_f32_16x16x32_bf16 a[48:51], v[228:231], v[228:231], 0
v_mul_f32 v56, s47, v52
v_mul_f32 v57, s47, v53
v_mul_f32 v58, s47, v54
v_mul_f32 v59, s47, v55
v_exp_f32 v56, v56
v_exp_f32 v57, v57
v_exp_f32 v58, v58
v_exp_f32 v59, v59
v_add_f32 v56, 1.0, v56
v_add_f32 v57, 1.0, v57
v_add_f32 v58, 1.0, v58
v_add_f32 v59, 1.0, v59
v_rcp_f32 v56, v56
v_rcp_f32 v57, v57
v_rcp_f32 v58, v58
v_rcp_f32 v59, v59
v_mul_f32 v56, v52, v56
v_mul_f32 v57, v53, v57
v_mul_f32 v58, v54, v58
v_mul_f32 v59, v55, v59
v_accvgpr_read_b32 v52, a112
v_accvgpr_read_b32 v53, a113
v_accvgpr_read_b32 v54, a114
v_accvgpr_read_b32 v55, a115
v_mfma_f32_16x16x32_bf16 a[112:115], v[228:231], v[228:231], 0
v_mul_f32 v56, v52, v56
v_mul_f32 v57, v53, v57
v_mul_f32 v58, v54, v58
v_mul_f32 v59, v55, v59
v_cvt_pk_bf16_f32 v62, v56, v57
v_cvt_pk_bf16_f32 v63, v58, v59
s_mov_b32 s31, 0
buffer_store_dwordx4 v[60:63], v2, s[88:91], s31 offen offset:1024 sc1
v_accvgpr_read_b32 v40, a4
v_accvgpr_read_b32 v41, a5
v_accvgpr_read_b32 v42, a6
v_accvgpr_read_b32 v43, a7
v_mfma_f32_16x16x32_bf16 a[4:7], v[228:231], v[228:231], 0
v_mul_f32 v44, s47, v40
v_mul_f32 v45, s47, v41
v_mul_f32 v46, s47, v42
v_mul_f32 v47, s47, v43
v_exp_f32 v44, v44
v_exp_f32 v45, v45
v_exp_f32 v46, v46
v_exp_f32 v47, v47
v_add_f32 v44, 1.0, v44
v_add_f32 v45, 1.0, v45
v_add_f32 v46, 1.0, v46
v_add_f32 v47, 1.0, v47
v_rcp_f32 v44, v44
v_rcp_f32 v45, v45
v_rcp_f32 v46, v46
v_rcp_f32 v47, v47
v_mul_f32 v44, v40, v44
v_mul_f32 v45, v41, v45
v_mul_f32 v46, v42, v46
v_mul_f32 v47, v43, v47
v_accvgpr_read_b32 v40, a68
v_accvgpr_read_b32 v41, a69
v_accvgpr_read_b32 v42, a70
v_accvgpr_read_b32 v43, a71
v_mfma_f32_16x16x32_bf16 a[68:71], v[228:231], v[228:231], 0
v_mul_f32 v44, v40, v44
v_mul_f32 v45, v41, v45
v_mul_f32 v46, v42, v46
v_mul_f32 v47, v43, v47
v_cvt_pk_bf16_f32 v48, v44, v45
v_cvt_pk_bf16_f32 v49, v46, v47
v_accvgpr_read_b32 v40, a20
v_accvgpr_read_b32 v41, a21
v_accvgpr_read_b32 v42, a22
v_accvgpr_read_b32 v43, a23
v_mfma_f32_16x16x32_bf16 a[20:23], v[228:231], v[228:231], 0
v_mul_f32 v44, s47, v40
v_mul_f32 v45, s47, v41
v_mul_f32 v46, s47, v42
v_mul_f32 v47, s47, v43
v_exp_f32 v44, v44
v_exp_f32 v45, v45
v_exp_f32 v46, v46
v_exp_f32 v47, v47
v_add_f32 v44, 1.0, v44
v_add_f32 v45, 1.0, v45
v_add_f32 v46, 1.0, v46
v_add_f32 v47, 1.0, v47
v_rcp_f32 v44, v44
v_rcp_f32 v45, v45
v_rcp_f32 v46, v46
v_rcp_f32 v47, v47
v_mul_f32 v44, v40, v44
v_mul_f32 v45, v41, v45
v_mul_f32 v46, v42, v46
v_mul_f32 v47, v43, v47
v_accvgpr_read_b32 v40, a84
v_accvgpr_read_b32 v41, a85
v_accvgpr_read_b32 v42, a86
v_accvgpr_read_b32 v43, a87
v_mfma_f32_16x16x32_bf16 a[84:87], v[228:231], v[228:231], 0
v_mul_f32 v44, v40, v44
v_mul_f32 v45, v41, v45
v_mul_f32 v46, v42, v46
v_mul_f32 v47, v43, v47
v_cvt_pk_bf16_f32 v50, v44, v45
v_cvt_pk_bf16_f32 v51, v46, v47
s_mov_b32 s30, 376832
buffer_store_dwordx4 v[48:51], v2, s[88:91], s30 offen offset:0 sc1
v_accvgpr_read_b32 v52, a36
v_accvgpr_read_b32 v53, a37
v_accvgpr_read_b32 v54, a38
v_accvgpr_read_b32 v55, a39
v_mfma_f32_16x16x32_bf16 a[36:39], v[228:231], v[228:231], 0
v_mul_f32 v56, s47, v52
v_mul_f32 v57, s47, v53
v_mul_f32 v58, s47, v54
v_mul_f32 v59, s47, v55
v_exp_f32 v56, v56
v_exp_f32 v57, v57
v_exp_f32 v58, v58
v_exp_f32 v59, v59
v_add_f32 v56, 1.0, v56
v_add_f32 v57, 1.0, v57
v_add_f32 v58, 1.0, v58
v_add_f32 v59, 1.0, v59
v_rcp_f32 v56, v56
v_rcp_f32 v57, v57
v_rcp_f32 v58, v58
v_rcp_f32 v59, v59
v_mul_f32 v56, v52, v56
v_mul_f32 v57, v53, v57
v_mul_f32 v58, v54, v58
v_mul_f32 v59, v55, v59
v_accvgpr_read_b32 v52, a100
v_accvgpr_read_b32 v53, a101
v_accvgpr_read_b32 v54, a102
v_accvgpr_read_b32 v55, a103
v_mfma_f32_16x16x32_bf16 a[100:103], v[228:231], v[228:231], 0
v_mul_f32 v56, v52, v56
v_mul_f32 v57, v53, v57
v_mul_f32 v58, v54, v58
v_mul_f32 v59, v55, v59
v_cvt_pk_bf16_f32 v60, v56, v57
v_cvt_pk_bf16_f32 v61, v58, v59
v_accvgpr_read_b32 v52, a52
v_accvgpr_read_b32 v53, a53
v_accvgpr_read_b32 v54, a54
v_accvgpr_read_b32 v55, a55
v_mfma_f32_16x16x32_bf16 a[52:55], v[228:231], v[228:231], 0
v_mul_f32 v56, s47, v52
v_mul_f32 v57, s47, v53
v_mul_f32 v58, s47, v54
v_mul_f32 v59, s47, v55
v_exp_f32 v56, v56
v_exp_f32 v57, v57
v_exp_f32 v58, v58
v_exp_f32 v59, v59
v_add_f32 v56, 1.0, v56
v_add_f32 v57, 1.0, v57
v_add_f32 v58, 1.0, v58
v_add_f32 v59, 1.0, v59
v_rcp_f32 v56, v56
v_rcp_f32 v57, v57
v_rcp_f32 v58, v58
v_rcp_f32 v59, v59
v_mul_f32 v56, v52, v56
v_mul_f32 v57, v53, v57
v_mul_f32 v58, v54, v58
v_mul_f32 v59, v55, v59
v_accvgpr_read_b32 v52, a116
v_accvgpr_read_b32 v53, a117
v_accvgpr_read_b32 v54, a118
v_accvgpr_read_b32 v55, a119
v_mfma_f32_16x16x32_bf16 a[116:119], v[228:231], v[228:231], 0
v_mul_f32 v56, v52, v56
v_mul_f32 v57, v53, v57
v_mul_f32 v58, v54, v58
v_mul_f32 v59, v55, v59
v_cvt_pk_bf16_f32 v62, v56, v57
v_cvt_pk_bf16_f32 v63, v58, v59
s_mov_b32 s31, 376832
buffer_store_dwordx4 v[60:63], v2, s[88:91], s31 offen offset:1024 sc1
s_waitcnt vmcnt(4)
L_C1E:
v_cmp_ne_u64 vcc, s[98:99], v[26:27]
s_nop 4
s_cbranch_vccz L_PK_A
s_mov_b32 s31, 1048576
L_PL_A:
s_sleep 2
global_load_dwordx2 v[26:27], v24, s[48:49] sc1
s_waitcnt vmcnt(0)
v_cmp_ne_u64 vcc, s[98:99], v[26:27]
s_sub_u32 s31, s31, 1
s_cbranch_scc1 L_PK_A
s_nop 4
s_cbranch_vccnz L_PL_A
L_PK_A:
v_readlane_b32 s39, v5, 0
v_readlane_b32 s40, v5, 1
v_readlane_b32 s41, v5, 2
s_mov_b32 s34, -1
s_lshr_b32 s24, s7, 1
s_lshl_b32 s24, s24, 16
s_lshl_b32 s25, s75, 12
s_add_u32 s43, s24, s25
s_add_u32 s42, s43, 16384
s_cmp_eq_u32 s74, 1
s_cbranch_scc0 L_C2
s_add_u32 m0, s43, 0
s_nop 0
buffer_load_dwordx4 v2, s[12:15], s39 offen sc1 lds
buffer_load_dwordx4 v2, s[12:15], s39 offen offset:1024 sc1 lds
buffer_load_dwordx4 v2, s[12:15], s39 offen offset:2048 sc1 lds
buffer_load_dwordx4 v2, s[12:15], s39 offen offset:3072 sc1 lds
s_add_u32 m0, s43, 8192
s_nop 0
buffer_load_dwordx4 v9, s[12:15], s39 offen sc1 lds
buffer_load_dwordx4 v9, s[12:15], s39 offen offset:1024 sc1 lds
buffer_load_dwordx4 v9, s[12:15], s39 offen offset:2048 sc1 lds
buffer_load_dwordx4 v9, s[12:15], s39 offen offset:3072 sc1 lds
s_add_u32 m0, s42, 0
s_nop 0
buffer_load_dwordx4 v2, s[12:15], s40 offen sc1 lds
buffer_load_dwordx4 v2, s[12:15], s40 offen offset:1024 sc1 lds
buffer_load_dwordx4 v2, s[12:15], s40 offen offset:2048 sc1 lds
buffer_load_dwordx4 v2, s[12:15], s40 offen offset:3072 sc1 lds
s_add_u32 m0, s43, 32768
s_nop 0
buffer_load_dwordx4 v2, s[16:19], s39 offen sc1 lds
buffer_load_dwordx4 v2, s[16:19], s39 offen offset:1024 sc1 lds
buffer_load_dwordx4 v2, s[16:19], s39 offen offset:2048 sc1 lds
buffer_load_dwordx4 v2, s[16:19], s39 offen offset:3072 sc1 lds
s_branch L_C2E
L_C2:
s_add_u32 m0, s43, 0
s_nop 0
buffer_load_dwordx4 v2, s[12:15], s39 offen sc1 lds
v_accvgpr_read_b32 v40, a8
v_accvgpr_read_b32 v41, a9
v_accvgpr_read_b32 v42, a10
v_accvgpr_read_b32 v43, a11
v_mfma_f32_16x16x32_bf16 a[8:11], v[228:231], v[228:231], 0
v_mul_f32 v44, s47, v40
v_mul_f32 v45, s47, v41
v_mul_f32 v46, s47, v42
v_mul_f32 v47, s47, v43
v_exp_f32 v44, v44
buffer_load_dwordx4 v2, s[12:15], s39 offen offset:1024 sc1 lds
v_exp_f32 v45, v45
v_exp_f32 v46, v46
v_exp_f32 v47, v47
v_add_f32 v44, 1.0, v44
v_add_f32 v45, 1.0, v45
v_add_f32 v46, 1.0, v46
v_add_f32 v47, 1.0, v47
v_rcp_f32 v44, v44
v_rcp_f32 v45, v45
v_rcp_f32 v46, v46
buffer_load_dwordx4 v2, s[12:15], s39 offen offset:2048 sc1 lds
v_rcp_f32 v47, v47
v_mul_f32 v44, v40, v44
v_mul_f32 v45, v41, v45
v_mul_f32 v46, v42, v46
v_mul_f32 v47, v43, v47
v_accvgpr_read_b32 v40, a72
v_accvgpr_read_b32 v41, a73
v_accvgpr_read_b32 v42, a74
v_accvgpr_read_b32 v43, a75
v_mfma_f32_16x16x32_bf16 a[72:75], v[228:231], v[228:231], 0
buffer_load_dwordx4 v2, s[12:15], s39 offen offset:3072 sc1 lds
v_mul_f32 v44, v40, v44
v_mul_f32 v45, v41, v45
v_mul_f32 v46, v42, v46
v_mul_f32 v47, v43, v47
v_cvt_pk_bf16_f32 v48, v44, v45
v_cvt_pk_bf16_f32 v49, v46, v47
v_accvgpr_read_b32 v40, a24
v_accvgpr_read_b32 v41, a25
v_accvgpr_read_b32 v42, a26
v_accvgpr_read_b32 v43, a27
s_add_u32 m0, s43, 8192
s_nop 0
buffer_load_dwordx4 v9, s[12:15], s39 offen sc1 lds
v_mfma_f32_16x16x32_bf16 a[24:27], v[228:231], v[228:231], 0
v_mul_f32 v44, s47, v40
v_mul_f32 v45, s47, v41
v_mul_f32 v46, s47, v42
v_mul_f32 v47, s47, v43
v_exp_f32 v44, v44
v_exp_f32 v45, v45
v_exp_f32 v46, v46
v_exp_f32 v47, v47
v_add_f32 v44, 1.0, v44
buffer_load_dwordx4 v9, s[12:15], s39 offen offset:1024 sc1 lds
v_add_f32 v45, 1.0, v45
v_add_f32 v46, 1.0, v46
v_add_f32 v47, 1.0, v47
v_rcp_f32 v44, v44
v_rcp_f32 v45, v45
v_rcp_f32 v46, v46
v_rcp_f32 v47, v47
v_mul_f32 v44, v40, v44
v_mul_f32 v45, v41, v45
v_mul_f32 v46, v42, v46
buffer_load_dwordx4 v9, s[12:15], s39 offen offset:2048 sc1 lds
v_mul_f32 v47, v43, v47
v_accvgpr_read_b32 v40, a88
v_accvgpr_read_b32 v41, a89
v_accvgpr_read_b32 v42, a90
v_accvgpr_read_b32 v43, a91
v_mfma_f32_16x16x32_bf16 a[88:91], v[228:231], v[228:231], 0
v_mul_f32 v44, v40, v44
v_mul_f32 v45, v41, v45
v_mul_f32 v46, v42, v46
v_mul_f32 v47, v43, v47
buffer_load_dwordx4 v9, s[12:15], s39 offen offset:3072 sc1 lds
v_cvt_pk_bf16_f32 v50, v44, v45
v_cvt_pk_bf16_f32 v51, v46, v47
s_mov_b32 s30, 753664
buffer_store_dwordx4 v[48:51], v2, s[88:91], s30 offen offset:0 sc1
v_accvgpr_read_b32 v52, a40
v_accvgpr_read_b32 v53, a41
v_accvgpr_read_b32 v54, a42
v_accvgpr_read_b32 v55, a43
v_mfma_f32_16x16x32_bf16 a[40:43], v[228:231], v[228:231], 0
v_mul_f32 v56, s47, v52
v_mul_f32 v57, s47, v53
s_add_u32 m0, s42, 0
s_nop 0
buffer_load_dwordx4 v2, s[12:15], s40 offen sc1 lds
v_mul_f32 v58, s47, v54
v_mul_f32 v59, s47, v55
v_exp_f32 v56, v56
v_exp_f32 v57, v57
v_exp_f32 v58, v58
v_exp_f32 v59, v59
v_add_f32 v56, 1.0, v56
v_add_f32 v57, 1.0, v57
v_add_f32 v58, 1.0, v58
v_add_f32 v59, 1.0, v59
buffer_load_dwordx4 v2, s[12:15], s40 offen offset:1024 sc1 lds
v_rcp_f32 v56, v56
v_rcp_f32 v57, v57
v_rcp_f32 v58, v58
v_rcp_f32 v59, v59
v_mul_f32 v56, v52, v56
v_mul_f32 v57, v53, v57
v_mul_f32 v58, v54, v58
v_mul_f32 v59, v55, v59
v_accvgpr_read_b32 v52, a104
v_accvgpr_read_b32 v53, a105
buffer_load_dwordx4 v2, s[12:15], s40 offen offset:2048 sc1 lds
v_accvgpr_read_b32 v54, a106
v_accvgpr_read_b32 v55, a107
v_mfma_f32_16x16x32_bf16 a[104:107], v[228:231], v[228:231], 0
v_mul_f32 v56, v52, v56
v_mul_f32 v57, v53, v57
)ASM"
R"ASM(v_mul_f32 v58, v54, v58
v_mul_f32 v59, v55, v59
v_cvt_pk_bf16_f32 v60, v56, v57
v_cvt_pk_bf16_f32 v61, v58, v59
v_accvgpr_read_b32 v52, a56
buffer_load_dwordx4 v2, s[12:15], s40 offen offset:3072 sc1 lds
v_accvgpr_read_b32 v53, a57
v_accvgpr_read_b32 v54, a58
v_accvgpr_read_b32 v55, a59
v_mfma_f32_16x16x32_bf16 a[56:59], v[228:231], v[228:231], 0
v_mul_f32 v56, s47, v52
v_mul_f32 v57, s47, v53
v_mul_f32 v58, s47, v54
v_mul_f32 v59, s47, v55
v_exp_f32 v56, v56
v_exp_f32 v57, v57
s_add_u32 m0, s43, 32768
s_nop 0
buffer_load_dwordx4 v2, s[16:19], s39 offen sc1 lds
v_exp_f32 v58, v58
v_exp_f32 v59, v59
v_add_f32 v56, 1.0, v56
v_add_f32 v57, 1.0, v57
v_add_f32 v58, 1.0, v58
v_add_f32 v59, 1.0, v59
v_rcp_f32 v56, v56
v_rcp_f32 v57, v57
v_rcp_f32 v58, v58
v_rcp_f32 v59, v59
buffer_load_dwordx4 v2, s[16:19], s39 offen offset:1024 sc1 lds
v_mul_f32 v56, v52, v56
v_mul_f32 v57, v53, v57
v_mul_f32 v58, v54, v58
v_mul_f32 v59, v55, v59
v_accvgpr_read_b32 v52, a120
v_accvgpr_read_b32 v53, a121
v_accvgpr_read_b32 v54, a122
v_accvgpr_read_b32 v55, a123
v_mfma_f32_16x16x32_bf16 a[120:123], v[228:231], v[228:231], 0
v_mul_f32 v56, v52, v56
buffer_load_dwordx4 v2, s[16:19], s39 offen offset:2048 sc1 lds
v_mul_f32 v57, v53, v57
v_mul_f32 v58, v54, v58
v_mul_f32 v59, v55, v59
v_cvt_pk_bf16_f32 v62, v56, v57
v_cvt_pk_bf16_f32 v63, v58, v59
s_mov_b32 s31, 753664
buffer_store_dwordx4 v[60:63], v2, s[88:91], s31 offen offset:1024 sc1
v_accvgpr_read_b32 v40, a12
v_accvgpr_read_b32 v41, a13
v_accvgpr_read_b32 v42, a14
v_accvgpr_read_b32 v43, a15
buffer_load_dwordx4 v2, s[16:19], s39 offen offset:3072 sc1 lds
v_mfma_f32_16x16x32_bf16 a[12:15], v[228:231], v[228:231], 0
v_mul_f32 v44, s47, v40
v_mul_f32 v45, s47, v41
v_mul_f32 v46, s47, v42
v_mul_f32 v47, s47, v43
v_exp_f32 v44, v44
v_exp_f32 v45, v45
v_exp_f32 v46, v46
v_exp_f32 v47, v47
v_add_f32 v44, 1.0, v44
v_add_f32 v45, 1.0, v45
v_add_f32 v46, 1.0, v46
v_add_f32 v47, 1.0, v47
v_rcp_f32 v44, v44
v_rcp_f32 v45, v45
v_rcp_f32 v46, v46
v_rcp_f32 v47, v47
v_mul_f32 v44, v40, v44
v_mul_f32 v45, v41, v45
v_mul_f32 v46, v42, v46
v_mul_f32 v47, v43, v47
v_accvgpr_read_b32 v40, a76
v_accvgpr_read_b32 v41, a77
v_accvgpr_read_b32 v42, a78
v_accvgpr_read_b32 v43, a79
v_mfma_f32_16x16x32_bf16 a[76:79], v[228:231], v[228:231], 0
v_mul_f32 v44, v40, v44
v_mul_f32 v45, v41, v45
v_mul_f32 v46, v42, v46
v_mul_f32 v47, v43, v47
v_cvt_pk_bf16_f32 v48, v44, v45
v_cvt_pk_bf16_f32 v49, v46, v47
v_accvgpr_read_b32 v40, a28
v_accvgpr_read_b32 v41, a29
v_accvgpr_read_b32 v42, a30
v_accvgpr_read_b32 v43, a31
v_mfma_f32_16x16x32_bf16 a[28:31], v[228:231], v[228:231], 0
v_mul_f32 v44, s47, v40
v_mul_f32 v45, s47, v41
v_mul_f32 v46, s47, v42
v_mul_f32 v47, s47, v43
v_exp_f32 v44, v44
v_exp_f32 v45, v45
v_exp_f32 v46, v46
v_exp_f32 v47, v47
v_add_f32 v44, 1.0, v44
v_add_f32 v45, 1.0, v45
v_add_f32 v46, 1.0, v46
v_add_f32 v47, 1.0, v47
v_rcp_f32 v44, v44
v_rcp_f32 v45, v45
v_rcp_f32 v46, v46
v_rcp_f32 v47, v47
v_mul_f32 v44, v40, v44
v_mul_f32 v45, v41, v45
v_mul_f32 v46, v42, v46
v_mul_f32 v47, v43, v47
v_accvgpr_read_b32 v40, a92
v_accvgpr_read_b32 v41, a93
v_accvgpr_read_b32 v42, a94
v_accvgpr_read_b32 v43, a95
v_mfma_f32_16x16x32_bf16 a[92:95], v[228:231], v[228:231], 0
v_mul_f32 v44, v40, v44
v_mul_f32 v45, v41, v45
v_mul_f32 v46, v42, v46
v_mul_f32 v47, v43, v47
v_cvt_pk_bf16_f32 v50, v44, v45
v_cvt_pk_bf16_f32 v51, v46, v47
s_mov_b32 s30, 1130496
buffer_store_dwordx4 v[48:51], v2, s[88:91], s30 offen offset:0 sc1
v_accvgpr_read_b32 v52, a44
v_accvgpr_read_b32 v53, a45
v_accvgpr_read_b32 v54, a46
v_accvgpr_read_b32 v55, a47
v_mfma_f32_16x16x32_bf16 a[44:47], v[228:231], v[228:231], 0
v_mul_f32 v56, s47, v52
v_mul_f32 v57, s47, v53
v_mul_f32 v58, s47, v54
v_mul_f32 v59, s47, v55
v_exp_f32 v56, v56
v_exp_f32 v57, v57
v_exp_f32 v58, v58
v_exp_f32 v59, v59
v_add_f32 v56, 1.0, v56
v_add_f32 v57, 1.0, v57
v_add_f32 v58, 1.0, v58
v_add_f32 v59, 1.0, v59
v_rcp_f32 v56, v56
v_rcp_f32 v57, v57
v_rcp_f32 v58, v58
v_rcp_f32 v59, v59
v_mul_f32 v56, v52, v56
v_mul_f32 v57, v53, v57
v_mul_f32 v58, v54, v58
v_mul_f32 v59, v55, v59
v_accvgpr_read_b32 v52, a108
v_accvgpr_read_b32 v53, a109
v_accvgpr_read_b32 v54, a110
v_accvgpr_read_b32 v55, a111
v_mfma_f32_16x16x32_bf16 a[108:111], v[228:231], v[228:231], 0
v_mul_f32 v56, v52, v56
v_mul_f32 v57, v53, v57
v_mul_f32 v58, v54, v58
v_mul_f32 v59, v55, v59
v_cvt_pk_bf16_f32 v60, v56, v57
v_cvt_pk_bf16_f32 v61, v58, v59
v_accvgpr_read_b32 v52, a60
v_accvgpr_read_b32 v53, a61
v_accvgpr_read_b32 v54, a62
v_accvgpr_read_b32 v55, a63
v_mfma_f32_16x16x32_bf16 a[60:63], v[228:231], v[228:231], 0
v_mul_f32 v56, s47, v52
v_mul_f32 v57, s47, v53
v_mul_f32 v58, s47, v54
v_mul_f32 v59, s47, v55
v_exp_f32 v56, v56
v_exp_f32 v57, v57
v_exp_f32 v58, v58
v_exp_f32 v59, v59
v_add_f32 v56, 1.0, v56
v_add_f32 v57, 1.0, v57
v_add_f32 v58, 1.0, v58
v_add_f32 v59, 1.0, v59
v_rcp_f32 v56, v56
v_rcp_f32 v57, v57
v_rcp_f32 v58, v58
v_rcp_f32 v59, v59
v_mul_f32 v56, v52, v56
v_mul_f32 v57, v53, v57
v_mul_f32 v58, v54, v58
v_mul_f32 v59, v55, v59
v_accvgpr_read_b32 v52, a124
v_accvgpr_read_b32 v53, a125
v_accvgpr_read_b32 v54, a126
v_accvgpr_read_b32 v55, a127
v_mfma_f32_16x16x32_bf16 a[124:127], v[228:231], v[228:231], 0
v_mul_f32 v56, v52, v56
v_mul_f32 v57, v53, v57
v_mul_f32 v58, v54, v58
v_mul_f32 v59, v55, v59
v_cvt_pk_bf16_f32 v62, v56, v57
v_cvt_pk_bf16_f32 v63, v58, v59
s_mov_b32 s31, 1130496
buffer_store_dwordx4 v[60:63], v2, s[88:91], s31 offen offset:1024 sc1
L_C2E:
s_add_u32 m0, s42, 8192
s_nop 0
buffer_load_dwordx4 v9, s[12:15], s40 offen sc1 lds
buffer_load_dwordx4 v9, s[12:15], s40 offen offset:1024 sc1 lds
buffer_load_dwordx4 v9, s[12:15], s40 offen offset:2048 sc1 lds
buffer_load_dwordx4 v9, s[12:15], s40 offen offset:3072 sc1 lds
v_mfma_f32_16x16x32_bf16 a[192:195], v[228:231], v[228:231], 0
v_mfma_f32_16x16x32_bf16 a[196:199], v[228:231], v[228:231], 0
v_mfma_f32_16x16x32_bf16 a[200:203], v[228:231], v[228:231], 0
v_mfma_f32_16x16x32_bf16 a[204:207], v[228:231], v[228:231], 0
s_cmp_eq_u32 s36, 1
s_cbranch_scc1 L_QX_skip
s_lshl_b32 s22, s75, 14
s_add_u32 s22, s22, 131072
v_add_u32 v20, s22, v2
ds_write_b128 v20, v[64:67] offset:0
ds_write_b128 v20, v[68:71] offset:1024
ds_write_b128 v20, v[72:75] offset:2048
ds_write_b128 v20, v[76:79] offset:3072
ds_write_b128 v20, v[80:83] offset:4096
ds_write_b128 v20, v[84:87] offset:5120
ds_write_b128 v20, v[88:91] offset:6144
ds_write_b128 v20, v[92:95] offset:7168
ds_write_b128 v20, v[96:99] offset:8192
ds_write_b128 v20, v[100:103] offset:9216
ds_write_b128 v20, v[104:107] offset:10240
ds_write_b128 v20, v[108:111] offset:11264
ds_write_b128 v20, v[112:115] offset:12288
ds_write_b128 v20, v[116:119] offset:13312
ds_write_b128 v20, v[120:123] offset:14336
ds_write_b128 v20, v[124:127] offset:15360
L_QX_skip:
s_mov_b32 s62, 0
s_waitcnt vmcnt(4) lgkmcnt(0)
s_barrier
s_cmp_eq_u32 s7, 0
s_cbranch_scc0 L_FS_C
s_lshl_b32 s31, s6, 6
s_add_u32 s31, s31, 49152
v_mov_b32 v42, s31
v_mov_b32 v40, s98
v_mov_b32 v41, s99
s_mov_b64 exec, 1
global_store_dwordx2 v42, v[40:41], s[48:49] sc1
s_mov_b64 exec, -1
L_FS_C:
ds_read_b128 a[128:131], v3 offset:0
ds_read_b128 a[132:135], v3 offset:1024
ds_read_b128 a[136:139], v3 offset:2048
ds_read_b128 a[140:143], v3 offset:3072
ds_read_b128 a[144:147], v3 offset:4096
ds_read_b128 a[148:151], v3 offset:5120
ds_read_b128 a[152:155], v3 offset:6144
ds_read_b128 a[156:159], v3 offset:7168
s_cmp_eq_u32 s44, 0
s_cbranch_scc1 L_START_D
s_nop 9
s_waitcnt lgkmcnt(0)
v_mfma_f32_16x16x32_bf16 v[128:131], a[128:131], v[64:67], 0
ds_read_b128 a[160:163], v3 offset:8192
v_mfma_f32_16x16x32_bf16 v[132:135], a[128:131], v[80:83], 0
s_add_u32 m0, s43, 40960
v_mfma_f32_16x16x32_bf16 v[136:139], a[128:131], v[96:99], 0
buffer_load_dwordx4 v9, s[16:19], s39 offen sc1 lds
v_mfma_f32_16x16x32_bf16 v[140:143], a[128:131], v[112:115], 0
ds_read_b128 a[164:167], v3 offset:9216
v_mfma_f32_16x16x32_bf16 v[128:131], a[132:135], v[68:71], v[128:131]
ds_read_b128 a[168:171], v3 offset:10240
v_mfma_f32_16x16x32_bf16 v[132:135], a[132:135], v[84:87], v[132:135]
s_getpc_b64 s[94:95]
L_CP_D3:
s_add_u32 s94, s94, L_EVENT-L_CP_D3
s_addc_u32 s95, s95, 0
v_mfma_f32_16x16x32_bf16 v[136:139], a[132:135], v[100:103], v[136:139]
ds_read_b128 a[172:175], v3 offset:11264
s_lshr_b32 s22, s6, 3
v_mfma_f32_16x16x32_bf16 v[140:143], a[132:135], v[116:119], v[140:143]
s_lshl_b32 s22, s22, 2
s_add_u32 s22, s22, s7
v_mfma_f32_16x16x32_bf16 v[128:131], a[136:139], v[72:75], v[128:131]
ds_read_b128 a[176:179], v3 offset:12288
s_mul_i32 s22, s22, 384
v_mfma_f32_16x16x32_bf16 v[132:135], a[136:139], v[88:91], v[132:135]
buffer_load_dwordx4 v9, s[16:19], s39 offen offset:1024 sc1 lds
v_mfma_f32_16x16x32_bf16 v[136:139], a[136:139], v[104:107], v[136:139]
ds_read_b128 a[180:183], v3 offset:13312
v_min_u32 v22, 2, v1
v_mfma_f32_16x16x32_bf16 v[140:143], a[136:139], v[120:123], v[140:143]
v_lshlrev_b32 v22, 7, v22
v_add_u32 v22, s22, v22
v_mfma_f32_16x16x32_bf16 v[128:131], a[140:143], v[76:79], v[128:131]
ds_read_b128 a[184:187], v3 offset:14336
v_min_u32 v22, L_CODE_END-L_EVENT-4, v22
v_mfma_f32_16x16x32_bf16 v[132:135], a[140:143], v[92:95], v[132:135]
s_and_b32 s20, s6, 7
s_lshr_b32 s21, s6, 3
v_mfma_f32_16x16x32_bf16 v[136:139], a[140:143], v[108:111], v[136:139]
ds_read_b128 a[188:191], v3 offset:15360
s_and_b32 s91, s20, 3
v_mfma_f32_16x16x32_bf16 v[140:143], a[140:143], v[124:127], v[140:143]
s_lshr_b32 s22, s20, 2
s_lshl_b32 s22, s22, 3
v_mfma_f32_16x16x32_bf16 v[144:147], a[144:147], v[64:67], 0
buffer_load_dwordx4 v9, s[16:19], s39 offen offset:2048 sc1 lds
v_mfma_f32_16x16x32_bf16 v[148:151], a[144:147], v[80:83], 0
v_exp_f32 v128, v128
v_mfma_f32_16x16x32_bf16 v[152:155], a[144:147], v[96:99], 0
v_exp_f32 v129, v129
v_mfma_f32_16x16x32_bf16 v[156:159], a[144:147], v[112:115], 0
v_exp_f32 v130, v130
v_mfma_f32_16x16x32_bf16 v[144:147], a[148:151], v[68:71], v[144:147]
v_exp_f32 v131, v131
v_mfma_f32_16x16x32_bf16 v[148:151], a[148:151], v[84:87], v[148:151]
v_exp_f32 v132, v132
v_mfma_f32_16x16x32_bf16 v[152:155], a[148:151], v[100:103], v[152:155]
v_exp_f32 v133, v133
v_mfma_f32_16x16x32_bf16 v[156:159], a[148:151], v[116:119], v[156:159]
buffer_load_dwordx4 v9, s[16:19], s39 offen offset:3072 sc1 lds
v_mfma_f32_16x16x32_bf16 v[144:147], a[152:155], v[72:75], v[144:147]
v_exp_f32 v134, v134
v_mfma_f32_16x16x32_bf16 v[148:151], a[152:155], v[88:91], v[148:151]
v_exp_f32 v135, v135
v_mfma_f32_16x16x32_bf16 v[152:155], a[152:155], v[104:107], v[152:155]
v_exp_f32 v136, v136
v_mfma_f32_16x16x32_bf16 v[156:159], a[152:155], v[120:123], v[156:159]
v_exp_f32 v137, v137
v_mfma_f32_16x16x32_bf16 v[144:147], a[156:159], v[76:79], v[144:147]
v_exp_f32 v138, v138
v_mfma_f32_16x16x32_bf16 v[148:151], a[156:159], v[92:95], v[148:151]
v_exp_f32 v139, v139
v_mfma_f32_16x16x32_bf16 v[152:155], a[156:159], v[108:111], v[152:155]
v_exp_f32 v140, v140
v_mfma_f32_16x16x32_bf16 v[156:159], a[156:159], v[124:127], v[156:159]
s_waitcnt vmcnt(8) lgkmcnt(0)
s_barrier
v_xor_b32 v3, 16384, v3
v_xor_b32 v4, 16384, v4
v_mfma_f32_16x16x32_bf16 v[160:163], a[160:163], v[64:67], 0
ds_read_b128 a[128:131], v3 offset:0
s_add_u32 m0, s43, 0
v_mfma_f32_16x16x32_bf16 v[164:167], a[160:163], v[80:83], 0
buffer_load_dwordx4 v2, s[12:15], s41 offen sc1 lds
v_mfma_f32_16x16x32_bf16 v[168:171], a[160:163], v[96:99], 0
ds_read_b128 a[132:135], v3 offset:1024
v_cvt_pk_bf16_f32 v192, v128, v129
v_mfma_f32_16x16x32_bf16 v[172:175], a[160:163], v[112:115], 0
buffer_load_dwordx4 v2, s[12:15], s41 offen offset:1024 sc1 lds
v_mfma_f32_16x16x32_bf16 v[160:163], a[164:167], v[68:71], v[160:163]
ds_read_b128 a[136:139], v3 offset:2048
v_cvt_pk_bf16_f32 v193, v130, v131
v_mfma_f32_16x16x32_bf16 v[164:167], a[164:167], v[84:87], v[164:167]
buffer_load_dwordx4 v2, s[12:15], s41 offen offset:2048 sc1 lds
v_mfma_f32_16x16x32_bf16 v[168:171], a[164:167], v[100:103], v[168:171]
ds_read_b128 a[140:143], v3 offset:3072
v_cvt_pk_bf16_f32 v196, v132, v133
v_mfma_f32_16x16x32_bf16 v[172:175], a[164:167], v[116:119], v[172:175]
v_exp_f32 v141, v141
v_mfma_f32_16x16x32_bf16 v[160:163], a[168:171], v[72:75], v[160:163]
buffer_load_dwordx4 v2, s[12:15], s41 offen offset:3072 sc1 lds
v_mfma_f32_16x16x32_bf16 v[164:167], a[168:171], v[88:91], v[164:167]
ds_read_b128 a[144:147], v3 offset:4096
s_add_u32 m0, s42, 32768
v_mfma_f32_16x16x32_bf16 v[168:171], a[168:171], v[104:107], v[168:171]
buffer_load_dwordx4 v2, s[16:19], s40 offen sc1 lds
v_mfma_f32_16x16x32_bf16 v[172:175], a[168:171], v[120:123], v[172:175]
ds_read_b128 a[148:151], v3 offset:5120
v_cvt_pk_bf16_f32 v197, v134, v135
v_mfma_f32_16x16x32_bf16 v[160:163], a[172:175], v[76:79], v[160:163]
v_exp_f32 v142, v142
v_mfma_f32_16x16x32_bf16 v[164:167], a[172:175], v[92:95], v[164:167]
buffer_load_dwordx4 v2, s[16:19], s40 offen offset:1024 sc1 lds
v_mfma_f32_16x16x32_bf16 v[168:171], a[172:175], v[108:111], v[168:171]
v_exp_f32 v143, v143
v_mfma_f32_16x16x32_bf16 v[172:175], a[172:175], v[124:127], v[172:175]
buffer_load_dwordx4 v2, s[16:19], s40 offen offset:2048 sc1 lds
v_mfma_f32_16x16x32_bf16 v[176:179], a[176:179], v[64:67], 0
ds_read_b128 a[152:155], v3 offset:6144
ds_read_b128 a[156:159], v3 offset:7168
v_mfma_f32_16x16x32_bf16 v[180:183], a[176:179], v[80:83], 0
buffer_load_dwordx4 v2, s[16:19], s40 offen offset:3072 sc1 lds
v_mfma_f32_16x16x32_bf16 v[184:187], a[176:179], v[96:99], 0
v_exp_f32 v144, v144
v_mfma_f32_16x16x32_bf16 v[188:191], a[176:179], v[112:115], 0
s_add_u32 m0, s43, 8192
v_cvt_pk_bf16_f32 v200, v136, v137
v_mfma_f32_16x16x32_bf16 v[176:179], a[180:183], v[68:71], v[176:179]
buffer_load_dwordx4 v9, s[12:15], s41 offen sc1 lds
v_mfma_f32_16x16x32_bf16 v[180:183], a[180:183], v[84:87], v[180:183]
v_exp_f32 v145, v145
v_mfma_f32_16x16x32_bf16 v[184:187], a[180:183], v[100:103], v[184:187]
buffer_load_dwordx4 v9, s[12:15], s41 offen offset:1024 sc1 lds
v_mfma_f32_16x16x32_bf16 v[188:191], a[180:183], v[116:119], v[188:191]
v_exp_f32 v146, v146
v_mfma_f32_16x16x32_bf16 v[176:179], a[184:187], v[72:75], v[176:179]
v_exp_f32 v147, v147
v_mfma_f32_16x16x32_bf16 v[180:183], a[184:187], v[88:91], v[180:183]
buffer_load_dwordx4 v9, s[12:15], s41 offen offset:2048 sc1 lds
v_mfma_f32_16x16x32_bf16 v[184:187], a[184:187], v[104:107], v[184:187]
v_exp_f32 v148, v148
v_mfma_f32_16x16x32_bf16 v[188:191], a[184:187], v[120:123], v[188:191]
buffer_load_dwordx4 v9, s[12:15], s41 offen offset:3072 sc1 lds
v_mfma_f32_16x16x32_bf16 v[176:179], a[188:191], v[76:79], v[176:179]
v_exp_f32 v149, v149
v_mfma_f32_16x16x32_bf16 v[180:183], a[188:191], v[92:95], v[180:183]
v_exp_f32 v150, v150
v_mfma_f32_16x16x32_bf16 v[184:187], a[188:191], v[108:111], v[184:187]
v_exp_f32 v151, v151
v_exp_f32 v152, v152
v_exp_f32 v153, v153
v_exp_f32 v154, v154
v_exp_f32 v155, v155
v_exp_f32 v156, v156
v_exp_f32 v157, v157
v_exp_f32 v158, v158
v_exp_f32 v159, v159
v_mfma_f32_16x16x32_bf16 v[188:191], a[188:191], v[124:127], v[188:191]
s_xor_b32 s42, s42, 16384
s_xor_b32 s43, s43, 16384
s_mov_b32 s39, s40
s_mov_b32 s40, s41
s_add_u32 s34, s34, 1
s_add_u32 s74, s34, 3
v_readlane_b32 s41, v5, s74
v_exp_f32 v160, v160
v_exp_f32 v161, v161
v_exp_f32 v162, v162
v_exp_f32 v163, v163
v_exp_f32 v164, v164
v_exp_f32 v165, v165
v_exp_f32 v166, v166
v_exp_f32 v167, v167
v_exp_f32 v168, v168
v_exp_f32 v169, v169
v_exp_f32 v170, v170
v_exp_f32 v171, v171
v_exp_f32 v172, v172
v_exp_f32 v173, v173
v_exp_f32 v174, v174
v_exp_f32 v175, v175
v_cvt_pk_bf16_f32 v201, v138, v139
v_cvt_pk_bf16_f32 v204, v140, v141
v_cvt_pk_bf16_f32 v205, v142, v143
v_cvt_pk_bf16_f32 v194, v144, v145
v_cvt_pk_bf16_f32 v195, v146, v147
v_cvt_pk_bf16_f32 v198, v148, v149
v_cvt_pk_bf16_f32 v199, v150, v151
v_cvt_pk_bf16_f32 v202, v152, v153
v_cvt_pk_bf16_f32 v203, v154, v155
v_cvt_pk_bf16_f32 v206, v156, v157
v_cvt_pk_bf16_f32 v207, v158, v159
s_lshr_b32 s23, s21, 2
s_add_u32 s92, s22, s23
s_and_b32 s93, s21, 3
s_lshl_b32 s24, s93, 8
s_mul_i32 s25, s91, 458752
s_add_u32 s25, s25, s24
s_add_u32 s46, s76, s25
s_addc_u32 s47, s77, 0
s_sub_u32 s26, 1835008, s25
s_mov_b32 s8, s46
s_and_b32 s9, s47, 65535
s_mov_b32 s10, s26
s_mov_b32 s11, 131072
s_lshl_b32 s25, s91, 20
s_add_u32 s25, s25, s24
s_add_u32 s46, s78, s25
s_addc_u32 s47, s79, 0
s_sub_u32 s26, 4194304, s25
s_mov_b32 s52, s46
s_and_b32 s53, s47, 65535
s_mov_b32 s54, s26
s_mov_b32 s55, 131072
s_lshl_b32 s24, s92, 3
s_lshl_b32 s25, s7, 1
s_add_u32 s24, s24, s25
s_mul_i32 s24, s24, 188416
s_add_u32 s24, s24, 38797312
s_add_u32 s46, s48, s24
s_addc_u32 s47, s49, 0
s_mov_b32 s84, s46
s_and_b32 s85, s47, 65535
s_mov_b32 s86, 376832
s_mov_b32 s87, 131072
s_mul_i32 s88, s91, 14336
s_add_u32 s88, s88, 131072
s_lshl_b32 s90, s91, 15
s_lshl_b32 s89, s7, 12
s_branch L_TOP
L_START_D:
s_nop 15
s_nop 3
s_lshl_b32 s22, s75, 4
v_subrev_u32 v21, s22, v6
v_add_u32 v24, 16, v21
v_cmp_ge_i32 s[64:65], 0, v21
v_cmp_ge_i32 s[66:67], -1, v21
v_cmp_ge_i32 s[68:69], -2, v21
v_cmp_ge_i32 s[70:71], -3, v21
v_cmp_ge_i32 s[72:73], 0, v24
v_cmp_ge_i32 s[28:29], -1, v24
v_cmp_ge_i32 s[30:31], -2, v24
v_cmp_ge_i32 s[96:97], -3, v24
s_nop 3
s_waitcnt lgkmcnt(0)
v_mfma_f32_16x16x32_bf16 v[128:131], a[128:131], v[64:67], 0
ds_read_b128 a[160:163], v3 offset:8192
v_mfma_f32_16x16x32_bf16 v[132:135], a[128:131], v[80:83], 0
s_add_u32 m0, s43, 40960
v_mfma_f32_16x16x32_bf16 v[136:139], a[128:131], v[96:99], 0
buffer_load_dwordx4 v9, s[16:19], s39 offen sc1 lds
v_mfma_f32_16x16x32_bf16 v[140:143], a[128:131], v[112:115], 0
ds_read_b128 a[164:167], v3 offset:9216
v_mfma_f32_16x16x32_bf16 v[128:131], a[132:135], v[68:71], v[128:131]
ds_read_b128 a[168:171], v3 offset:10240
v_mfma_f32_16x16x32_bf16 v[132:135], a[132:135], v[84:87], v[132:135]
s_getpc_b64 s[94:95]
L_CP_D4:
s_add_u32 s94, s94, L_EVENT-L_CP_D4
s_addc_u32 s95, s95, 0
v_mfma_f32_16x16x32_bf16 v[136:139], a[132:135], v[100:103], v[136:139]
ds_read_b128 a[172:175], v3 offset:11264
s_lshr_b32 s22, s6, 3
v_mfma_f32_16x16x32_bf16 v[140:143], a[132:135], v[116:119], v[140:143]
s_lshl_b32 s22, s22, 2
s_add_u32 s22, s22, s7
v_mfma_f32_16x16x32_bf16 v[128:131], a[136:139], v[72:75], v[128:131]
ds_read_b128 a[176:179], v3 offset:12288
s_mul_i32 s22, s22, 384
v_mfma_f32_16x16x32_bf16 v[132:135], a[136:139], v[88:91], v[132:135]
buffer_load_dwordx4 v9, s[16:19], s39 offen offset:1024 sc1 lds
v_mfma_f32_16x16x32_bf16 v[136:139], a[136:139], v[104:107], v[136:139]
ds_read_b128 a[180:183], v3 offset:13312
v_min_u32 v22, 2, v1
v_mfma_f32_16x16x32_bf16 v[140:143], a[136:139], v[120:123], v[140:143]
v_lshlrev_b32 v22, 7, v22
v_add_u32 v22, s22, v22
v_mfma_f32_16x16x32_bf16 v[128:131], a[140:143], v[76:79], v[128:131]
ds_read_b128 a[184:187], v3 offset:14336
v_min_u32 v22, L_CODE_END-L_EVENT-4, v22
v_mfma_f32_16x16x32_bf16 v[132:135], a[140:143], v[92:95], v[132:135]
s_and_b32 s20, s6, 7
s_lshr_b32 s21, s6, 3
v_mfma_f32_16x16x32_bf16 v[136:139], a[140:143], v[108:111], v[136:139]
ds_read_b128 a[188:191], v3 offset:15360
s_and_b32 s91, s20, 3
v_mfma_f32_16x16x32_bf16 v[140:143], a[140:143], v[124:127], v[140:143]
s_lshr_b32 s22, s20, 2
s_lshl_b32 s22, s22, 3
v_mfma_f32_16x16x32_bf16 v[144:147], a[144:147], v[64:67], 0
buffer_load_dwordx4 v9, s[16:19], s39 offen offset:2048 sc1 lds
v_mfma_f32_16x16x32_bf16 v[148:151], a[144:147], v[80:83], 0
v_cndmask_b32 v128, v7, v128, s[64:65]
v_cndmask_b32 v129, v7, v129, s[66:67]
v_mfma_f32_16x16x32_bf16 v[152:155], a[144:147], v[96:99], 0
v_cndmask_b32 v130, v7, v130, s[68:69]
v_cndmask_b32 v131, v7, v131, s[70:71]
v_mfma_f32_16x16x32_bf16 v[156:159], a[144:147], v[112:115], 0
v_exp_f32 v128, v128
v_mfma_f32_16x16x32_bf16 v[144:147], a[148:151], v[68:71], v[144:147]
v_exp_f32 v129, v129
v_mfma_f32_16x16x32_bf16 v[148:151], a[148:151], v[84:87], v[148:151]
v_exp_f32 v130, v130
v_mfma_f32_16x16x32_bf16 v[152:155], a[148:151], v[100:103], v[152:155]
v_exp_f32 v131, v131
v_mfma_f32_16x16x32_bf16 v[156:159], a[148:151], v[116:119], v[156:159]
buffer_load_dwordx4 v9, s[16:19], s39 offen offset:3072 sc1 lds
v_mfma_f32_16x16x32_bf16 v[144:147], a[152:155], v[72:75], v[144:147]
v_exp_f32 v132, v132
v_mfma_f32_16x16x32_bf16 v[148:151], a[152:155], v[88:91], v[148:151]
v_exp_f32 v133, v133
v_mfma_f32_16x16x32_bf16 v[152:155], a[152:155], v[104:107], v[152:155]
v_exp_f32 v134, v134
v_mfma_f32_16x16x32_bf16 v[156:159], a[152:155], v[120:123], v[156:159]
v_exp_f32 v135, v135
v_mfma_f32_16x16x32_bf16 v[144:147], a[156:159], v[76:79], v[144:147]
v_exp_f32 v136, v136
v_mfma_f32_16x16x32_bf16 v[148:151], a[156:159], v[92:95], v[148:151]
v_exp_f32 v137, v137
v_mfma_f32_16x16x32_bf16 v[152:155], a[156:159], v[108:111], v[152:155]
v_exp_f32 v138, v138
v_mfma_f32_16x16x32_bf16 v[156:159], a[156:159], v[124:127], v[156:159]
s_waitcnt vmcnt(8) lgkmcnt(0)
s_barrier
v_xor_b32 v3, 16384, v3
v_xor_b32 v4, 16384, v4
v_mfma_f32_16x16x32_bf16 v[164:167], a[160:163], v[80:83], 0
ds_read_b128 a[128:131], v3 offset:0
s_add_u32 m0, s43, 0
v_mfma_f32_16x16x32_bf16 v[168:171], a[160:163], v[96:99], 0
buffer_load_dwordx4 v2, s[12:15], s41 offen sc1 lds
v_mfma_f32_16x16x32_bf16 v[172:175], a[160:163], v[112:115], 0
buffer_load_dwordx4 v2, s[12:15], s41 offen offset:1024 sc1 lds
v_mfma_f32_16x16x32_bf16 v[180:183], a[176:179], v[80:83], 0
ds_read_b128 a[132:135], v3 offset:1024
v_cndmask_b32 v144, v7, v144, s[72:73]
v_mfma_f32_16x16x32_bf16 v[184:187], a[176:179], v[96:99], 0
buffer_load_dwordx4 v2, s[12:15], s41 offen offset:2048 sc1 lds
v_mfma_f32_16x16x32_bf16 v[188:191], a[176:179], v[112:115], 0
ds_read_b128 a[136:139], v3 offset:2048
v_cndmask_b32 v145, v7, v145, s[28:29]
v_mfma_f32_16x16x32_bf16 v[164:167], a[164:167], v[84:87], v[164:167]
buffer_load_dwordx4 v2, s[12:15], s41 offen offset:3072 sc1 lds
v_mfma_f32_16x16x32_bf16 v[168:171], a[164:167], v[100:103], v[168:171]
ds_read_b128 a[140:143], v3 offset:3072
s_add_u32 m0, s42, 32768
v_mfma_f32_16x16x32_bf16 v[172:175], a[164:167], v[116:119], v[172:175]
buffer_load_dwordx4 v2, s[16:19], s40 offen sc1 lds
v_mfma_f32_16x16x32_bf16 v[180:183], a[180:183], v[84:87], v[180:183]
buffer_load_dwordx4 v2, s[16:19], s40 offen offset:1024 sc1 lds
v_mfma_f32_16x16x32_bf16 v[184:187], a[180:183], v[100:103], v[184:187]
ds_read_b128 a[144:147], v3 offset:4096
ds_read_b128 a[148:151], v3 offset:5120
v_mfma_f32_16x16x32_bf16 v[188:191], a[180:183], v[116:119], v[188:191]
buffer_load_dwordx4 v2, s[16:19], s40 offen offset:2048 sc1 lds
v_mfma_f32_16x16x32_bf16 v[164:167], a[168:171], v[88:91], v[164:167]
v_exp_f32 v139, v139
v_mfma_f32_16x16x32_bf16 v[168:171], a[168:171], v[104:107], v[168:171]
buffer_load_dwordx4 v2, s[16:19], s40 offen offset:3072 sc1 lds
v_mfma_f32_16x16x32_bf16 v[172:175], a[168:171], v[120:123], v[172:175]
ds_read_b128 a[152:155], v3 offset:6144
s_add_u32 m0, s43, 8192
v_mfma_f32_16x16x32_bf16 v[180:183], a[184:187], v[88:91], v[180:183]
buffer_load_dwordx4 v9, s[12:15], s41 offen sc1 lds
v_mfma_f32_16x16x32_bf16 v[184:187], a[184:187], v[104:107], v[184:187]
buffer_load_dwordx4 v9, s[12:15], s41 offen offset:1024 sc1 lds
v_mfma_f32_16x16x32_bf16 v[188:191], a[184:187], v[120:123], v[188:191]
v_exp_f32 v140, v140
v_mfma_f32_16x16x32_bf16 v[164:167], a[172:175], v[92:95], v[164:167]
v_exp_f32 v141, v141
v_exp_f32 v142, v142
v_exp_f32 v143, v143
v_mfma_f32_16x16x32_bf16 v[168:171], a[172:175], v[108:111], v[168:171]
buffer_load_dwordx4 v9, s[12:15], s41 offen offset:2048 sc1 lds
v_mfma_f32_16x16x32_bf16 v[172:175], a[172:175], v[124:127], v[172:175]
ds_read_b128 a[156:159], v3 offset:7168
v_cndmask_b32 v146, v7, v146, s[30:31]
v_cndmask_b32 v147, v7, v147, s[96:97]
v_mfma_f32_16x16x32_bf16 v[180:183], a[188:191], v[92:95], v[180:183]
buffer_load_dwordx4 v9, s[12:15], s41 offen offset:3072 sc1 lds
v_mfma_f32_16x16x32_bf16 v[184:187], a[188:191], v[108:111], v[184:187]
v_exp_f32 v144, v144
v_exp_f32 v145, v145
v_exp_f32 v146, v146
v_exp_f32 v147, v147
v_exp_f32 v148, v148
v_exp_f32 v149, v149
v_exp_f32 v150, v150
v_exp_f32 v151, v151
v_exp_f32 v152, v152
v_exp_f32 v153, v153
v_exp_f32 v154, v154
v_exp_f32 v155, v155
v_exp_f32 v156, v156
v_exp_f32 v157, v157
v_exp_f32 v158, v158
v_exp_f32 v159, v159
v_mfma_f32_16x16x32_bf16 v[188:191], a[188:191], v[124:127], v[188:191]
s_xor_b32 s42, s42, 16384
s_xor_b32 s43, s43, 16384
s_mov_b32 s39, s40
s_mov_b32 s40, s41
s_add_u32 s34, s34, 1
s_add_u32 s74, s34, 3
v_readlane_b32 s41, v5, s74
v_cndmask_b32 v164, v7, v164, s[64:65]
v_exp_f32 v164, v164
v_cndmask_b32 v165, v7, v165, s[66:67]
v_exp_f32 v165, v165
v_cndmask_b32 v166, v7, v166, s[68:69]
v_exp_f32 v166, v166
v_cndmask_b32 v167, v7, v167, s[70:71]
v_exp_f32 v167, v167
v_exp_f32 v168, v168
v_exp_f32 v169, v169
v_exp_f32 v170, v170
v_exp_f32 v171, v171
v_exp_f32 v172, v172
v_exp_f32 v173, v173
v_exp_f32 v174, v174
v_exp_f32 v175, v175
v_cndmask_b32 v180, v7, v180, s[72:73]
v_cndmask_b32 v181, v7, v181, s[28:29]
v_cndmask_b32 v182, v7, v182, s[30:31]
v_cndmask_b32 v183, v7, v183, s[96:97]
v_cvt_pk_bf16_f32 v192, v128, v129
v_cvt_pk_bf16_f32 v193, v130, v131
v_cvt_pk_bf16_f32 v196, v132, v133
v_cvt_pk_bf16_f32 v197, v134, v135
v_cvt_pk_bf16_f32 v200, v136, v137
v_cvt_pk_bf16_f32 v201, v138, v139
v_cvt_pk_bf16_f32 v204, v140, v141
v_cvt_pk_bf16_f32 v205, v142, v143
v_cvt_pk_bf16_f32 v194, v144, v145
v_cvt_pk_bf16_f32 v195, v146, v147
v_cvt_pk_bf16_f32 v198, v148, v149
v_cvt_pk_bf16_f32 v199, v150, v151
v_cvt_pk_bf16_f32 v202, v152, v153
v_cvt_pk_bf16_f32 v203, v154, v155
v_cvt_pk_bf16_f32 v206, v156, v157
v_cvt_pk_bf16_f32 v207, v158, v159
s_lshr_b32 s23, s21, 2
s_add_u32 s92, s22, s23
s_and_b32 s93, s21, 3
s_lshl_b32 s24, s93, 8
s_mul_i32 s25, s91, 458752
s_add_u32 s25, s25, s24
s_add_u32 s46, s76, s25
s_addc_u32 s47, s77, 0
s_sub_u32 s26, 1835008, s25
s_mov_b32 s8, s46
s_and_b32 s9, s47, 65535
s_mov_b32 s10, s26
s_mov_b32 s11, 131072
s_lshl_b32 s25, s91, 20
s_add_u32 s25, s25, s24
s_add_u32 s46, s78, s25
s_addc_u32 s47, s79, 0
s_sub_u32 s26, 4194304, s25
s_mov_b32 s52, s46
s_and_b32 s53, s47, 65535
s_mov_b32 s54, s26
s_mov_b32 s55, 131072
s_lshl_b32 s24, s92, 3
s_lshl_b32 s25, s7, 1
s_add_u32 s24, s24, s25
s_mul_i32 s24, s24, 188416
s_add_u32 s24, s24, 38797312
s_add_u32 s46, s48, s24
s_addc_u32 s47, s49, 0
s_mov_b32 s84, s46
s_and_b32 s85, s47, 65535
s_mov_b32 s86, 376832
s_mov_b32 s87, 131072
s_mul_i32 s88, s91, 14336
s_add_u32 s88, s88, 131072
s_lshl_b32 s90, s91, 15
s_lshl_b32 s89, s7, 12
s_branch L_BB
L_TOP:
s_bitcmp1_b32 s60, s34
s_cbranch_scc1 L_EVENT
L_FULL0:
s_nop 0
s_waitcnt lgkmcnt(0)
v_mfma_f32_16x16x32_bf16 v[128:131], a[128:131], v[64:67], 0
ds_read_b128 a[160:163], v4 offset:0
v_cvt_pk_bf16_f32 v208, v160, v161
v_mfma_f32_16x16x32_bf16 v[132:135], a[128:131], v[80:83], 0
s_add_u32 m0, s43, 40960
v_cvt_pk_bf16_f32 v209, v162, v163
v_mfma_f32_16x16x32_bf16 v[136:139], a[128:131], v[96:99], 0
buffer_load_dwordx4 v9, s[16:19], s39 offen sc1 lds
v_mfma_f32_16x16x32_bf16 v[140:143], a[128:131], v[112:115], 0
global_load_dword v23, v22, s[94:95]
v_mfma_f32_16x16x32_bf16 v[128:131], a[132:135], v[68:71], v[128:131]
ds_read_b128 a[164:167], v4 offset:1024
ds_read_b128 a[168:171], v4 offset:2048
v_mfma_f32_16x16x32_bf16 v[132:135], a[132:135], v[84:87], v[132:135]
v_exp_f32 v176, v176
v_mfma_f32_16x16x32_bf16 v[136:139], a[132:135], v[100:103], v[136:139]
ds_read_b128 a[172:175], v4 offset:3072
v_cvt_pk_bf16_f32 v212, v164, v165
v_mfma_f32_16x16x32_bf16 v[140:143], a[132:135], v[116:119], v[140:143]
v_exp_f32 v177, v177
v_mfma_f32_16x16x32_bf16 v[128:131], a[136:139], v[72:75], v[128:131]
ds_read_b128 a[176:179], v4 offset:4096
v_cvt_pk_bf16_f32 v213, v166, v167
v_mfma_f32_16x16x32_bf16 v[132:135], a[136:139], v[88:91], v[132:135]
buffer_load_dwordx4 v9, s[16:19], s39 offen offset:1024 sc1 lds
v_mfma_f32_16x16x32_bf16 v[136:139], a[136:139], v[104:107], v[136:139]
ds_read_b128 a[180:183], v4 offset:5120
v_cvt_pk_bf16_f32 v216, v168, v169
v_mfma_f32_16x16x32_bf16 v[140:143], a[136:139], v[120:123], v[140:143]
v_exp_f32 v178, v178
v_mfma_f32_16x16x32_bf16 v[128:131], a[140:143], v[76:79], v[128:131]
ds_read_b128 a[184:187], v4 offset:6144
v_cvt_pk_bf16_f32 v217, v170, v171
v_mfma_f32_16x16x32_bf16 v[132:135], a[140:143], v[92:95], v[132:135]
v_exp_f32 v179, v179
v_mfma_f32_16x16x32_bf16 v[136:139], a[140:143], v[108:111], v[136:139]
ds_read_b128 a[188:191], v4 offset:7168
v_cvt_pk_bf16_f32 v220, v172, v173
v_mfma_f32_16x16x32_bf16 v[140:143], a[140:143], v[124:127], v[140:143]
v_exp_f32 v180, v180
v_mfma_f32_16x16x32_bf16 v[144:147], a[144:147], v[64:67], 0
buffer_load_dwordx4 v9, s[16:19], s39 offen offset:2048 sc1 lds
v_mfma_f32_16x16x32_bf16 v[148:151], a[144:147], v[80:83], 0
v_exp_f32 v181, v181
v_mfma_f32_16x16x32_bf16 v[152:155], a[144:147], v[96:99], 0
v_exp_f32 v182, v182
v_mfma_f32_16x16x32_bf16 v[156:159], a[144:147], v[112:115], 0
v_exp_f32 v183, v183
v_mfma_f32_16x16x32_bf16 v[144:147], a[148:151], v[68:71], v[144:147]
v_exp_f32 v184, v184
v_mfma_f32_16x16x32_bf16 v[148:151], a[148:151], v[84:87], v[148:151]
v_exp_f32 v185, v185
v_mfma_f32_16x16x32_bf16 v[152:155], a[148:151], v[100:103], v[152:155]
v_exp_f32 v186, v186
v_mfma_f32_16x16x32_bf16 v[156:159], a[148:151], v[116:119], v[156:159]
buffer_load_dwordx4 v9, s[16:19], s39 offen offset:3072 sc1 lds
v_mfma_f32_16x16x32_bf16 v[144:147], a[152:155], v[72:75], v[144:147]
v_exp_f32 v187, v187
v_mfma_f32_16x16x32_bf16 v[148:151], a[152:155], v[88:91], v[148:151]
v_exp_f32 v188, v188
v_mfma_f32_16x16x32_bf16 v[152:155], a[152:155], v[104:107], v[152:155]
v_exp_f32 v189, v189
v_mfma_f32_16x16x32_bf16 v[156:159], a[152:155], v[120:123], v[156:159]
v_exp_f32 v190, v190
v_mfma_f32_16x16x32_bf16 v[144:147], a[156:159], v[76:79], v[144:147]
v_exp_f32 v191, v191
v_mfma_f32_16x16x32_bf16 v[148:151], a[156:159], v[92:95], v[148:151]
v_cvt_pk_bf16_f32 v221, v174, v175
v_cvt_pk_bf16_f32 v210, v176, v177
v_mfma_f32_16x16x32_bf16 v[152:155], a[156:159], v[108:111], v[152:155]
v_cvt_pk_bf16_f32 v211, v178, v179
v_cvt_pk_bf16_f32 v214, v180, v181
v_mfma_f32_16x16x32_bf16 v[156:159], a[156:159], v[124:127], v[156:159]
s_waitcnt vmcnt(17) lgkmcnt(0)
s_barrier
v_mfma_f32_16x16x32_bf16 a[0:3], a[160:163], v[192:195], a[0:3]
)ASM"
R"ASM(ds_read_b128 a[128:131], v3 offset:8192
v_cvt_pk_bf16_f32 v215, v182, v183
v_mfma_f32_16x16x32_bf16 a[4:7], a[160:163], v[196:199], a[4:7]
s_add_u32 m0, s43, 0
v_cvt_pk_bf16_f32 v218, v184, v185
v_mfma_f32_16x16x32_bf16 a[8:11], a[160:163], v[200:203], a[8:11]
buffer_load_dwordx4 v2, s[12:15], s41 offen sc1 lds
v_mfma_f32_16x16x32_bf16 a[12:15], a[160:163], v[204:207], a[12:15]
ds_read_b128 a[132:135], v3 offset:9216
v_cvt_pk_bf16_f32 v219, v186, v187
v_mfma_f32_16x16x32_bf16 a[16:19], a[164:167], v[192:195], a[16:19]
ds_read_b128 a[136:139], v3 offset:10240
v_cvt_pk_bf16_f32 v222, v188, v189
v_mfma_f32_16x16x32_bf16 a[20:23], a[164:167], v[196:199], a[20:23]
v_cvt_pk_bf16_f32 v223, v190, v191
v_mfma_f32_16x16x32_bf16 a[24:27], a[164:167], v[200:203], a[24:27]
ds_read_b128 a[140:143], v3 offset:11264
v_mfma_f32_16x16x32_bf16 a[28:31], a[164:167], v[204:207], a[28:31]
v_exp_f32 v128, v128
v_mfma_f32_16x16x32_bf16 a[32:35], a[168:171], v[192:195], a[32:35]
ds_read_b128 a[144:147], v3 offset:12288
v_mfma_f32_16x16x32_bf16 a[36:39], a[168:171], v[196:199], a[36:39]
v_exp_f32 v129, v129
v_mfma_f32_16x16x32_bf16 a[40:43], a[168:171], v[200:203], a[40:43]
buffer_load_dwordx4 v2, s[12:15], s41 offen offset:1024 sc1 lds
v_mfma_f32_16x16x32_bf16 a[44:47], a[168:171], v[204:207], a[44:47]
ds_read_b128 a[148:151], v3 offset:13312
v_mfma_f32_16x16x32_bf16 a[48:51], a[172:175], v[192:195], a[48:51]
ds_read_b128 a[152:155], v3 offset:14336
v_mfma_f32_16x16x32_bf16 a[52:55], a[172:175], v[196:199], a[52:55]
v_exp_f32 v130, v130
v_mfma_f32_16x16x32_bf16 a[56:59], a[172:175], v[200:203], a[56:59]
ds_read_b128 a[156:159], v3 offset:15360
v_mfma_f32_16x16x32_bf16 a[60:63], a[172:175], v[204:207], a[60:63]
v_exp_f32 v131, v131
v_mfma_f32_16x16x32_bf16 a[192:195], v[224:227], v[192:195], a[192:195]
v_exp_f32 v132, v132
v_mfma_f32_16x16x32_bf16 a[196:199], v[224:227], v[196:199], a[196:199]
v_exp_f32 v133, v133
v_mfma_f32_16x16x32_bf16 a[64:67], a[176:179], v[192:195], a[64:67]
buffer_load_dwordx4 v2, s[12:15], s41 offen offset:2048 sc1 lds
v_mfma_f32_16x16x32_bf16 a[68:71], a[176:179], v[196:199], a[68:71]
v_exp_f32 v134, v134
v_mfma_f32_16x16x32_bf16 a[72:75], a[176:179], v[200:203], a[72:75]
v_exp_f32 v135, v135
v_mfma_f32_16x16x32_bf16 a[76:79], a[176:179], v[204:207], a[76:79]
v_exp_f32 v136, v136
v_mfma_f32_16x16x32_bf16 a[80:83], a[180:183], v[192:195], a[80:83]
v_exp_f32 v137, v137
v_mfma_f32_16x16x32_bf16 a[84:87], a[180:183], v[196:199], a[84:87]
v_exp_f32 v138, v138
v_mfma_f32_16x16x32_bf16 a[88:91], a[180:183], v[200:203], a[88:91]
v_exp_f32 v139, v139
v_mfma_f32_16x16x32_bf16 a[92:95], a[180:183], v[204:207], a[92:95]
v_exp_f32 v140, v140
v_mfma_f32_16x16x32_bf16 a[96:99], a[184:187], v[192:195], a[96:99]
buffer_load_dwordx4 v2, s[12:15], s41 offen offset:3072 sc1 lds
v_mfma_f32_16x16x32_bf16 a[100:103], a[184:187], v[196:199], a[100:103]
v_exp_f32 v141, v141
v_mfma_f32_16x16x32_bf16 a[104:107], a[184:187], v[200:203], a[104:107]
v_exp_f32 v142, v142
v_mfma_f32_16x16x32_bf16 a[108:111], a[184:187], v[204:207], a[108:111]
v_exp_f32 v143, v143
v_mfma_f32_16x16x32_bf16 a[112:115], a[188:191], v[192:195], a[112:115]
v_exp_f32 v144, v144
v_mfma_f32_16x16x32_bf16 a[116:119], a[188:191], v[196:199], a[116:119]
v_exp_f32 v145, v145
v_mfma_f32_16x16x32_bf16 a[120:123], a[188:191], v[200:203], a[120:123]
v_exp_f32 v146, v146
v_mfma_f32_16x16x32_bf16 a[124:127], a[188:191], v[204:207], a[124:127]
s_add_u32 m0, s42, 32768
v_mfma_f32_16x16x32_bf16 a[200:203], v[224:227], v[200:203], a[200:203]
buffer_load_dwordx4 v2, s[16:19], s40 offen sc1 lds
v_mfma_f32_16x16x32_bf16 a[204:207], v[224:227], v[204:207], a[204:207]
s_waitcnt lgkmcnt(0)
v_mfma_f32_16x16x32_bf16 v[160:163], a[128:131], v[64:67], 0
ds_read_b128 a[160:163], v4 offset:8192
v_cvt_pk_bf16_f32 v192, v128, v129
v_mfma_f32_16x16x32_bf16 v[164:167], a[128:131], v[80:83], 0
v_exp_f32 v147, v147
v_mfma_f32_16x16x32_bf16 v[168:171], a[128:131], v[96:99], 0
ds_read_b128 a[164:167], v4 offset:9216
v_cvt_pk_bf16_f32 v193, v130, v131
v_mfma_f32_16x16x32_bf16 v[172:175], a[128:131], v[112:115], 0
v_exp_f32 v148, v148
v_mfma_f32_16x16x32_bf16 v[160:163], a[132:135], v[68:71], v[160:163]
ds_read_b128 a[168:171], v4 offset:10240
v_cvt_pk_bf16_f32 v196, v132, v133
v_mfma_f32_16x16x32_bf16 v[164:167], a[132:135], v[84:87], v[164:167]
v_exp_f32 v149, v149
v_mfma_f32_16x16x32_bf16 v[168:171], a[132:135], v[100:103], v[168:171]
buffer_load_dwordx4 v2, s[16:19], s40 offen offset:1024 sc1 lds
v_mfma_f32_16x16x32_bf16 v[172:175], a[132:135], v[116:119], v[172:175]
ds_read_b128 a[172:175], v4 offset:11264
v_cvt_pk_bf16_f32 v197, v134, v135
v_mfma_f32_16x16x32_bf16 v[160:163], a[136:139], v[72:75], v[160:163]
ds_read_b128 a[176:179], v4 offset:12288
v_cvt_pk_bf16_f32 v200, v136, v137
v_mfma_f32_16x16x32_bf16 v[164:167], a[136:139], v[88:91], v[164:167]
v_exp_f32 v150, v150
v_mfma_f32_16x16x32_bf16 v[168:171], a[136:139], v[104:107], v[168:171]
ds_read_b128 a[180:183], v4 offset:13312
v_cvt_pk_bf16_f32 v201, v138, v139
v_mfma_f32_16x16x32_bf16 v[172:175], a[136:139], v[120:123], v[172:175]
v_exp_f32 v151, v151
v_mfma_f32_16x16x32_bf16 v[160:163], a[140:143], v[76:79], v[160:163]
ds_read_b128 a[184:187], v4 offset:14336
v_cvt_pk_bf16_f32 v204, v140, v141
v_mfma_f32_16x16x32_bf16 v[164:167], a[140:143], v[92:95], v[164:167]
v_exp_f32 v152, v152
v_mfma_f32_16x16x32_bf16 v[168:171], a[140:143], v[108:111], v[168:171]
buffer_load_dwordx4 v2, s[16:19], s40 offen offset:2048 sc1 lds
v_mfma_f32_16x16x32_bf16 v[172:175], a[140:143], v[124:127], v[172:175]
ds_read_b128 a[188:191], v4 offset:15360
v_cvt_pk_bf16_f32 v205, v142, v143
v_mfma_f32_16x16x32_bf16 v[176:179], a[144:147], v[64:67], 0
v_exp_f32 v153, v153
v_mfma_f32_16x16x32_bf16 v[180:183], a[144:147], v[80:83], 0
v_exp_f32 v154, v154
v_mfma_f32_16x16x32_bf16 v[184:187], a[144:147], v[96:99], 0
v_exp_f32 v155, v155
v_mfma_f32_16x16x32_bf16 v[188:191], a[144:147], v[112:115], 0
v_exp_f32 v156, v156
v_mfma_f32_16x16x32_bf16 v[176:179], a[148:151], v[68:71], v[176:179]
v_exp_f32 v157, v157
v_mfma_f32_16x16x32_bf16 v[180:183], a[148:151], v[84:87], v[180:183]
v_exp_f32 v158, v158
v_mfma_f32_16x16x32_bf16 v[184:187], a[148:151], v[100:103], v[184:187]
buffer_load_dwordx4 v2, s[16:19], s40 offen offset:3072 sc1 lds
v_mfma_f32_16x16x32_bf16 v[188:191], a[148:151], v[116:119], v[188:191]
v_exp_f32 v159, v159
v_mfma_f32_16x16x32_bf16 v[176:179], a[152:155], v[72:75], v[176:179]
v_exp_f32 v160, v160
v_mfma_f32_16x16x32_bf16 v[180:183], a[152:155], v[88:91], v[180:183]
v_exp_f32 v161, v161
v_mfma_f32_16x16x32_bf16 v[184:187], a[152:155], v[104:107], v[184:187]
v_exp_f32 v162, v162
v_mfma_f32_16x16x32_bf16 v[188:191], a[152:155], v[120:123], v[188:191]
v_exp_f32 v163, v163
v_mfma_f32_16x16x32_bf16 v[176:179], a[156:159], v[76:79], v[176:179]
v_exp_f32 v164, v164
v_mfma_f32_16x16x32_bf16 v[180:183], a[156:159], v[92:95], v[180:183]
v_exp_f32 v165, v165
v_mfma_f32_16x16x32_bf16 v[184:187], a[156:159], v[108:111], v[184:187]
v_exp_f32 v166, v166
v_mfma_f32_16x16x32_bf16 v[188:191], a[156:159], v[124:127], v[188:191]
s_waitcnt vmcnt(17) lgkmcnt(0)
s_barrier
v_xor_b32 v3, 16384, v3
v_xor_b32 v4, 16384, v4
v_mfma_f32_16x16x32_bf16 a[0:3], a[160:163], v[208:211], a[0:3]
ds_read_b128 a[128:131], v3 offset:0
v_cvt_pk_bf16_f32 v194, v144, v145
v_mfma_f32_16x16x32_bf16 a[4:7], a[160:163], v[212:215], a[4:7]
s_add_u32 m0, s43, 8192
v_cvt_pk_bf16_f32 v195, v146, v147
v_mfma_f32_16x16x32_bf16 a[8:11], a[160:163], v[216:219], a[8:11]
buffer_load_dwordx4 v9, s[12:15], s41 offen sc1 lds
v_mfma_f32_16x16x32_bf16 a[12:15], a[160:163], v[220:223], a[12:15]
ds_read_b128 a[132:135], v3 offset:1024
v_cvt_pk_bf16_f32 v198, v148, v149
v_mfma_f32_16x16x32_bf16 a[16:19], a[164:167], v[208:211], a[16:19]
ds_read_b128 a[136:139], v3 offset:2048
v_cvt_pk_bf16_f32 v199, v150, v151
v_mfma_f32_16x16x32_bf16 a[20:23], a[164:167], v[212:215], a[20:23]
v_exp_f32 v167, v167
v_mfma_f32_16x16x32_bf16 a[24:27], a[164:167], v[216:219], a[24:27]
ds_read_b128 a[140:143], v3 offset:3072
v_cvt_pk_bf16_f32 v202, v152, v153
v_mfma_f32_16x16x32_bf16 a[28:31], a[164:167], v[220:223], a[28:31]
v_exp_f32 v168, v168
v_mfma_f32_16x16x32_bf16 a[32:35], a[168:171], v[208:211], a[32:35]
buffer_load_dwordx4 v9, s[12:15], s41 offen offset:1024 sc1 lds
v_mfma_f32_16x16x32_bf16 a[36:39], a[168:171], v[212:215], a[36:39]
ds_read_b128 a[144:147], v3 offset:4096
v_cvt_pk_bf16_f32 v203, v154, v155
v_mfma_f32_16x16x32_bf16 a[40:43], a[168:171], v[216:219], a[40:43]
ds_read_b128 a[148:151], v3 offset:5120
v_cvt_pk_bf16_f32 v206, v156, v157
v_mfma_f32_16x16x32_bf16 a[44:47], a[168:171], v[220:223], a[44:47]
v_exp_f32 v169, v169
v_mfma_f32_16x16x32_bf16 a[48:51], a[172:175], v[208:211], a[48:51]
ds_read_b128 a[152:155], v3 offset:6144
v_cvt_pk_bf16_f32 v207, v158, v159
v_mfma_f32_16x16x32_bf16 a[52:55], a[172:175], v[212:215], a[52:55]
v_exp_f32 v170, v170
v_mfma_f32_16x16x32_bf16 a[56:59], a[172:175], v[216:219], a[56:59]
buffer_load_dwordx4 v9, s[12:15], s41 offen offset:2048 sc1 lds
v_mfma_f32_16x16x32_bf16 a[60:63], a[172:175], v[220:223], a[60:63]
ds_read_b128 a[156:159], v3 offset:7168
v_mfma_f32_16x16x32_bf16 a[192:195], v[224:227], v[208:211], a[192:195]
v_exp_f32 v171, v171
v_mfma_f32_16x16x32_bf16 a[196:199], v[224:227], v[212:215], a[196:199]
v_exp_f32 v172, v172
v_mfma_f32_16x16x32_bf16 a[64:67], a[176:179], v[208:211], a[64:67]
v_exp_f32 v173, v173
v_mfma_f32_16x16x32_bf16 a[68:71], a[176:179], v[212:215], a[68:71]
v_exp_f32 v174, v174
v_mfma_f32_16x16x32_bf16 a[72:75], a[176:179], v[216:219], a[72:75]
buffer_load_dwordx4 v9, s[12:15], s41 offen offset:3072 sc1 lds
v_mfma_f32_16x16x32_bf16 a[76:79], a[176:179], v[220:223], a[76:79]
s_xor_b32 s42, s42, 16384
s_xor_b32 s43, s43, 16384
v_mfma_f32_16x16x32_bf16 a[80:83], a[180:183], v[208:211], a[80:83]
s_mov_b32 s39, s40
s_mov_b32 s40, s41
v_mfma_f32_16x16x32_bf16 a[84:87], a[180:183], v[212:215], a[84:87]
s_add_u32 s34, s34, 1
s_add_u32 s74, s34, 3
v_mfma_f32_16x16x32_bf16 a[88:91], a[180:183], v[216:219], a[88:91]
v_exp_f32 v175, v175
v_mfma_f32_16x16x32_bf16 a[92:95], a[180:183], v[220:223], a[92:95]
v_readlane_b32 s41, v5, s74
v_mfma_f32_16x16x32_bf16 a[96:99], a[184:187], v[208:211], a[96:99]
v_mfma_f32_16x16x32_bf16 a[100:103], a[184:187], v[212:215], a[100:103]
v_mfma_f32_16x16x32_bf16 a[104:107], a[184:187], v[216:219], a[104:107]
v_mfma_f32_16x16x32_bf16 a[108:111], a[184:187], v[220:223], a[108:111]
v_mfma_f32_16x16x32_bf16 a[112:115], a[188:191], v[208:211], a[112:115]
v_mfma_f32_16x16x32_bf16 a[116:119], a[188:191], v[212:215], a[116:119]
v_mfma_f32_16x16x32_bf16 a[120:123], a[188:191], v[216:219], a[120:123]
v_mfma_f32_16x16x32_bf16 a[124:127], a[188:191], v[220:223], a[124:127]
v_mfma_f32_16x16x32_bf16 a[200:203], v[224:227], v[216:219], a[200:203]
v_mfma_f32_16x16x32_bf16 a[204:207], v[224:227], v[220:223], a[204:207]
s_bitcmp1_b32 s60, s34
s_cbranch_scc0 L_FULL0
L_EVENT:
s_bitcmp1_b32 s51, s34
s_cbranch_scc1 L_SPECIAL
s_nop 0
s_waitcnt lgkmcnt(0)
v_mfma_f32_16x16x32_bf16 v[128:131], a[128:131], v[64:67], 0
ds_read_b128 a[160:163], v4 offset:0
s_lshl_b32 s22, s75, 4
v_mfma_f32_16x16x32_bf16 v[132:135], a[128:131], v[80:83], 0
s_add_u32 m0, s43, 40960
v_subrev_u32 v21, s22, v6
v_mfma_f32_16x16x32_bf16 v[136:139], a[128:131], v[96:99], 0
buffer_load_dwordx4 v9, s[16:19], s39 offen sc1 lds
v_mfma_f32_16x16x32_bf16 v[140:143], a[128:131], v[112:115], 0
ds_read_b128 a[164:167], v4 offset:1024
v_add_u32 v24, 16, v21
v_mfma_f32_16x16x32_bf16 v[128:131], a[132:135], v[68:71], v[128:131]
ds_read_b128 a[168:171], v4 offset:2048
v_cmp_ge_i32 s[64:65], 0, v21
v_mfma_f32_16x16x32_bf16 v[132:135], a[132:135], v[84:87], v[132:135]
v_cmp_ge_i32 s[66:67], -1, v21
v_cvt_pk_bf16_f32 v208, v160, v161
v_mfma_f32_16x16x32_bf16 v[136:139], a[132:135], v[100:103], v[136:139]
ds_read_b128 a[172:175], v4 offset:3072
v_cmp_ge_i32 s[68:69], -2, v21
v_mfma_f32_16x16x32_bf16 v[140:143], a[132:135], v[116:119], v[140:143]
v_cmp_ge_i32 s[70:71], -3, v21
v_cvt_pk_bf16_f32 v209, v162, v163
v_mfma_f32_16x16x32_bf16 v[128:131], a[136:139], v[72:75], v[128:131]
ds_read_b128 a[176:179], v4 offset:4096
v_cmp_ge_i32 s[72:73], 0, v24
v_mfma_f32_16x16x32_bf16 v[132:135], a[136:139], v[88:91], v[132:135]
buffer_load_dwordx4 v9, s[16:19], s39 offen offset:1024 sc1 lds
v_mfma_f32_16x16x32_bf16 v[136:139], a[136:139], v[104:107], v[136:139]
ds_read_b128 a[180:183], v4 offset:5120
v_cmp_ge_i32 s[28:29], -1, v24
v_mfma_f32_16x16x32_bf16 v[140:143], a[136:139], v[120:123], v[140:143]
v_cmp_ge_i32 s[30:31], -2, v24
v_cvt_pk_bf16_f32 v212, v164, v165
v_mfma_f32_16x16x32_bf16 v[128:131], a[140:143], v[76:79], v[128:131]
ds_read_b128 a[184:187], v4 offset:6144
v_cmp_ge_i32 s[96:97], -3, v24
v_mfma_f32_16x16x32_bf16 v[132:135], a[140:143], v[92:95], v[132:135]
v_exp_f32 v176, v176
v_mfma_f32_16x16x32_bf16 v[136:139], a[140:143], v[108:111], v[136:139]
ds_read_b128 a[188:191], v4 offset:7168
v_cvt_pk_bf16_f32 v213, v166, v167
v_mfma_f32_16x16x32_bf16 v[140:143], a[140:143], v[124:127], v[140:143]
v_exp_f32 v177, v177
v_mfma_f32_16x16x32_bf16 v[144:147], a[144:147], v[64:67], 0
buffer_load_dwordx4 v9, s[16:19], s39 offen offset:2048 sc1 lds
v_mfma_f32_16x16x32_bf16 v[148:151], a[144:147], v[80:83], 0
v_exp_f32 v178, v178
v_mfma_f32_16x16x32_bf16 v[152:155], a[144:147], v[96:99], 0
v_exp_f32 v179, v179
v_mfma_f32_16x16x32_bf16 v[156:159], a[144:147], v[112:115], 0
v_exp_f32 v180, v180
v_mfma_f32_16x16x32_bf16 v[144:147], a[148:151], v[68:71], v[144:147]
v_exp_f32 v181, v181
v_mfma_f32_16x16x32_bf16 v[148:151], a[148:151], v[84:87], v[148:151]
v_exp_f32 v182, v182
v_mfma_f32_16x16x32_bf16 v[152:155], a[148:151], v[100:103], v[152:155]
v_exp_f32 v183, v183
v_mfma_f32_16x16x32_bf16 v[156:159], a[148:151], v[116:119], v[156:159]
buffer_load_dwordx4 v9, s[16:19], s39 offen offset:3072 sc1 lds
v_mfma_f32_16x16x32_bf16 v[144:147], a[152:155], v[72:75], v[144:147]
v_exp_f32 v184, v184
v_mfma_f32_16x16x32_bf16 v[148:151], a[152:155], v[88:91], v[148:151]
v_exp_f32 v185, v185
v_mfma_f32_16x16x32_bf16 v[152:155], a[152:155], v[104:107], v[152:155]
v_exp_f32 v186, v186
v_mfma_f32_16x16x32_bf16 v[156:159], a[152:155], v[120:123], v[156:159]
v_exp_f32 v187, v187
v_mfma_f32_16x16x32_bf16 v[144:147], a[156:159], v[76:79], v[144:147]
v_exp_f32 v188, v188
v_mfma_f32_16x16x32_bf16 v[148:151], a[156:159], v[92:95], v[148:151]
v_exp_f32 v189, v189
v_mfma_f32_16x16x32_bf16 v[152:155], a[156:159], v[108:111], v[152:155]
v_exp_f32 v190, v190
v_mfma_f32_16x16x32_bf16 v[156:159], a[156:159], v[124:127], v[156:159]
s_waitcnt vmcnt(16) lgkmcnt(0)
s_barrier
v_mfma_f32_16x16x32_bf16 a[0:3], a[160:163], v[192:195], a[0:3]
ds_read_b128 a[128:131], v3 offset:8192
v_cvt_pk_bf16_f32 v216, v168, v169
v_mfma_f32_16x16x32_bf16 a[4:7], a[160:163], v[196:199], a[4:7]
s_add_u32 m0, s43, 0
v_cvt_pk_bf16_f32 v217, v170, v171
v_mfma_f32_16x16x32_bf16 a[8:11], a[160:163], v[200:203], a[8:11]
buffer_load_dwordx4 v2, s[12:15], s41 offen sc1 lds
v_mfma_f32_16x16x32_bf16 a[12:15], a[160:163], v[204:207], a[12:15]
v_exp_f32 v191, v191
v_mfma_f32_16x16x32_bf16 a[16:19], a[164:167], v[192:195], a[16:19]
ds_read_b128 a[132:135], v3 offset:9216
ds_read_b128 a[136:139], v3 offset:10240
v_mfma_f32_16x16x32_bf16 a[20:23], a[164:167], v[196:199], a[20:23]
v_cvt_pk_bf16_f32 v220, v172, v173
v_cvt_pk_bf16_f32 v221, v174, v175
v_mfma_f32_16x16x32_bf16 a[24:27], a[164:167], v[200:203], a[24:27]
ds_read_b128 a[140:143], v3 offset:11264
v_cvt_pk_bf16_f32 v210, v176, v177
v_mfma_f32_16x16x32_bf16 a[28:31], a[164:167], v[204:207], a[28:31]
v_cvt_pk_bf16_f32 v211, v178, v179
v_cvt_pk_bf16_f32 v214, v180, v181
v_mfma_f32_16x16x32_bf16 a[32:35], a[168:171], v[192:195], a[32:35]
ds_read_b128 a[144:147], v3 offset:12288
v_cvt_pk_bf16_f32 v215, v182, v183
v_mfma_f32_16x16x32_bf16 a[36:39], a[168:171], v[196:199], a[36:39]
buffer_load_dwordx4 v2, s[12:15], s41 offen offset:1024 sc1 lds
v_mfma_f32_16x16x32_bf16 a[40:43], a[168:171], v[200:203], a[40:43]
ds_read_b128 a[148:151], v3 offset:13312
v_cvt_pk_bf16_f32 v218, v184, v185
v_mfma_f32_16x16x32_bf16 a[44:47], a[168:171], v[204:207], a[44:47]
v_cvt_pk_bf16_f32 v219, v186, v187
v_cvt_pk_bf16_f32 v222, v188, v189
v_mfma_f32_16x16x32_bf16 a[48:51], a[172:175], v[192:195], a[48:51]
ds_read_b128 a[152:155], v3 offset:14336
v_cvt_pk_bf16_f32 v223, v190, v191
v_mfma_f32_16x16x32_bf16 a[52:55], a[172:175], v[196:199], a[52:55]
v_cndmask_b32 v128, v7, v128, s[64:65]
v_cndmask_b32 v129, v7, v129, s[66:67]
v_mfma_f32_16x16x32_bf16 a[56:59], a[172:175], v[200:203], a[56:59]
ds_read_b128 a[156:159], v3 offset:15360
v_cndmask_b32 v130, v7, v130, s[68:69]
v_mfma_f32_16x16x32_bf16 a[60:63], a[172:175], v[204:207], a[60:63]
v_cndmask_b32 v131, v7, v131, s[70:71]
v_cndmask_b32 v144, v7, v144, s[72:73]
v_mfma_f32_16x16x32_bf16 a[192:195], v[224:227], v[192:195], a[192:195]
buffer_load_dwordx4 v2, s[12:15], s41 offen offset:2048 sc1 lds
v_mfma_f32_16x16x32_bf16 a[196:199], v[224:227], v[196:199], a[196:199]
v_exp_f32 v128, v128
v_mfma_f32_16x16x32_bf16 a[64:67], a[176:179], v[192:195], a[64:67]
v_exp_f32 v129, v129
v_mfma_f32_16x16x32_bf16 a[68:71], a[176:179], v[196:199], a[68:71]
v_exp_f32 v130, v130
v_mfma_f32_16x16x32_bf16 a[72:75], a[176:179], v[200:203], a[72:75]
v_exp_f32 v131, v131
v_mfma_f32_16x16x32_bf16 a[76:79], a[176:179], v[204:207], a[76:79]
v_exp_f32 v132, v132
v_mfma_f32_16x16x32_bf16 a[80:83], a[180:183], v[192:195], a[80:83]
v_exp_f32 v133, v133
v_mfma_f32_16x16x32_bf16 a[84:87], a[180:183], v[196:199], a[84:87]
buffer_load_dwordx4 v2, s[12:15], s41 offen offset:3072 sc1 lds
v_mfma_f32_16x16x32_bf16 a[88:91], a[180:183], v[200:203], a[88:91]
v_exp_f32 v134, v134
v_mfma_f32_16x16x32_bf16 a[92:95], a[180:183], v[204:207], a[92:95]
v_exp_f32 v135, v135
v_mfma_f32_16x16x32_bf16 a[96:99], a[184:187], v[192:195], a[96:99]
v_exp_f32 v136, v136
v_mfma_f32_16x16x32_bf16 a[100:103], a[184:187], v[196:199], a[100:103]
v_exp_f32 v137, v137
v_mfma_f32_16x16x32_bf16 a[104:107], a[184:187], v[200:203], a[104:107]
v_exp_f32 v138, v138
v_mfma_f32_16x16x32_bf16 a[108:111], a[184:187], v[204:207], a[108:111]
s_add_u32 m0, s42, 32768
v_cndmask_b32 v145, v7, v145, s[28:29]
v_mfma_f32_16x16x32_bf16 a[112:115], a[188:191], v[192:195], a[112:115]
buffer_load_dwordx4 v2, s[16:19], s40 offen sc1 lds
v_mfma_f32_16x16x32_bf16 a[116:119], a[188:191], v[196:199], a[116:119]
v_exp_f32 v139, v139
v_mfma_f32_16x16x32_bf16 a[120:123], a[188:191], v[200:203], a[120:123]
v_exp_f32 v140, v140
v_mfma_f32_16x16x32_bf16 a[124:127], a[188:191], v[204:207], a[124:127]
v_exp_f32 v141, v141
v_mfma_f32_16x16x32_bf16 a[200:203], v[224:227], v[200:203], a[200:203]
v_exp_f32 v142, v142
v_mfma_f32_16x16x32_bf16 a[204:207], v[224:227], v[204:207], a[204:207]
v_cndmask_b32 v146, v7, v146, s[30:31]
s_waitcnt lgkmcnt(0)
v_mfma_f32_16x16x32_bf16 v[164:167], a[128:131], v[80:83], 0
ds_read_b128 a[160:163], v4 offset:8192
v_cndmask_b32 v147, v7, v147, s[96:97]
v_mfma_f32_16x16x32_bf16 v[168:171], a[128:131], v[96:99], 0
buffer_load_dwordx4 v2, s[16:19], s40 offen offset:1024 sc1 lds
v_mfma_f32_16x16x32_bf16 v[172:175], a[128:131], v[112:115], 0
v_exp_f32 v143, v143
v_mfma_f32_16x16x32_bf16 v[180:183], a[144:147], v[80:83], 0
ds_read_b128 a[164:167], v4 offset:9216
v_cvt_pk_bf16_f32 v192, v128, v129
v_mfma_f32_16x16x32_bf16 v[184:187], a[144:147], v[96:99], 0
ds_read_b128 a[168:171], v4 offset:10240
v_cvt_pk_bf16_f32 v193, v130, v131
v_mfma_f32_16x16x32_bf16 v[188:191], a[144:147], v[112:115], 0
v_exp_f32 v144, v144
v_mfma_f32_16x16x32_bf16 v[164:167], a[132:135], v[84:87], v[164:167]
ds_read_b128 a[172:175], v4 offset:11264
v_cvt_pk_bf16_f32 v196, v132, v133
v_mfma_f32_16x16x32_bf16 v[168:171], a[132:135], v[100:103], v[168:171]
v_exp_f32 v145, v145
v_mfma_f32_16x16x32_bf16 v[172:175], a[132:135], v[116:119], v[172:175]
buffer_load_dwordx4 v2, s[16:19], s40 offen offset:2048 sc1 lds
v_mfma_f32_16x16x32_bf16 v[180:183], a[148:151], v[84:87], v[180:183]
ds_read_b128 a[176:179], v4 offset:12288
v_cvt_pk_bf16_f32 v197, v134, v135
v_mfma_f32_16x16x32_bf16 v[184:187], a[148:151], v[100:103], v[184:187]
ds_read_b128 a[180:183], v4 offset:13312
v_cvt_pk_bf16_f32 v200, v136, v137
v_mfma_f32_16x16x32_bf16 v[188:191], a[148:151], v[116:119], v[188:191]
v_exp_f32 v146, v146
v_mfma_f32_16x16x32_bf16 v[164:167], a[136:139], v[88:91], v[164:167]
ds_read_b128 a[184:187], v4 offset:14336
v_cvt_pk_bf16_f32 v201, v138, v139
v_mfma_f32_16x16x32_bf16 v[168:171], a[136:139], v[104:107], v[168:171]
v_exp_f32 v147, v147
v_mfma_f32_16x16x32_bf16 v[172:175], a[136:139], v[120:123], v[172:175]
ds_read_b128 a[188:191], v4 offset:15360
v_cvt_pk_bf16_f32 v204, v140, v141
v_mfma_f32_16x16x32_bf16 v[180:183], a[152:155], v[88:91], v[180:183]
buffer_load_dwordx4 v2, s[16:19], s40 offen offset:3072 sc1 lds
v_mfma_f32_16x16x32_bf16 v[184:187], a[152:155], v[104:107], v[184:187]
v_exp_f32 v148, v148
v_mfma_f32_16x16x32_bf16 v[188:191], a[152:155], v[120:123], v[188:191]
v_exp_f32 v149, v149
v_mfma_f32_16x16x32_bf16 v[164:167], a[140:143], v[92:95], v[164:167]
v_exp_f32 v150, v150
v_mfma_f32_16x16x32_bf16 v[168:171], a[140:143], v[108:111], v[168:171]
v_exp_f32 v151, v151
v_mfma_f32_16x16x32_bf16 v[172:175], a[140:143], v[124:127], v[172:175]
v_exp_f32 v152, v152
v_mfma_f32_16x16x32_bf16 v[180:183], a[156:159], v[92:95], v[180:183]
v_exp_f32 v153, v153
v_mfma_f32_16x16x32_bf16 v[184:187], a[156:159], v[108:111], v[184:187]
v_exp_f32 v154, v154
v_mfma_f32_16x16x32_bf16 v[188:191], a[156:159], v[124:127], v[188:191]
s_waitcnt vmcnt(16) lgkmcnt(0)
s_barrier
v_xor_b32 v3, 16384, v3
v_xor_b32 v4, 16384, v4
v_mfma_f32_16x16x32_bf16 a[0:3], a[160:163], v[208:211], a[0:3]
ds_read_b128 a[128:131], v3 offset:0
s_add_u32 m0, s43, 8192
v_mfma_f32_16x16x32_bf16 a[4:7], a[160:163], v[212:215], a[4:7]
buffer_load_dwordx4 v9, s[12:15], s41 offen sc1 lds
v_mfma_f32_16x16x32_bf16 a[8:11], a[160:163], v[216:219], a[8:11]
ds_read_b128 a[132:135], v3 offset:1024
v_cndmask_b32 v164, v7, v164, s[64:65]
v_mfma_f32_16x16x32_bf16 a[12:15], a[160:163], v[220:223], a[12:15]
v_exp_f32 v155, v155
v_mfma_f32_16x16x32_bf16 a[16:19], a[164:167], v[208:211], a[16:19]
ds_read_b128 a[136:139], v3 offset:2048
v_cndmask_b32 v165, v7, v165, s[66:67]
v_mfma_f32_16x16x32_bf16 a[20:23], a[164:167], v[212:215], a[20:23]
v_exp_f32 v156, v156
v_mfma_f32_16x16x32_bf16 a[24:27], a[164:167], v[216:219], a[24:27]
ds_read_b128 a[140:143], v3 offset:3072
v_cndmask_b32 v166, v7, v166, s[68:69]
v_mfma_f32_16x16x32_bf16 a[28:31], a[164:167], v[220:223], a[28:31]
buffer_load_dwordx4 v9, s[12:15], s41 offen offset:1024 sc1 lds
v_mfma_f32_16x16x32_bf16 a[32:35], a[168:171], v[208:211], a[32:35]
ds_read_b128 a[144:147], v3 offset:4096
v_cndmask_b32 v167, v7, v167, s[70:71]
v_mfma_f32_16x16x32_bf16 a[36:39], a[168:171], v[212:215], a[36:39]
v_exp_f32 v157, v157
v_mfma_f32_16x16x32_bf16 a[40:43], a[168:171], v[216:219], a[40:43]
ds_read_b128 a[148:151], v3 offset:5120
v_cndmask_b32 v180, v7, v180, s[72:73]
v_mfma_f32_16x16x32_bf16 a[44:47], a[168:171], v[220:223], a[44:47]
v_exp_f32 v158, v158
v_mfma_f32_16x16x32_bf16 a[48:51], a[172:175], v[208:211], a[48:51]
ds_read_b128 a[152:155], v3 offset:6144
v_cndmask_b32 v181, v7, v181, s[28:29]
v_mfma_f32_16x16x32_bf16 a[52:55], a[172:175], v[212:215], a[52:55]
buffer_load_dwordx4 v9, s[12:15], s41 offen offset:2048 sc1 lds
v_mfma_f32_16x16x32_bf16 a[56:59], a[172:175], v[216:219], a[56:59]
ds_read_b128 a[156:159], v3 offset:7168
v_cndmask_b32 v182, v7, v182, s[30:31]
v_mfma_f32_16x16x32_bf16 a[60:63], a[172:175], v[220:223], a[60:63]
v_exp_f32 v159, v159
v_mfma_f32_16x16x32_bf16 a[192:195], v[224:227], v[208:211], a[192:195]
v_cndmask_b32 v183, v7, v183, s[96:97]
v_cvt_pk_bf16_f32 v205, v142, v143
v_mfma_f32_16x16x32_bf16 a[196:199], v[224:227], v[212:215], a[196:199]
v_exp_f32 v164, v164
v_mfma_f32_16x16x32_bf16 a[64:67], a[176:179], v[208:211], a[64:67]
v_exp_f32 v165, v165
v_mfma_f32_16x16x32_bf16 a[68:71], a[176:179], v[212:215], a[68:71]
buffer_load_dwordx4 v9, s[12:15], s41 offen offset:3072 sc1 lds
v_mfma_f32_16x16x32_bf16 a[72:75], a[176:179], v[216:219], a[72:75]
s_xor_b32 s42, s42, 16384
s_xor_b32 s43, s43, 16384
v_mfma_f32_16x16x32_bf16 a[76:79], a[176:179], v[220:223], a[76:79]
s_mov_b32 s39, s40
s_mov_b32 s40, s41
v_mfma_f32_16x16x32_bf16 a[80:83], a[180:183], v[208:211], a[80:83]
s_add_u32 s34, s34, 1
s_add_u32 s74, s34, 3
v_mfma_f32_16x16x32_bf16 a[84:87], a[180:183], v[212:215], a[84:87]
v_exp_f32 v166, v166
v_mfma_f32_16x16x32_bf16 a[88:91], a[180:183], v[216:219], a[88:91]
v_exp_f32 v167, v167
v_mfma_f32_16x16x32_bf16 a[92:95], a[180:183], v[220:223], a[92:95]
v_exp_f32 v168, v168
v_mfma_f32_16x16x32_bf16 a[96:99], a[184:187], v[208:211], a[96:99]
v_exp_f32 v169, v169
v_mfma_f32_16x16x32_bf16 a[100:103], a[184:187], v[212:215], a[100:103]
v_exp_f32 v170, v170
v_mfma_f32_16x16x32_bf16 a[104:107], a[184:187], v[216:219], a[104:107]
v_exp_f32 v171, v171
v_mfma_f32_16x16x32_bf16 a[108:111], a[184:187], v[220:223], a[108:111]
v_exp_f32 v172, v172
v_mfma_f32_16x16x32_bf16 a[112:115], a[188:191], v[208:211], a[112:115]
v_exp_f32 v173, v173
v_mfma_f32_16x16x32_bf16 a[116:119], a[188:191], v[212:215], a[116:119]
v_exp_f32 v174, v174
v_mfma_f32_16x16x32_bf16 a[120:123], a[188:191], v[216:219], a[120:123]
v_exp_f32 v175, v175
v_mfma_f32_16x16x32_bf16 a[124:127], a[188:191], v[220:223], a[124:127]
v_readlane_b32 s41, v5, s74
v_cvt_pk_bf16_f32 v194, v144, v145
v_mfma_f32_16x16x32_bf16 a[200:203], v[224:227], v[216:219], a[200:203]
v_cvt_pk_bf16_f32 v195, v146, v147
v_cvt_pk_bf16_f32 v198, v148, v149
v_mfma_f32_16x16x32_bf16 a[204:207], v[224:227], v[220:223], a[204:207]
v_cvt_pk_bf16_f32 v199, v150, v151
v_cvt_pk_bf16_f32 v202, v152, v153
v_cvt_pk_bf16_f32 v203, v154, v155
v_cvt_pk_bf16_f32 v206, v156, v157
v_cvt_pk_bf16_f32 v207, v158, v159
L_BB:
s_nop 15
s_nop 3
s_cmp_eq_u32 s36, 1
s_cbranch_scc1 L_BBS
s_nop 0
s_waitcnt lgkmcnt(0)
v_mfma_f32_16x16x32_bf16 v[136:139], a[128:131], v[96:99], 0
ds_read_b128 a[160:163], v4 offset:0
v_cvt_pk_bf16_f32 v212, v164, v165
v_mfma_f32_16x16x32_bf16 v[140:143], a[128:131], v[112:115], 0
ds_read_b128 a[164:167], v4 offset:1024
s_add_u32 m0, s43, 40960
v_mfma_f32_16x16x32_bf16 v[152:155], a[144:147], v[96:99], 0
ds_read_b128 a[168:171], v4 offset:2048
v_cvt_pk_bf16_f32 v213, v166, v167
v_mfma_f32_16x16x32_bf16 v[156:159], a[144:147], v[112:115], 0
buffer_load_dwordx4 v9, s[16:19], s39 offen sc1 lds
ds_read_b128 a[172:175], v4 offset:3072
v_mfma_f32_16x16x32_bf16 v[136:139], a[132:135], v[100:103], v[136:139]
ds_read_b128 a[176:179], v4 offset:4096
v_cvt_pk_bf16_f32 v216, v168, v169
v_mfma_f32_16x16x32_bf16 v[140:143], a[132:135], v[116:119], v[140:143]
ds_read_b128 a[180:183], v4 offset:5120
v_cvt_pk_bf16_f32 v217, v170, v171
v_mfma_f32_16x16x32_bf16 v[152:155], a[148:151], v[100:103], v[152:155]
buffer_load_dwordx4 v9, s[16:19], s39 offen offset:1024 sc1 lds
ds_read_b128 a[184:187], v4 offset:6144
v_mfma_f32_16x16x32_bf16 v[156:159], a[148:151], v[116:119], v[156:159]
ds_read_b128 a[188:191], v4 offset:7168
v_cvt_pk_bf16_f32 v220, v172, v173
v_mfma_f32_16x16x32_bf16 v[136:139], a[136:139], v[104:107], v[136:139]
buffer_load_dwordx4 v9, s[16:19], s39 offen offset:2048 sc1 lds
v_mfma_f32_16x16x32_bf16 v[140:143], a[136:139], v[120:123], v[140:143]
v_exp_f32 v180, v180
v_mfma_f32_16x16x32_bf16 v[152:155], a[152:155], v[104:107], v[152:155]
v_exp_f32 v181, v181
v_mfma_f32_16x16x32_bf16 v[156:159], a[152:155], v[120:123], v[156:159]
buffer_load_dwordx4 v9, s[16:19], s39 offen offset:3072 sc1 lds
v_mfma_f32_16x16x32_bf16 v[136:139], a[140:143], v[108:111], v[136:139]
v_exp_f32 v182, v182
v_mfma_f32_16x16x32_bf16 v[140:143], a[140:143], v[124:127], v[140:143]
v_exp_f32 v183, v183
v_mfma_f32_16x16x32_bf16 v[152:155], a[156:159], v[108:111], v[152:155]
v_exp_f32 v184, v184
v_mfma_f32_16x16x32_bf16 v[156:159], a[156:159], v[124:127], v[156:159]
s_waitcnt vmcnt(16) lgkmcnt(0)
s_barrier
v_mfma_f32_16x16x32_bf16 a[0:3], a[160:163], v[192:195], a[0:3]
ds_read_b128 a[128:131], v3 offset:8192
v_cvt_pk_bf16_f32 v221, v174, v175
v_mfma_f32_16x16x32_bf16 a[4:7], a[160:163], v[196:199], a[4:7]
s_add_u32 m0, s43, 0
v_cvt_pk_bf16_f32 v214, v180, v181
v_mfma_f32_16x16x32_bf16 a[8:11], a[160:163], v[200:203], a[8:11]
buffer_load_dwordx4 v2, s[12:15], s41 offen sc1 lds
v_mfma_f32_16x16x32_bf16 a[12:15], a[160:163], v[204:207], a[12:15]
v_exp_f32 v185, v185
v_mfma_f32_16x16x32_bf16 a[16:19], a[164:167], v[192:195], a[16:19]
v_exp_f32 v186, v186
v_mfma_f32_16x16x32_bf16 a[20:23], a[164:167], v[196:199], a[20:23]
v_exp_f32 v187, v187
v_mfma_f32_16x16x32_bf16 a[24:27], a[164:167], v[200:203], a[24:27]
v_exp_f32 v188, v188
v_mfma_f32_16x16x32_bf16 a[28:31], a[164:167], v[204:207], a[28:31]
buffer_load_dwordx4 v2, s[12:15], s41 offen offset:1024 sc1 lds
v_mfma_f32_16x16x32_bf16 a[32:35], a[168:171], v[192:195], a[32:35]
ds_read_b128 a[132:135], v3 offset:9216
v_exp_f32 v189, v189
v_exp_f32 v190, v190
v_exp_f32 v191, v191
v_mfma_f32_16x16x32_bf16 a[36:39], a[168:171], v[196:199], a[36:39]
ds_read_b128 a[136:139], v3 offset:10240
ds_read_b128 a[140:143], v3 offset:11264
v_mfma_f32_16x16x32_bf16 a[40:43], a[168:171], v[200:203], a[40:43]
ds_read_b128 a[144:147], v3 offset:12288
ds_read_b128 a[148:151], v3 offset:13312
v_mfma_f32_16x16x32_bf16 a[44:47], a[168:171], v[204:207], a[44:47]
v_cvt_pk_bf16_f32 v215, v182, v183
v_cvt_pk_bf16_f32 v218, v184, v185
v_mfma_f32_16x16x32_bf16 a[48:51], a[172:175], v[192:195], a[48:51]
buffer_load_dwordx4 v2, s[12:15], s41 offen offset:2048 sc1 lds
v_mfma_f32_16x16x32_bf16 a[52:55], a[172:175], v[196:199], a[52:55]
ds_read_b128 a[152:155], v3 offset:14336
v_cvt_pk_bf16_f32 v219, v186, v187
v_mfma_f32_16x16x32_bf16 a[56:59], a[172:175], v[200:203], a[56:59]
ds_read_b128 a[156:159], v3 offset:15360
v_cvt_pk_bf16_f32 v222, v188, v189
v_mfma_f32_16x16x32_bf16 a[60:63], a[172:175], v[204:207], a[60:63]
v_cvt_pk_bf16_f32 v223, v190, v191
v_cndmask_b32 v136, v7, v136, s[64:65]
v_mfma_f32_16x16x32_bf16 a[192:195], v[224:227], v[192:195], a[192:195]
)ASM"
R"ASM(v_cndmask_b32 v137, v7, v137, s[66:67]
v_cndmask_b32 v138, v7, v138, s[68:69]
v_mfma_f32_16x16x32_bf16 a[196:199], v[224:227], v[196:199], a[196:199]
buffer_load_dwordx4 v2, s[12:15], s41 offen offset:3072 sc1 lds
v_mfma_f32_16x16x32_bf16 a[64:67], a[176:179], v[192:195], a[64:67]
v_cndmask_b32 v139, v7, v139, s[70:71]
v_cndmask_b32 v152, v7, v152, s[72:73]
v_mfma_f32_16x16x32_bf16 a[68:71], a[176:179], v[196:199], a[68:71]
v_exp_f32 v136, v136
v_mfma_f32_16x16x32_bf16 a[72:75], a[176:179], v[200:203], a[72:75]
v_exp_f32 v137, v137
v_mfma_f32_16x16x32_bf16 a[76:79], a[176:179], v[204:207], a[76:79]
s_add_u32 m0, s42, 32768
v_cndmask_b32 v153, v7, v153, s[28:29]
v_mfma_f32_16x16x32_bf16 a[80:83], a[180:183], v[192:195], a[80:83]
buffer_load_dwordx4 v2, s[16:19], s40 offen sc1 lds
v_mfma_f32_16x16x32_bf16 a[84:87], a[180:183], v[196:199], a[84:87]
v_exp_f32 v138, v138
v_mfma_f32_16x16x32_bf16 a[88:91], a[180:183], v[200:203], a[88:91]
v_exp_f32 v139, v139
v_mfma_f32_16x16x32_bf16 a[92:95], a[180:183], v[204:207], a[92:95]
v_exp_f32 v140, v140
v_mfma_f32_16x16x32_bf16 a[96:99], a[184:187], v[192:195], a[96:99]
v_exp_f32 v141, v141
v_mfma_f32_16x16x32_bf16 a[100:103], a[184:187], v[196:199], a[100:103]
buffer_load_dwordx4 v2, s[16:19], s40 offen offset:1024 sc1 lds
v_mfma_f32_16x16x32_bf16 a[104:107], a[184:187], v[200:203], a[104:107]
v_exp_f32 v142, v142
v_mfma_f32_16x16x32_bf16 a[108:111], a[184:187], v[204:207], a[108:111]
v_exp_f32 v143, v143
v_mfma_f32_16x16x32_bf16 a[112:115], a[188:191], v[192:195], a[112:115]
v_cndmask_b32 v154, v7, v154, s[30:31]
v_cndmask_b32 v155, v7, v155, s[96:97]
v_mfma_f32_16x16x32_bf16 a[116:119], a[188:191], v[196:199], a[116:119]
v_exp_f32 v152, v152
v_mfma_f32_16x16x32_bf16 a[120:123], a[188:191], v[200:203], a[120:123]
buffer_load_dwordx4 v2, s[16:19], s40 offen offset:2048 sc1 lds
v_mfma_f32_16x16x32_bf16 a[124:127], a[188:191], v[204:207], a[124:127]
v_exp_f32 v153, v153
v_mfma_f32_16x16x32_bf16 a[200:203], v[224:227], v[200:203], a[200:203]
v_exp_f32 v154, v154
v_mfma_f32_16x16x32_bf16 a[204:207], v[224:227], v[204:207], a[204:207]
s_waitcnt lgkmcnt(0)
v_mfma_f32_16x16x32_bf16 v[172:175], a[128:131], v[112:115], 0
ds_read_b128 a[160:163], v4 offset:8192
v_cvt_pk_bf16_f32 v200, v136, v137
v_mfma_f32_16x16x32_bf16 v[188:191], a[144:147], v[112:115], 0
ds_read_b128 a[164:167], v4 offset:9216
v_cvt_pk_bf16_f32 v201, v138, v139
v_mfma_f32_16x16x32_bf16 v[172:175], a[132:135], v[116:119], v[172:175]
buffer_load_dwordx4 v2, s[16:19], s40 offen offset:3072 sc1 lds
ds_read_b128 a[168:171], v4 offset:10240
v_mfma_f32_16x16x32_bf16 v[188:191], a[148:151], v[116:119], v[188:191]
ds_read_b128 a[172:175], v4 offset:11264
v_cvt_pk_bf16_f32 v204, v140, v141
v_mfma_f32_16x16x32_bf16 v[172:175], a[136:139], v[120:123], v[172:175]
ds_read_b128 a[176:179], v4 offset:12288
v_cvt_pk_bf16_f32 v205, v142, v143
v_mfma_f32_16x16x32_bf16 v[188:191], a[152:155], v[120:123], v[188:191]
ds_read_b128 a[180:183], v4 offset:13312
v_cvt_pk_bf16_f32 v202, v152, v153
v_mfma_f32_16x16x32_bf16 v[172:175], a[140:143], v[124:127], v[172:175]
ds_read_b128 a[184:187], v4 offset:14336
v_mfma_f32_16x16x32_bf16 v[188:191], a[156:159], v[124:127], v[188:191]
ds_read_b128 a[188:191], v4 offset:15360
s_waitcnt vmcnt(16) lgkmcnt(0)
s_barrier
v_xor_b32 v3, 16384, v3
v_xor_b32 v4, 16384, v4
v_mfma_f32_16x16x32_bf16 a[4:7], a[160:163], v[212:215], a[4:7]
ds_read_b128 a[128:131], v3 offset:0
s_add_u32 m0, s43, 8192
v_mfma_f32_16x16x32_bf16 a[8:11], a[160:163], v[216:219], a[8:11]
buffer_load_dwordx4 v9, s[12:15], s41 offen sc1 lds
v_mfma_f32_16x16x32_bf16 a[12:15], a[160:163], v[220:223], a[12:15]
ds_read_b128 a[132:135], v3 offset:1024
v_cndmask_b32 v172, v7, v172, s[64:65]
v_mfma_f32_16x16x32_bf16 a[20:23], a[164:167], v[212:215], a[20:23]
v_exp_f32 v155, v155
v_mfma_f32_16x16x32_bf16 a[24:27], a[164:167], v[216:219], a[24:27]
ds_read_b128 a[136:139], v3 offset:2048
v_cndmask_b32 v173, v7, v173, s[66:67]
v_mfma_f32_16x16x32_bf16 a[28:31], a[164:167], v[220:223], a[28:31]
buffer_load_dwordx4 v9, s[12:15], s41 offen offset:1024 sc1 lds
v_mfma_f32_16x16x32_bf16 a[36:39], a[168:171], v[212:215], a[36:39]
ds_read_b128 a[140:143], v3 offset:3072
v_cndmask_b32 v174, v7, v174, s[68:69]
v_mfma_f32_16x16x32_bf16 a[40:43], a[168:171], v[216:219], a[40:43]
v_exp_f32 v156, v156
v_mfma_f32_16x16x32_bf16 a[44:47], a[168:171], v[220:223], a[44:47]
ds_read_b128 a[144:147], v3 offset:4096
v_cndmask_b32 v175, v7, v175, s[70:71]
v_mfma_f32_16x16x32_bf16 a[52:55], a[172:175], v[212:215], a[52:55]
buffer_load_dwordx4 v9, s[12:15], s41 offen offset:2048 sc1 lds
v_mfma_f32_16x16x32_bf16 a[56:59], a[172:175], v[216:219], a[56:59]
ds_read_b128 a[148:151], v3 offset:5120
v_cndmask_b32 v188, v7, v188, s[72:73]
v_mfma_f32_16x16x32_bf16 a[60:63], a[172:175], v[220:223], a[60:63]
v_exp_f32 v157, v157
v_mfma_f32_16x16x32_bf16 a[196:199], v[224:227], v[212:215], a[196:199]
ds_read_b128 a[152:155], v3 offset:6144
v_cndmask_b32 v189, v7, v189, s[28:29]
v_mfma_f32_16x16x32_bf16 a[200:203], v[224:227], v[216:219], a[200:203]
buffer_load_dwordx4 v9, s[12:15], s41 offen offset:3072 sc1 lds
v_mfma_f32_16x16x32_bf16 a[68:71], a[176:179], v[212:215], a[68:71]
v_exp_f32 v158, v158
v_mfma_f32_16x16x32_bf16 a[72:75], a[176:179], v[216:219], a[72:75]
v_exp_f32 v159, v159
v_mfma_f32_16x16x32_bf16 a[76:79], a[176:179], v[220:223], a[76:79]
ds_read_b128 a[156:159], v3 offset:7168
s_xor_b32 s42, s42, 16384
v_mfma_f32_16x16x32_bf16 a[84:87], a[180:183], v[212:215], a[84:87]
s_xor_b32 s43, s43, 16384
s_mov_b32 s39, s40
v_mfma_f32_16x16x32_bf16 a[88:91], a[180:183], v[216:219], a[88:91]
s_mov_b32 s40, s41
s_add_u32 s34, s34, 1
v_mfma_f32_16x16x32_bf16 a[92:95], a[180:183], v[220:223], a[92:95]
s_add_u32 s74, s34, 3
v_cndmask_b32 v190, v7, v190, s[30:31]
v_mfma_f32_16x16x32_bf16 a[100:103], a[184:187], v[212:215], a[100:103]
v_cndmask_b32 v191, v7, v191, s[96:97]
v_readlane_b32 s41, v5, s74
v_mfma_f32_16x16x32_bf16 a[104:107], a[184:187], v[216:219], a[104:107]
v_exp_f32 v172, v172
v_mfma_f32_16x16x32_bf16 a[108:111], a[184:187], v[220:223], a[108:111]
v_exp_f32 v173, v173
v_mfma_f32_16x16x32_bf16 a[116:119], a[188:191], v[212:215], a[116:119]
v_exp_f32 v174, v174
v_mfma_f32_16x16x32_bf16 a[120:123], a[188:191], v[216:219], a[120:123]
v_exp_f32 v175, v175
v_mfma_f32_16x16x32_bf16 a[124:127], a[188:191], v[220:223], a[124:127]
v_cvt_pk_bf16_f32 v203, v154, v155
v_cvt_pk_bf16_f32 v206, v156, v157
v_mfma_f32_16x16x32_bf16 a[204:207], v[224:227], v[220:223], a[204:207]
v_cvt_pk_bf16_f32 v207, v158, v159
s_branch L_BC
L_BBS:
s_nop 15
s_nop 3
s_nop 0
s_waitcnt lgkmcnt(0)
v_mfma_f32_16x16x32_bf16 v[136:139], a[128:131], v[96:99], 0
ds_read_b128 a[160:163], v4 offset:0
s_lshl_b32 s22, s75, 14
v_mfma_f32_16x16x32_bf16 v[140:143], a[128:131], v[112:115], 0
ds_read_b128 a[164:167], v4 offset:1024
s_add_u32 m0, s43, 40960
v_mfma_f32_16x16x32_bf16 v[152:155], a[144:147], v[96:99], 0
ds_read_b128 a[168:171], v4 offset:2048
s_add_u32 s22, s22, 131072
v_mfma_f32_16x16x32_bf16 v[156:159], a[144:147], v[112:115], 0
buffer_load_dwordx4 v9, s[16:19], s39 offen sc1 lds
ds_read_b128 a[172:175], v4 offset:3072
v_add_u32 v20, s22, v2
v_mfma_f32_16x16x32_bf16 v[136:139], a[132:135], v[100:103], v[136:139]
ds_read_b128 a[176:179], v4 offset:4096
v_mov_b32 v244, v64
v_mov_b32 v245, v65
v_mov_b32 v246, v66
v_mov_b32 v247, v67
v_mfma_f32_16x16x32_bf16 v[140:143], a[132:135], v[116:119], v[140:143]
ds_read_b128 a[180:183], v4 offset:5120
ds_read_b128 v[64:67], v20 offset:0
v_mfma_f32_16x16x32_bf16 v[152:155], a[148:151], v[100:103], v[152:155]
buffer_load_dwordx4 v9, s[16:19], s39 offen offset:1024 sc1 lds
ds_read_b128 a[184:187], v4 offset:6144
v_mfma_f32_16x16x32_bf16 v[156:159], a[148:151], v[116:119], v[156:159]
ds_read_b128 a[188:191], v4 offset:7168
ds_write_b128 v20, v[68:71] offset:0
v_mfma_f32_16x16x32_bf16 v[136:139], a[136:139], v[104:107], v[136:139]
buffer_load_dwordx4 v9, s[16:19], s39 offen offset:2048 sc1 lds
v_mfma_f32_16x16x32_bf16 v[140:143], a[136:139], v[120:123], v[140:143]
ds_read_b128 v[68:71], v20 offset:1024
v_cvt_pk_bf16_f32 v212, v164, v165
v_mfma_f32_16x16x32_bf16 v[152:155], a[152:155], v[104:107], v[152:155]
v_exp_f32 v180, v180
v_mfma_f32_16x16x32_bf16 v[156:159], a[152:155], v[120:123], v[156:159]
buffer_load_dwordx4 v9, s[16:19], s39 offen offset:3072 sc1 lds
v_mfma_f32_16x16x32_bf16 v[136:139], a[140:143], v[108:111], v[136:139]
ds_write_b128 v20, v[72:75] offset:1024
v_cvt_pk_bf16_f32 v213, v166, v167
v_mfma_f32_16x16x32_bf16 v[140:143], a[140:143], v[124:127], v[140:143]
v_exp_f32 v181, v181
v_mfma_f32_16x16x32_bf16 v[152:155], a[156:159], v[108:111], v[152:155]
ds_read_b128 v[72:75], v20 offset:2048
v_cvt_pk_bf16_f32 v216, v168, v169
v_mfma_f32_16x16x32_bf16 v[156:159], a[156:159], v[124:127], v[156:159]
s_waitcnt vmcnt(16) lgkmcnt(0)
s_barrier
v_mfma_f32_16x16x32_bf16 a[0:3], a[160:163], v[192:195], a[0:3]
ds_read_b128 a[128:131], v3 offset:8192
v_cvt_pk_bf16_f32 v217, v170, v171
v_mfma_f32_16x16x32_bf16 a[4:7], a[160:163], v[196:199], a[4:7]
ds_write_b128 v20, v[76:79] offset:2048
s_add_u32 m0, s43, 0
v_mfma_f32_16x16x32_bf16 a[8:11], a[160:163], v[200:203], a[8:11]
buffer_load_dwordx4 v2, s[12:15], s41 offen sc1 lds
v_mfma_f32_16x16x32_bf16 a[12:15], a[160:163], v[204:207], a[12:15]
ds_read_b128 v[76:79], v20 offset:3072
ds_read_b128 a[132:135], v3 offset:9216
v_mfma_f32_16x16x32_bf16 a[16:19], a[164:167], v[192:195], a[16:19]
v_exp_f32 v182, v182
v_mfma_f32_16x16x32_bf16 a[20:23], a[164:167], v[196:199], a[20:23]
v_exp_f32 v183, v183
v_mfma_f32_16x16x32_bf16 a[24:27], a[164:167], v[200:203], a[24:27]
ds_write_b128 v20, v[80:83] offset:3072
ds_read_b128 a[136:139], v3 offset:10240
v_mfma_f32_16x16x32_bf16 a[28:31], a[164:167], v[204:207], a[28:31]
buffer_load_dwordx4 v2, s[12:15], s41 offen offset:1024 sc1 lds
v_mfma_f32_16x16x32_bf16 a[32:35], a[168:171], v[192:195], a[32:35]
v_exp_f32 v184, v184
v_exp_f32 v185, v185
v_exp_f32 v186, v186
v_exp_f32 v187, v187
v_exp_f32 v188, v188
v_exp_f32 v189, v189
v_exp_f32 v190, v190
v_exp_f32 v191, v191
v_mfma_f32_16x16x32_bf16 a[36:39], a[168:171], v[196:199], a[36:39]
ds_read_b128 v[80:83], v20 offset:4096
ds_read_b128 a[140:143], v3 offset:11264
v_mfma_f32_16x16x32_bf16 a[40:43], a[168:171], v[200:203], a[40:43]
ds_write_b128 v20, v[84:87] offset:4096
ds_read_b128 a[144:147], v3 offset:12288
v_mfma_f32_16x16x32_bf16 a[44:47], a[168:171], v[204:207], a[44:47]
ds_read_b128 a[148:151], v3 offset:13312
v_cvt_pk_bf16_f32 v220, v172, v173
v_mfma_f32_16x16x32_bf16 a[48:51], a[172:175], v[192:195], a[48:51]
buffer_load_dwordx4 v2, s[12:15], s41 offen offset:2048 sc1 lds
v_mfma_f32_16x16x32_bf16 a[52:55], a[172:175], v[196:199], a[52:55]
ds_read_b128 v[84:87], v20 offset:5120
ds_read_b128 a[152:155], v3 offset:14336
v_mfma_f32_16x16x32_bf16 a[56:59], a[172:175], v[200:203], a[56:59]
ds_write_b128 v20, v[88:91] offset:5120
ds_read_b128 a[156:159], v3 offset:15360
v_mfma_f32_16x16x32_bf16 a[60:63], a[172:175], v[204:207], a[60:63]
v_cvt_pk_bf16_f32 v221, v174, v175
v_cvt_pk_bf16_f32 v214, v180, v181
v_mfma_f32_16x16x32_bf16 a[192:195], v[224:227], v[192:195], a[192:195]
ds_read_b128 v[88:91], v20 offset:6144
v_cvt_pk_bf16_f32 v215, v182, v183
v_mfma_f32_16x16x32_bf16 a[196:199], v[224:227], v[196:199], a[196:199]
buffer_load_dwordx4 v2, s[12:15], s41 offen offset:3072 sc1 lds
v_mfma_f32_16x16x32_bf16 a[64:67], a[176:179], v[192:195], a[64:67]
ds_write_b128 v20, v[92:95] offset:6144
v_cvt_pk_bf16_f32 v218, v184, v185
v_mfma_f32_16x16x32_bf16 a[68:71], a[176:179], v[196:199], a[68:71]
v_cvt_pk_bf16_f32 v219, v186, v187
v_cvt_pk_bf16_f32 v222, v188, v189
v_mfma_f32_16x16x32_bf16 a[72:75], a[176:179], v[200:203], a[72:75]
ds_read_b128 v[92:95], v20 offset:7168
v_cvt_pk_bf16_f32 v223, v190, v191
v_mfma_f32_16x16x32_bf16 a[76:79], a[176:179], v[204:207], a[76:79]
s_add_u32 m0, s42, 32768
v_cndmask_b32 v136, v7, v136, s[64:65]
v_mfma_f32_16x16x32_bf16 a[80:83], a[180:183], v[192:195], a[80:83]
buffer_load_dwordx4 v2, s[16:19], s40 offen sc1 lds
v_mfma_f32_16x16x32_bf16 a[84:87], a[180:183], v[196:199], a[84:87]
ds_write_b128 v20, v[96:99] offset:7168
v_cndmask_b32 v137, v7, v137, s[66:67]
v_mfma_f32_16x16x32_bf16 a[88:91], a[180:183], v[200:203], a[88:91]
ds_read_b128 v[96:99], v20 offset:8192
v_cndmask_b32 v138, v7, v138, s[68:69]
v_mfma_f32_16x16x32_bf16 a[92:95], a[180:183], v[204:207], a[92:95]
v_cndmask_b32 v139, v7, v139, s[70:71]
v_cndmask_b32 v152, v7, v152, s[72:73]
v_mfma_f32_16x16x32_bf16 a[96:99], a[184:187], v[192:195], a[96:99]
ds_write_b128 v20, v[100:103] offset:8192
v_cndmask_b32 v153, v7, v153, s[28:29]
v_mfma_f32_16x16x32_bf16 a[100:103], a[184:187], v[196:199], a[100:103]
buffer_load_dwordx4 v2, s[16:19], s40 offen offset:1024 sc1 lds
v_mfma_f32_16x16x32_bf16 a[104:107], a[184:187], v[200:203], a[104:107]
ds_read_b128 v[100:103], v20 offset:9216
v_cndmask_b32 v154, v7, v154, s[30:31]
v_mfma_f32_16x16x32_bf16 a[108:111], a[184:187], v[204:207], a[108:111]
v_exp_f32 v136, v136
v_mfma_f32_16x16x32_bf16 a[112:115], a[188:191], v[192:195], a[112:115]
ds_write_b128 v20, v[104:107] offset:9216
v_cndmask_b32 v155, v7, v155, s[96:97]
v_mfma_f32_16x16x32_bf16 a[116:119], a[188:191], v[196:199], a[116:119]
v_exp_f32 v137, v137
v_mfma_f32_16x16x32_bf16 a[120:123], a[188:191], v[200:203], a[120:123]
buffer_load_dwordx4 v2, s[16:19], s40 offen offset:2048 sc1 lds
v_mfma_f32_16x16x32_bf16 a[124:127], a[188:191], v[204:207], a[124:127]
ds_read_b128 v[104:107], v20 offset:10240
v_mfma_f32_16x16x32_bf16 a[200:203], v[224:227], v[200:203], a[200:203]
ds_write_b128 v20, v[108:111] offset:10240
v_mfma_f32_16x16x32_bf16 a[204:207], v[224:227], v[204:207], a[204:207]
s_waitcnt lgkmcnt(0)
v_mfma_f32_16x16x32_bf16 v[172:175], a[128:131], v[112:115], 0
ds_read_b128 a[160:163], v4 offset:8192
ds_read_b128 v[108:111], v20 offset:11264
v_mfma_f32_16x16x32_bf16 v[188:191], a[144:147], v[112:115], 0
ds_read_b128 a[164:167], v4 offset:9216
v_cvt_pk_bf16_f32 v200, v136, v137
v_mfma_f32_16x16x32_bf16 v[172:175], a[132:135], v[116:119], v[172:175]
buffer_load_dwordx4 v2, s[16:19], s40 offen offset:3072 sc1 lds
ds_read_b128 a[168:171], v4 offset:10240
v_mfma_f32_16x16x32_bf16 v[188:191], a[148:151], v[116:119], v[188:191]
ds_read_b128 a[172:175], v4 offset:11264
v_mfma_f32_16x16x32_bf16 v[172:175], a[136:139], v[120:123], v[172:175]
ds_read_b128 a[176:179], v4 offset:12288
v_mfma_f32_16x16x32_bf16 v[188:191], a[152:155], v[120:123], v[188:191]
ds_read_b128 a[180:183], v4 offset:13312
v_mfma_f32_16x16x32_bf16 v[172:175], a[140:143], v[124:127], v[172:175]
ds_read_b128 a[184:187], v4 offset:14336
v_mfma_f32_16x16x32_bf16 v[188:191], a[156:159], v[124:127], v[188:191]
ds_read_b128 a[188:191], v4 offset:15360
v_exp_f32 v138, v138
v_exp_f32 v139, v139
v_exp_f32 v140, v140
v_exp_f32 v141, v141
v_exp_f32 v142, v142
v_exp_f32 v143, v143
s_waitcnt vmcnt(16) lgkmcnt(0)
s_barrier
v_xor_b32 v3, 16384, v3
v_xor_b32 v4, 16384, v4
v_mfma_f32_16x16x32_bf16 a[4:7], a[160:163], v[212:215], a[4:7]
ds_read_b128 a[128:131], v3 offset:0
s_add_u32 m0, s43, 8192
v_mfma_f32_16x16x32_bf16 a[8:11], a[160:163], v[216:219], a[8:11]
buffer_load_dwordx4 v9, s[12:15], s41 offen sc1 lds
v_mfma_f32_16x16x32_bf16 a[12:15], a[160:163], v[220:223], a[12:15]
ds_write_b128 v20, v[112:115] offset:11264
ds_read_b128 a[132:135], v3 offset:1024
v_mfma_f32_16x16x32_bf16 a[20:23], a[164:167], v[212:215], a[20:23]
ds_read_b128 v[112:115], v20 offset:12288
v_cndmask_b32 v172, v7, v172, s[64:65]
v_mfma_f32_16x16x32_bf16 a[24:27], a[164:167], v[216:219], a[24:27]
ds_read_b128 a[136:139], v3 offset:2048
v_cndmask_b32 v173, v7, v173, s[66:67]
v_mfma_f32_16x16x32_bf16 a[28:31], a[164:167], v[220:223], a[28:31]
buffer_load_dwordx4 v9, s[12:15], s41 offen offset:1024 sc1 lds
v_mfma_f32_16x16x32_bf16 a[36:39], a[168:171], v[212:215], a[36:39]
ds_write_b128 v20, v[116:119] offset:12288
ds_read_b128 a[140:143], v3 offset:3072
v_mfma_f32_16x16x32_bf16 a[40:43], a[168:171], v[216:219], a[40:43]
v_exp_f32 v152, v152
v_mfma_f32_16x16x32_bf16 a[44:47], a[168:171], v[220:223], a[44:47]
ds_read_b128 a[144:147], v3 offset:4096
v_cndmask_b32 v174, v7, v174, s[68:69]
v_mfma_f32_16x16x32_bf16 a[52:55], a[172:175], v[212:215], a[52:55]
buffer_load_dwordx4 v9, s[12:15], s41 offen offset:2048 sc1 lds
v_mfma_f32_16x16x32_bf16 a[56:59], a[172:175], v[216:219], a[56:59]
ds_read_b128 v[116:119], v20 offset:13312
ds_read_b128 a[148:151], v3 offset:5120
v_mfma_f32_16x16x32_bf16 a[60:63], a[172:175], v[220:223], a[60:63]
v_exp_f32 v153, v153
v_mfma_f32_16x16x32_bf16 a[196:199], v[224:227], v[212:215], a[196:199]
ds_write_b128 v20, v[120:123] offset:13312
ds_read_b128 a[152:155], v3 offset:6144
v_mfma_f32_16x16x32_bf16 a[200:203], v[224:227], v[216:219], a[200:203]
buffer_load_dwordx4 v9, s[12:15], s41 offen offset:3072 sc1 lds
v_mfma_f32_16x16x32_bf16 a[68:71], a[176:179], v[212:215], a[68:71]
v_exp_f32 v154, v154
v_mfma_f32_16x16x32_bf16 a[72:75], a[176:179], v[216:219], a[72:75]
ds_read_b128 v[120:123], v20 offset:14336
ds_read_b128 a[156:159], v3 offset:7168
v_mfma_f32_16x16x32_bf16 a[76:79], a[176:179], v[220:223], a[76:79]
v_exp_f32 v155, v155
v_mfma_f32_16x16x32_bf16 a[84:87], a[180:183], v[212:215], a[84:87]
v_exp_f32 v156, v156
v_mfma_f32_16x16x32_bf16 a[88:91], a[180:183], v[216:219], a[88:91]
v_exp_f32 v157, v157
v_mfma_f32_16x16x32_bf16 a[92:95], a[180:183], v[220:223], a[92:95]
ds_write_b128 v20, v[124:127] offset:14336
v_exp_f32 v158, v158
v_exp_f32 v159, v159
v_mfma_f32_16x16x32_bf16 a[100:103], a[184:187], v[212:215], a[100:103]
s_xor_b32 s42, s42, 16384
s_xor_b32 s43, s43, 16384
v_mfma_f32_16x16x32_bf16 a[104:107], a[184:187], v[216:219], a[104:107]
ds_read_b128 v[124:127], v20 offset:15360
s_mov_b32 s39, s40
v_mfma_f32_16x16x32_bf16 a[108:111], a[184:187], v[220:223], a[108:111]
s_mov_b32 s40, s41
s_add_u32 s34, s34, 1
v_mfma_f32_16x16x32_bf16 a[116:119], a[188:191], v[212:215], a[116:119]
s_add_u32 s74, s34, 3
v_cndmask_b32 v175, v7, v175, s[70:71]
v_mfma_f32_16x16x32_bf16 a[120:123], a[188:191], v[216:219], a[120:123]
v_cndmask_b32 v188, v7, v188, s[72:73]
v_cndmask_b32 v189, v7, v189, s[28:29]
v_cndmask_b32 v190, v7, v190, s[30:31]
v_cndmask_b32 v191, v7, v191, s[96:97]
v_mfma_f32_16x16x32_bf16 a[124:127], a[188:191], v[220:223], a[124:127]
s_mov_b32 s62, 1
v_readlane_b32 s41, v5, s74
v_mfma_f32_16x16x32_bf16 a[204:207], v[224:227], v[220:223], a[204:207]
v_exp_f32 v172, v172
v_exp_f32 v173, v173
v_exp_f32 v174, v174
v_exp_f32 v175, v175
v_cvt_pk_bf16_f32 v201, v138, v139
v_cvt_pk_bf16_f32 v204, v140, v141
v_cvt_pk_bf16_f32 v205, v142, v143
v_cvt_pk_bf16_f32 v202, v152, v153
v_cvt_pk_bf16_f32 v203, v154, v155
v_cvt_pk_bf16_f32 v206, v156, v157
v_cvt_pk_bf16_f32 v207, v158, v159
L_BC:
s_nop 15
s_nop 3
s_nop 0
s_waitcnt lgkmcnt(0)
v_mfma_f32_16x16x32_bf16 v[128:131], a[128:131], v[64:67], 0
ds_read_b128 a[160:163], v4 offset:0
v_cvt_pk_bf16_f32 v220, v172, v173
v_mfma_f32_16x16x32_bf16 v[132:135], a[128:131], v[80:83], 0
s_add_u32 m0, s43, 40960
v_cvt_pk_bf16_f32 v221, v174, v175
v_mfma_f32_16x16x32_bf16 v[136:139], a[128:131], v[96:99], 0
buffer_load_dwordx4 v9, s[16:19], s39 offen sc1 lds
v_mfma_f32_16x16x32_bf16 v[140:143], a[128:131], v[112:115], 0
ds_read_b128 a[164:167], v4 offset:1024
v_mfma_f32_16x16x32_bf16 v[128:131], a[132:135], v[68:71], v[128:131]
ds_read_b128 a[168:171], v4 offset:2048
v_mfma_f32_16x16x32_bf16 v[132:135], a[132:135], v[84:87], v[132:135]
v_exp_f32 v188, v188
v_mfma_f32_16x16x32_bf16 v[136:139], a[132:135], v[100:103], v[136:139]
ds_read_b128 a[172:175], v4 offset:3072
v_mfma_f32_16x16x32_bf16 v[140:143], a[132:135], v[116:119], v[140:143]
v_exp_f32 v189, v189
v_mfma_f32_16x16x32_bf16 v[128:131], a[136:139], v[72:75], v[128:131]
ds_read_b128 a[176:179], v4 offset:4096
v_cvt_pk_bf16_f32 v222, v188, v189
v_mfma_f32_16x16x32_bf16 v[132:135], a[136:139], v[88:91], v[132:135]
buffer_load_dwordx4 v9, s[16:19], s39 offen offset:1024 sc1 lds
v_mfma_f32_16x16x32_bf16 v[136:139], a[136:139], v[104:107], v[136:139]
ds_read_b128 a[180:183], v4 offset:5120
v_mfma_f32_16x16x32_bf16 v[140:143], a[136:139], v[120:123], v[140:143]
v_exp_f32 v190, v190
v_mfma_f32_16x16x32_bf16 v[128:131], a[140:143], v[76:79], v[128:131]
ds_read_b128 a[184:187], v4 offset:6144
v_mfma_f32_16x16x32_bf16 v[132:135], a[140:143], v[92:95], v[132:135]
v_exp_f32 v191, v191
v_mfma_f32_16x16x32_bf16 v[136:139], a[140:143], v[108:111], v[136:139]
ds_read_b128 a[188:191], v4 offset:7168
v_cvt_pk_bf16_f32 v223, v190, v191
v_mfma_f32_16x16x32_bf16 v[140:143], a[140:143], v[124:127], v[140:143]
v_mfma_f32_16x16x32_bf16 v[144:147], a[144:147], v[64:67], 0
buffer_load_dwordx4 v9, s[16:19], s39 offen offset:2048 sc1 lds
v_mfma_f32_16x16x32_bf16 v[148:151], a[144:147], v[80:83], 0
v_exp_f32 v128, v128
v_mfma_f32_16x16x32_bf16 v[152:155], a[144:147], v[96:99], 0
v_exp_f32 v129, v129
v_mfma_f32_16x16x32_bf16 v[156:159], a[144:147], v[112:115], 0
v_exp_f32 v130, v130
v_mfma_f32_16x16x32_bf16 v[144:147], a[148:151], v[68:71], v[144:147]
v_exp_f32 v131, v131
v_mfma_f32_16x16x32_bf16 v[148:151], a[148:151], v[84:87], v[148:151]
v_exp_f32 v132, v132
v_mfma_f32_16x16x32_bf16 v[152:155], a[148:151], v[100:103], v[152:155]
v_exp_f32 v133, v133
v_mfma_f32_16x16x32_bf16 v[156:159], a[148:151], v[116:119], v[156:159]
buffer_load_dwordx4 v9, s[16:19], s39 offen offset:3072 sc1 lds
v_mfma_f32_16x16x32_bf16 v[144:147], a[152:155], v[72:75], v[144:147]
v_exp_f32 v134, v134
v_mfma_f32_16x16x32_bf16 v[148:151], a[152:155], v[88:91], v[148:151]
v_exp_f32 v135, v135
v_mfma_f32_16x16x32_bf16 v[152:155], a[152:155], v[104:107], v[152:155]
v_exp_f32 v136, v136
v_mfma_f32_16x16x32_bf16 v[156:159], a[152:155], v[120:123], v[156:159]
v_exp_f32 v137, v137
v_mfma_f32_16x16x32_bf16 v[144:147], a[156:159], v[76:79], v[144:147]
v_exp_f32 v138, v138
v_mfma_f32_16x16x32_bf16 v[148:151], a[156:159], v[92:95], v[148:151]
v_exp_f32 v139, v139
v_mfma_f32_16x16x32_bf16 v[152:155], a[156:159], v[108:111], v[152:155]
v_exp_f32 v140, v140
v_mfma_f32_16x16x32_bf16 v[156:159], a[156:159], v[124:127], v[156:159]
s_waitcnt vmcnt(16) lgkmcnt(0)
s_barrier
v_mfma_f32_16x16x32_bf16 a[8:11], a[160:163], v[200:203], a[8:11]
ds_read_b128 a[128:131], v3 offset:8192
v_mfma_f32_16x16x32_bf16 a[12:15], a[160:163], v[204:207], a[12:15]
s_add_u32 m0, s43, 0
v_mfma_f32_16x16x32_bf16 a[24:27], a[164:167], v[200:203], a[24:27]
ds_read_b128 a[132:135], v3 offset:9216
v_mfma_f32_16x16x32_bf16 a[28:31], a[164:167], v[204:207], a[28:31]
buffer_load_dwordx4 v2, s[12:15], s41 offen sc1 lds
v_mfma_f32_16x16x32_bf16 a[40:43], a[168:171], v[200:203], a[40:43]
ds_read_b128 a[136:139], v3 offset:10240
v_mfma_f32_16x16x32_bf16 a[44:47], a[168:171], v[204:207], a[44:47]
v_exp_f32 v141, v141
v_mfma_f32_16x16x32_bf16 a[56:59], a[172:175], v[200:203], a[56:59]
ds_read_b128 a[140:143], v3 offset:11264
v_mfma_f32_16x16x32_bf16 a[60:63], a[172:175], v[204:207], a[60:63]
buffer_load_dwordx4 v2, s[12:15], s41 offen offset:1024 sc1 lds
v_mfma_f32_16x16x32_bf16 a[200:203], v[224:227], v[200:203], a[200:203]
ds_read_b128 a[144:147], v3 offset:12288
v_mfma_f32_16x16x32_bf16 a[72:75], a[176:179], v[200:203], a[72:75]
v_exp_f32 v142, v142
v_mfma_f32_16x16x32_bf16 a[76:79], a[176:179], v[204:207], a[76:79]
ds_read_b128 a[148:151], v3 offset:13312
v_mfma_f32_16x16x32_bf16 a[88:91], a[180:183], v[200:203], a[88:91]
v_exp_f32 v143, v143
v_mfma_f32_16x16x32_bf16 a[92:95], a[180:183], v[204:207], a[92:95]
ds_read_b128 a[152:155], v3 offset:14336
v_mfma_f32_16x16x32_bf16 a[104:107], a[184:187], v[200:203], a[104:107]
buffer_load_dwordx4 v2, s[12:15], s41 offen offset:2048 sc1 lds
v_mfma_f32_16x16x32_bf16 a[108:111], a[184:187], v[204:207], a[108:111]
ds_read_b128 a[156:159], v3 offset:15360
v_mfma_f32_16x16x32_bf16 a[120:123], a[188:191], v[200:203], a[120:123]
v_exp_f32 v144, v144
v_mfma_f32_16x16x32_bf16 a[124:127], a[188:191], v[204:207], a[124:127]
v_exp_f32 v145, v145
v_mfma_f32_16x16x32_bf16 a[204:207], v[224:227], v[204:207], a[204:207]
s_waitcnt lgkmcnt(0)
v_mfma_f32_16x16x32_bf16 v[160:163], a[128:131], v[64:67], 0
ds_read_b128 a[160:163], v4 offset:8192
v_cvt_pk_bf16_f32 v192, v128, v129
v_mfma_f32_16x16x32_bf16 v[164:167], a[128:131], v[80:83], 0
buffer_load_dwordx4 v2, s[12:15], s41 offen offset:3072 sc1 lds
v_mfma_f32_16x16x32_bf16 v[168:171], a[128:131], v[96:99], 0
ds_read_b128 a[164:167], v4 offset:9216
v_cvt_pk_bf16_f32 v193, v130, v131
v_mfma_f32_16x16x32_bf16 v[172:175], a[128:131], v[112:115], 0
v_exp_f32 v146, v146
v_mfma_f32_16x16x32_bf16 v[160:163], a[132:135], v[68:71], v[160:163]
ds_read_b128 a[168:171], v4 offset:10240
v_cvt_pk_bf16_f32 v196, v132, v133
v_mfma_f32_16x16x32_bf16 v[164:167], a[132:135], v[84:87], v[164:167]
v_exp_f32 v147, v147
v_mfma_f32_16x16x32_bf16 v[168:171], a[132:135], v[100:103], v[168:171]
ds_read_b128 a[172:175], v4 offset:11264
s_add_u32 m0, s42, 32768
v_mfma_f32_16x16x32_bf16 v[172:175], a[132:135], v[116:119], v[172:175]
buffer_load_dwordx4 v2, s[16:19], s40 offen sc1 lds
v_mfma_f32_16x16x32_bf16 v[160:163], a[136:139], v[72:75], v[160:163]
ds_read_b128 a[176:179], v4 offset:12288
v_cvt_pk_bf16_f32 v197, v134, v135
v_mfma_f32_16x16x32_bf16 v[164:167], a[136:139], v[88:91], v[164:167]
v_exp_f32 v148, v148
v_mfma_f32_16x16x32_bf16 v[168:171], a[136:139], v[104:107], v[168:171]
ds_read_b128 a[180:183], v4 offset:13312
v_cvt_pk_bf16_f32 v200, v136, v137
v_mfma_f32_16x16x32_bf16 v[172:175], a[136:139], v[120:123], v[172:175]
v_exp_f32 v149, v149
v_mfma_f32_16x16x32_bf16 v[160:163], a[140:143], v[76:79], v[160:163]
buffer_load_dwordx4 v2, s[16:19], s40 offen offset:1024 sc1 lds
v_mfma_f32_16x16x32_bf16 v[164:167], a[140:143], v[92:95], v[164:167]
ds_read_b128 a[184:187], v4 offset:14336
v_cvt_pk_bf16_f32 v201, v138, v139
v_mfma_f32_16x16x32_bf16 v[168:171], a[140:143], v[108:111], v[168:171]
ds_read_b128 a[188:191], v4 offset:15360
v_cvt_pk_bf16_f32 v204, v140, v141
v_mfma_f32_16x16x32_bf16 v[172:175], a[140:143], v[124:127], v[172:175]
v_exp_f32 v150, v150
v_mfma_f32_16x16x32_bf16 v[176:179], a[144:147], v[64:67], 0
v_exp_f32 v151, v151
v_mfma_f32_16x16x32_bf16 v[180:183], a[144:147], v[80:83], 0
v_exp_f32 v152, v152
v_mfma_f32_16x16x32_bf16 v[184:187], a[144:147], v[96:99], 0
buffer_load_dwordx4 v2, s[16:19], s40 offen offset:2048 sc1 lds
v_mfma_f32_16x16x32_bf16 v[188:191], a[144:147], v[112:115], 0
v_exp_f32 v153, v153
v_mfma_f32_16x16x32_bf16 v[176:179], a[148:151], v[68:71], v[176:179]
v_exp_f32 v154, v154
v_mfma_f32_16x16x32_bf16 v[180:183], a[148:151], v[84:87], v[180:183]
v_exp_f32 v155, v155
v_mfma_f32_16x16x32_bf16 v[184:187], a[148:151], v[100:103], v[184:187]
v_exp_f32 v156, v156
v_mfma_f32_16x16x32_bf16 v[188:191], a[148:151], v[116:119], v[188:191]
v_exp_f32 v157, v157
v_mfma_f32_16x16x32_bf16 v[176:179], a[152:155], v[72:75], v[176:179]
buffer_load_dwordx4 v2, s[16:19], s40 offen offset:3072 sc1 lds
v_mfma_f32_16x16x32_bf16 v[180:183], a[152:155], v[88:91], v[180:183]
v_exp_f32 v158, v158
v_mfma_f32_16x16x32_bf16 v[184:187], a[152:155], v[104:107], v[184:187]
v_exp_f32 v159, v159
v_mfma_f32_16x16x32_bf16 v[188:191], a[152:155], v[120:123], v[188:191]
v_exp_f32 v160, v160
v_mfma_f32_16x16x32_bf16 v[176:179], a[156:159], v[76:79], v[176:179]
v_exp_f32 v161, v161
v_mfma_f32_16x16x32_bf16 v[180:183], a[156:159], v[92:95], v[180:183]
v_exp_f32 v162, v162
v_mfma_f32_16x16x32_bf16 v[184:187], a[156:159], v[108:111], v[184:187]
v_exp_f32 v163, v163
v_mfma_f32_16x16x32_bf16 v[188:191], a[156:159], v[124:127], v[188:191]
s_waitcnt vmcnt(16) lgkmcnt(0)
s_barrier
v_xor_b32 v3, 16384, v3
v_xor_b32 v4, 16384, v4
v_mfma_f32_16x16x32_bf16 a[12:15], a[160:163], v[220:223], a[12:15]
ds_read_b128 a[128:131], v3 offset:0
s_add_u32 m0, s43, 8192
v_mfma_f32_16x16x32_bf16 a[28:31], a[164:167], v[220:223], a[28:31]
ds_read_b128 a[132:135], v3 offset:1024
v_cvt_pk_bf16_f32 v205, v142, v143
v_mfma_f32_16x16x32_bf16 a[44:47], a[168:171], v[220:223], a[44:47]
buffer_load_dwordx4 v9, s[12:15], s41 offen sc1 lds
ds_read_b128 a[136:139], v3 offset:2048
v_mfma_f32_16x16x32_bf16 a[60:63], a[172:175], v[220:223], a[60:63]
buffer_load_dwordx4 v9, s[12:15], s41 offen offset:1024 sc1 lds
ds_read_b128 a[140:143], v3 offset:3072
v_mfma_f32_16x16x32_bf16 a[204:207], v[224:227], v[220:223], a[204:207]
buffer_load_dwordx4 v9, s[12:15], s41 offen offset:2048 sc1 lds
ds_read_b128 a[144:147], v3 offset:4096
v_mfma_f32_16x16x32_bf16 a[76:79], a[176:179], v[220:223], a[76:79]
buffer_load_dwordx4 v9, s[12:15], s41 offen offset:3072 sc1 lds
ds_read_b128 a[148:151], v3 offset:5120
v_mfma_f32_16x16x32_bf16 a[92:95], a[180:183], v[220:223], a[92:95]
ds_read_b128 a[152:155], v3 offset:6144
s_xor_b32 s42, s42, 16384
s_xor_b32 s43, s43, 16384
s_mov_b32 s39, s40
s_mov_b32 s40, s41
s_add_u32 s34, s34, 1
s_add_u32 s74, s34, 3
v_mfma_f32_16x16x32_bf16 a[108:111], a[184:187], v[220:223], a[108:111]
ds_read_b128 a[156:159], v3 offset:7168
v_readlane_b32 s41, v5, s74
v_mfma_f32_16x16x32_bf16 a[124:127], a[188:191], v[220:223], a[124:127]
v_exp_f32 v164, v164
v_exp_f32 v165, v165
v_exp_f32 v166, v166
v_exp_f32 v167, v167
v_exp_f32 v168, v168
v_exp_f32 v169, v169
v_exp_f32 v170, v170
v_exp_f32 v171, v171
v_exp_f32 v172, v172
v_exp_f32 v173, v173
v_exp_f32 v174, v174
v_exp_f32 v175, v175
v_cvt_pk_bf16_f32 v194, v144, v145
v_cvt_pk_bf16_f32 v195, v146, v147
v_cvt_pk_bf16_f32 v198, v148, v149
v_cvt_pk_bf16_f32 v199, v150, v151
v_cvt_pk_bf16_f32 v202, v152, v153
v_cvt_pk_bf16_f32 v203, v154, v155
v_cvt_pk_bf16_f32 v206, v156, v157
v_cvt_pk_bf16_f32 v207, v158, v159
s_bitcmp1_b32 s60, s34
s_cbranch_scc0 L_FULL0
s_branch L_EVENT
L_SPECIAL:
s_nop 15
s_nop 3
s_cmp_eq_u32 s62, 1
s_cbranch_scc0 L_BIDLE
global_load_dwordx2 v[16:17], v37, s[48:49] sc1
s_or_b32 s22, s6, s7
s_cmp_eq_u32 s22, 0
s_cbranch_scc0 L_NOBP0
v_lshlrev_b32 v8, 8, v1
v_add_u32 v8, 32768, v8
global_load_dwordx2 v[10:11], v8, s[48:49] offset:0 sc1
global_load_dwordx2 v[12:13], v8, s[48:49] offset:64 sc1
global_load_dwordx2 v[14:15], v8, s[48:49] offset:128 sc1
global_load_dwordx2 v[18:19], v8, s[48:49] offset:192 sc1
)ASM"
R"ASM(L_NOBP0:
v_accvgpr_read_b32 v24, a192
v_accvgpr_read_b32 v25, a196
v_accvgpr_read_b32 v26, a200
v_accvgpr_read_b32 v27, a204
v_max3_f32 v248, v24, v25, v26
v_max_f32 v248, v248, v27
v_min3_f32 v249, v24, v25, v26
v_min_f32 v249, v249, v27
v_add_f32 v250, v24, v25
v_add_f32 v251, v26, v27
v_add_f32 v250, v250, v251
v_mov_b32 v252, 0x53800000
v_mov_b32 v253, 0x2b800000
v_cmp_nle_f32 s[46:47], v248, v252
v_cmp_nge_f32 s[30:31], v249, v253
v_cmp_u_f32 vcc, v250, v250
s_nop 2
s_or_b64 s[46:47], s[46:47], s[30:31]
s_nop 0
s_or_b64 vcc, vcc, s[46:47]
s_cbranch_scc1 L_EV_BAD
v_rcp_f32 v24, v24
v_rcp_f32 v25, v25
v_rcp_f32 v26, v26
v_rcp_f32 v27, v27
v_accvgpr_read_b32 v40, a0
v_accvgpr_read_b32 v41, a1
v_accvgpr_read_b32 v42, a2
v_accvgpr_read_b32 v43, a3
v_accvgpr_read_b32 v44, a16
v_accvgpr_read_b32 v45, a17
v_accvgpr_read_b32 v46, a18
v_accvgpr_read_b32 v47, a19
v_mfma_f32_16x16x32_bf16 a[0:3], v[228:231], v[228:231], 0
v_mul_f32 v40, v40, v24
v_mul_f32 v41, v41, v24
v_mul_f32 v42, v42, v24
v_mul_f32 v43, v43, v24
v_mul_f32 v44, v44, v24
v_mul_f32 v45, v45, v24
v_mul_f32 v46, v46, v24
v_mul_f32 v47, v47, v24
v_mfma_f32_16x16x32_bf16 a[16:19], v[228:231], v[228:231], 0
v_cvt_pk_bf16_f32 v48, v40, v41
v_cvt_pk_bf16_f32 v49, v42, v43
v_cvt_pk_bf16_f32 v50, v44, v45
v_cvt_pk_bf16_f32 v51, v46, v47
s_mov_b32 s23, 0
buffer_store_dwordx4 v[48:51], v2, s[56:59], s23 offen offset:0 sc1
v_accvgpr_read_b32 v52, a32
v_accvgpr_read_b32 v53, a33
v_accvgpr_read_b32 v54, a34
v_accvgpr_read_b32 v55, a35
v_accvgpr_read_b32 v56, a48
v_accvgpr_read_b32 v57, a49
v_accvgpr_read_b32 v58, a50
v_accvgpr_read_b32 v59, a51
v_mfma_f32_16x16x32_bf16 a[32:35], v[228:231], v[228:231], 0
v_mul_f32 v52, v52, v24
v_mul_f32 v53, v53, v24
v_mul_f32 v54, v54, v24
v_mul_f32 v55, v55, v24
v_mul_f32 v56, v56, v24
v_mul_f32 v57, v57, v24
v_mul_f32 v58, v58, v24
v_mul_f32 v59, v59, v24
v_mfma_f32_16x16x32_bf16 a[48:51], v[228:231], v[228:231], 0
v_cvt_pk_bf16_f32 v60, v52, v53
v_cvt_pk_bf16_f32 v61, v54, v55
v_cvt_pk_bf16_f32 v62, v56, v57
v_cvt_pk_bf16_f32 v63, v58, v59
buffer_store_dwordx4 v[60:63], v2, s[56:59], s23 offen offset:1024 sc1
v_accvgpr_read_b32 v40, a64
v_accvgpr_read_b32 v41, a65
v_accvgpr_read_b32 v42, a66
v_accvgpr_read_b32 v43, a67
v_accvgpr_read_b32 v44, a80
v_accvgpr_read_b32 v45, a81
v_accvgpr_read_b32 v46, a82
v_accvgpr_read_b32 v47, a83
v_mfma_f32_16x16x32_bf16 a[64:67], v[228:231], v[228:231], 0
v_mul_f32 v40, v40, v24
v_mul_f32 v41, v41, v24
v_mul_f32 v42, v42, v24
v_mul_f32 v43, v43, v24
v_mul_f32 v44, v44, v24
v_mul_f32 v45, v45, v24
v_mul_f32 v46, v46, v24
v_mul_f32 v47, v47, v24
v_mfma_f32_16x16x32_bf16 a[80:83], v[228:231], v[228:231], 0
v_cvt_pk_bf16_f32 v48, v40, v41
v_cvt_pk_bf16_f32 v49, v42, v43
v_cvt_pk_bf16_f32 v50, v44, v45
v_cvt_pk_bf16_f32 v51, v46, v47
buffer_store_dwordx4 v[48:51], v2, s[56:59], s23 offen offset:2048 sc1
v_accvgpr_read_b32 v52, a96
v_accvgpr_read_b32 v53, a97
v_accvgpr_read_b32 v54, a98
v_accvgpr_read_b32 v55, a99
v_accvgpr_read_b32 v56, a112
v_accvgpr_read_b32 v57, a113
v_accvgpr_read_b32 v58, a114
v_accvgpr_read_b32 v59, a115
v_mfma_f32_16x16x32_bf16 a[96:99], v[228:231], v[228:231], 0
v_mul_f32 v52, v52, v24
v_mul_f32 v53, v53, v24
v_mul_f32 v54, v54, v24
v_mul_f32 v55, v55, v24
v_mul_f32 v56, v56, v24
v_mul_f32 v57, v57, v24
v_mul_f32 v58, v58, v24
v_mul_f32 v59, v59, v24
v_mfma_f32_16x16x32_bf16 a[112:115], v[228:231], v[228:231], 0
v_cvt_pk_bf16_f32 v60, v52, v53
v_cvt_pk_bf16_f32 v61, v54, v55
v_cvt_pk_bf16_f32 v62, v56, v57
v_cvt_pk_bf16_f32 v63, v58, v59
buffer_store_dwordx4 v[60:63], v2, s[56:59], s23 offen offset:3072 sc1
v_accvgpr_read_b32 v40, a4
v_accvgpr_read_b32 v41, a5
v_accvgpr_read_b32 v42, a6
v_accvgpr_read_b32 v43, a7
v_accvgpr_read_b32 v44, a20
v_accvgpr_read_b32 v45, a21
v_accvgpr_read_b32 v46, a22
v_accvgpr_read_b32 v47, a23
v_mfma_f32_16x16x32_bf16 a[4:7], v[228:231], v[228:231], 0
v_mul_f32 v40, v40, v25
v_mul_f32 v41, v41, v25
v_mul_f32 v42, v42, v25
v_mul_f32 v43, v43, v25
v_mul_f32 v44, v44, v25
v_mul_f32 v45, v45, v25
v_mul_f32 v46, v46, v25
v_mul_f32 v47, v47, v25
v_mfma_f32_16x16x32_bf16 a[20:23], v[228:231], v[228:231], 0
v_cvt_pk_bf16_f32 v48, v40, v41
v_cvt_pk_bf16_f32 v49, v42, v43
v_cvt_pk_bf16_f32 v50, v44, v45
v_cvt_pk_bf16_f32 v51, v46, v47
s_mov_b32 s23, 376832
buffer_store_dwordx4 v[48:51], v2, s[56:59], s23 offen offset:0 sc1
v_accvgpr_read_b32 v52, a36
v_accvgpr_read_b32 v53, a37
v_accvgpr_read_b32 v54, a38
v_accvgpr_read_b32 v55, a39
v_accvgpr_read_b32 v56, a52
v_accvgpr_read_b32 v57, a53
v_accvgpr_read_b32 v58, a54
v_accvgpr_read_b32 v59, a55
v_mfma_f32_16x16x32_bf16 a[36:39], v[228:231], v[228:231], 0
v_mul_f32 v52, v52, v25
v_mul_f32 v53, v53, v25
v_mul_f32 v54, v54, v25
v_mul_f32 v55, v55, v25
v_mul_f32 v56, v56, v25
v_mul_f32 v57, v57, v25
v_mul_f32 v58, v58, v25
v_mul_f32 v59, v59, v25
v_mfma_f32_16x16x32_bf16 a[52:55], v[228:231], v[228:231], 0
v_cvt_pk_bf16_f32 v60, v52, v53
v_cvt_pk_bf16_f32 v61, v54, v55
v_cvt_pk_bf16_f32 v62, v56, v57
v_cvt_pk_bf16_f32 v63, v58, v59
buffer_store_dwordx4 v[60:63], v2, s[56:59], s23 offen offset:1024 sc1
v_accvgpr_read_b32 v40, a68
v_accvgpr_read_b32 v41, a69
v_accvgpr_read_b32 v42, a70
v_accvgpr_read_b32 v43, a71
v_accvgpr_read_b32 v44, a84
v_accvgpr_read_b32 v45, a85
v_accvgpr_read_b32 v46, a86
v_accvgpr_read_b32 v47, a87
v_mfma_f32_16x16x32_bf16 a[68:71], v[228:231], v[228:231], 0
v_mul_f32 v40, v40, v25
v_mul_f32 v41, v41, v25
v_mul_f32 v42, v42, v25
v_mul_f32 v43, v43, v25
v_mul_f32 v44, v44, v25
v_mul_f32 v45, v45, v25
v_mul_f32 v46, v46, v25
v_mul_f32 v47, v47, v25
v_mfma_f32_16x16x32_bf16 a[84:87], v[228:231], v[228:231], 0
v_cvt_pk_bf16_f32 v48, v40, v41
v_cvt_pk_bf16_f32 v49, v42, v43
v_cvt_pk_bf16_f32 v50, v44, v45
v_cvt_pk_bf16_f32 v51, v46, v47
buffer_store_dwordx4 v[48:51], v2, s[56:59], s23 offen offset:2048 sc1
v_accvgpr_read_b32 v52, a100
v_accvgpr_read_b32 v53, a101
v_accvgpr_read_b32 v54, a102
v_accvgpr_read_b32 v55, a103
v_accvgpr_read_b32 v56, a116
v_accvgpr_read_b32 v57, a117
v_accvgpr_read_b32 v58, a118
v_accvgpr_read_b32 v59, a119
v_mfma_f32_16x16x32_bf16 a[100:103], v[228:231], v[228:231], 0
v_mul_f32 v52, v52, v25
v_mul_f32 v53, v53, v25
v_mul_f32 v54, v54, v25
v_mul_f32 v55, v55, v25
v_mul_f32 v56, v56, v25
v_mul_f32 v57, v57, v25
v_mul_f32 v58, v58, v25
v_mul_f32 v59, v59, v25
v_mfma_f32_16x16x32_bf16 a[116:119], v[228:231], v[228:231], 0
v_cvt_pk_bf16_f32 v60, v52, v53
v_cvt_pk_bf16_f32 v61, v54, v55
v_cvt_pk_bf16_f32 v62, v56, v57
v_cvt_pk_bf16_f32 v63, v58, v59
buffer_store_dwordx4 v[60:63], v2, s[56:59], s23 offen offset:3072 sc1
v_accvgpr_read_b32 v40, a8
v_accvgpr_read_b32 v41, a9
v_accvgpr_read_b32 v42, a10
v_accvgpr_read_b32 v43, a11
v_accvgpr_read_b32 v44, a24
v_accvgpr_read_b32 v45, a25
v_accvgpr_read_b32 v46, a26
v_accvgpr_read_b32 v47, a27
v_mfma_f32_16x16x32_bf16 a[8:11], v[228:231], v[228:231], 0
v_mul_f32 v40, v40, v26
v_mul_f32 v41, v41, v26
v_mul_f32 v42, v42, v26
v_mul_f32 v43, v43, v26
v_mul_f32 v44, v44, v26
v_mul_f32 v45, v45, v26
v_mul_f32 v46, v46, v26
v_mul_f32 v47, v47, v26
v_mfma_f32_16x16x32_bf16 a[24:27], v[228:231], v[228:231], 0
v_cvt_pk_bf16_f32 v48, v40, v41
v_cvt_pk_bf16_f32 v49, v42, v43
v_cvt_pk_bf16_f32 v50, v44, v45
v_cvt_pk_bf16_f32 v51, v46, v47
s_mov_b32 s23, 753664
buffer_store_dwordx4 v[48:51], v2, s[56:59], s23 offen offset:0 sc1
v_accvgpr_read_b32 v52, a40
v_accvgpr_read_b32 v53, a41
v_accvgpr_read_b32 v54, a42
v_accvgpr_read_b32 v55, a43
v_accvgpr_read_b32 v56, a56
v_accvgpr_read_b32 v57, a57
v_accvgpr_read_b32 v58, a58
v_accvgpr_read_b32 v59, a59
v_mfma_f32_16x16x32_bf16 a[40:43], v[228:231], v[228:231], 0
v_mul_f32 v52, v52, v26
v_mul_f32 v53, v53, v26
v_mul_f32 v54, v54, v26
v_mul_f32 v55, v55, v26
v_mul_f32 v56, v56, v26
v_mul_f32 v57, v57, v26
v_mul_f32 v58, v58, v26
v_mul_f32 v59, v59, v26
v_mfma_f32_16x16x32_bf16 a[56:59], v[228:231], v[228:231], 0
v_cvt_pk_bf16_f32 v60, v52, v53
v_cvt_pk_bf16_f32 v61, v54, v55
v_cvt_pk_bf16_f32 v62, v56, v57
v_cvt_pk_bf16_f32 v63, v58, v59
buffer_store_dwordx4 v[60:63], v2, s[56:59], s23 offen offset:1024 sc1
v_accvgpr_read_b32 v40, a72
v_accvgpr_read_b32 v41, a73
v_accvgpr_read_b32 v42, a74
v_accvgpr_read_b32 v43, a75
v_accvgpr_read_b32 v44, a88
v_accvgpr_read_b32 v45, a89
v_accvgpr_read_b32 v46, a90
v_accvgpr_read_b32 v47, a91
v_mfma_f32_16x16x32_bf16 a[72:75], v[228:231], v[228:231], 0
v_mul_f32 v40, v40, v26
v_mul_f32 v41, v41, v26
v_mul_f32 v42, v42, v26
v_mul_f32 v43, v43, v26
v_mul_f32 v44, v44, v26
v_mul_f32 v45, v45, v26
v_mul_f32 v46, v46, v26
v_mul_f32 v47, v47, v26
v_mfma_f32_16x16x32_bf16 a[88:91], v[228:231], v[228:231], 0
v_cvt_pk_bf16_f32 v48, v40, v41
v_cvt_pk_bf16_f32 v49, v42, v43
v_cvt_pk_bf16_f32 v50, v44, v45
v_cvt_pk_bf16_f32 v51, v46, v47
buffer_store_dwordx4 v[48:51], v2, s[56:59], s23 offen offset:2048 sc1
v_accvgpr_read_b32 v52, a104
v_accvgpr_read_b32 v53, a105
v_accvgpr_read_b32 v54, a106
v_accvgpr_read_b32 v55, a107
v_accvgpr_read_b32 v56, a120
v_accvgpr_read_b32 v57, a121
v_accvgpr_read_b32 v58, a122
v_accvgpr_read_b32 v59, a123
v_mfma_f32_16x16x32_bf16 a[104:107], v[228:231], v[228:231], 0
v_mul_f32 v52, v52, v26
v_mul_f32 v53, v53, v26
v_mul_f32 v54, v54, v26
v_mul_f32 v55, v55, v26
v_mul_f32 v56, v56, v26
v_mul_f32 v57, v57, v26
v_mul_f32 v58, v58, v26
v_mul_f32 v59, v59, v26
v_mfma_f32_16x16x32_bf16 a[120:123], v[228:231], v[228:231], 0
v_cvt_pk_bf16_f32 v60, v52, v53
v_cvt_pk_bf16_f32 v61, v54, v55
v_cvt_pk_bf16_f32 v62, v56, v57
v_cvt_pk_bf16_f32 v63, v58, v59
buffer_store_dwordx4 v[60:63], v2, s[56:59], s23 offen offset:3072 sc1
v_accvgpr_read_b32 v40, a12
v_accvgpr_read_b32 v41, a13
v_accvgpr_read_b32 v42, a14
v_accvgpr_read_b32 v43, a15
v_accvgpr_read_b32 v44, a28
v_accvgpr_read_b32 v45, a29
v_accvgpr_read_b32 v46, a30
v_accvgpr_read_b32 v47, a31
v_mfma_f32_16x16x32_bf16 a[12:15], v[228:231], v[228:231], 0
v_mul_f32 v40, v40, v27
v_mul_f32 v41, v41, v27
v_mul_f32 v42, v42, v27
v_mul_f32 v43, v43, v27
v_mul_f32 v44, v44, v27
v_mul_f32 v45, v45, v27
v_mul_f32 v46, v46, v27
v_mul_f32 v47, v47, v27
v_mfma_f32_16x16x32_bf16 a[28:31], v[228:231], v[228:231], 0
v_cvt_pk_bf16_f32 v48, v40, v41
v_cvt_pk_bf16_f32 v49, v42, v43
v_cvt_pk_bf16_f32 v50, v44, v45
v_cvt_pk_bf16_f32 v51, v46, v47
s_mov_b32 s23, 1130496
buffer_store_dwordx4 v[48:51], v2, s[56:59], s23 offen offset:0 sc1
v_accvgpr_read_b32 v52, a44
v_accvgpr_read_b32 v53, a45
v_accvgpr_read_b32 v54, a46
v_accvgpr_read_b32 v55, a47
v_accvgpr_read_b32 v56, a60
v_accvgpr_read_b32 v57, a61
v_accvgpr_read_b32 v58, a62
v_accvgpr_read_b32 v59, a63
v_mfma_f32_16x16x32_bf16 a[44:47], v[228:231], v[228:231], 0
v_mul_f32 v52, v52, v27
v_mul_f32 v53, v53, v27
v_mul_f32 v54, v54, v27
v_mul_f32 v55, v55, v27
v_mul_f32 v56, v56, v27
v_mul_f32 v57, v57, v27
v_mul_f32 v58, v58, v27
v_mul_f32 v59, v59, v27
v_mfma_f32_16x16x32_bf16 a[60:63], v[228:231], v[228:231], 0
v_cvt_pk_bf16_f32 v60, v52, v53
v_cvt_pk_bf16_f32 v61, v54, v55
v_cvt_pk_bf16_f32 v62, v56, v57
v_cvt_pk_bf16_f32 v63, v58, v59
buffer_store_dwordx4 v[60:63], v2, s[56:59], s23 offen offset:1024 sc1
v_accvgpr_read_b32 v40, a76
v_accvgpr_read_b32 v41, a77
v_accvgpr_read_b32 v42, a78
v_accvgpr_read_b32 v43, a79
v_accvgpr_read_b32 v44, a92
v_accvgpr_read_b32 v45, a93
v_accvgpr_read_b32 v46, a94
v_accvgpr_read_b32 v47, a95
v_mfma_f32_16x16x32_bf16 a[76:79], v[228:231], v[228:231], 0
v_mul_f32 v40, v40, v27
v_mul_f32 v41, v41, v27
v_mul_f32 v42, v42, v27
v_mul_f32 v43, v43, v27
v_mul_f32 v44, v44, v27
v_mul_f32 v45, v45, v27
v_mul_f32 v46, v46, v27
v_mul_f32 v47, v47, v27
v_mfma_f32_16x16x32_bf16 a[92:95], v[228:231], v[228:231], 0
v_cvt_pk_bf16_f32 v48, v40, v41
v_cvt_pk_bf16_f32 v49, v42, v43
v_cvt_pk_bf16_f32 v50, v44, v45
v_cvt_pk_bf16_f32 v51, v46, v47
buffer_store_dwordx4 v[48:51], v2, s[56:59], s23 offen offset:2048 sc1
v_accvgpr_read_b32 v52, a108
v_accvgpr_read_b32 v53, a109
v_accvgpr_read_b32 v54, a110
v_accvgpr_read_b32 v55, a111
v_accvgpr_read_b32 v56, a124
v_accvgpr_read_b32 v57, a125
v_accvgpr_read_b32 v58, a126
v_accvgpr_read_b32 v59, a127
v_mfma_f32_16x16x32_bf16 a[108:111], v[228:231], v[228:231], 0
v_mul_f32 v52, v52, v27
v_mul_f32 v53, v53, v27
v_mul_f32 v54, v54, v27
v_mul_f32 v55, v55, v27
v_mul_f32 v56, v56, v27
v_mul_f32 v57, v57, v27
v_mul_f32 v58, v58, v27
v_mul_f32 v59, v59, v27
v_mfma_f32_16x16x32_bf16 a[124:127], v[228:231], v[228:231], 0
v_cvt_pk_bf16_f32 v60, v52, v53
v_cvt_pk_bf16_f32 v61, v54, v55
v_cvt_pk_bf16_f32 v62, v56, v57
v_cvt_pk_bf16_f32 v63, v58, v59
buffer_store_dwordx4 v[60:63], v2, s[56:59], s23 offen offset:3072 sc1
L_EV_DONE:
v_mfma_f32_16x16x32_bf16 a[192:195], v[228:231], v[228:231], 0
v_mfma_f32_16x16x32_bf16 a[196:199], v[228:231], v[228:231], 0
v_mfma_f32_16x16x32_bf16 a[200:203], v[228:231], v[228:231], 0
v_mfma_f32_16x16x32_bf16 a[204:207], v[228:231], v[228:231], 0
s_sub_u32 s22, 15, s44
s_lshl_b32 s22, s22, 3
s_lshl_b32 s23, s75, 0
s_add_u32 s50, s22, s23
s_lshr_b32 s22, s50, 3
s_lshl_b32 s22, s22, 15
s_lshl_b32 s23, s75, 12
s_add_u32 s37, s22, s23
s_mul_i32 s22, s50, 184
s_lshl_b32 s23, s45, 2
s_add_u32 s22, s22, s23
s_lshl_b32 s22, s22, 10
s_add_u32 s22, s22, 38797312
s_add_u32 s46, s48, s22
s_addc_u32 s47, s49, 0
s_mov_b32 s56, s46
s_and_b32 s57, s47, 65535
s_mov_b32 s58, 1318912
s_mov_b32 s59, 131072
s_mov_b32 s62, 0
s_waitcnt vmcnt(16)
v_cmp_ne_u64 vcc, s[98:99], v[16:17]
s_nop 4
s_cbranch_vccz L_PK_PC
s_mov_b32 s31, 1048576
L_PL_PC:
s_sleep 2
global_load_dwordx2 v[16:17], v37, s[48:49] sc1
s_waitcnt vmcnt(0)
v_cmp_ne_u64 vcc, s[98:99], v[16:17]
s_sub_u32 s31, s31, 1
s_cbranch_scc1 L_PK_PC
s_nop 4
s_cbranch_vccnz L_PL_PC
L_PK_PC:
s_or_b32 s22, s6, s7
s_cmp_eq_u32 s22, 0
s_cbranch_scc0 L_NOBUMP
s_mov_b32 s31, 1048576
L_BP_CHK:
v_cmp_ne_u64 s[46:47], s[98:99], v[10:11]
v_cmp_ne_u64 s[26:27], s[98:99], v[12:13]
v_cmp_ne_u64 s[22:23], s[98:99], v[14:15]
v_cmp_ne_u64 vcc, s[98:99], v[18:19]
s_nop 4
s_or_b64 s[46:47], s[46:47], s[26:27]
s_or_b64 s[22:23], s[22:23], vcc
s_or_b64 s[46:47], s[46:47], s[22:23]
s_cbranch_scc0 L_BP_OK
s_sub_u32 s31, s31, 1
s_cbranch_scc1 L_BP_OK
s_sleep 2
global_load_dwordx2 v[10:11], v8, s[48:49] offset:0 sc1
global_load_dwordx2 v[12:13], v8, s[48:49] offset:64 sc1
global_load_dwordx2 v[14:15], v8, s[48:49] offset:128 sc1
global_load_dwordx2 v[18:19], v8, s[48:49] offset:192 sc1
s_waitcnt vmcnt(0)
s_branch L_BP_CHK
L_BP_OK:
v_mov_b32 v8, 0
v_mov_b32 v10, s98
v_mov_b32 v11, s99
s_mov_b64 exec, 1
global_store_dwordx2 v8, v[10:11], s[48:49] sc1
s_mov_b64 exec, -1
L_NOBUMP:
s_nop 9
L_EV3:
s_cmp_eq_u32 s34, 16
s_cbranch_scc1 L_FINAL
s_branch L_FULL0
L_BIDLE:
s_nop 15
s_nop 3
s_cmp_eq_u32 s34, 16
s_cbranch_scc1 L_FINAL
s_and_b32 s22, s45, 3
s_mul_i32 s22, s22, 7
s_add_u32 s22, s22, s44
s_lshl_b32 s22, s22, 1
s_add_u32 s22, s22, s75
s_mul_i32 s22, s22, 106
s_and_b32 s23, s6, 3
s_lshr_b32 s24, s6, 2
s_and_b32 s24, s24, 1
s_mul_i32 s25, s23, 458752
s_mul_i32 s26, s24, 229376
s_add_u32 s25, s25, s26
s_add_u32 s26, s76, s25
s_addc_u32 s27, s77, 0
s_lshl_b32 s25, s23, 20
s_lshl_b32 s24, s24, 19
s_add_u32 s25, s25, s24
s_sub_u32 s28, s78, 229376
s_subb_u32 s29, s79, 0
s_add_u32 s28, s28, s25
s_addc_u32 s29, s29, 0
v_min_u32 v42, 15, v1
v_add_u32 v42, s22, v42
v_mov_b32 v43, 5887
v_min_u32 v42, v43, v42
v_cmp_gt_u32 vcc, 1792, v42
v_lshlrev_b32 v42, 7, v42
v_mov_b32 v40, s28
v_mov_b32 v43, s26
v_cndmask_b32 v40, v40, v43
v_mov_b32 v41, s29
v_mov_b32 v43, s27
v_cndmask_b32 v41, v41, v43
v_add_co_u32 v40, vcc, v40, v42
s_nop 1
v_addc_co_u32 v41, vcc, 0, v41, vcc
global_load_dword v23, v[40:41], off
s_sleep 4
v_min_u32 v42, 15, v1
v_add_u32 v42, s22, v42
v_add_u32 v42, 16, v42
v_mov_b32 v43, 5887
v_min_u32 v42, v43, v42
v_cmp_gt_u32 vcc, 1792, v42
v_lshlrev_b32 v42, 7, v42
v_mov_b32 v40, s28
v_mov_b32 v43, s26
v_cndmask_b32 v40, v40, v43
v_mov_b32 v41, s29
v_mov_b32 v43, s27
v_cndmask_b32 v41, v41, v43
v_add_co_u32 v40, vcc, v40, v42
s_nop 1
v_addc_co_u32 v41, vcc, 0, v41, vcc
global_load_dword v23, v[40:41], off
s_sleep 4
v_min_u32 v42, 15, v1
v_add_u32 v42, s22, v42
v_add_u32 v42, 32, v42
v_mov_b32 v43, 5887
v_min_u32 v42, v43, v42
v_cmp_gt_u32 vcc, 1792, v42
v_lshlrev_b32 v42, 7, v42
v_mov_b32 v40, s28
v_mov_b32 v43, s26
v_cndmask_b32 v40, v40, v43
v_mov_b32 v41, s29
v_mov_b32 v43, s27
v_cndmask_b32 v41, v41, v43
v_add_co_u32 v40, vcc, v40, v42
s_nop 1
v_addc_co_u32 v41, vcc, 0, v41, vcc
global_load_dword v23, v[40:41], off
s_sleep 4
v_min_u32 v42, 15, v1
v_add_u32 v42, s22, v42
v_add_u32 v42, 48, v42
v_mov_b32 v43, 5887
v_min_u32 v42, v43, v42
v_cmp_gt_u32 vcc, 1792, v42
v_lshlrev_b32 v42, 7, v42
v_mov_b32 v40, s28
v_mov_b32 v43, s26
v_cndmask_b32 v40, v40, v43
v_mov_b32 v41, s29
v_mov_b32 v43, s27
v_cndmask_b32 v41, v41, v43
v_add_co_u32 v40, vcc, v40, v42
s_nop 1
v_addc_co_u32 v41, vcc, 0, v41, vcc
global_load_dword v23, v[40:41], off
s_sleep 4
v_min_u32 v42, 15, v1
v_add_u32 v42, s22, v42
v_add_u32 v42, 64, v42
v_mov_b32 v43, 5887
v_min_u32 v42, v43, v42
v_cmp_gt_u32 vcc, 1792, v42
v_lshlrev_b32 v42, 7, v42
v_mov_b32 v40, s28
v_mov_b32 v43, s26
v_cndmask_b32 v40, v40, v43
v_mov_b32 v41, s29
v_mov_b32 v43, s27
v_cndmask_b32 v41, v41, v43
v_add_co_u32 v40, vcc, v40, v42
s_nop 1
v_addc_co_u32 v41, vcc, 0, v41, vcc
global_load_dword v23, v[40:41], off
s_sleep 4
v_min_u32 v42, 15, v1
v_add_u32 v42, s22, v42
v_add_u32 v42, 80, v42
v_mov_b32 v43, 5887
v_min_u32 v42, v43, v42
v_cmp_gt_u32 vcc, 1792, v42
v_lshlrev_b32 v42, 7, v42
v_mov_b32 v40, s28
v_mov_b32 v43, s26
v_cndmask_b32 v40, v40, v43
v_mov_b32 v41, s29
v_mov_b32 v43, s27
v_cndmask_b32 v41, v41, v43
v_add_co_u32 v40, vcc, v40, v42
s_nop 1
v_addc_co_u32 v41, vcc, 0, v41, vcc
global_load_dword v23, v[40:41], off
s_sleep 4
v_min_u32 v42, 9, v1
v_add_u32 v42, s22, v42
v_add_u32 v42, 96, v42
v_mov_b32 v43, 5887
v_min_u32 v42, v43, v42
v_cmp_gt_u32 vcc, 1792, v42
v_lshlrev_b32 v42, 7, v42
v_mov_b32 v40, s28
v_mov_b32 v43, s26
v_cndmask_b32 v40, v40, v43
v_mov_b32 v41, s29
v_mov_b32 v43, s27
v_cndmask_b32 v41, v41, v43
v_add_co_u32 v40, vcc, v40, v42
s_nop 1
v_addc_co_u32 v41, vcc, 0, v41, vcc
global_load_dword v23, v[40:41], off
s_nop 3
s_branch L_FULL0
L_EV_BAD:
s_waitcnt vmcnt(0) lgkmcnt(0)
s_nop 15
s_nop 3
s_lshl_b32 s22, s6, 2
s_add_u32 s22, s22, s7
s_lshl_b32 s22, s22, 16
s_add_u32 s22, s22, 134217728
s_add_u32 s46, s48, s22
s_addc_u32 s47, s49, 0
v_add_u32 v21, 0, v2
global_store_dwordx4 v21, v[64:67], s[46:47]
v_add_u32 v21, 1024, v2
global_store_dwordx4 v21, v[68:71], s[46:47]
v_add_u32 v21, 2048, v2
global_store_dwordx4 v21, v[72:75], s[46:47]
v_add_u32 v21, 3072, v2
global_store_dwordx4 v21, v[76:79], s[46:47]
v_add_u32 v21, 4096, v2
global_store_dwordx4 v21, v[80:83], s[46:47]
v_add_u32 v21, 5120, v2
global_store_dwordx4 v21, v[84:87], s[46:47]
v_add_u32 v21, 6144, v2
global_store_dwordx4 v21, v[88:91], s[46:47]
v_add_u32 v21, 7168, v2
global_store_dwordx4 v21, v[92:95], s[46:47]
v_add_u32 v21, 8192, v2
global_store_dwordx4 v21, v[96:99], s[46:47]
v_add_u32 v21, 9216, v2
global_store_dwordx4 v21, v[100:103], s[46:47]
v_add_u32 v21, 10240, v2
global_store_dwordx4 v21, v[104:107], s[46:47]
v_add_u32 v21, 11264, v2
global_store_dwordx4 v21, v[108:111], s[46:47]
v_add_u32 v21, 12288, v2
global_store_dwordx4 v21, v[112:115], s[46:47]
v_add_u32 v21, 13312, v2
global_store_dwordx4 v21, v[116:119], s[46:47]
v_add_u32 v21, 14336, v2
global_store_dwordx4 v21, v[120:123], s[46:47]
v_add_u32 v21, 15360, v2
global_store_dwordx4 v21, v[124:127], s[46:47]
v_add_u32 v21, 16384, v2
global_store_dwordx4 v21, v[128:131], s[46:47]
v_add_u32 v21, 17408, v2
global_store_dwordx4 v21, v[132:135], s[46:47]
v_add_u32 v21, 18432, v2
global_store_dwordx4 v21, v[136:139], s[46:47]
v_add_u32 v21, 19456, v2
global_store_dwordx4 v21, v[140:143], s[46:47]
v_add_u32 v21, 20480, v2
global_store_dwordx4 v21, v[144:147], s[46:47]
v_add_u32 v21, 21504, v2
global_store_dwordx4 v21, v[148:151], s[46:47]
v_add_u32 v21, 22528, v2
global_store_dwordx4 v21, v[152:155], s[46:47]
v_add_u32 v21, 23552, v2
global_store_dwordx4 v21, v[156:159], s[46:47]
v_add_u32 v21, 24576, v2
global_store_dwordx4 v21, v[160:163], s[46:47]
v_add_u32 v21, 25600, v2
global_store_dwordx4 v21, v[164:167], s[46:47]
v_add_u32 v21, 26624, v2
global_store_dwordx4 v21, v[168:171], s[46:47]
v_add_u32 v21, 27648, v2
global_store_dwordx4 v21, v[172:175], s[46:47]
v_add_u32 v21, 28672, v2
global_store_dwordx4 v21, v[176:179], s[46:47]
v_add_u32 v21, 29696, v2
global_store_dwordx4 v21, v[180:183], s[46:47]
v_add_u32 v21, 30720, v2
global_store_dwordx4 v21, v[184:187], s[46:47]
v_add_u32 v21, 31744, v2
global_store_dwordx4 v21, v[188:191], s[46:47]
v_add_u32 v21, 32768, v2
global_store_dwordx4 v21, v[192:195], s[46:47]
v_add_u32 v21, 33792, v2
global_store_dwordx4 v21, v[196:199], s[46:47]
v_add_u32 v21, 34816, v2
global_store_dwordx4 v21, v[200:203], s[46:47]
v_add_u32 v21, 35840, v2
global_store_dwordx4 v21, v[204:207], s[46:47]
s_waitcnt vmcnt(0)
s_lshl_b32 s22, s75, 14
s_add_u32 s22, s22, 131072
v_add_u32 v20, s22, v2
v_mov_b32 v64, v244
v_mov_b32 v65, v245
v_mov_b32 v66, v246
v_mov_b32 v67, v247
ds_read_b128 v[68:71], v20 offset:0
ds_read_b128 v[72:75], v20 offset:1024
ds_read_b128 v[76:79], v20 offset:2048
ds_read_b128 v[80:83], v20 offset:3072
ds_read_b128 v[84:87], v20 offset:4096
ds_read_b128 v[88:91], v20 offset:5120
ds_read_b128 v[92:95], v20 offset:6144
ds_read_b128 v[96:99], v20 offset:7168
ds_read_b128 v[100:103], v20 offset:8192
ds_read_b128 v[104:107], v20 offset:9216
ds_read_b128 v[108:111], v20 offset:10240
ds_read_b128 v[112:115], v20 offset:11264
ds_read_b128 v[116:119], v20 offset:12288
ds_read_b128 v[120:123], v20 offset:13312
ds_read_b128 v[124:127], v20 offset:14336
s_lshr_b32 s22, s50, 3
s_lshl_b32 s22, s22, 15
s_lshl_b32 s23, s75, 12
s_add_u32 s37, s22, s23
s_mov_b32 s61, 0
s_branch L_SAFE
L_SAFE_RET0:
s_lshl_b32 s22, s6, 2
s_add_u32 s22, s22, s7
s_lshl_b32 s22, s22, 16
s_add_u32 s22, s22, 134217728
s_add_u32 s46, s48, s22
s_addc_u32 s47, s49, 0
v_add_u32 v21, 0, v2
global_load_dwordx4 v[64:67], v21, s[46:47]
v_add_u32 v21, 1024, v2
s_waitcnt lgkmcnt(14)
global_load_dwordx4 v[68:71], v21, s[46:47]
v_add_u32 v21, 2048, v2
s_waitcnt lgkmcnt(13)
global_load_dwordx4 v[72:75], v21, s[46:47]
v_add_u32 v21, 3072, v2
s_waitcnt lgkmcnt(12)
global_load_dwordx4 v[76:79], v21, s[46:47]
v_add_u32 v21, 4096, v2
s_waitcnt lgkmcnt(11)
global_load_dwordx4 v[80:83], v21, s[46:47]
v_add_u32 v21, 5120, v2
s_waitcnt lgkmcnt(10)
global_load_dwordx4 v[84:87], v21, s[46:47]
v_add_u32 v21, 6144, v2
s_waitcnt lgkmcnt(9)
global_load_dwordx4 v[88:91], v21, s[46:47]
v_add_u32 v21, 7168, v2
s_waitcnt lgkmcnt(8)
global_load_dwordx4 v[92:95], v21, s[46:47]
v_add_u32 v21, 8192, v2
s_waitcnt lgkmcnt(7)
global_load_dwordx4 v[96:99], v21, s[46:47]
v_add_u32 v21, 9216, v2
s_waitcnt lgkmcnt(6)
global_load_dwordx4 v[100:103], v21, s[46:47]
v_add_u32 v21, 10240, v2
s_waitcnt lgkmcnt(5)
global_load_dwordx4 v[104:107], v21, s[46:47]
v_add_u32 v21, 11264, v2
s_waitcnt lgkmcnt(4)
global_load_dwordx4 v[108:111], v21, s[46:47]
v_add_u32 v21, 12288, v2
s_waitcnt lgkmcnt(3)
global_load_dwordx4 v[112:115], v21, s[46:47]
v_add_u32 v21, 13312, v2
s_waitcnt lgkmcnt(2)
global_load_dwordx4 v[116:119], v21, s[46:47]
v_add_u32 v21, 14336, v2
s_waitcnt lgkmcnt(1)
global_load_dwordx4 v[120:123], v21, s[46:47]
v_add_u32 v21, 15360, v2
s_waitcnt lgkmcnt(0)
global_load_dwordx4 v[124:127], v21, s[46:47]
v_add_u32 v21, 16384, v2
global_load_dwordx4 v[128:131], v21, s[46:47]
v_add_u32 v21, 17408, v2
global_load_dwordx4 v[132:135], v21, s[46:47]
v_add_u32 v21, 18432, v2
global_load_dwordx4 v[136:139], v21, s[46:47]
v_add_u32 v21, 19456, v2
global_load_dwordx4 v[140:143], v21, s[46:47]
v_add_u32 v21, 20480, v2
global_load_dwordx4 v[144:147], v21, s[46:47]
v_add_u32 v21, 21504, v2
global_load_dwordx4 v[148:151], v21, s[46:47]
v_add_u32 v21, 22528, v2
global_load_dwordx4 v[152:155], v21, s[46:47]
v_add_u32 v21, 23552, v2
global_load_dwordx4 v[156:159], v21, s[46:47]
v_add_u32 v21, 24576, v2
global_load_dwordx4 v[160:163], v21, s[46:47]
v_add_u32 v21, 25600, v2
global_load_dwordx4 v[164:167], v21, s[46:47]
v_add_u32 v21, 26624, v2
global_load_dwordx4 v[168:171], v21, s[46:47]
v_add_u32 v21, 27648, v2
global_load_dwordx4 v[172:175], v21, s[46:47]
v_add_u32 v21, 28672, v2
global_load_dwordx4 v[176:179], v21, s[46:47]
v_add_u32 v21, 29696, v2
global_load_dwordx4 v[180:183], v21, s[46:47]
v_add_u32 v21, 30720, v2
global_load_dwordx4 v[184:187], v21, s[46:47]
v_add_u32 v21, 31744, v2
global_load_dwordx4 v[188:191], v21, s[46:47]
v_add_u32 v21, 32768, v2
global_load_dwordx4 v[192:195], v21, s[46:47]
v_add_u32 v21, 33792, v2
global_load_dwordx4 v[196:199], v21, s[46:47]
v_add_u32 v21, 34816, v2
global_load_dwordx4 v[200:203], v21, s[46:47]
v_add_u32 v21, 35840, v2
global_load_dwordx4 v[204:207], v21, s[46:47]
v_accvgpr_write_b32 a0, 0
v_accvgpr_write_b32 a1, 0
v_accvgpr_write_b32 a2, 0
v_accvgpr_write_b32 a3, 0
v_accvgpr_write_b32 a4, 0
v_accvgpr_write_b32 a5, 0
v_accvgpr_write_b32 a6, 0
v_accvgpr_write_b32 a7, 0
v_accvgpr_write_b32 a8, 0
v_accvgpr_write_b32 a9, 0
v_accvgpr_write_b32 a10, 0
v_accvgpr_write_b32 a11, 0
v_accvgpr_write_b32 a12, 0
v_accvgpr_write_b32 a13, 0
v_accvgpr_write_b32 a14, 0
v_accvgpr_write_b32 a15, 0
v_accvgpr_write_b32 a16, 0
v_accvgpr_write_b32 a17, 0
v_accvgpr_write_b32 a18, 0
v_accvgpr_write_b32 a19, 0
v_accvgpr_write_b32 a20, 0
v_accvgpr_write_b32 a21, 0
v_accvgpr_write_b32 a22, 0
v_accvgpr_write_b32 a23, 0
v_accvgpr_write_b32 a24, 0
v_accvgpr_write_b32 a25, 0
v_accvgpr_write_b32 a26, 0
v_accvgpr_write_b32 a27, 0
v_accvgpr_write_b32 a28, 0
v_accvgpr_write_b32 a29, 0
v_accvgpr_write_b32 a30, 0
v_accvgpr_write_b32 a31, 0
v_accvgpr_write_b32 a32, 0
v_accvgpr_write_b32 a33, 0
v_accvgpr_write_b32 a34, 0
v_accvgpr_write_b32 a35, 0
v_accvgpr_write_b32 a36, 0
v_accvgpr_write_b32 a37, 0
v_accvgpr_write_b32 a38, 0
v_accvgpr_write_b32 a39, 0
v_accvgpr_write_b32 a40, 0
v_accvgpr_write_b32 a41, 0
v_accvgpr_write_b32 a42, 0
v_accvgpr_write_b32 a43, 0
v_accvgpr_write_b32 a44, 0
v_accvgpr_write_b32 a45, 0
v_accvgpr_write_b32 a46, 0
v_accvgpr_write_b32 a47, 0
v_accvgpr_write_b32 a48, 0
v_accvgpr_write_b32 a49, 0
v_accvgpr_write_b32 a50, 0
v_accvgpr_write_b32 a51, 0
v_accvgpr_write_b32 a52, 0
v_accvgpr_write_b32 a53, 0
v_accvgpr_write_b32 a54, 0
v_accvgpr_write_b32 a55, 0
v_accvgpr_write_b32 a56, 0
v_accvgpr_write_b32 a57, 0
v_accvgpr_write_b32 a58, 0
v_accvgpr_write_b32 a59, 0
v_accvgpr_write_b32 a60, 0
v_accvgpr_write_b32 a61, 0
v_accvgpr_write_b32 a62, 0
v_accvgpr_write_b32 a63, 0
v_accvgpr_write_b32 a64, 0
v_accvgpr_write_b32 a65, 0
v_accvgpr_write_b32 a66, 0
v_accvgpr_write_b32 a67, 0
v_accvgpr_write_b32 a68, 0
v_accvgpr_write_b32 a69, 0
v_accvgpr_write_b32 a70, 0
v_accvgpr_write_b32 a71, 0
v_accvgpr_write_b32 a72, 0
v_accvgpr_write_b32 a73, 0
v_accvgpr_write_b32 a74, 0
v_accvgpr_write_b32 a75, 0
v_accvgpr_write_b32 a76, 0
v_accvgpr_write_b32 a77, 0
v_accvgpr_write_b32 a78, 0
v_accvgpr_write_b32 a79, 0
v_accvgpr_write_b32 a80, 0
v_accvgpr_write_b32 a81, 0
v_accvgpr_write_b32 a82, 0
v_accvgpr_write_b32 a83, 0
v_accvgpr_write_b32 a84, 0
v_accvgpr_write_b32 a85, 0
v_accvgpr_write_b32 a86, 0
v_accvgpr_write_b32 a87, 0
v_accvgpr_write_b32 a88, 0
v_accvgpr_write_b32 a89, 0
v_accvgpr_write_b32 a90, 0
v_accvgpr_write_b32 a91, 0
v_accvgpr_write_b32 a92, 0
v_accvgpr_write_b32 a93, 0
v_accvgpr_write_b32 a94, 0
v_accvgpr_write_b32 a95, 0
v_accvgpr_write_b32 a96, 0
v_accvgpr_write_b32 a97, 0
v_accvgpr_write_b32 a98, 0
v_accvgpr_write_b32 a99, 0
v_accvgpr_write_b32 a100, 0
v_accvgpr_write_b32 a101, 0
v_accvgpr_write_b32 a102, 0
v_accvgpr_write_b32 a103, 0
v_accvgpr_write_b32 a104, 0
v_accvgpr_write_b32 a105, 0
v_accvgpr_write_b32 a106, 0
v_accvgpr_write_b32 a107, 0
v_accvgpr_write_b32 a108, 0
v_accvgpr_write_b32 a109, 0
v_accvgpr_write_b32 a110, 0
v_accvgpr_write_b32 a111, 0
v_accvgpr_write_b32 a112, 0
v_accvgpr_write_b32 a113, 0
v_accvgpr_write_b32 a114, 0
v_accvgpr_write_b32 a115, 0
v_accvgpr_write_b32 a116, 0
v_accvgpr_write_b32 a117, 0
v_accvgpr_write_b32 a118, 0
v_accvgpr_write_b32 a119, 0
v_accvgpr_write_b32 a120, 0
v_accvgpr_write_b32 a121, 0
v_accvgpr_write_b32 a122, 0
v_accvgpr_write_b32 a123, 0
v_accvgpr_write_b32 a124, 0
v_accvgpr_write_b32 a125, 0
v_accvgpr_write_b32 a126, 0
v_accvgpr_write_b32 a127, 0
ds_read_b128 a[128:131], v3 offset:0
ds_read_b128 a[132:135], v3 offset:1024
ds_read_b128 a[136:139], v3 offset:2048
ds_read_b128 a[140:143], v3 offset:3072
ds_read_b128 a[144:147], v3 offset:4096
ds_read_b128 a[148:151], v3 offset:5120
ds_read_b128 a[152:155], v3 offset:6144
ds_read_b128 a[156:159], v3 offset:7168
s_waitcnt vmcnt(0) lgkmcnt(0)
s_nop 15
s_nop 3
s_branch L_EV_DONE
L_SAFE:
s_waitcnt vmcnt(0) lgkmcnt(0)
s_nop 15
s_nop 3
v_mov_b32 v232, v7
v_mov_b32 v236, 0
v_mov_b32 v233, v7
v_mov_b32 v237, 0
v_mov_b32 v234, v7
v_mov_b32 v238, 0
v_mov_b32 v235, v7
v_mov_b32 v239, 0
v_accvgpr_write_b32 a0, 0
v_accvgpr_write_b32 a1, 0
v_accvgpr_write_b32 a2, 0
v_accvgpr_write_b32 a3, 0
v_accvgpr_write_b32 a4, 0
v_accvgpr_write_b32 a5, 0
v_accvgpr_write_b32 a6, 0
v_accvgpr_write_b32 a7, 0
v_accvgpr_write_b32 a8, 0
v_accvgpr_write_b32 a9, 0
v_accvgpr_write_b32 a10, 0
v_accvgpr_write_b32 a11, 0
v_accvgpr_write_b32 a12, 0
v_accvgpr_write_b32 a13, 0
v_accvgpr_write_b32 a14, 0
v_accvgpr_write_b32 a15, 0
v_accvgpr_write_b32 a16, 0
v_accvgpr_write_b32 a17, 0
v_accvgpr_write_b32 a18, 0
v_accvgpr_write_b32 a19, 0
v_accvgpr_write_b32 a20, 0
v_accvgpr_write_b32 a21, 0
v_accvgpr_write_b32 a22, 0
v_accvgpr_write_b32 a23, 0
v_accvgpr_write_b32 a24, 0
v_accvgpr_write_b32 a25, 0
v_accvgpr_write_b32 a26, 0
v_accvgpr_write_b32 a27, 0
v_accvgpr_write_b32 a28, 0
v_accvgpr_write_b32 a29, 0
v_accvgpr_write_b32 a30, 0
v_accvgpr_write_b32 a31, 0
v_accvgpr_write_b32 a32, 0
v_accvgpr_write_b32 a33, 0
v_accvgpr_write_b32 a34, 0
v_accvgpr_write_b32 a35, 0
v_accvgpr_write_b32 a36, 0
v_accvgpr_write_b32 a37, 0
v_accvgpr_write_b32 a38, 0
v_accvgpr_write_b32 a39, 0
v_accvgpr_write_b32 a40, 0
v_accvgpr_write_b32 a41, 0
v_accvgpr_write_b32 a42, 0
v_accvgpr_write_b32 a43, 0
v_accvgpr_write_b32 a44, 0
v_accvgpr_write_b32 a45, 0
)ASM"
R"ASM(v_accvgpr_write_b32 a46, 0
v_accvgpr_write_b32 a47, 0
v_accvgpr_write_b32 a48, 0
v_accvgpr_write_b32 a49, 0
v_accvgpr_write_b32 a50, 0
v_accvgpr_write_b32 a51, 0
v_accvgpr_write_b32 a52, 0
v_accvgpr_write_b32 a53, 0
v_accvgpr_write_b32 a54, 0
v_accvgpr_write_b32 a55, 0
v_accvgpr_write_b32 a56, 0
v_accvgpr_write_b32 a57, 0
v_accvgpr_write_b32 a58, 0
v_accvgpr_write_b32 a59, 0
v_accvgpr_write_b32 a60, 0
v_accvgpr_write_b32 a61, 0
v_accvgpr_write_b32 a62, 0
v_accvgpr_write_b32 a63, 0
v_accvgpr_write_b32 a64, 0
v_accvgpr_write_b32 a65, 0
v_accvgpr_write_b32 a66, 0
v_accvgpr_write_b32 a67, 0
v_accvgpr_write_b32 a68, 0
v_accvgpr_write_b32 a69, 0
v_accvgpr_write_b32 a70, 0
v_accvgpr_write_b32 a71, 0
v_accvgpr_write_b32 a72, 0
v_accvgpr_write_b32 a73, 0
v_accvgpr_write_b32 a74, 0
v_accvgpr_write_b32 a75, 0
v_accvgpr_write_b32 a76, 0
v_accvgpr_write_b32 a77, 0
v_accvgpr_write_b32 a78, 0
v_accvgpr_write_b32 a79, 0
v_accvgpr_write_b32 a80, 0
v_accvgpr_write_b32 a81, 0
v_accvgpr_write_b32 a82, 0
v_accvgpr_write_b32 a83, 0
v_accvgpr_write_b32 a84, 0
v_accvgpr_write_b32 a85, 0
v_accvgpr_write_b32 a86, 0
v_accvgpr_write_b32 a87, 0
v_accvgpr_write_b32 a88, 0
v_accvgpr_write_b32 a89, 0
v_accvgpr_write_b32 a90, 0
v_accvgpr_write_b32 a91, 0
v_accvgpr_write_b32 a92, 0
v_accvgpr_write_b32 a93, 0
v_accvgpr_write_b32 a94, 0
v_accvgpr_write_b32 a95, 0
v_accvgpr_write_b32 a96, 0
v_accvgpr_write_b32 a97, 0
v_accvgpr_write_b32 a98, 0
v_accvgpr_write_b32 a99, 0
v_accvgpr_write_b32 a100, 0
v_accvgpr_write_b32 a101, 0
v_accvgpr_write_b32 a102, 0
v_accvgpr_write_b32 a103, 0
v_accvgpr_write_b32 a104, 0
v_accvgpr_write_b32 a105, 0
v_accvgpr_write_b32 a106, 0
v_accvgpr_write_b32 a107, 0
v_accvgpr_write_b32 a108, 0
v_accvgpr_write_b32 a109, 0
v_accvgpr_write_b32 a110, 0
v_accvgpr_write_b32 a111, 0
v_accvgpr_write_b32 a112, 0
v_accvgpr_write_b32 a113, 0
v_accvgpr_write_b32 a114, 0
v_accvgpr_write_b32 a115, 0
v_accvgpr_write_b32 a116, 0
v_accvgpr_write_b32 a117, 0
v_accvgpr_write_b32 a118, 0
v_accvgpr_write_b32 a119, 0
v_accvgpr_write_b32 a120, 0
v_accvgpr_write_b32 a121, 0
v_accvgpr_write_b32 a122, 0
v_accvgpr_write_b32 a123, 0
v_accvgpr_write_b32 a124, 0
v_accvgpr_write_b32 a125, 0
v_accvgpr_write_b32 a126, 0
v_accvgpr_write_b32 a127, 0
s_lshl_b32 s22, s75, 12
s_mov_b32 s24, s22
L_SAFE_LOOP:
s_waitcnt vmcnt(0) lgkmcnt(0)
s_nop 15
s_nop 3
s_sub_u32 s22, s24, s37
s_lshr_b32 s22, s22, 8
s_lshl_b32 s23, s75, 4
s_sub_u32 s22, s23, s22
s_cmp_lt_u32 s24, s37
s_cselect_b32 s22, 1024, s22
v_subrev_u32 v255, s22, v6
s_lshl_b32 s22, s75, 12
s_sub_u32 s22, s24, s22
s_add_u32 s23, s22, 0
buffer_load_dwordx4 a[128:131], v2, s[12:15], s23 offen offset:0 sc1
buffer_load_dwordx4 a[132:135], v2, s[12:15], s23 offen offset:1024 sc1
buffer_load_dwordx4 a[136:139], v2, s[12:15], s23 offen offset:2048 sc1
buffer_load_dwordx4 a[140:143], v2, s[12:15], s23 offen offset:3072 sc1
s_add_u32 s23, s22, 4096
buffer_load_dwordx4 a[144:147], v2, s[12:15], s23 offen offset:0 sc1
buffer_load_dwordx4 a[148:151], v2, s[12:15], s23 offen offset:1024 sc1
buffer_load_dwordx4 a[152:155], v2, s[12:15], s23 offen offset:2048 sc1
buffer_load_dwordx4 a[156:159], v2, s[12:15], s23 offen offset:3072 sc1
s_add_u32 s23, s22, 8192
buffer_load_dwordx4 a[160:163], v2, s[12:15], s23 offen offset:0 sc1
buffer_load_dwordx4 a[164:167], v2, s[12:15], s23 offen offset:1024 sc1
buffer_load_dwordx4 a[168:171], v2, s[12:15], s23 offen offset:2048 sc1
buffer_load_dwordx4 a[172:175], v2, s[12:15], s23 offen offset:3072 sc1
s_add_u32 s23, s22, 12288
buffer_load_dwordx4 a[176:179], v2, s[12:15], s23 offen offset:0 sc1
buffer_load_dwordx4 a[180:183], v2, s[12:15], s23 offen offset:1024 sc1
buffer_load_dwordx4 a[184:187], v2, s[12:15], s23 offen offset:2048 sc1
buffer_load_dwordx4 a[188:191], v2, s[12:15], s23 offen offset:3072 sc1
s_waitcnt vmcnt(0)
v_mfma_f32_16x16x32_bf16 v[128:131], a[128:131], v[64:67], 0
v_mfma_f32_16x16x32_bf16 v[132:135], a[128:131], v[80:83], 0
v_mfma_f32_16x16x32_bf16 v[136:139], a[128:131], v[96:99], 0
v_mfma_f32_16x16x32_bf16 v[140:143], a[128:131], v[112:115], 0
v_mfma_f32_16x16x32_bf16 v[128:131], a[132:135], v[68:71], v[128:131]
v_mfma_f32_16x16x32_bf16 v[132:135], a[132:135], v[84:87], v[132:135]
v_mfma_f32_16x16x32_bf16 v[136:139], a[132:135], v[100:103], v[136:139]
v_mfma_f32_16x16x32_bf16 v[140:143], a[132:135], v[116:119], v[140:143]
v_mfma_f32_16x16x32_bf16 v[128:131], a[136:139], v[72:75], v[128:131]
v_mfma_f32_16x16x32_bf16 v[132:135], a[136:139], v[88:91], v[132:135]
v_mfma_f32_16x16x32_bf16 v[136:139], a[136:139], v[104:107], v[136:139]
v_mfma_f32_16x16x32_bf16 v[140:143], a[136:139], v[120:123], v[140:143]
v_mfma_f32_16x16x32_bf16 v[128:131], a[140:143], v[76:79], v[128:131]
v_mfma_f32_16x16x32_bf16 v[132:135], a[140:143], v[92:95], v[132:135]
v_mfma_f32_16x16x32_bf16 v[136:139], a[140:143], v[108:111], v[136:139]
v_mfma_f32_16x16x32_bf16 v[140:143], a[140:143], v[124:127], v[140:143]
v_mfma_f32_16x16x32_bf16 v[144:147], a[144:147], v[64:67], 0
v_mfma_f32_16x16x32_bf16 v[148:151], a[144:147], v[80:83], 0
v_mfma_f32_16x16x32_bf16 v[152:155], a[144:147], v[96:99], 0
v_mfma_f32_16x16x32_bf16 v[156:159], a[144:147], v[112:115], 0
v_mfma_f32_16x16x32_bf16 v[144:147], a[148:151], v[68:71], v[144:147]
v_mfma_f32_16x16x32_bf16 v[148:151], a[148:151], v[84:87], v[148:151]
v_mfma_f32_16x16x32_bf16 v[152:155], a[148:151], v[100:103], v[152:155]
v_mfma_f32_16x16x32_bf16 v[156:159], a[148:151], v[116:119], v[156:159]
v_mfma_f32_16x16x32_bf16 v[144:147], a[152:155], v[72:75], v[144:147]
v_mfma_f32_16x16x32_bf16 v[148:151], a[152:155], v[88:91], v[148:151]
v_mfma_f32_16x16x32_bf16 v[152:155], a[152:155], v[104:107], v[152:155]
v_mfma_f32_16x16x32_bf16 v[156:159], a[152:155], v[120:123], v[156:159]
v_mfma_f32_16x16x32_bf16 v[144:147], a[156:159], v[76:79], v[144:147]
v_mfma_f32_16x16x32_bf16 v[148:151], a[156:159], v[92:95], v[148:151]
v_mfma_f32_16x16x32_bf16 v[152:155], a[156:159], v[108:111], v[152:155]
v_mfma_f32_16x16x32_bf16 v[156:159], a[156:159], v[124:127], v[156:159]
v_mfma_f32_16x16x32_bf16 v[160:163], a[160:163], v[64:67], 0
v_mfma_f32_16x16x32_bf16 v[164:167], a[160:163], v[80:83], 0
v_mfma_f32_16x16x32_bf16 v[168:171], a[160:163], v[96:99], 0
v_mfma_f32_16x16x32_bf16 v[172:175], a[160:163], v[112:115], 0
v_mfma_f32_16x16x32_bf16 v[160:163], a[164:167], v[68:71], v[160:163]
v_mfma_f32_16x16x32_bf16 v[164:167], a[164:167], v[84:87], v[164:167]
v_mfma_f32_16x16x32_bf16 v[168:171], a[164:167], v[100:103], v[168:171]
v_mfma_f32_16x16x32_bf16 v[172:175], a[164:167], v[116:119], v[172:175]
v_mfma_f32_16x16x32_bf16 v[160:163], a[168:171], v[72:75], v[160:163]
v_mfma_f32_16x16x32_bf16 v[164:167], a[168:171], v[88:91], v[164:167]
v_mfma_f32_16x16x32_bf16 v[168:171], a[168:171], v[104:107], v[168:171]
v_mfma_f32_16x16x32_bf16 v[172:175], a[168:171], v[120:123], v[172:175]
v_mfma_f32_16x16x32_bf16 v[160:163], a[172:175], v[76:79], v[160:163]
v_mfma_f32_16x16x32_bf16 v[164:167], a[172:175], v[92:95], v[164:167]
v_mfma_f32_16x16x32_bf16 v[168:171], a[172:175], v[108:111], v[168:171]
v_mfma_f32_16x16x32_bf16 v[172:175], a[172:175], v[124:127], v[172:175]
v_mfma_f32_16x16x32_bf16 v[176:179], a[176:179], v[64:67], 0
v_mfma_f32_16x16x32_bf16 v[180:183], a[176:179], v[80:83], 0
v_mfma_f32_16x16x32_bf16 v[184:187], a[176:179], v[96:99], 0
v_mfma_f32_16x16x32_bf16 v[188:191], a[176:179], v[112:115], 0
v_mfma_f32_16x16x32_bf16 v[176:179], a[180:183], v[68:71], v[176:179]
v_mfma_f32_16x16x32_bf16 v[180:183], a[180:183], v[84:87], v[180:183]
v_mfma_f32_16x16x32_bf16 v[184:187], a[180:183], v[100:103], v[184:187]
v_mfma_f32_16x16x32_bf16 v[188:191], a[180:183], v[116:119], v[188:191]
v_mfma_f32_16x16x32_bf16 v[176:179], a[184:187], v[72:75], v[176:179]
v_mfma_f32_16x16x32_bf16 v[180:183], a[184:187], v[88:91], v[180:183]
v_mfma_f32_16x16x32_bf16 v[184:187], a[184:187], v[104:107], v[184:187]
v_mfma_f32_16x16x32_bf16 v[188:191], a[184:187], v[120:123], v[188:191]
v_mfma_f32_16x16x32_bf16 v[176:179], a[188:191], v[76:79], v[176:179]
v_mfma_f32_16x16x32_bf16 v[180:183], a[188:191], v[92:95], v[180:183]
v_mfma_f32_16x16x32_bf16 v[184:187], a[188:191], v[108:111], v[184:187]
v_mfma_f32_16x16x32_bf16 v[188:191], a[188:191], v[124:127], v[188:191]
s_nop 9
s_add_u32 s23, s22, 0
buffer_load_dwordx4 a[128:131], v2, s[16:19], s23 offen offset:0 sc1
buffer_load_dwordx4 a[132:135], v2, s[16:19], s23 offen offset:1024 sc1
buffer_load_dwordx4 a[136:139], v2, s[16:19], s23 offen offset:2048 sc1
buffer_load_dwordx4 a[140:143], v2, s[16:19], s23 offen offset:3072 sc1
s_add_u32 s23, s22, 4096
buffer_load_dwordx4 a[144:147], v2, s[16:19], s23 offen offset:0 sc1
buffer_load_dwordx4 a[148:151], v2, s[16:19], s23 offen offset:1024 sc1
buffer_load_dwordx4 a[152:155], v2, s[16:19], s23 offen offset:2048 sc1
buffer_load_dwordx4 a[156:159], v2, s[16:19], s23 offen offset:3072 sc1
s_add_u32 s23, s22, 8192
buffer_load_dwordx4 a[160:163], v2, s[16:19], s23 offen offset:0 sc1
buffer_load_dwordx4 a[164:167], v2, s[16:19], s23 offen offset:1024 sc1
buffer_load_dwordx4 a[168:171], v2, s[16:19], s23 offen offset:2048 sc1
buffer_load_dwordx4 a[172:175], v2, s[16:19], s23 offen offset:3072 sc1
s_add_u32 s23, s22, 12288
buffer_load_dwordx4 a[176:179], v2, s[16:19], s23 offen offset:0 sc1
buffer_load_dwordx4 a[180:183], v2, s[16:19], s23 offen offset:1024 sc1
buffer_load_dwordx4 a[184:187], v2, s[16:19], s23 offen offset:2048 sc1
buffer_load_dwordx4 a[188:191], v2, s[16:19], s23 offen offset:3072 sc1
v_mov_b32 v254, 0
v_cmp_ge_i32 vcc, v254, v255
s_nop 1
v_cndmask_b32 v128, v7, v128, vcc
v_mov_b32 v254, -1
v_cmp_ge_i32 vcc, v254, v255
s_nop 1
v_cndmask_b32 v129, v7, v129, vcc
v_mov_b32 v254, -2
v_cmp_ge_i32 vcc, v254, v255
s_nop 1
v_cndmask_b32 v130, v7, v130, vcc
v_mov_b32 v254, -3
v_cmp_ge_i32 vcc, v254, v255
s_nop 1
v_cndmask_b32 v131, v7, v131, vcc
v_mov_b32 v254, 32
v_cmp_ge_i32 vcc, v254, v255
s_nop 1
v_cndmask_b32 v132, v7, v132, vcc
v_mov_b32 v254, 31
v_cmp_ge_i32 vcc, v254, v255
s_nop 1
v_cndmask_b32 v133, v7, v133, vcc
v_mov_b32 v254, 30
v_cmp_ge_i32 vcc, v254, v255
s_nop 1
v_cndmask_b32 v134, v7, v134, vcc
v_mov_b32 v254, 29
v_cmp_ge_i32 vcc, v254, v255
s_nop 1
v_cndmask_b32 v135, v7, v135, vcc
v_mov_b32 v254, 64
v_cmp_ge_i32 vcc, v254, v255
s_nop 1
v_cndmask_b32 v136, v7, v136, vcc
v_mov_b32 v254, 63
v_cmp_ge_i32 vcc, v254, v255
s_nop 1
v_cndmask_b32 v137, v7, v137, vcc
v_mov_b32 v254, 62
v_cmp_ge_i32 vcc, v254, v255
s_nop 1
v_cndmask_b32 v138, v7, v138, vcc
v_mov_b32 v254, 61
v_cmp_ge_i32 vcc, v254, v255
s_nop 1
v_cndmask_b32 v139, v7, v139, vcc
v_mov_b32 v254, 96
v_cmp_ge_i32 vcc, v254, v255
s_nop 1
v_cndmask_b32 v140, v7, v140, vcc
v_mov_b32 v254, 95
v_cmp_ge_i32 vcc, v254, v255
s_nop 1
v_cndmask_b32 v141, v7, v141, vcc
v_mov_b32 v254, 94
v_cmp_ge_i32 vcc, v254, v255
s_nop 1
v_cndmask_b32 v142, v7, v142, vcc
v_mov_b32 v254, 93
v_cmp_ge_i32 vcc, v254, v255
s_nop 1
v_cndmask_b32 v143, v7, v143, vcc
v_mov_b32 v254, -16
v_cmp_ge_i32 vcc, v254, v255
s_nop 1
v_cndmask_b32 v144, v7, v144, vcc
v_mov_b32 v254, -17
v_cmp_ge_i32 vcc, v254, v255
s_nop 1
v_cndmask_b32 v145, v7, v145, vcc
v_mov_b32 v254, -18
v_cmp_ge_i32 vcc, v254, v255
s_nop 1
v_cndmask_b32 v146, v7, v146, vcc
v_mov_b32 v254, -19
v_cmp_ge_i32 vcc, v254, v255
s_nop 1
v_cndmask_b32 v147, v7, v147, vcc
v_mov_b32 v254, 16
v_cmp_ge_i32 vcc, v254, v255
s_nop 1
v_cndmask_b32 v148, v7, v148, vcc
v_mov_b32 v254, 15
v_cmp_ge_i32 vcc, v254, v255
s_nop 1
v_cndmask_b32 v149, v7, v149, vcc
v_mov_b32 v254, 14
v_cmp_ge_i32 vcc, v254, v255
s_nop 1
v_cndmask_b32 v150, v7, v150, vcc
v_mov_b32 v254, 13
v_cmp_ge_i32 vcc, v254, v255
s_nop 1
v_cndmask_b32 v151, v7, v151, vcc
v_mov_b32 v254, 48
v_cmp_ge_i32 vcc, v254, v255
s_nop 1
v_cndmask_b32 v152, v7, v152, vcc
v_mov_b32 v254, 47
v_cmp_ge_i32 vcc, v254, v255
s_nop 1
v_cndmask_b32 v153, v7, v153, vcc
v_mov_b32 v254, 46
v_cmp_ge_i32 vcc, v254, v255
s_nop 1
v_cndmask_b32 v154, v7, v154, vcc
v_mov_b32 v254, 45
v_cmp_ge_i32 vcc, v254, v255
s_nop 1
v_cndmask_b32 v155, v7, v155, vcc
v_mov_b32 v254, 80
v_cmp_ge_i32 vcc, v254, v255
s_nop 1
v_cndmask_b32 v156, v7, v156, vcc
v_mov_b32 v254, 79
v_cmp_ge_i32 vcc, v254, v255
s_nop 1
v_cndmask_b32 v157, v7, v157, vcc
v_mov_b32 v254, 78
v_cmp_ge_i32 vcc, v254, v255
s_nop 1
v_cndmask_b32 v158, v7, v158, vcc
v_mov_b32 v254, 77
v_cmp_ge_i32 vcc, v254, v255
s_nop 1
v_cndmask_b32 v159, v7, v159, vcc
v_mov_b32 v254, -32
v_cmp_ge_i32 vcc, v254, v255
s_nop 1
v_cndmask_b32 v160, v7, v160, vcc
v_mov_b32 v254, -33
v_cmp_ge_i32 vcc, v254, v255
s_nop 1
v_cndmask_b32 v161, v7, v161, vcc
v_mov_b32 v254, -34
v_cmp_ge_i32 vcc, v254, v255
s_nop 1
v_cndmask_b32 v162, v7, v162, vcc
v_mov_b32 v254, -35
v_cmp_ge_i32 vcc, v254, v255
s_nop 1
v_cndmask_b32 v163, v7, v163, vcc
v_mov_b32 v254, 0
v_cmp_ge_i32 vcc, v254, v255
s_nop 1
v_cndmask_b32 v164, v7, v164, vcc
v_mov_b32 v254, -1
v_cmp_ge_i32 vcc, v254, v255
s_nop 1
v_cndmask_b32 v165, v7, v165, vcc
v_mov_b32 v254, -2
v_cmp_ge_i32 vcc, v254, v255
s_nop 1
v_cndmask_b32 v166, v7, v166, vcc
v_mov_b32 v254, -3
v_cmp_ge_i32 vcc, v254, v255
s_nop 1
v_cndmask_b32 v167, v7, v167, vcc
v_mov_b32 v254, 32
v_cmp_ge_i32 vcc, v254, v255
s_nop 1
v_cndmask_b32 v168, v7, v168, vcc
v_mov_b32 v254, 31
v_cmp_ge_i32 vcc, v254, v255
s_nop 1
v_cndmask_b32 v169, v7, v169, vcc
v_mov_b32 v254, 30
v_cmp_ge_i32 vcc, v254, v255
s_nop 1
v_cndmask_b32 v170, v7, v170, vcc
v_mov_b32 v254, 29
v_cmp_ge_i32 vcc, v254, v255
s_nop 1
v_cndmask_b32 v171, v7, v171, vcc
v_mov_b32 v254, 64
v_cmp_ge_i32 vcc, v254, v255
s_nop 1
v_cndmask_b32 v172, v7, v172, vcc
v_mov_b32 v254, 63
v_cmp_ge_i32 vcc, v254, v255
s_nop 1
v_cndmask_b32 v173, v7, v173, vcc
v_mov_b32 v254, 62
v_cmp_ge_i32 vcc, v254, v255
s_nop 1
v_cndmask_b32 v174, v7, v174, vcc
v_mov_b32 v254, 61
v_cmp_ge_i32 vcc, v254, v255
s_nop 1
v_cndmask_b32 v175, v7, v175, vcc
v_mov_b32 v254, -48
v_cmp_ge_i32 vcc, v254, v255
s_nop 1
v_cndmask_b32 v176, v7, v176, vcc
v_mov_b32 v254, -49
v_cmp_ge_i32 vcc, v254, v255
s_nop 1
v_cndmask_b32 v177, v7, v177, vcc
v_mov_b32 v254, -50
v_cmp_ge_i32 vcc, v254, v255
s_nop 1
v_cndmask_b32 v178, v7, v178, vcc
v_mov_b32 v254, -51
v_cmp_ge_i32 vcc, v254, v255
s_nop 1
v_cndmask_b32 v179, v7, v179, vcc
v_mov_b32 v254, -16
v_cmp_ge_i32 vcc, v254, v255
s_nop 1
v_cndmask_b32 v180, v7, v180, vcc
v_mov_b32 v254, -17
v_cmp_ge_i32 vcc, v254, v255
s_nop 1
v_cndmask_b32 v181, v7, v181, vcc
v_mov_b32 v254, -18
v_cmp_ge_i32 vcc, v254, v255
s_nop 1
v_cndmask_b32 v182, v7, v182, vcc
v_mov_b32 v254, -19
v_cmp_ge_i32 vcc, v254, v255
s_nop 1
v_cndmask_b32 v183, v7, v183, vcc
v_mov_b32 v254, 16
v_cmp_ge_i32 vcc, v254, v255
s_nop 1
v_cndmask_b32 v184, v7, v184, vcc
v_mov_b32 v254, 15
v_cmp_ge_i32 vcc, v254, v255
s_nop 1
v_cndmask_b32 v185, v7, v185, vcc
v_mov_b32 v254, 14
v_cmp_ge_i32 vcc, v254, v255
s_nop 1
v_cndmask_b32 v186, v7, v186, vcc
v_mov_b32 v254, 13
v_cmp_ge_i32 vcc, v254, v255
s_nop 1
v_cndmask_b32 v187, v7, v187, vcc
v_mov_b32 v254, 48
v_cmp_ge_i32 vcc, v254, v255
s_nop 1
v_cndmask_b32 v188, v7, v188, vcc
v_mov_b32 v254, 47
v_cmp_ge_i32 vcc, v254, v255
s_nop 1
v_cndmask_b32 v189, v7, v189, vcc
v_mov_b32 v254, 46
v_cmp_ge_i32 vcc, v254, v255
s_nop 1
v_cndmask_b32 v190, v7, v190, vcc
v_mov_b32 v254, 45
v_cmp_ge_i32 vcc, v254, v255
s_nop 1
v_cndmask_b32 v191, v7, v191, vcc
v_max3_f32 v250, v128, v129, v130
v_max3_f32 v250, v250, v131, v144
v_max3_f32 v250, v250, v145, v146
v_max3_f32 v250, v250, v147, v160
v_max3_f32 v250, v250, v161, v162
v_max3_f32 v250, v250, v163, v176
v_max3_f32 v250, v250, v177, v178
v_max_f32 v250, v250, v179
v_mov_b32 v254, v250
s_nop 1
v_permlane16_swap_b32 v250, v254
v_max_f32 v250, v250, v254
v_mov_b32 v254, v250
s_nop 1
v_permlane32_swap_b32 v250, v254
v_max_f32 v250, v250, v254
v_max3_f32 v251, v132, v133, v134
v_max3_f32 v251, v251, v135, v148
v_max3_f32 v251, v251, v149, v150
v_max3_f32 v251, v251, v151, v164
v_max3_f32 v251, v251, v165, v166
v_max3_f32 v251, v251, v167, v180
v_max3_f32 v251, v251, v181, v182
v_max_f32 v251, v251, v183
v_mov_b32 v254, v251
s_nop 1
v_permlane16_swap_b32 v251, v254
v_max_f32 v251, v251, v254
v_mov_b32 v254, v251
s_nop 1
v_permlane32_swap_b32 v251, v254
v_max_f32 v251, v251, v254
v_max3_f32 v252, v136, v137, v138
v_max3_f32 v252, v252, v139, v152
v_max3_f32 v252, v252, v153, v154
v_max3_f32 v252, v252, v155, v168
v_max3_f32 v252, v252, v169, v170
v_max3_f32 v252, v252, v171, v184
v_max3_f32 v252, v252, v185, v186
v_max_f32 v252, v252, v187
v_mov_b32 v254, v252
s_nop 1
v_permlane16_swap_b32 v252, v254
v_max_f32 v252, v252, v254
v_mov_b32 v254, v252
s_nop 1
v_permlane32_swap_b32 v252, v254
v_max_f32 v252, v252, v254
v_max3_f32 v253, v140, v141, v142
v_max3_f32 v253, v253, v143, v156
v_max3_f32 v253, v253, v157, v158
v_max3_f32 v253, v253, v159, v172
v_max3_f32 v253, v253, v173, v174
v_max3_f32 v253, v253, v175, v188
v_max3_f32 v253, v253, v189, v190
v_max_f32 v253, v253, v191
v_mov_b32 v254, v253
s_nop 1
v_permlane16_swap_b32 v253, v254
v_max_f32 v253, v253, v254
v_mov_b32 v254, v253
s_nop 1
v_permlane32_swap_b32 v253, v254
v_max_f32 v253, v253, v254
v_max_f32 v250, v250, v232
v_sub_f32 v240, v232, v250
v_exp_f32 v240, v240
v_mov_b32 v232, v250
v_max_f32 v251, v251, v233
v_sub_f32 v241, v233, v251
v_exp_f32 v241, v241
v_mov_b32 v233, v251
v_max_f32 v252, v252, v234
v_sub_f32 v242, v234, v252
v_exp_f32 v242, v242
v_mov_b32 v234, v252
v_max_f32 v253, v253, v235
v_sub_f32 v243, v235, v253
v_exp_f32 v243, v243
v_mov_b32 v235, v253
v_accvgpr_read_b32 v24, a0
v_mul_f32 v24, v24, v240
v_accvgpr_write_b32 a0, v24
v_accvgpr_read_b32 v24, a1
v_mul_f32 v24, v24, v240
v_accvgpr_write_b32 a1, v24
v_accvgpr_read_b32 v24, a2
v_mul_f32 v24, v24, v240
v_accvgpr_write_b32 a2, v24
v_accvgpr_read_b32 v24, a3
v_mul_f32 v24, v24, v240
v_accvgpr_write_b32 a3, v24
v_accvgpr_read_b32 v24, a4
v_mul_f32 v24, v24, v241
v_accvgpr_write_b32 a4, v24
v_accvgpr_read_b32 v24, a5
v_mul_f32 v24, v24, v241
v_accvgpr_write_b32 a5, v24
v_accvgpr_read_b32 v24, a6
v_mul_f32 v24, v24, v241
v_accvgpr_write_b32 a6, v24
v_accvgpr_read_b32 v24, a7
v_mul_f32 v24, v24, v241
v_accvgpr_write_b32 a7, v24
v_accvgpr_read_b32 v24, a8
v_mul_f32 v24, v24, v242
v_accvgpr_write_b32 a8, v24
v_accvgpr_read_b32 v24, a9
v_mul_f32 v24, v24, v242
v_accvgpr_write_b32 a9, v24
v_accvgpr_read_b32 v24, a10
v_mul_f32 v24, v24, v242
v_accvgpr_write_b32 a10, v24
v_accvgpr_read_b32 v24, a11
v_mul_f32 v24, v24, v242
v_accvgpr_write_b32 a11, v24
v_accvgpr_read_b32 v24, a12
v_mul_f32 v24, v24, v243
v_accvgpr_write_b32 a12, v24
v_accvgpr_read_b32 v24, a13
v_mul_f32 v24, v24, v243
v_accvgpr_write_b32 a13, v24
v_accvgpr_read_b32 v24, a14
v_mul_f32 v24, v24, v243
v_accvgpr_write_b32 a14, v24
v_accvgpr_read_b32 v24, a15
v_mul_f32 v24, v24, v243
v_accvgpr_write_b32 a15, v24
v_accvgpr_read_b32 v24, a16
v_mul_f32 v24, v24, v240
v_accvgpr_write_b32 a16, v24
v_accvgpr_read_b32 v24, a17
v_mul_f32 v24, v24, v240
v_accvgpr_write_b32 a17, v24
v_accvgpr_read_b32 v24, a18
v_mul_f32 v24, v24, v240
v_accvgpr_write_b32 a18, v24
v_accvgpr_read_b32 v24, a19
v_mul_f32 v24, v24, v240
v_accvgpr_write_b32 a19, v24
v_accvgpr_read_b32 v24, a20
v_mul_f32 v24, v24, v241
v_accvgpr_write_b32 a20, v24
v_accvgpr_read_b32 v24, a21
v_mul_f32 v24, v24, v241
v_accvgpr_write_b32 a21, v24
v_accvgpr_read_b32 v24, a22
v_mul_f32 v24, v24, v241
v_accvgpr_write_b32 a22, v24
v_accvgpr_read_b32 v24, a23
v_mul_f32 v24, v24, v241
v_accvgpr_write_b32 a23, v24
v_accvgpr_read_b32 v24, a24
v_mul_f32 v24, v24, v242
v_accvgpr_write_b32 a24, v24
v_accvgpr_read_b32 v24, a25
v_mul_f32 v24, v24, v242
v_accvgpr_write_b32 a25, v24
v_accvgpr_read_b32 v24, a26
v_mul_f32 v24, v24, v242
v_accvgpr_write_b32 a26, v24
v_accvgpr_read_b32 v24, a27
v_mul_f32 v24, v24, v242
v_accvgpr_write_b32 a27, v24
v_accvgpr_read_b32 v24, a28
v_mul_f32 v24, v24, v243
v_accvgpr_write_b32 a28, v24
v_accvgpr_read_b32 v24, a29
v_mul_f32 v24, v24, v243
v_accvgpr_write_b32 a29, v24
v_accvgpr_read_b32 v24, a30
v_mul_f32 v24, v24, v243
v_accvgpr_write_b32 a30, v24
v_accvgpr_read_b32 v24, a31
v_mul_f32 v24, v24, v243
v_accvgpr_write_b32 a31, v24
v_accvgpr_read_b32 v24, a32
v_mul_f32 v24, v24, v240
v_accvgpr_write_b32 a32, v24
v_accvgpr_read_b32 v24, a33
v_mul_f32 v24, v24, v240
v_accvgpr_write_b32 a33, v24
v_accvgpr_read_b32 v24, a34
v_mul_f32 v24, v24, v240
v_accvgpr_write_b32 a34, v24
v_accvgpr_read_b32 v24, a35
v_mul_f32 v24, v24, v240
v_accvgpr_write_b32 a35, v24
v_accvgpr_read_b32 v24, a36
v_mul_f32 v24, v24, v241
v_accvgpr_write_b32 a36, v24
v_accvgpr_read_b32 v24, a37
v_mul_f32 v24, v24, v241
v_accvgpr_write_b32 a37, v24
v_accvgpr_read_b32 v24, a38
v_mul_f32 v24, v24, v241
v_accvgpr_write_b32 a38, v24
v_accvgpr_read_b32 v24, a39
v_mul_f32 v24, v24, v241
v_accvgpr_write_b32 a39, v24
v_accvgpr_read_b32 v24, a40
v_mul_f32 v24, v24, v242
v_accvgpr_write_b32 a40, v24
v_accvgpr_read_b32 v24, a41
v_mul_f32 v24, v24, v242
v_accvgpr_write_b32 a41, v24
v_accvgpr_read_b32 v24, a42
v_mul_f32 v24, v24, v242
v_accvgpr_write_b32 a42, v24
v_accvgpr_read_b32 v24, a43
v_mul_f32 v24, v24, v242
v_accvgpr_write_b32 a43, v24
v_accvgpr_read_b32 v24, a44
v_mul_f32 v24, v24, v243
v_accvgpr_write_b32 a44, v24
v_accvgpr_read_b32 v24, a45
v_mul_f32 v24, v24, v243
v_accvgpr_write_b32 a45, v24
v_accvgpr_read_b32 v24, a46
v_mul_f32 v24, v24, v243
v_accvgpr_write_b32 a46, v24
v_accvgpr_read_b32 v24, a47
v_mul_f32 v24, v24, v243
v_accvgpr_write_b32 a47, v24
v_accvgpr_read_b32 v24, a48
v_mul_f32 v24, v24, v240
v_accvgpr_write_b32 a48, v24
v_accvgpr_read_b32 v24, a49
v_mul_f32 v24, v24, v240
v_accvgpr_write_b32 a49, v24
v_accvgpr_read_b32 v24, a50
v_mul_f32 v24, v24, v240
v_accvgpr_write_b32 a50, v24
v_accvgpr_read_b32 v24, a51
v_mul_f32 v24, v24, v240
v_accvgpr_write_b32 a51, v24
v_accvgpr_read_b32 v24, a52
v_mul_f32 v24, v24, v241
v_accvgpr_write_b32 a52, v24
v_accvgpr_read_b32 v24, a53
v_mul_f32 v24, v24, v241
v_accvgpr_write_b32 a53, v24
v_accvgpr_read_b32 v24, a54
v_mul_f32 v24, v24, v241
v_accvgpr_write_b32 a54, v24
v_accvgpr_read_b32 v24, a55
v_mul_f32 v24, v24, v241
v_accvgpr_write_b32 a55, v24
v_accvgpr_read_b32 v24, a56
v_mul_f32 v24, v24, v242
v_accvgpr_write_b32 a56, v24
v_accvgpr_read_b32 v24, a57
v_mul_f32 v24, v24, v242
v_accvgpr_write_b32 a57, v24
v_accvgpr_read_b32 v24, a58
v_mul_f32 v24, v24, v242
v_accvgpr_write_b32 a58, v24
v_accvgpr_read_b32 v24, a59
v_mul_f32 v24, v24, v242
v_accvgpr_write_b32 a59, v24
v_accvgpr_read_b32 v24, a60
v_mul_f32 v24, v24, v243
v_accvgpr_write_b32 a60, v24
v_accvgpr_read_b32 v24, a61
v_mul_f32 v24, v24, v243
v_accvgpr_write_b32 a61, v24
v_accvgpr_read_b32 v24, a62
v_mul_f32 v24, v24, v243
v_accvgpr_write_b32 a62, v24
v_accvgpr_read_b32 v24, a63
v_mul_f32 v24, v24, v243
v_accvgpr_write_b32 a63, v24
v_accvgpr_read_b32 v24, a64
v_mul_f32 v24, v24, v240
v_accvgpr_write_b32 a64, v24
v_accvgpr_read_b32 v24, a65
v_mul_f32 v24, v24, v240
v_accvgpr_write_b32 a65, v24
v_accvgpr_read_b32 v24, a66
v_mul_f32 v24, v24, v240
v_accvgpr_write_b32 a66, v24
v_accvgpr_read_b32 v24, a67
v_mul_f32 v24, v24, v240
v_accvgpr_write_b32 a67, v24
v_accvgpr_read_b32 v24, a68
v_mul_f32 v24, v24, v241
v_accvgpr_write_b32 a68, v24
v_accvgpr_read_b32 v24, a69
v_mul_f32 v24, v24, v241
v_accvgpr_write_b32 a69, v24
v_accvgpr_read_b32 v24, a70
v_mul_f32 v24, v24, v241
v_accvgpr_write_b32 a70, v24
v_accvgpr_read_b32 v24, a71
v_mul_f32 v24, v24, v241
v_accvgpr_write_b32 a71, v24
v_accvgpr_read_b32 v24, a72
v_mul_f32 v24, v24, v242
v_accvgpr_write_b32 a72, v24
v_accvgpr_read_b32 v24, a73
v_mul_f32 v24, v24, v242
v_accvgpr_write_b32 a73, v24
v_accvgpr_read_b32 v24, a74
v_mul_f32 v24, v24, v242
v_accvgpr_write_b32 a74, v24
v_accvgpr_read_b32 v24, a75
v_mul_f32 v24, v24, v242
v_accvgpr_write_b32 a75, v24
v_accvgpr_read_b32 v24, a76
v_mul_f32 v24, v24, v243
v_accvgpr_write_b32 a76, v24
v_accvgpr_read_b32 v24, a77
v_mul_f32 v24, v24, v243
v_accvgpr_write_b32 a77, v24
v_accvgpr_read_b32 v24, a78
v_mul_f32 v24, v24, v243
v_accvgpr_write_b32 a78, v24
v_accvgpr_read_b32 v24, a79
v_mul_f32 v24, v24, v243
v_accvgpr_write_b32 a79, v24
v_accvgpr_read_b32 v24, a80
v_mul_f32 v24, v24, v240
v_accvgpr_write_b32 a80, v24
v_accvgpr_read_b32 v24, a81
v_mul_f32 v24, v24, v240
v_accvgpr_write_b32 a81, v24
v_accvgpr_read_b32 v24, a82
v_mul_f32 v24, v24, v240
v_accvgpr_write_b32 a82, v24
v_accvgpr_read_b32 v24, a83
v_mul_f32 v24, v24, v240
v_accvgpr_write_b32 a83, v24
v_accvgpr_read_b32 v24, a84
v_mul_f32 v24, v24, v241
v_accvgpr_write_b32 a84, v24
v_accvgpr_read_b32 v24, a85
v_mul_f32 v24, v24, v241
v_accvgpr_write_b32 a85, v24
v_accvgpr_read_b32 v24, a86
v_mul_f32 v24, v24, v241
v_accvgpr_write_b32 a86, v24
v_accvgpr_read_b32 v24, a87
v_mul_f32 v24, v24, v241
v_accvgpr_write_b32 a87, v24
v_accvgpr_read_b32 v24, a88
v_mul_f32 v24, v24, v242
v_accvgpr_write_b32 a88, v24
v_accvgpr_read_b32 v24, a89
v_mul_f32 v24, v24, v242
v_accvgpr_write_b32 a89, v24
v_accvgpr_read_b32 v24, a90
v_mul_f32 v24, v24, v242
v_accvgpr_write_b32 a90, v24
v_accvgpr_read_b32 v24, a91
v_mul_f32 v24, v24, v242
v_accvgpr_write_b32 a91, v24
v_accvgpr_read_b32 v24, a92
v_mul_f32 v24, v24, v243
v_accvgpr_write_b32 a92, v24
v_accvgpr_read_b32 v24, a93
v_mul_f32 v24, v24, v243
v_accvgpr_write_b32 a93, v24
v_accvgpr_read_b32 v24, a94
v_mul_f32 v24, v24, v243
v_accvgpr_write_b32 a94, v24
v_accvgpr_read_b32 v24, a95
v_mul_f32 v24, v24, v243
v_accvgpr_write_b32 a95, v24
v_accvgpr_read_b32 v24, a96
v_mul_f32 v24, v24, v240
v_accvgpr_write_b32 a96, v24
v_accvgpr_read_b32 v24, a97
v_mul_f32 v24, v24, v240
v_accvgpr_write_b32 a97, v24
v_accvgpr_read_b32 v24, a98
v_mul_f32 v24, v24, v240
v_accvgpr_write_b32 a98, v24
v_accvgpr_read_b32 v24, a99
v_mul_f32 v24, v24, v240
v_accvgpr_write_b32 a99, v24
v_accvgpr_read_b32 v24, a100
v_mul_f32 v24, v24, v241
v_accvgpr_write_b32 a100, v24
v_accvgpr_read_b32 v24, a101
v_mul_f32 v24, v24, v241
v_accvgpr_write_b32 a101, v24
v_accvgpr_read_b32 v24, a102
v_mul_f32 v24, v24, v241
v_accvgpr_write_b32 a102, v24
v_accvgpr_read_b32 v24, a103
v_mul_f32 v24, v24, v241
v_accvgpr_write_b32 a103, v24
v_accvgpr_read_b32 v24, a104
v_mul_f32 v24, v24, v242
v_accvgpr_write_b32 a104, v24
v_accvgpr_read_b32 v24, a105
v_mul_f32 v24, v24, v242
v_accvgpr_write_b32 a105, v24
v_accvgpr_read_b32 v24, a106
v_mul_f32 v24, v24, v242
v_accvgpr_write_b32 a106, v24
v_accvgpr_read_b32 v24, a107
v_mul_f32 v24, v24, v242
v_accvgpr_write_b32 a107, v24
v_accvgpr_read_b32 v24, a108
v_mul_f32 v24, v24, v243
v_accvgpr_write_b32 a108, v24
v_accvgpr_read_b32 v24, a109
v_mul_f32 v24, v24, v243
v_accvgpr_write_b32 a109, v24
v_accvgpr_read_b32 v24, a110
v_mul_f32 v24, v24, v243
v_accvgpr_write_b32 a110, v24
v_accvgpr_read_b32 v24, a111
v_mul_f32 v24, v24, v243
v_accvgpr_write_b32 a111, v24
v_accvgpr_read_b32 v24, a112
v_mul_f32 v24, v24, v240
v_accvgpr_write_b32 a112, v24
v_accvgpr_read_b32 v24, a113
v_mul_f32 v24, v24, v240
v_accvgpr_write_b32 a113, v24
v_accvgpr_read_b32 v24, a114
v_mul_f32 v24, v24, v240
v_accvgpr_write_b32 a114, v24
v_accvgpr_read_b32 v24, a115
v_mul_f32 v24, v24, v240
v_accvgpr_write_b32 a115, v24
v_accvgpr_read_b32 v24, a116
v_mul_f32 v24, v24, v241
v_accvgpr_write_b32 a116, v24
v_accvgpr_read_b32 v24, a117
v_mul_f32 v24, v24, v241
v_accvgpr_write_b32 a117, v24
v_accvgpr_read_b32 v24, a118
v_mul_f32 v24, v24, v241
v_accvgpr_write_b32 a118, v24
v_accvgpr_read_b32 v24, a119
v_mul_f32 v24, v24, v241
v_accvgpr_write_b32 a119, v24
v_accvgpr_read_b32 v24, a120
v_mul_f32 v24, v24, v242
v_accvgpr_write_b32 a120, v24
v_accvgpr_read_b32 v24, a121
v_mul_f32 v24, v24, v242
v_accvgpr_write_b32 a121, v24
v_accvgpr_read_b32 v24, a122
v_mul_f32 v24, v24, v242
v_accvgpr_write_b32 a122, v24
v_accvgpr_read_b32 v24, a123
v_mul_f32 v24, v24, v242
v_accvgpr_write_b32 a123, v24
v_accvgpr_read_b32 v24, a124
v_mul_f32 v24, v24, v243
v_accvgpr_write_b32 a124, v24
v_accvgpr_read_b32 v24, a125
v_mul_f32 v24, v24, v243
v_accvgpr_write_b32 a125, v24
v_accvgpr_read_b32 v24, a126
v_mul_f32 v24, v24, v243
v_accvgpr_write_b32 a126, v24
v_accvgpr_read_b32 v24, a127
v_mul_f32 v24, v24, v243
v_accvgpr_write_b32 a127, v24
v_sub_f32 v128, v128, v250
v_exp_f32 v128, v128
v_sub_f32 v129, v129, v250
v_exp_f32 v129, v129
v_sub_f32 v130, v130, v250
v_exp_f32 v130, v130
v_sub_f32 v131, v131, v250
v_exp_f32 v131, v131
v_sub_f32 v132, v132, v251
v_exp_f32 v132, v132
v_sub_f32 v133, v133, v251
v_exp_f32 v133, v133
v_sub_f32 v134, v134, v251
v_exp_f32 v134, v134
v_sub_f32 v135, v135, v251
v_exp_f32 v135, v135
v_sub_f32 v136, v136, v252
v_exp_f32 v136, v136
v_sub_f32 v137, v137, v252
v_exp_f32 v137, v137
v_sub_f32 v138, v138, v252
v_exp_f32 v138, v138
v_sub_f32 v139, v139, v252
v_exp_f32 v139, v139
v_sub_f32 v140, v140, v253
v_exp_f32 v140, v140
v_sub_f32 v141, v141, v253
v_exp_f32 v141, v141
v_sub_f32 v142, v142, v253
v_exp_f32 v142, v142
v_sub_f32 v143, v143, v253
v_exp_f32 v143, v143
v_sub_f32 v144, v144, v250
v_exp_f32 v144, v144
v_sub_f32 v145, v145, v250
v_exp_f32 v145, v145
v_sub_f32 v146, v146, v250
v_exp_f32 v146, v146
v_sub_f32 v147, v147, v250
v_exp_f32 v147, v147
v_sub_f32 v148, v148, v251
v_exp_f32 v148, v148
v_sub_f32 v149, v149, v251
v_exp_f32 v149, v149
v_sub_f32 v150, v150, v251
v_exp_f32 v150, v150
v_sub_f32 v151, v151, v251
v_exp_f32 v151, v151
v_sub_f32 v152, v152, v252
v_exp_f32 v152, v152
v_sub_f32 v153, v153, v252
v_exp_f32 v153, v153
v_sub_f32 v154, v154, v252
v_exp_f32 v154, v154
v_sub_f32 v155, v155, v252
v_exp_f32 v155, v155
v_sub_f32 v156, v156, v253
v_exp_f32 v156, v156
v_sub_f32 v157, v157, v253
v_exp_f32 v157, v157
v_sub_f32 v158, v158, v253
v_exp_f32 v158, v158
v_sub_f32 v159, v159, v253
v_exp_f32 v159, v159
v_sub_f32 v160, v160, v250
v_exp_f32 v160, v160
v_sub_f32 v161, v161, v250
v_exp_f32 v161, v161
v_sub_f32 v162, v162, v250
v_exp_f32 v162, v162
v_sub_f32 v163, v163, v250
v_exp_f32 v163, v163
v_sub_f32 v164, v164, v251
v_exp_f32 v164, v164
v_sub_f32 v165, v165, v251
v_exp_f32 v165, v165
v_sub_f32 v166, v166, v251
v_exp_f32 v166, v166
v_sub_f32 v167, v167, v251
v_exp_f32 v167, v167
v_sub_f32 v168, v168, v252
v_exp_f32 v168, v168
v_sub_f32 v169, v169, v252
v_exp_f32 v169, v169
v_sub_f32 v170, v170, v252
v_exp_f32 v170, v170
v_sub_f32 v171, v171, v252
)ASM"
R"ASM(v_exp_f32 v171, v171
v_sub_f32 v172, v172, v253
v_exp_f32 v172, v172
v_sub_f32 v173, v173, v253
v_exp_f32 v173, v173
v_sub_f32 v174, v174, v253
v_exp_f32 v174, v174
v_sub_f32 v175, v175, v253
v_exp_f32 v175, v175
v_sub_f32 v176, v176, v250
v_exp_f32 v176, v176
v_sub_f32 v177, v177, v250
v_exp_f32 v177, v177
v_sub_f32 v178, v178, v250
v_exp_f32 v178, v178
v_sub_f32 v179, v179, v250
v_exp_f32 v179, v179
v_sub_f32 v180, v180, v251
v_exp_f32 v180, v180
v_sub_f32 v181, v181, v251
v_exp_f32 v181, v181
v_sub_f32 v182, v182, v251
v_exp_f32 v182, v182
v_sub_f32 v183, v183, v251
v_exp_f32 v183, v183
v_sub_f32 v184, v184, v252
v_exp_f32 v184, v184
v_sub_f32 v185, v185, v252
v_exp_f32 v185, v185
v_sub_f32 v186, v186, v252
v_exp_f32 v186, v186
v_sub_f32 v187, v187, v252
v_exp_f32 v187, v187
v_sub_f32 v188, v188, v253
v_exp_f32 v188, v188
v_sub_f32 v189, v189, v253
v_exp_f32 v189, v189
v_sub_f32 v190, v190, v253
v_exp_f32 v190, v190
v_sub_f32 v191, v191, v253
v_exp_f32 v191, v191
v_mul_f32 v236, v236, v240
v_add_f32 v236, v128, v236
v_add_f32 v236, v129, v236
v_add_f32 v236, v130, v236
v_add_f32 v236, v131, v236
v_add_f32 v236, v144, v236
v_add_f32 v236, v145, v236
v_add_f32 v236, v146, v236
v_add_f32 v236, v147, v236
v_add_f32 v236, v160, v236
v_add_f32 v236, v161, v236
v_add_f32 v236, v162, v236
v_add_f32 v236, v163, v236
v_add_f32 v236, v176, v236
v_add_f32 v236, v177, v236
v_add_f32 v236, v178, v236
v_add_f32 v236, v179, v236
v_mul_f32 v237, v237, v241
v_add_f32 v237, v132, v237
v_add_f32 v237, v133, v237
v_add_f32 v237, v134, v237
v_add_f32 v237, v135, v237
v_add_f32 v237, v148, v237
v_add_f32 v237, v149, v237
v_add_f32 v237, v150, v237
v_add_f32 v237, v151, v237
v_add_f32 v237, v164, v237
v_add_f32 v237, v165, v237
v_add_f32 v237, v166, v237
v_add_f32 v237, v167, v237
v_add_f32 v237, v180, v237
v_add_f32 v237, v181, v237
v_add_f32 v237, v182, v237
v_add_f32 v237, v183, v237
v_mul_f32 v238, v238, v242
v_add_f32 v238, v136, v238
v_add_f32 v238, v137, v238
v_add_f32 v238, v138, v238
v_add_f32 v238, v139, v238
v_add_f32 v238, v152, v238
v_add_f32 v238, v153, v238
v_add_f32 v238, v154, v238
v_add_f32 v238, v155, v238
v_add_f32 v238, v168, v238
v_add_f32 v238, v169, v238
v_add_f32 v238, v170, v238
v_add_f32 v238, v171, v238
v_add_f32 v238, v184, v238
v_add_f32 v238, v185, v238
v_add_f32 v238, v186, v238
v_add_f32 v238, v187, v238
v_mul_f32 v239, v239, v243
v_add_f32 v239, v140, v239
v_add_f32 v239, v141, v239
v_add_f32 v239, v142, v239
v_add_f32 v239, v143, v239
v_add_f32 v239, v156, v239
v_add_f32 v239, v157, v239
v_add_f32 v239, v158, v239
v_add_f32 v239, v159, v239
v_add_f32 v239, v172, v239
v_add_f32 v239, v173, v239
v_add_f32 v239, v174, v239
v_add_f32 v239, v175, v239
v_add_f32 v239, v188, v239
v_add_f32 v239, v189, v239
v_add_f32 v239, v190, v239
v_add_f32 v239, v191, v239
v_cvt_pk_bf16_f32 v192, v128, v129
v_cvt_pk_bf16_f32 v193, v130, v131
v_cvt_pk_bf16_f32 v196, v132, v133
v_cvt_pk_bf16_f32 v197, v134, v135
v_cvt_pk_bf16_f32 v200, v136, v137
v_cvt_pk_bf16_f32 v201, v138, v139
v_cvt_pk_bf16_f32 v204, v140, v141
v_cvt_pk_bf16_f32 v205, v142, v143
v_cvt_pk_bf16_f32 v194, v144, v145
v_cvt_pk_bf16_f32 v195, v146, v147
v_cvt_pk_bf16_f32 v198, v148, v149
v_cvt_pk_bf16_f32 v199, v150, v151
v_cvt_pk_bf16_f32 v202, v152, v153
v_cvt_pk_bf16_f32 v203, v154, v155
v_cvt_pk_bf16_f32 v206, v156, v157
v_cvt_pk_bf16_f32 v207, v158, v159
v_cvt_pk_bf16_f32 v208, v160, v161
v_cvt_pk_bf16_f32 v209, v162, v163
v_cvt_pk_bf16_f32 v212, v164, v165
v_cvt_pk_bf16_f32 v213, v166, v167
v_cvt_pk_bf16_f32 v216, v168, v169
v_cvt_pk_bf16_f32 v217, v170, v171
v_cvt_pk_bf16_f32 v220, v172, v173
v_cvt_pk_bf16_f32 v221, v174, v175
v_cvt_pk_bf16_f32 v210, v176, v177
v_cvt_pk_bf16_f32 v211, v178, v179
v_cvt_pk_bf16_f32 v214, v180, v181
v_cvt_pk_bf16_f32 v215, v182, v183
v_cvt_pk_bf16_f32 v218, v184, v185
v_cvt_pk_bf16_f32 v219, v186, v187
v_cvt_pk_bf16_f32 v222, v188, v189
v_cvt_pk_bf16_f32 v223, v190, v191
s_waitcnt vmcnt(0)
s_nop 3
v_mfma_f32_16x16x32_bf16 a[0:3], a[128:131], v[192:195], a[0:3]
v_mfma_f32_16x16x32_bf16 a[4:7], a[128:131], v[196:199], a[4:7]
v_mfma_f32_16x16x32_bf16 a[8:11], a[128:131], v[200:203], a[8:11]
v_mfma_f32_16x16x32_bf16 a[12:15], a[128:131], v[204:207], a[12:15]
v_mfma_f32_16x16x32_bf16 a[16:19], a[132:135], v[192:195], a[16:19]
v_mfma_f32_16x16x32_bf16 a[20:23], a[132:135], v[196:199], a[20:23]
v_mfma_f32_16x16x32_bf16 a[24:27], a[132:135], v[200:203], a[24:27]
v_mfma_f32_16x16x32_bf16 a[28:31], a[132:135], v[204:207], a[28:31]
v_mfma_f32_16x16x32_bf16 a[32:35], a[136:139], v[192:195], a[32:35]
v_mfma_f32_16x16x32_bf16 a[36:39], a[136:139], v[196:199], a[36:39]
v_mfma_f32_16x16x32_bf16 a[40:43], a[136:139], v[200:203], a[40:43]
v_mfma_f32_16x16x32_bf16 a[44:47], a[136:139], v[204:207], a[44:47]
v_mfma_f32_16x16x32_bf16 a[48:51], a[140:143], v[192:195], a[48:51]
v_mfma_f32_16x16x32_bf16 a[52:55], a[140:143], v[196:199], a[52:55]
v_mfma_f32_16x16x32_bf16 a[56:59], a[140:143], v[200:203], a[56:59]
v_mfma_f32_16x16x32_bf16 a[60:63], a[140:143], v[204:207], a[60:63]
v_mfma_f32_16x16x32_bf16 a[64:67], a[144:147], v[192:195], a[64:67]
v_mfma_f32_16x16x32_bf16 a[68:71], a[144:147], v[196:199], a[68:71]
v_mfma_f32_16x16x32_bf16 a[72:75], a[144:147], v[200:203], a[72:75]
v_mfma_f32_16x16x32_bf16 a[76:79], a[144:147], v[204:207], a[76:79]
v_mfma_f32_16x16x32_bf16 a[80:83], a[148:151], v[192:195], a[80:83]
v_mfma_f32_16x16x32_bf16 a[84:87], a[148:151], v[196:199], a[84:87]
v_mfma_f32_16x16x32_bf16 a[88:91], a[148:151], v[200:203], a[88:91]
v_mfma_f32_16x16x32_bf16 a[92:95], a[148:151], v[204:207], a[92:95]
v_mfma_f32_16x16x32_bf16 a[96:99], a[152:155], v[192:195], a[96:99]
v_mfma_f32_16x16x32_bf16 a[100:103], a[152:155], v[196:199], a[100:103]
v_mfma_f32_16x16x32_bf16 a[104:107], a[152:155], v[200:203], a[104:107]
v_mfma_f32_16x16x32_bf16 a[108:111], a[152:155], v[204:207], a[108:111]
v_mfma_f32_16x16x32_bf16 a[112:115], a[156:159], v[192:195], a[112:115]
v_mfma_f32_16x16x32_bf16 a[116:119], a[156:159], v[196:199], a[116:119]
v_mfma_f32_16x16x32_bf16 a[120:123], a[156:159], v[200:203], a[120:123]
v_mfma_f32_16x16x32_bf16 a[124:127], a[156:159], v[204:207], a[124:127]
v_mfma_f32_16x16x32_bf16 a[0:3], a[160:163], v[208:211], a[0:3]
v_mfma_f32_16x16x32_bf16 a[4:7], a[160:163], v[212:215], a[4:7]
v_mfma_f32_16x16x32_bf16 a[8:11], a[160:163], v[216:219], a[8:11]
v_mfma_f32_16x16x32_bf16 a[12:15], a[160:163], v[220:223], a[12:15]
v_mfma_f32_16x16x32_bf16 a[16:19], a[164:167], v[208:211], a[16:19]
v_mfma_f32_16x16x32_bf16 a[20:23], a[164:167], v[212:215], a[20:23]
v_mfma_f32_16x16x32_bf16 a[24:27], a[164:167], v[216:219], a[24:27]
v_mfma_f32_16x16x32_bf16 a[28:31], a[164:167], v[220:223], a[28:31]
v_mfma_f32_16x16x32_bf16 a[32:35], a[168:171], v[208:211], a[32:35]
v_mfma_f32_16x16x32_bf16 a[36:39], a[168:171], v[212:215], a[36:39]
v_mfma_f32_16x16x32_bf16 a[40:43], a[168:171], v[216:219], a[40:43]
v_mfma_f32_16x16x32_bf16 a[44:47], a[168:171], v[220:223], a[44:47]
v_mfma_f32_16x16x32_bf16 a[48:51], a[172:175], v[208:211], a[48:51]
v_mfma_f32_16x16x32_bf16 a[52:55], a[172:175], v[212:215], a[52:55]
v_mfma_f32_16x16x32_bf16 a[56:59], a[172:175], v[216:219], a[56:59]
v_mfma_f32_16x16x32_bf16 a[60:63], a[172:175], v[220:223], a[60:63]
v_mfma_f32_16x16x32_bf16 a[64:67], a[176:179], v[208:211], a[64:67]
v_mfma_f32_16x16x32_bf16 a[68:71], a[176:179], v[212:215], a[68:71]
v_mfma_f32_16x16x32_bf16 a[72:75], a[176:179], v[216:219], a[72:75]
v_mfma_f32_16x16x32_bf16 a[76:79], a[176:179], v[220:223], a[76:79]
v_mfma_f32_16x16x32_bf16 a[80:83], a[180:183], v[208:211], a[80:83]
v_mfma_f32_16x16x32_bf16 a[84:87], a[180:183], v[212:215], a[84:87]
v_mfma_f32_16x16x32_bf16 a[88:91], a[180:183], v[216:219], a[88:91]
v_mfma_f32_16x16x32_bf16 a[92:95], a[180:183], v[220:223], a[92:95]
v_mfma_f32_16x16x32_bf16 a[96:99], a[184:187], v[208:211], a[96:99]
v_mfma_f32_16x16x32_bf16 a[100:103], a[184:187], v[212:215], a[100:103]
v_mfma_f32_16x16x32_bf16 a[104:107], a[184:187], v[216:219], a[104:107]
v_mfma_f32_16x16x32_bf16 a[108:111], a[184:187], v[220:223], a[108:111]
v_mfma_f32_16x16x32_bf16 a[112:115], a[188:191], v[208:211], a[112:115]
v_mfma_f32_16x16x32_bf16 a[116:119], a[188:191], v[212:215], a[116:119]
v_mfma_f32_16x16x32_bf16 a[120:123], a[188:191], v[216:219], a[120:123]
v_mfma_f32_16x16x32_bf16 a[124:127], a[188:191], v[220:223], a[124:127]
s_nop 9
s_add_u32 s22, s37, 16384
s_cmp_ge_u32 s24, s22
s_cbranch_scc1 L_SAFE_END
s_add_u32 s24, s24, 16384
s_branch L_SAFE_LOOP
L_SAFE_END:
s_waitcnt vmcnt(0) lgkmcnt(0)
s_nop 15
s_nop 3
v_mov_b32 v254, v236
s_nop 1
v_permlane16_swap_b32 v236, v254
v_add_f32 v236, v236, v254
v_mov_b32 v254, v236
s_nop 1
v_permlane32_swap_b32 v236, v254
v_add_f32 v236, v236, v254
v_rcp_f32 v236, v236
v_mov_b32 v254, v237
s_nop 1
v_permlane16_swap_b32 v237, v254
v_add_f32 v237, v237, v254
v_mov_b32 v254, v237
s_nop 1
v_permlane32_swap_b32 v237, v254
v_add_f32 v237, v237, v254
v_rcp_f32 v237, v237
v_mov_b32 v254, v238
s_nop 1
v_permlane16_swap_b32 v238, v254
v_add_f32 v238, v238, v254
v_mov_b32 v254, v238
s_nop 1
v_permlane32_swap_b32 v238, v254
v_add_f32 v238, v238, v254
v_rcp_f32 v238, v238
v_mov_b32 v254, v239
s_nop 1
v_permlane16_swap_b32 v239, v254
v_add_f32 v239, v239, v254
v_mov_b32 v254, v239
s_nop 1
v_permlane32_swap_b32 v239, v254
v_add_f32 v239, v239, v254
v_rcp_f32 v239, v239
v_accvgpr_read_b32 v40, a0
v_accvgpr_read_b32 v41, a1
v_accvgpr_read_b32 v42, a2
v_accvgpr_read_b32 v43, a3
v_accvgpr_read_b32 v44, a16
v_accvgpr_read_b32 v45, a17
v_accvgpr_read_b32 v46, a18
v_accvgpr_read_b32 v47, a19
v_mul_f32 v40, v40, v236
v_mul_f32 v41, v41, v236
v_mul_f32 v42, v42, v236
v_mul_f32 v43, v43, v236
v_mul_f32 v44, v44, v236
v_mul_f32 v45, v45, v236
v_mul_f32 v46, v46, v236
v_mul_f32 v47, v47, v236
v_cvt_pk_bf16_f32 v48, v40, v41
v_cvt_pk_bf16_f32 v49, v42, v43
v_cvt_pk_bf16_f32 v50, v44, v45
v_cvt_pk_bf16_f32 v51, v46, v47
s_mov_b32 s23, 0
buffer_store_dwordx4 v[48:51], v2, s[56:59], s23 offen offset:0 sc1
v_accvgpr_read_b32 v52, a32
v_accvgpr_read_b32 v53, a33
v_accvgpr_read_b32 v54, a34
v_accvgpr_read_b32 v55, a35
v_accvgpr_read_b32 v56, a48
v_accvgpr_read_b32 v57, a49
v_accvgpr_read_b32 v58, a50
v_accvgpr_read_b32 v59, a51
v_mul_f32 v52, v52, v236
v_mul_f32 v53, v53, v236
v_mul_f32 v54, v54, v236
v_mul_f32 v55, v55, v236
v_mul_f32 v56, v56, v236
v_mul_f32 v57, v57, v236
v_mul_f32 v58, v58, v236
v_mul_f32 v59, v59, v236
v_cvt_pk_bf16_f32 v60, v52, v53
v_cvt_pk_bf16_f32 v61, v54, v55
v_cvt_pk_bf16_f32 v62, v56, v57
v_cvt_pk_bf16_f32 v63, v58, v59
buffer_store_dwordx4 v[60:63], v2, s[56:59], s23 offen offset:1024 sc1
v_accvgpr_read_b32 v40, a64
v_accvgpr_read_b32 v41, a65
v_accvgpr_read_b32 v42, a66
v_accvgpr_read_b32 v43, a67
v_accvgpr_read_b32 v44, a80
v_accvgpr_read_b32 v45, a81
v_accvgpr_read_b32 v46, a82
v_accvgpr_read_b32 v47, a83
v_mul_f32 v40, v40, v236
v_mul_f32 v41, v41, v236
v_mul_f32 v42, v42, v236
v_mul_f32 v43, v43, v236
v_mul_f32 v44, v44, v236
v_mul_f32 v45, v45, v236
v_mul_f32 v46, v46, v236
v_mul_f32 v47, v47, v236
v_cvt_pk_bf16_f32 v48, v40, v41
v_cvt_pk_bf16_f32 v49, v42, v43
v_cvt_pk_bf16_f32 v50, v44, v45
v_cvt_pk_bf16_f32 v51, v46, v47
buffer_store_dwordx4 v[48:51], v2, s[56:59], s23 offen offset:2048 sc1
v_accvgpr_read_b32 v52, a96
v_accvgpr_read_b32 v53, a97
v_accvgpr_read_b32 v54, a98
v_accvgpr_read_b32 v55, a99
v_accvgpr_read_b32 v56, a112
v_accvgpr_read_b32 v57, a113
v_accvgpr_read_b32 v58, a114
v_accvgpr_read_b32 v59, a115
v_mul_f32 v52, v52, v236
v_mul_f32 v53, v53, v236
v_mul_f32 v54, v54, v236
v_mul_f32 v55, v55, v236
v_mul_f32 v56, v56, v236
v_mul_f32 v57, v57, v236
v_mul_f32 v58, v58, v236
v_mul_f32 v59, v59, v236
v_cvt_pk_bf16_f32 v60, v52, v53
v_cvt_pk_bf16_f32 v61, v54, v55
v_cvt_pk_bf16_f32 v62, v56, v57
v_cvt_pk_bf16_f32 v63, v58, v59
buffer_store_dwordx4 v[60:63], v2, s[56:59], s23 offen offset:3072 sc1
v_accvgpr_read_b32 v40, a4
v_accvgpr_read_b32 v41, a5
v_accvgpr_read_b32 v42, a6
v_accvgpr_read_b32 v43, a7
v_accvgpr_read_b32 v44, a20
v_accvgpr_read_b32 v45, a21
v_accvgpr_read_b32 v46, a22
v_accvgpr_read_b32 v47, a23
v_mul_f32 v40, v40, v237
v_mul_f32 v41, v41, v237
v_mul_f32 v42, v42, v237
v_mul_f32 v43, v43, v237
v_mul_f32 v44, v44, v237
v_mul_f32 v45, v45, v237
v_mul_f32 v46, v46, v237
v_mul_f32 v47, v47, v237
v_cvt_pk_bf16_f32 v48, v40, v41
v_cvt_pk_bf16_f32 v49, v42, v43
v_cvt_pk_bf16_f32 v50, v44, v45
v_cvt_pk_bf16_f32 v51, v46, v47
s_mov_b32 s23, 376832
buffer_store_dwordx4 v[48:51], v2, s[56:59], s23 offen offset:0 sc1
v_accvgpr_read_b32 v52, a36
v_accvgpr_read_b32 v53, a37
v_accvgpr_read_b32 v54, a38
v_accvgpr_read_b32 v55, a39
v_accvgpr_read_b32 v56, a52
v_accvgpr_read_b32 v57, a53
v_accvgpr_read_b32 v58, a54
v_accvgpr_read_b32 v59, a55
v_mul_f32 v52, v52, v237
v_mul_f32 v53, v53, v237
v_mul_f32 v54, v54, v237
v_mul_f32 v55, v55, v237
v_mul_f32 v56, v56, v237
v_mul_f32 v57, v57, v237
v_mul_f32 v58, v58, v237
v_mul_f32 v59, v59, v237
v_cvt_pk_bf16_f32 v60, v52, v53
v_cvt_pk_bf16_f32 v61, v54, v55
v_cvt_pk_bf16_f32 v62, v56, v57
v_cvt_pk_bf16_f32 v63, v58, v59
buffer_store_dwordx4 v[60:63], v2, s[56:59], s23 offen offset:1024 sc1
v_accvgpr_read_b32 v40, a68
v_accvgpr_read_b32 v41, a69
v_accvgpr_read_b32 v42, a70
v_accvgpr_read_b32 v43, a71
v_accvgpr_read_b32 v44, a84
v_accvgpr_read_b32 v45, a85
v_accvgpr_read_b32 v46, a86
v_accvgpr_read_b32 v47, a87
v_mul_f32 v40, v40, v237
v_mul_f32 v41, v41, v237
v_mul_f32 v42, v42, v237
v_mul_f32 v43, v43, v237
v_mul_f32 v44, v44, v237
v_mul_f32 v45, v45, v237
v_mul_f32 v46, v46, v237
v_mul_f32 v47, v47, v237
v_cvt_pk_bf16_f32 v48, v40, v41
v_cvt_pk_bf16_f32 v49, v42, v43
v_cvt_pk_bf16_f32 v50, v44, v45
v_cvt_pk_bf16_f32 v51, v46, v47
buffer_store_dwordx4 v[48:51], v2, s[56:59], s23 offen offset:2048 sc1
v_accvgpr_read_b32 v52, a100
v_accvgpr_read_b32 v53, a101
v_accvgpr_read_b32 v54, a102
v_accvgpr_read_b32 v55, a103
v_accvgpr_read_b32 v56, a116
v_accvgpr_read_b32 v57, a117
v_accvgpr_read_b32 v58, a118
v_accvgpr_read_b32 v59, a119
v_mul_f32 v52, v52, v237
v_mul_f32 v53, v53, v237
v_mul_f32 v54, v54, v237
v_mul_f32 v55, v55, v237
v_mul_f32 v56, v56, v237
v_mul_f32 v57, v57, v237
v_mul_f32 v58, v58, v237
v_mul_f32 v59, v59, v237
v_cvt_pk_bf16_f32 v60, v52, v53
v_cvt_pk_bf16_f32 v61, v54, v55
v_cvt_pk_bf16_f32 v62, v56, v57
v_cvt_pk_bf16_f32 v63, v58, v59
buffer_store_dwordx4 v[60:63], v2, s[56:59], s23 offen offset:3072 sc1
v_accvgpr_read_b32 v40, a8
v_accvgpr_read_b32 v41, a9
v_accvgpr_read_b32 v42, a10
v_accvgpr_read_b32 v43, a11
v_accvgpr_read_b32 v44, a24
v_accvgpr_read_b32 v45, a25
v_accvgpr_read_b32 v46, a26
v_accvgpr_read_b32 v47, a27
v_mul_f32 v40, v40, v238
v_mul_f32 v41, v41, v238
v_mul_f32 v42, v42, v238
v_mul_f32 v43, v43, v238
v_mul_f32 v44, v44, v238
v_mul_f32 v45, v45, v238
v_mul_f32 v46, v46, v238
v_mul_f32 v47, v47, v238
v_cvt_pk_bf16_f32 v48, v40, v41
v_cvt_pk_bf16_f32 v49, v42, v43
v_cvt_pk_bf16_f32 v50, v44, v45
v_cvt_pk_bf16_f32 v51, v46, v47
s_mov_b32 s23, 753664
buffer_store_dwordx4 v[48:51], v2, s[56:59], s23 offen offset:0 sc1
v_accvgpr_read_b32 v52, a40
v_accvgpr_read_b32 v53, a41
v_accvgpr_read_b32 v54, a42
v_accvgpr_read_b32 v55, a43
v_accvgpr_read_b32 v56, a56
v_accvgpr_read_b32 v57, a57
v_accvgpr_read_b32 v58, a58
v_accvgpr_read_b32 v59, a59
v_mul_f32 v52, v52, v238
v_mul_f32 v53, v53, v238
v_mul_f32 v54, v54, v238
v_mul_f32 v55, v55, v238
v_mul_f32 v56, v56, v238
v_mul_f32 v57, v57, v238
v_mul_f32 v58, v58, v238
v_mul_f32 v59, v59, v238
v_cvt_pk_bf16_f32 v60, v52, v53
v_cvt_pk_bf16_f32 v61, v54, v55
v_cvt_pk_bf16_f32 v62, v56, v57
v_cvt_pk_bf16_f32 v63, v58, v59
buffer_store_dwordx4 v[60:63], v2, s[56:59], s23 offen offset:1024 sc1
v_accvgpr_read_b32 v40, a72
v_accvgpr_read_b32 v41, a73
v_accvgpr_read_b32 v42, a74
v_accvgpr_read_b32 v43, a75
v_accvgpr_read_b32 v44, a88
v_accvgpr_read_b32 v45, a89
v_accvgpr_read_b32 v46, a90
v_accvgpr_read_b32 v47, a91
v_mul_f32 v40, v40, v238
v_mul_f32 v41, v41, v238
v_mul_f32 v42, v42, v238
v_mul_f32 v43, v43, v238
v_mul_f32 v44, v44, v238
v_mul_f32 v45, v45, v238
v_mul_f32 v46, v46, v238
v_mul_f32 v47, v47, v238
v_cvt_pk_bf16_f32 v48, v40, v41
v_cvt_pk_bf16_f32 v49, v42, v43
v_cvt_pk_bf16_f32 v50, v44, v45
v_cvt_pk_bf16_f32 v51, v46, v47
buffer_store_dwordx4 v[48:51], v2, s[56:59], s23 offen offset:2048 sc1
v_accvgpr_read_b32 v52, a104
v_accvgpr_read_b32 v53, a105
v_accvgpr_read_b32 v54, a106
v_accvgpr_read_b32 v55, a107
v_accvgpr_read_b32 v56, a120
v_accvgpr_read_b32 v57, a121
v_accvgpr_read_b32 v58, a122
v_accvgpr_read_b32 v59, a123
v_mul_f32 v52, v52, v238
v_mul_f32 v53, v53, v238
v_mul_f32 v54, v54, v238
v_mul_f32 v55, v55, v238
v_mul_f32 v56, v56, v238
v_mul_f32 v57, v57, v238
v_mul_f32 v58, v58, v238
v_mul_f32 v59, v59, v238
v_cvt_pk_bf16_f32 v60, v52, v53
v_cvt_pk_bf16_f32 v61, v54, v55
v_cvt_pk_bf16_f32 v62, v56, v57
v_cvt_pk_bf16_f32 v63, v58, v59
buffer_store_dwordx4 v[60:63], v2, s[56:59], s23 offen offset:3072 sc1
v_accvgpr_read_b32 v40, a12
v_accvgpr_read_b32 v41, a13
v_accvgpr_read_b32 v42, a14
v_accvgpr_read_b32 v43, a15
v_accvgpr_read_b32 v44, a28
v_accvgpr_read_b32 v45, a29
v_accvgpr_read_b32 v46, a30
v_accvgpr_read_b32 v47, a31
v_mul_f32 v40, v40, v239
v_mul_f32 v41, v41, v239
v_mul_f32 v42, v42, v239
v_mul_f32 v43, v43, v239
v_mul_f32 v44, v44, v239
v_mul_f32 v45, v45, v239
v_mul_f32 v46, v46, v239
v_mul_f32 v47, v47, v239
v_cvt_pk_bf16_f32 v48, v40, v41
v_cvt_pk_bf16_f32 v49, v42, v43
v_cvt_pk_bf16_f32 v50, v44, v45
v_cvt_pk_bf16_f32 v51, v46, v47
s_mov_b32 s23, 1130496
buffer_store_dwordx4 v[48:51], v2, s[56:59], s23 offen offset:0 sc1
v_accvgpr_read_b32 v52, a44
v_accvgpr_read_b32 v53, a45
v_accvgpr_read_b32 v54, a46
v_accvgpr_read_b32 v55, a47
v_accvgpr_read_b32 v56, a60
v_accvgpr_read_b32 v57, a61
v_accvgpr_read_b32 v58, a62
v_accvgpr_read_b32 v59, a63
v_mul_f32 v52, v52, v239
v_mul_f32 v53, v53, v239
v_mul_f32 v54, v54, v239
v_mul_f32 v55, v55, v239
v_mul_f32 v56, v56, v239
v_mul_f32 v57, v57, v239
v_mul_f32 v58, v58, v239
v_mul_f32 v59, v59, v239
v_cvt_pk_bf16_f32 v60, v52, v53
v_cvt_pk_bf16_f32 v61, v54, v55
v_cvt_pk_bf16_f32 v62, v56, v57
v_cvt_pk_bf16_f32 v63, v58, v59
buffer_store_dwordx4 v[60:63], v2, s[56:59], s23 offen offset:1024 sc1
v_accvgpr_read_b32 v40, a76
v_accvgpr_read_b32 v41, a77
v_accvgpr_read_b32 v42, a78
v_accvgpr_read_b32 v43, a79
v_accvgpr_read_b32 v44, a92
v_accvgpr_read_b32 v45, a93
v_accvgpr_read_b32 v46, a94
v_accvgpr_read_b32 v47, a95
v_mul_f32 v40, v40, v239
v_mul_f32 v41, v41, v239
v_mul_f32 v42, v42, v239
v_mul_f32 v43, v43, v239
v_mul_f32 v44, v44, v239
v_mul_f32 v45, v45, v239
v_mul_f32 v46, v46, v239
v_mul_f32 v47, v47, v239
v_cvt_pk_bf16_f32 v48, v40, v41
v_cvt_pk_bf16_f32 v49, v42, v43
v_cvt_pk_bf16_f32 v50, v44, v45
v_cvt_pk_bf16_f32 v51, v46, v47
buffer_store_dwordx4 v[48:51], v2, s[56:59], s23 offen offset:2048 sc1
v_accvgpr_read_b32 v52, a108
v_accvgpr_read_b32 v53, a109
v_accvgpr_read_b32 v54, a110
v_accvgpr_read_b32 v55, a111
v_accvgpr_read_b32 v56, a124
v_accvgpr_read_b32 v57, a125
v_accvgpr_read_b32 v58, a126
v_accvgpr_read_b32 v59, a127
v_mul_f32 v52, v52, v239
v_mul_f32 v53, v53, v239
v_mul_f32 v54, v54, v239
v_mul_f32 v55, v55, v239
v_mul_f32 v56, v56, v239
v_mul_f32 v57, v57, v239
v_mul_f32 v58, v58, v239
v_mul_f32 v59, v59, v239
v_cvt_pk_bf16_f32 v60, v52, v53
v_cvt_pk_bf16_f32 v61, v54, v55
v_cvt_pk_bf16_f32 v62, v56, v57
v_cvt_pk_bf16_f32 v63, v58, v59
buffer_store_dwordx4 v[60:63], v2, s[56:59], s23 offen offset:3072 sc1
s_waitcnt vmcnt(0) lgkmcnt(0)
s_nop 15
s_nop 3
s_cmp_eq_u32 s61, 0
s_cbranch_scc1 L_SAFE_RET0
s_branch L_SAFE_RET1
L_FINAL:
s_nop 15
s_nop 3
v_add_u32 v18, 188416, v2
v_add_u32 v9, 16384, v39
s_waitcnt lgkmcnt(0)
ds_read_b128 a[160:163], v4 offset:0
ds_read_b128 a[164:167], v4 offset:1024
ds_read_b128 a[168:171], v4 offset:2048
ds_read_b128 a[172:175], v4 offset:3072
ds_read_b128 a[176:179], v4 offset:4096
ds_read_b128 a[180:183], v4 offset:5120
ds_read_b128 a[184:187], v4 offset:6144
ds_read_b128 a[188:191], v4 offset:7168
s_waitcnt vmcnt(0) lgkmcnt(0)
s_barrier
v_mfma_f32_16x16x32_bf16 a[0:3], a[160:163], v[192:195], a[0:3]
ds_read_b128 a[128:131], v4 offset:8192
v_cvt_pk_bf16_f32 v208, v160, v161
v_mfma_f32_16x16x32_bf16 a[4:7], a[160:163], v[196:199], a[4:7]
v_exp_f32 v176, v176
v_mfma_f32_16x16x32_bf16 a[8:11], a[160:163], v[200:203], a[8:11]
ds_read_b128 a[132:135], v4 offset:9216
v_cvt_pk_bf16_f32 v209, v162, v163
v_mfma_f32_16x16x32_bf16 a[12:15], a[160:163], v[204:207], a[12:15]
v_exp_f32 v177, v177
v_mfma_f32_16x16x32_bf16 a[16:19], a[164:167], v[192:195], a[16:19]
global_load_dwordx4 v[10:13], v39, s[82:83]
v_mfma_f32_16x16x32_bf16 a[20:23], a[164:167], v[196:199], a[20:23]
ds_read_b128 a[136:139], v4 offset:10240
v_cvt_pk_bf16_f32 v212, v164, v165
v_mfma_f32_16x16x32_bf16 a[24:27], a[164:167], v[200:203], a[24:27]
ds_read_b128 a[140:143], v4 offset:11264
v_cvt_pk_bf16_f32 v213, v166, v167
v_mfma_f32_16x16x32_bf16 a[28:31], a[164:167], v[204:207], a[28:31]
v_exp_f32 v178, v178
v_mfma_f32_16x16x32_bf16 a[32:35], a[168:171], v[192:195], a[32:35]
global_load_dwordx4 v[14:17], v9, s[82:83]
v_mfma_f32_16x16x32_bf16 a[36:39], a[168:171], v[196:199], a[36:39]
ds_read_b128 a[144:147], v4 offset:12288
v_cvt_pk_bf16_f32 v216, v168, v169
v_mfma_f32_16x16x32_bf16 a[40:43], a[168:171], v[200:203], a[40:43]
ds_read_b128 a[148:151], v4 offset:13312
v_cvt_pk_bf16_f32 v217, v170, v171
v_mfma_f32_16x16x32_bf16 a[44:47], a[168:171], v[204:207], a[44:47]
v_exp_f32 v179, v179
v_mfma_f32_16x16x32_bf16 a[48:51], a[172:175], v[192:195], a[48:51]
s_mov_b32 s28, 0
s_add_u32 m0, s89, 0
s_nop 0
buffer_load_dwordx4 v32, s[8:11], s28 offen lds
v_mfma_f32_16x16x32_bf16 a[52:55], a[172:175], v[196:199], a[52:55]
ds_read_b128 a[152:155], v4 offset:14336
v_cvt_pk_bf16_f32 v220, v172, v173
v_mfma_f32_16x16x32_bf16 a[56:59], a[172:175], v[200:203], a[56:59]
v_exp_f32 v180, v180
v_mfma_f32_16x16x32_bf16 a[60:63], a[172:175], v[204:207], a[60:63]
v_exp_f32 v181, v181
v_mfma_f32_16x16x32_bf16 a[192:195], v[224:227], v[192:195], a[192:195]
buffer_load_dwordx4 v33, s[8:11], s28 offen offset:1024 lds
v_mfma_f32_16x16x32_bf16 a[196:199], v[224:227], v[196:199], a[196:199]
v_exp_f32 v182, v182
v_mfma_f32_16x16x32_bf16 a[64:67], a[176:179], v[192:195], a[64:67]
v_exp_f32 v183, v183
v_mfma_f32_16x16x32_bf16 a[68:71], a[176:179], v[196:199], a[68:71]
v_exp_f32 v184, v184
v_mfma_f32_16x16x32_bf16 a[72:75], a[176:179], v[200:203], a[72:75]
ds_read_b128 a[156:159], v4 offset:15360
v_exp_f32 v185, v185
v_exp_f32 v186, v186
v_exp_f32 v187, v187
v_exp_f32 v188, v188
v_exp_f32 v189, v189
v_exp_f32 v190, v190
v_exp_f32 v191, v191
v_mfma_f32_16x16x32_bf16 a[76:79], a[176:179], v[204:207], a[76:79]
buffer_load_dwordx4 v34, s[8:11], s28 offen offset:2048 lds
v_mfma_f32_16x16x32_bf16 a[80:83], a[180:183], v[192:195], a[80:83]
v_cvt_pk_bf16_f32 v221, v174, v175
v_cvt_pk_bf16_f32 v210, v176, v177
v_mfma_f32_16x16x32_bf16 a[84:87], a[180:183], v[196:199], a[84:87]
v_cvt_pk_bf16_f32 v211, v178, v179
v_cvt_pk_bf16_f32 v214, v180, v181
v_mfma_f32_16x16x32_bf16 a[88:91], a[180:183], v[200:203], a[88:91]
buffer_load_dwordx4 v35, s[8:11], s28 offen offset:3072 lds
v_mfma_f32_16x16x32_bf16 a[92:95], a[180:183], v[204:207], a[92:95]
v_cvt_pk_bf16_f32 v215, v182, v183
v_cvt_pk_bf16_f32 v218, v184, v185
v_mfma_f32_16x16x32_bf16 a[96:99], a[184:187], v[192:195], a[96:99]
v_cvt_pk_bf16_f32 v219, v186, v187
v_cvt_pk_bf16_f32 v222, v188, v189
v_mfma_f32_16x16x32_bf16 a[100:103], a[184:187], v[196:199], a[100:103]
v_cvt_pk_bf16_f32 v223, v190, v191
v_mfma_f32_16x16x32_bf16 a[104:107], a[184:187], v[200:203], a[104:107]
s_mov_b32 s28, 65536
s_add_u32 m0, s89, 16384
s_nop 0
buffer_load_dwordx4 v32, s[8:11], s28 offen lds
v_mfma_f32_16x16x32_bf16 a[108:111], a[184:187], v[204:207], a[108:111]
v_mfma_f32_16x16x32_bf16 a[112:115], a[188:191], v[192:195], a[112:115]
v_mfma_f32_16x16x32_bf16 a[116:119], a[188:191], v[196:199], a[116:119]
v_mfma_f32_16x16x32_bf16 a[120:123], a[188:191], v[200:203], a[120:123]
buffer_load_dwordx4 v33, s[8:11], s28 offen offset:1024 lds
v_mfma_f32_16x16x32_bf16 a[124:127], a[188:191], v[204:207], a[124:127]
v_mfma_f32_16x16x32_bf16 a[200:203], v[224:227], v[200:203], a[200:203]
v_mfma_f32_16x16x32_bf16 a[204:207], v[224:227], v[204:207], a[204:207]
s_waitcnt lgkmcnt(0)
v_mfma_f32_16x16x32_bf16 a[0:3], a[128:131], v[208:211], a[0:3]
v_mfma_f32_16x16x32_bf16 a[4:7], a[128:131], v[212:215], a[4:7]
buffer_load_dwordx4 v34, s[8:11], s28 offen offset:2048 lds
v_mfma_f32_16x16x32_bf16 a[8:11], a[128:131], v[216:219], a[8:11]
v_mfma_f32_16x16x32_bf16 a[12:15], a[128:131], v[220:223], a[12:15]
v_mfma_f32_16x16x32_bf16 a[16:19], a[132:135], v[208:211], a[16:19]
s_lshr_b32 s22, s7, 1
s_lshl_b32 s22, s22, 16
v_mfma_f32_16x16x32_bf16 a[20:23], a[132:135], v[212:215], a[20:23]
buffer_load_dwordx4 v35, s[8:11], s28 offen offset:3072 lds
v_mfma_f32_16x16x32_bf16 a[24:27], a[132:135], v[216:219], a[24:27]
s_lshl_b32 s23, s75, 14
s_add_u32 s23, s23, 32768
v_mfma_f32_16x16x32_bf16 a[28:31], a[132:135], v[220:223], a[28:31]
s_add_u32 s24, s22, s23
v_add_u32 v20, s24, v2
v_mfma_f32_16x16x32_bf16 a[32:35], a[136:139], v[208:211], a[32:35]
s_xor_b32 s22, s22, 65536
s_add_u32 s24, s22, s23
v_mfma_f32_16x16x32_bf16 a[36:39], a[136:139], v[212:215], a[36:39]
s_mov_b32 s28, 131072
s_add_u32 m0, s89, 65536
s_nop 0
buffer_load_dwordx4 v32, s[8:11], s28 offen lds
v_mfma_f32_16x16x32_bf16 a[40:43], a[136:139], v[216:219], a[40:43]
v_add_u32 v21, s24, v2
s_lshl_b32 s22, s7, 10
v_mfma_f32_16x16x32_bf16 a[44:47], a[136:139], v[220:223], a[44:47]
s_add_u32 s22, s22, 131072
v_add_u32 v22, s22, v2
v_mfma_f32_16x16x32_bf16 a[48:51], a[140:143], v[208:211], a[48:51]
s_xor_b32 s22, s22, 2048
v_add_u32 v23, s22, v2
v_mfma_f32_16x16x32_bf16 a[52:55], a[140:143], v[212:215], a[52:55]
buffer_load_dwordx4 v33, s[8:11], s28 offen offset:1024 lds
v_mfma_f32_16x16x32_bf16 a[56:59], a[140:143], v[216:219], a[56:59]
v_mfma_f32_16x16x32_bf16 a[60:63], a[140:143], v[220:223], a[60:63]
v_mfma_f32_16x16x32_bf16 a[192:195], v[224:227], v[208:211], a[192:195]
v_mfma_f32_16x16x32_bf16 a[196:199], v[224:227], v[212:215], a[196:199]
buffer_load_dwordx4 v34, s[8:11], s28 offen offset:2048 lds
v_mfma_f32_16x16x32_bf16 a[64:67], a[144:147], v[208:211], a[64:67]
v_mfma_f32_16x16x32_bf16 a[68:71], a[144:147], v[212:215], a[68:71]
v_mfma_f32_16x16x32_bf16 a[72:75], a[144:147], v[216:219], a[72:75]
v_mfma_f32_16x16x32_bf16 a[76:79], a[144:147], v[220:223], a[76:79]
buffer_load_dwordx4 v35, s[8:11], s28 offen offset:3072 lds
v_mfma_f32_16x16x32_bf16 a[80:83], a[148:151], v[208:211], a[80:83]
v_mfma_f32_16x16x32_bf16 a[84:87], a[148:151], v[212:215], a[84:87]
v_mfma_f32_16x16x32_bf16 a[88:91], a[148:151], v[216:219], a[88:91]
v_mfma_f32_16x16x32_bf16 a[92:95], a[148:151], v[220:223], a[92:95]
s_add_u32 s29, s88, 0
buffer_load_dwordx4 a[208:211], v2, s[84:87], s29 offen offset:0 sc1
v_mfma_f32_16x16x32_bf16 a[96:99], a[152:155], v[208:211], a[96:99]
v_mfma_f32_16x16x32_bf16 a[100:103], a[152:155], v[212:215], a[100:103]
v_mfma_f32_16x16x32_bf16 a[104:107], a[152:155], v[216:219], a[104:107]
v_mfma_f32_16x16x32_bf16 a[108:111], a[152:155], v[220:223], a[108:111]
buffer_load_dwordx4 a[212:215], v18, s[84:87], s29 offen offset:0 sc1
v_mfma_f32_16x16x32_bf16 a[112:115], a[156:159], v[208:211], a[112:115]
v_mfma_f32_16x16x32_bf16 a[116:119], a[156:159], v[212:215], a[116:119]
v_mfma_f32_16x16x32_bf16 a[120:123], a[156:159], v[216:219], a[120:123]
v_mfma_f32_16x16x32_bf16 a[124:127], a[156:159], v[220:223], a[124:127]
v_mfma_f32_16x16x32_bf16 a[200:203], v[224:227], v[216:219], a[200:203]
v_mfma_f32_16x16x32_bf16 a[204:207], v[224:227], v[220:223], a[204:207]
s_nop 9
s_barrier
v_accvgpr_read_b32 v24, a192
v_accvgpr_read_b32 v25, a196
v_accvgpr_read_b32 v26, a200
v_accvgpr_read_b32 v27, a204
ds_write_b128 v22, v[24:27]
s_cmp_eq_u32 s36, 1
s_cbranch_scc0 L_CB_B
ds_write_b128 v20, a[64:67] offset:0
ds_write_b128 v20, a[68:71] offset:1024
buffer_load_dwordx4 a[216:219], v2, s[84:87], s29 offen offset:1024 sc1
ds_write_b128 v20, a[72:75] offset:2048
ds_write_b128 v20, a[76:79] offset:3072
buffer_load_dwordx4 a[220:223], v18, s[84:87], s29 offen offset:1024 sc1
ds_write_b128 v20, a[80:83] offset:4096
ds_write_b128 v20, a[84:87] offset:5120
s_mov_b32 s28, 196608
s_add_u32 m0, s89, 81920
s_nop 0
buffer_load_dwordx4 v32, s[8:11], s28 offen lds
ds_write_b128 v20, a[88:91] offset:6144
ds_write_b128 v20, a[92:95] offset:7168
buffer_load_dwordx4 v33, s[8:11], s28 offen offset:1024 lds
ds_write_b128 v20, a[96:99] offset:8192
ds_write_b128 v20, a[100:103] offset:9216
buffer_load_dwordx4 v34, s[8:11], s28 offen offset:2048 lds
ds_write_b128 v20, a[104:107] offset:10240
ds_write_b128 v20, a[108:111] offset:11264
buffer_load_dwordx4 v35, s[8:11], s28 offen offset:3072 lds
ds_write_b128 v20, a[112:115] offset:12288
ds_write_b128 v20, a[116:119] offset:13312
buffer_load_dwordx4 a[224:227], v2, s[84:87], s29 offen offset:2048 sc1
ds_write_b128 v20, a[120:123] offset:14336
ds_write_b128 v20, a[124:127] offset:15360
buffer_load_dwordx4 a[228:231], v18, s[84:87], s29 offen offset:2048 sc1
s_waitcnt lgkmcnt(0)
s_waitcnt vmcnt(18)
s_barrier
ds_read_b128 v[28:31], v23
ds_read_b128 v[128:131], v21 offset:0
ds_read_b128 v[144:147], v21 offset:4096
ds_read_b128 v[160:163], v21 offset:8192
ds_read_b128 v[176:179], v21 offset:12288
ds_read_b128 v[132:135], v21 offset:1024
ds_read_b128 v[148:151], v21 offset:5120
ds_read_b128 v[164:167], v21 offset:9216
ds_read_b128 v[180:183], v21 offset:13312
ds_read_b128 v[136:139], v21 offset:2048
ds_read_b128 v[152:155], v21 offset:6144
ds_read_b128 v[168:171], v21 offset:10240
ds_read_b128 v[184:187], v21 offset:14336
ds_read_b128 v[140:143], v21 offset:3072
ds_read_b128 v[156:159], v21 offset:7168
ds_read_b128 v[172:175], v21 offset:11264
ds_read_b128 v[188:191], v21 offset:15360
buffer_load_dwordx4 a[232:235], v2, s[84:87], s29 offen offset:3072 sc1
buffer_load_dwordx4 a[236:239], v18, s[84:87], s29 offen offset:3072 sc1
s_add_u32 s29, s88, 4096
buffer_load_dwordx4 a[240:243], v2, s[84:87], s29 offen offset:0 sc1
)ASM"
R"ASM(buffer_load_dwordx4 a[244:247], v18, s[84:87], s29 offen offset:0 sc1
buffer_load_dwordx4 a[248:251], v2, s[84:87], s29 offen offset:1024 sc1
buffer_load_dwordx4 a[252:255], v18, s[84:87], s29 offen offset:1024 sc1
s_waitcnt lgkmcnt(15)
v_add_f32 v200, v24, v28
v_add_f32 v201, v25, v29
v_add_f32 v202, v26, v30
v_add_f32 v203, v27, v31
v_max3_f32 v248, v200, v201, v202
v_max_f32 v248, v248, v203
v_min3_f32 v249, v200, v201, v202
v_min_f32 v249, v249, v203
v_add_f32 v250, v200, v201
v_add_f32 v251, v202, v203
v_add_f32 v250, v250, v251
v_mov_b32 v252, 0x53800000
v_mov_b32 v253, 0x2b800000
v_cmp_nle_f32 s[46:47], v248, v252
v_cmp_nge_f32 s[30:31], v249, v253
v_cmp_u_f32 vcc, v250, v250
s_nop 2
s_or_b64 s[46:47], s[46:47], s[30:31]
s_nop 0
s_or_b64 vcc, vcc, s[46:47]
s_cbranch_scc1 L_CB_BAD
v_rcp_f32 v200, v200
v_rcp_f32 v201, v201
v_rcp_f32 v202, v202
v_rcp_f32 v203, v203
v_accvgpr_read_b32 v40, a0
v_accvgpr_read_b32 v41, a1
v_accvgpr_read_b32 v42, a2
v_accvgpr_read_b32 v43, a3
v_accvgpr_read_b32 v44, a16
v_accvgpr_read_b32 v45, a17
v_accvgpr_read_b32 v46, a18
v_accvgpr_read_b32 v47, a19
v_add_f32 v40, v128, v40
v_add_f32 v41, v129, v41
v_add_f32 v42, v130, v42
v_add_f32 v43, v131, v43
s_waitcnt lgkmcnt(14)
v_add_f32 v44, v144, v44
v_add_f32 v45, v145, v45
v_add_f32 v46, v146, v46
v_add_f32 v47, v147, v47
v_mul_f32 v40, v40, v200
v_mul_f32 v41, v41, v200
v_mul_f32 v42, v42, v200
v_mul_f32 v43, v43, v200
v_mul_f32 v44, v44, v200
v_mul_f32 v45, v45, v200
v_mul_f32 v46, v46, v200
v_mul_f32 v47, v47, v200
v_cvt_pk_bf16_f32 v48, v40, v41
v_cvt_pk_bf16_f32 v49, v42, v43
v_cvt_pk_bf16_f32 v50, v44, v45
v_cvt_pk_bf16_f32 v51, v46, v47
s_mov_b32 s23, 0
buffer_store_dwordx4 v[48:51], v2, s[56:59], s23 offen offset:0 sc1
v_accvgpr_read_b32 v52, a32
v_accvgpr_read_b32 v53, a33
v_accvgpr_read_b32 v54, a34
v_accvgpr_read_b32 v55, a35
v_accvgpr_read_b32 v56, a48
v_accvgpr_read_b32 v57, a49
v_accvgpr_read_b32 v58, a50
v_accvgpr_read_b32 v59, a51
s_waitcnt lgkmcnt(13)
v_add_f32 v52, v160, v52
v_add_f32 v53, v161, v53
v_add_f32 v54, v162, v54
v_add_f32 v55, v163, v55
s_waitcnt lgkmcnt(12)
v_add_f32 v56, v176, v56
v_add_f32 v57, v177, v57
v_add_f32 v58, v178, v58
v_add_f32 v59, v179, v59
v_mul_f32 v52, v52, v200
v_mul_f32 v53, v53, v200
v_mul_f32 v54, v54, v200
v_mul_f32 v55, v55, v200
v_mul_f32 v56, v56, v200
v_mul_f32 v57, v57, v200
v_mul_f32 v58, v58, v200
v_mul_f32 v59, v59, v200
v_cvt_pk_bf16_f32 v60, v52, v53
v_cvt_pk_bf16_f32 v61, v54, v55
v_cvt_pk_bf16_f32 v62, v56, v57
v_cvt_pk_bf16_f32 v63, v58, v59
buffer_store_dwordx4 v[60:63], v2, s[56:59], s23 offen offset:1024 sc1
v_accvgpr_read_b32 v40, a4
v_accvgpr_read_b32 v41, a5
v_accvgpr_read_b32 v42, a6
v_accvgpr_read_b32 v43, a7
v_accvgpr_read_b32 v44, a20
v_accvgpr_read_b32 v45, a21
v_accvgpr_read_b32 v46, a22
v_accvgpr_read_b32 v47, a23
s_waitcnt lgkmcnt(11)
v_add_f32 v40, v132, v40
v_add_f32 v41, v133, v41
v_add_f32 v42, v134, v42
v_add_f32 v43, v135, v43
s_waitcnt lgkmcnt(10)
v_add_f32 v44, v148, v44
v_add_f32 v45, v149, v45
v_add_f32 v46, v150, v46
v_add_f32 v47, v151, v47
v_mul_f32 v40, v40, v201
v_mul_f32 v41, v41, v201
v_mul_f32 v42, v42, v201
v_mul_f32 v43, v43, v201
v_mul_f32 v44, v44, v201
v_mul_f32 v45, v45, v201
v_mul_f32 v46, v46, v201
v_mul_f32 v47, v47, v201
v_cvt_pk_bf16_f32 v48, v40, v41
v_cvt_pk_bf16_f32 v49, v42, v43
v_cvt_pk_bf16_f32 v50, v44, v45
v_cvt_pk_bf16_f32 v51, v46, v47
s_mov_b32 s23, 376832
buffer_store_dwordx4 v[48:51], v2, s[56:59], s23 offen offset:0 sc1
v_accvgpr_read_b32 v52, a36
v_accvgpr_read_b32 v53, a37
v_accvgpr_read_b32 v54, a38
v_accvgpr_read_b32 v55, a39
v_accvgpr_read_b32 v56, a52
v_accvgpr_read_b32 v57, a53
v_accvgpr_read_b32 v58, a54
v_accvgpr_read_b32 v59, a55
s_waitcnt lgkmcnt(9)
v_add_f32 v52, v164, v52
v_add_f32 v53, v165, v53
v_add_f32 v54, v166, v54
v_add_f32 v55, v167, v55
s_waitcnt lgkmcnt(8)
v_add_f32 v56, v180, v56
v_add_f32 v57, v181, v57
v_add_f32 v58, v182, v58
v_add_f32 v59, v183, v59
v_mul_f32 v52, v52, v201
v_mul_f32 v53, v53, v201
v_mul_f32 v54, v54, v201
v_mul_f32 v55, v55, v201
v_mul_f32 v56, v56, v201
v_mul_f32 v57, v57, v201
v_mul_f32 v58, v58, v201
v_mul_f32 v59, v59, v201
v_cvt_pk_bf16_f32 v60, v52, v53
v_cvt_pk_bf16_f32 v61, v54, v55
v_cvt_pk_bf16_f32 v62, v56, v57
v_cvt_pk_bf16_f32 v63, v58, v59
buffer_store_dwordx4 v[60:63], v2, s[56:59], s23 offen offset:1024 sc1
v_accvgpr_read_b32 v40, a8
v_accvgpr_read_b32 v41, a9
v_accvgpr_read_b32 v42, a10
v_accvgpr_read_b32 v43, a11
v_accvgpr_read_b32 v44, a24
v_accvgpr_read_b32 v45, a25
v_accvgpr_read_b32 v46, a26
v_accvgpr_read_b32 v47, a27
s_waitcnt lgkmcnt(7)
v_add_f32 v40, v136, v40
v_add_f32 v41, v137, v41
v_add_f32 v42, v138, v42
v_add_f32 v43, v139, v43
s_waitcnt lgkmcnt(6)
v_add_f32 v44, v152, v44
v_add_f32 v45, v153, v45
v_add_f32 v46, v154, v46
v_add_f32 v47, v155, v47
v_mul_f32 v40, v40, v202
v_mul_f32 v41, v41, v202
v_mul_f32 v42, v42, v202
v_mul_f32 v43, v43, v202
v_mul_f32 v44, v44, v202
v_mul_f32 v45, v45, v202
v_mul_f32 v46, v46, v202
v_mul_f32 v47, v47, v202
v_cvt_pk_bf16_f32 v48, v40, v41
v_cvt_pk_bf16_f32 v49, v42, v43
v_cvt_pk_bf16_f32 v50, v44, v45
v_cvt_pk_bf16_f32 v51, v46, v47
s_mov_b32 s23, 753664
buffer_store_dwordx4 v[48:51], v2, s[56:59], s23 offen offset:0 sc1
v_accvgpr_read_b32 v52, a40
v_accvgpr_read_b32 v53, a41
v_accvgpr_read_b32 v54, a42
v_accvgpr_read_b32 v55, a43
v_accvgpr_read_b32 v56, a56
v_accvgpr_read_b32 v57, a57
v_accvgpr_read_b32 v58, a58
v_accvgpr_read_b32 v59, a59
s_waitcnt lgkmcnt(5)
v_add_f32 v52, v168, v52
v_add_f32 v53, v169, v53
v_add_f32 v54, v170, v54
v_add_f32 v55, v171, v55
s_waitcnt lgkmcnt(4)
v_add_f32 v56, v184, v56
v_add_f32 v57, v185, v57
v_add_f32 v58, v186, v58
v_add_f32 v59, v187, v59
v_mul_f32 v52, v52, v202
v_mul_f32 v53, v53, v202
v_mul_f32 v54, v54, v202
v_mul_f32 v55, v55, v202
v_mul_f32 v56, v56, v202
v_mul_f32 v57, v57, v202
v_mul_f32 v58, v58, v202
v_mul_f32 v59, v59, v202
v_cvt_pk_bf16_f32 v60, v52, v53
v_cvt_pk_bf16_f32 v61, v54, v55
v_cvt_pk_bf16_f32 v62, v56, v57
v_cvt_pk_bf16_f32 v63, v58, v59
buffer_store_dwordx4 v[60:63], v2, s[56:59], s23 offen offset:1024 sc1
v_accvgpr_read_b32 v40, a12
v_accvgpr_read_b32 v41, a13
v_accvgpr_read_b32 v42, a14
v_accvgpr_read_b32 v43, a15
v_accvgpr_read_b32 v44, a28
v_accvgpr_read_b32 v45, a29
v_accvgpr_read_b32 v46, a30
v_accvgpr_read_b32 v47, a31
s_waitcnt lgkmcnt(3)
v_add_f32 v40, v140, v40
v_add_f32 v41, v141, v41
v_add_f32 v42, v142, v42
v_add_f32 v43, v143, v43
s_waitcnt lgkmcnt(2)
v_add_f32 v44, v156, v44
v_add_f32 v45, v157, v45
v_add_f32 v46, v158, v46
v_add_f32 v47, v159, v47
v_mul_f32 v40, v40, v203
v_mul_f32 v41, v41, v203
v_mul_f32 v42, v42, v203
v_mul_f32 v43, v43, v203
v_mul_f32 v44, v44, v203
v_mul_f32 v45, v45, v203
v_mul_f32 v46, v46, v203
v_mul_f32 v47, v47, v203
v_cvt_pk_bf16_f32 v48, v40, v41
v_cvt_pk_bf16_f32 v49, v42, v43
v_cvt_pk_bf16_f32 v50, v44, v45
v_cvt_pk_bf16_f32 v51, v46, v47
s_mov_b32 s23, 1130496
buffer_store_dwordx4 v[48:51], v2, s[56:59], s23 offen offset:0 sc1
v_accvgpr_read_b32 v52, a44
v_accvgpr_read_b32 v53, a45
v_accvgpr_read_b32 v54, a46
v_accvgpr_read_b32 v55, a47
v_accvgpr_read_b32 v56, a60
v_accvgpr_read_b32 v57, a61
v_accvgpr_read_b32 v58, a62
v_accvgpr_read_b32 v59, a63
s_waitcnt lgkmcnt(1)
v_add_f32 v52, v172, v52
v_add_f32 v53, v173, v53
v_add_f32 v54, v174, v54
v_add_f32 v55, v175, v55
s_waitcnt lgkmcnt(0)
v_add_f32 v56, v188, v56
v_add_f32 v57, v189, v57
v_add_f32 v58, v190, v58
v_add_f32 v59, v191, v59
v_mul_f32 v52, v52, v203
v_mul_f32 v53, v53, v203
v_mul_f32 v54, v54, v203
v_mul_f32 v55, v55, v203
v_mul_f32 v56, v56, v203
v_mul_f32 v57, v57, v203
v_mul_f32 v58, v58, v203
v_mul_f32 v59, v59, v203
v_cvt_pk_bf16_f32 v60, v52, v53
v_cvt_pk_bf16_f32 v61, v54, v55
v_cvt_pk_bf16_f32 v62, v56, v57
v_cvt_pk_bf16_f32 v63, v58, v59
buffer_store_dwordx4 v[60:63], v2, s[56:59], s23 offen offset:1024 sc1
s_branch L_CB_END
L_CB_B:
s_nop 15
s_nop 3
ds_write_b128 v20, a[0:3] offset:0
ds_write_b128 v20, a[4:7] offset:1024
buffer_load_dwordx4 a[216:219], v2, s[84:87], s29 offen offset:1024 sc1
ds_write_b128 v20, a[8:11] offset:2048
ds_write_b128 v20, a[12:15] offset:3072
buffer_load_dwordx4 a[220:223], v18, s[84:87], s29 offen offset:1024 sc1
ds_write_b128 v20, a[16:19] offset:4096
ds_write_b128 v20, a[20:23] offset:5120
s_mov_b32 s28, 196608
s_add_u32 m0, s89, 81920
s_nop 0
buffer_load_dwordx4 v32, s[8:11], s28 offen lds
ds_write_b128 v20, a[24:27] offset:6144
ds_write_b128 v20, a[28:31] offset:7168
buffer_load_dwordx4 v33, s[8:11], s28 offen offset:1024 lds
ds_write_b128 v20, a[32:35] offset:8192
ds_write_b128 v20, a[36:39] offset:9216
buffer_load_dwordx4 v34, s[8:11], s28 offen offset:2048 lds
ds_write_b128 v20, a[40:43] offset:10240
ds_write_b128 v20, a[44:47] offset:11264
buffer_load_dwordx4 v35, s[8:11], s28 offen offset:3072 lds
ds_write_b128 v20, a[48:51] offset:12288
ds_write_b128 v20, a[52:55] offset:13312
buffer_load_dwordx4 a[224:227], v2, s[84:87], s29 offen offset:2048 sc1
ds_write_b128 v20, a[56:59] offset:14336
ds_write_b128 v20, a[60:63] offset:15360
buffer_load_dwordx4 a[228:231], v18, s[84:87], s29 offen offset:2048 sc1
s_waitcnt lgkmcnt(0)
s_waitcnt vmcnt(18)
s_barrier
ds_read_b128 v[28:31], v23
ds_read_b128 v[128:131], v21 offset:0
ds_read_b128 v[144:147], v21 offset:4096
ds_read_b128 v[160:163], v21 offset:8192
ds_read_b128 v[176:179], v21 offset:12288
ds_read_b128 v[132:135], v21 offset:1024
ds_read_b128 v[148:151], v21 offset:5120
ds_read_b128 v[164:167], v21 offset:9216
ds_read_b128 v[180:183], v21 offset:13312
ds_read_b128 v[136:139], v21 offset:2048
ds_read_b128 v[152:155], v21 offset:6144
ds_read_b128 v[168:171], v21 offset:10240
ds_read_b128 v[184:187], v21 offset:14336
ds_read_b128 v[140:143], v21 offset:3072
ds_read_b128 v[156:159], v21 offset:7168
ds_read_b128 v[172:175], v21 offset:11264
ds_read_b128 v[188:191], v21 offset:15360
buffer_load_dwordx4 a[232:235], v2, s[84:87], s29 offen offset:3072 sc1
buffer_load_dwordx4 a[236:239], v18, s[84:87], s29 offen offset:3072 sc1
s_add_u32 s29, s88, 4096
buffer_load_dwordx4 a[240:243], v2, s[84:87], s29 offen offset:0 sc1
buffer_load_dwordx4 a[244:247], v18, s[84:87], s29 offen offset:0 sc1
buffer_load_dwordx4 a[248:251], v2, s[84:87], s29 offen offset:1024 sc1
buffer_load_dwordx4 a[252:255], v18, s[84:87], s29 offen offset:1024 sc1
s_waitcnt lgkmcnt(15)
v_add_f32 v200, v24, v28
v_add_f32 v201, v25, v29
v_add_f32 v202, v26, v30
v_add_f32 v203, v27, v31
v_max3_f32 v248, v200, v201, v202
v_max_f32 v248, v248, v203
v_min3_f32 v249, v200, v201, v202
v_min_f32 v249, v249, v203
v_add_f32 v250, v200, v201
v_add_f32 v251, v202, v203
v_add_f32 v250, v250, v251
v_mov_b32 v252, 0x53800000
v_mov_b32 v253, 0x2b800000
v_cmp_nle_f32 s[46:47], v248, v252
v_cmp_nge_f32 s[30:31], v249, v253
v_cmp_u_f32 vcc, v250, v250
s_nop 2
s_or_b64 s[46:47], s[46:47], s[30:31]
s_nop 0
s_or_b64 vcc, vcc, s[46:47]
s_cbranch_scc1 L_CB_BAD
v_rcp_f32 v200, v200
v_rcp_f32 v201, v201
v_rcp_f32 v202, v202
v_rcp_f32 v203, v203
v_accvgpr_read_b32 v40, a64
v_accvgpr_read_b32 v41, a65
v_accvgpr_read_b32 v42, a66
v_accvgpr_read_b32 v43, a67
v_accvgpr_read_b32 v44, a80
v_accvgpr_read_b32 v45, a81
v_accvgpr_read_b32 v46, a82
v_accvgpr_read_b32 v47, a83
v_add_f32 v40, v128, v40
v_add_f32 v41, v129, v41
v_add_f32 v42, v130, v42
v_add_f32 v43, v131, v43
s_waitcnt lgkmcnt(14)
v_add_f32 v44, v144, v44
v_add_f32 v45, v145, v45
v_add_f32 v46, v146, v46
v_add_f32 v47, v147, v47
v_mul_f32 v40, v40, v200
v_mul_f32 v41, v41, v200
v_mul_f32 v42, v42, v200
v_mul_f32 v43, v43, v200
v_mul_f32 v44, v44, v200
v_mul_f32 v45, v45, v200
v_mul_f32 v46, v46, v200
v_mul_f32 v47, v47, v200
v_cvt_pk_bf16_f32 v48, v40, v41
v_cvt_pk_bf16_f32 v49, v42, v43
v_cvt_pk_bf16_f32 v50, v44, v45
v_cvt_pk_bf16_f32 v51, v46, v47
s_mov_b32 s23, 0
buffer_store_dwordx4 v[48:51], v2, s[56:59], s23 offen offset:2048 sc1
v_accvgpr_read_b32 v52, a96
v_accvgpr_read_b32 v53, a97
v_accvgpr_read_b32 v54, a98
v_accvgpr_read_b32 v55, a99
v_accvgpr_read_b32 v56, a112
v_accvgpr_read_b32 v57, a113
v_accvgpr_read_b32 v58, a114
v_accvgpr_read_b32 v59, a115
s_waitcnt lgkmcnt(13)
v_add_f32 v52, v160, v52
v_add_f32 v53, v161, v53
v_add_f32 v54, v162, v54
v_add_f32 v55, v163, v55
s_waitcnt lgkmcnt(12)
v_add_f32 v56, v176, v56
v_add_f32 v57, v177, v57
v_add_f32 v58, v178, v58
v_add_f32 v59, v179, v59
v_mul_f32 v52, v52, v200
v_mul_f32 v53, v53, v200
v_mul_f32 v54, v54, v200
v_mul_f32 v55, v55, v200
v_mul_f32 v56, v56, v200
v_mul_f32 v57, v57, v200
v_mul_f32 v58, v58, v200
v_mul_f32 v59, v59, v200
v_cvt_pk_bf16_f32 v60, v52, v53
v_cvt_pk_bf16_f32 v61, v54, v55
v_cvt_pk_bf16_f32 v62, v56, v57
v_cvt_pk_bf16_f32 v63, v58, v59
buffer_store_dwordx4 v[60:63], v2, s[56:59], s23 offen offset:3072 sc1
v_accvgpr_read_b32 v40, a68
v_accvgpr_read_b32 v41, a69
v_accvgpr_read_b32 v42, a70
v_accvgpr_read_b32 v43, a71
v_accvgpr_read_b32 v44, a84
v_accvgpr_read_b32 v45, a85
v_accvgpr_read_b32 v46, a86
v_accvgpr_read_b32 v47, a87
s_waitcnt lgkmcnt(11)
v_add_f32 v40, v132, v40
v_add_f32 v41, v133, v41
v_add_f32 v42, v134, v42
v_add_f32 v43, v135, v43
s_waitcnt lgkmcnt(10)
v_add_f32 v44, v148, v44
v_add_f32 v45, v149, v45
v_add_f32 v46, v150, v46
v_add_f32 v47, v151, v47
v_mul_f32 v40, v40, v201
v_mul_f32 v41, v41, v201
v_mul_f32 v42, v42, v201
v_mul_f32 v43, v43, v201
v_mul_f32 v44, v44, v201
v_mul_f32 v45, v45, v201
v_mul_f32 v46, v46, v201
v_mul_f32 v47, v47, v201
v_cvt_pk_bf16_f32 v48, v40, v41
v_cvt_pk_bf16_f32 v49, v42, v43
v_cvt_pk_bf16_f32 v50, v44, v45
v_cvt_pk_bf16_f32 v51, v46, v47
s_mov_b32 s23, 376832
buffer_store_dwordx4 v[48:51], v2, s[56:59], s23 offen offset:2048 sc1
v_accvgpr_read_b32 v52, a100
v_accvgpr_read_b32 v53, a101
v_accvgpr_read_b32 v54, a102
v_accvgpr_read_b32 v55, a103
v_accvgpr_read_b32 v56, a116
v_accvgpr_read_b32 v57, a117
v_accvgpr_read_b32 v58, a118
v_accvgpr_read_b32 v59, a119
s_waitcnt lgkmcnt(9)
v_add_f32 v52, v164, v52
v_add_f32 v53, v165, v53
v_add_f32 v54, v166, v54
v_add_f32 v55, v167, v55
s_waitcnt lgkmcnt(8)
v_add_f32 v56, v180, v56
v_add_f32 v57, v181, v57
v_add_f32 v58, v182, v58
v_add_f32 v59, v183, v59
v_mul_f32 v52, v52, v201
v_mul_f32 v53, v53, v201
v_mul_f32 v54, v54, v201
v_mul_f32 v55, v55, v201
v_mul_f32 v56, v56, v201
v_mul_f32 v57, v57, v201
v_mul_f32 v58, v58, v201
v_mul_f32 v59, v59, v201
v_cvt_pk_bf16_f32 v60, v52, v53
v_cvt_pk_bf16_f32 v61, v54, v55
v_cvt_pk_bf16_f32 v62, v56, v57
v_cvt_pk_bf16_f32 v63, v58, v59
buffer_store_dwordx4 v[60:63], v2, s[56:59], s23 offen offset:3072 sc1
v_accvgpr_read_b32 v40, a72
v_accvgpr_read_b32 v41, a73
v_accvgpr_read_b32 v42, a74
v_accvgpr_read_b32 v43, a75
v_accvgpr_read_b32 v44, a88
v_accvgpr_read_b32 v45, a89
v_accvgpr_read_b32 v46, a90
v_accvgpr_read_b32 v47, a91
s_waitcnt lgkmcnt(7)
v_add_f32 v40, v136, v40
v_add_f32 v41, v137, v41
v_add_f32 v42, v138, v42
v_add_f32 v43, v139, v43
s_waitcnt lgkmcnt(6)
v_add_f32 v44, v152, v44
v_add_f32 v45, v153, v45
v_add_f32 v46, v154, v46
v_add_f32 v47, v155, v47
v_mul_f32 v40, v40, v202
v_mul_f32 v41, v41, v202
v_mul_f32 v42, v42, v202
v_mul_f32 v43, v43, v202
v_mul_f32 v44, v44, v202
v_mul_f32 v45, v45, v202
v_mul_f32 v46, v46, v202
v_mul_f32 v47, v47, v202
v_cvt_pk_bf16_f32 v48, v40, v41
v_cvt_pk_bf16_f32 v49, v42, v43
v_cvt_pk_bf16_f32 v50, v44, v45
v_cvt_pk_bf16_f32 v51, v46, v47
s_mov_b32 s23, 753664
buffer_store_dwordx4 v[48:51], v2, s[56:59], s23 offen offset:2048 sc1
v_accvgpr_read_b32 v52, a104
v_accvgpr_read_b32 v53, a105
v_accvgpr_read_b32 v54, a106
v_accvgpr_read_b32 v55, a107
v_accvgpr_read_b32 v56, a120
v_accvgpr_read_b32 v57, a121
v_accvgpr_read_b32 v58, a122
v_accvgpr_read_b32 v59, a123
s_waitcnt lgkmcnt(5)
v_add_f32 v52, v168, v52
v_add_f32 v53, v169, v53
v_add_f32 v54, v170, v54
v_add_f32 v55, v171, v55
s_waitcnt lgkmcnt(4)
v_add_f32 v56, v184, v56
v_add_f32 v57, v185, v57
v_add_f32 v58, v186, v58
v_add_f32 v59, v187, v59
v_mul_f32 v52, v52, v202
v_mul_f32 v53, v53, v202
v_mul_f32 v54, v54, v202
v_mul_f32 v55, v55, v202
v_mul_f32 v56, v56, v202
v_mul_f32 v57, v57, v202
v_mul_f32 v58, v58, v202
v_mul_f32 v59, v59, v202
v_cvt_pk_bf16_f32 v60, v52, v53
v_cvt_pk_bf16_f32 v61, v54, v55
v_cvt_pk_bf16_f32 v62, v56, v57
v_cvt_pk_bf16_f32 v63, v58, v59
buffer_store_dwordx4 v[60:63], v2, s[56:59], s23 offen offset:3072 sc1
v_accvgpr_read_b32 v40, a76
v_accvgpr_read_b32 v41, a77
v_accvgpr_read_b32 v42, a78
v_accvgpr_read_b32 v43, a79
v_accvgpr_read_b32 v44, a92
v_accvgpr_read_b32 v45, a93
v_accvgpr_read_b32 v46, a94
v_accvgpr_read_b32 v47, a95
s_waitcnt lgkmcnt(3)
v_add_f32 v40, v140, v40
v_add_f32 v41, v141, v41
v_add_f32 v42, v142, v42
v_add_f32 v43, v143, v43
s_waitcnt lgkmcnt(2)
v_add_f32 v44, v156, v44
v_add_f32 v45, v157, v45
v_add_f32 v46, v158, v46
v_add_f32 v47, v159, v47
v_mul_f32 v40, v40, v203
v_mul_f32 v41, v41, v203
v_mul_f32 v42, v42, v203
v_mul_f32 v43, v43, v203
v_mul_f32 v44, v44, v203
v_mul_f32 v45, v45, v203
v_mul_f32 v46, v46, v203
v_mul_f32 v47, v47, v203
v_cvt_pk_bf16_f32 v48, v40, v41
v_cvt_pk_bf16_f32 v49, v42, v43
v_cvt_pk_bf16_f32 v50, v44, v45
v_cvt_pk_bf16_f32 v51, v46, v47
s_mov_b32 s23, 1130496
buffer_store_dwordx4 v[48:51], v2, s[56:59], s23 offen offset:2048 sc1
v_accvgpr_read_b32 v52, a108
v_accvgpr_read_b32 v53, a109
v_accvgpr_read_b32 v54, a110
v_accvgpr_read_b32 v55, a111
v_accvgpr_read_b32 v56, a124
v_accvgpr_read_b32 v57, a125
v_accvgpr_read_b32 v58, a126
v_accvgpr_read_b32 v59, a127
s_waitcnt lgkmcnt(1)
v_add_f32 v52, v172, v52
v_add_f32 v53, v173, v53
v_add_f32 v54, v174, v54
v_add_f32 v55, v175, v55
s_waitcnt lgkmcnt(0)
v_add_f32 v56, v188, v56
v_add_f32 v57, v189, v57
v_add_f32 v58, v190, v58
v_add_f32 v59, v191, v59
v_mul_f32 v52, v52, v203
v_mul_f32 v53, v53, v203
v_mul_f32 v54, v54, v203
v_mul_f32 v55, v55, v203
v_mul_f32 v56, v56, v203
v_mul_f32 v57, v57, v203
v_mul_f32 v58, v58, v203
v_mul_f32 v59, v59, v203
v_cvt_pk_bf16_f32 v60, v52, v53
v_cvt_pk_bf16_f32 v61, v54, v55
v_cvt_pk_bf16_f32 v62, v56, v57
v_cvt_pk_bf16_f32 v63, v58, v59
buffer_store_dwordx4 v[60:63], v2, s[56:59], s23 offen offset:3072 sc1
s_branch L_CB_END
L_CB_BAD:
s_waitcnt vmcnt(0)
s_cmp_eq_u32 s36, 1
s_cbranch_scc0 L_SAFE_RET1
s_sub_u32 s22, 15, s44
s_lshl_b32 s22, s22, 3
s_lshl_b32 s23, s75, 0
s_add_u32 s50, s22, s23
s_mov_b32 s61, 1
s_branch L_SAFE
L_SAFE_RET1:
s_add_u32 s29, s88, 0
buffer_load_dwordx4 a[208:211], v2, s[84:87], s29 offen offset:0 sc1
buffer_load_dwordx4 a[212:215], v18, s[84:87], s29 offen offset:0 sc1
buffer_load_dwordx4 a[216:219], v2, s[84:87], s29 offen offset:1024 sc1
buffer_load_dwordx4 a[220:223], v18, s[84:87], s29 offen offset:1024 sc1
buffer_load_dwordx4 a[224:227], v2, s[84:87], s29 offen offset:2048 sc1
buffer_load_dwordx4 a[228:231], v18, s[84:87], s29 offen offset:2048 sc1
buffer_load_dwordx4 a[232:235], v2, s[84:87], s29 offen offset:3072 sc1
buffer_load_dwordx4 a[236:239], v18, s[84:87], s29 offen offset:3072 sc1
s_add_u32 s29, s88, 4096
buffer_load_dwordx4 a[240:243], v2, s[84:87], s29 offen offset:0 sc1
buffer_load_dwordx4 a[244:247], v18, s[84:87], s29 offen offset:0 sc1
buffer_load_dwordx4 a[248:251], v2, s[84:87], s29 offen offset:1024 sc1
buffer_load_dwordx4 a[252:255], v18, s[84:87], s29 offen offset:1024 sc1
s_mov_b32 s28, 196608
s_add_u32 m0, s89, 81920
s_nop 0
buffer_load_dwordx4 v32, s[8:11], s28 offen lds
buffer_load_dwordx4 v33, s[8:11], s28 offen offset:1024 lds
buffer_load_dwordx4 v34, s[8:11], s28 offen offset:2048 lds
buffer_load_dwordx4 v35, s[8:11], s28 offen offset:3072 lds
s_waitcnt vmcnt(0)
L_CB_END:
v_xor_b32 v64, 0, v36
v_xor_b32 v65, 32, v36
v_xor_b32 v66, 64, v36
v_xor_b32 v67, 96, v36
v_xor_b32 v68, 128, v36
v_xor_b32 v69, 160, v36
v_xor_b32 v70, 192, v36
v_xor_b32 v71, 224, v36
L_EM0:
ds_read_b64_tr_b16 v[80:81], v64 offset:0
ds_read_b64_tr_b16 v[82:83], v64 offset:4096
ds_read_b64_tr_b16 v[84:85], v65 offset:0
ds_read_b64_tr_b16 v[86:87], v65 offset:4096
ds_read_b64_tr_b16 v[88:89], v66 offset:0
ds_read_b64_tr_b16 v[90:91], v66 offset:4096
ds_read_b64_tr_b16 v[92:93], v67 offset:0
ds_read_b64_tr_b16 v[94:95], v67 offset:4096
ds_read_b64_tr_b16 v[96:97], v68 offset:0
ds_read_b64_tr_b16 v[98:99], v68 offset:4096
ds_read_b64_tr_b16 v[100:101], v69 offset:0
ds_read_b64_tr_b16 v[102:103], v69 offset:4096
s_waitcnt vmcnt(23) lgkmcnt(10)
v_mfma_f32_16x16x32_bf16 a[0:3], v[80:83], a[208:211], 0
ds_read_b64_tr_b16 v[104:105], v70 offset:0
ds_read_b64_tr_b16 v[106:107], v70 offset:4096
s_waitcnt vmcnt(22)
v_mfma_f32_16x16x32_bf16 a[4:7], v[80:83], a[212:215], 0
v_add_u32 v72, 65536, v64
s_waitcnt lgkmcnt(10)
v_mfma_f32_16x16x32_bf16 a[8:11], v[84:87], a[208:211], 0
ds_read_b64_tr_b16 v[108:109], v71 offset:0
ds_read_b64_tr_b16 v[110:111], v71 offset:4096
s_add_u32 s29, s88, 4096
v_mfma_f32_16x16x32_bf16 a[12:15], v[84:87], a[212:215], 0
buffer_load_dwordx4 a[160:163], v2, s[84:87], s29 offen offset:2048 sc1
v_add_u32 v73, 65536, v65
s_waitcnt lgkmcnt(10)
v_mfma_f32_16x16x32_bf16 a[16:19], v[88:91], a[208:211], 0
ds_read_b64_tr_b16 v[80:81], v64 offset:8192
ds_read_b64_tr_b16 v[82:83], v64 offset:12288
v_mfma_f32_16x16x32_bf16 a[20:23], v[88:91], a[212:215], 0
v_add_u32 v74, 65536, v66
s_waitcnt lgkmcnt(10)
v_mfma_f32_16x16x32_bf16 a[24:27], v[92:95], a[208:211], 0
ds_read_b64_tr_b16 v[84:85], v65 offset:8192
ds_read_b64_tr_b16 v[86:87], v65 offset:12288
v_mfma_f32_16x16x32_bf16 a[28:31], v[92:95], a[212:215], 0
buffer_load_dwordx4 a[164:167], v18, s[84:87], s29 offen offset:2048 sc1
v_add_u32 v75, 65536, v67
s_waitcnt lgkmcnt(10)
v_mfma_f32_16x16x32_bf16 a[32:35], v[96:99], a[208:211], 0
ds_read_b64_tr_b16 v[88:89], v66 offset:8192
ds_read_b64_tr_b16 v[90:91], v66 offset:12288
v_mfma_f32_16x16x32_bf16 a[36:39], v[96:99], a[212:215], 0
v_add_u32 v76, 65536, v68
s_waitcnt lgkmcnt(10)
v_mfma_f32_16x16x32_bf16 a[40:43], v[100:103], a[208:211], 0
ds_read_b64_tr_b16 v[92:93], v67 offset:8192
ds_read_b64_tr_b16 v[94:95], v67 offset:12288
v_mfma_f32_16x16x32_bf16 a[44:47], v[100:103], a[212:215], 0
buffer_load_dwordx4 a[168:171], v2, s[84:87], s29 offen offset:3072 sc1
v_add_u32 v77, 65536, v69
s_waitcnt lgkmcnt(10)
v_mfma_f32_16x16x32_bf16 a[48:51], v[104:107], a[208:211], 0
ds_read_b64_tr_b16 v[96:97], v68 offset:8192
ds_read_b64_tr_b16 v[98:99], v68 offset:12288
v_mfma_f32_16x16x32_bf16 a[52:55], v[104:107], a[212:215], 0
v_add_u32 v78, 65536, v70
s_waitcnt lgkmcnt(10)
v_mfma_f32_16x16x32_bf16 a[56:59], v[108:111], a[208:211], 0
ds_read_b64_tr_b16 v[100:101], v69 offset:8192
ds_read_b64_tr_b16 v[102:103], v69 offset:12288
v_mfma_f32_16x16x32_bf16 a[60:63], v[108:111], a[212:215], 0
buffer_load_dwordx4 a[172:175], v18, s[84:87], s29 offen offset:3072 sc1
v_add_u32 v79, 65536, v71
s_waitcnt vmcnt(25) lgkmcnt(10)
v_mfma_f32_16x16x32_bf16 a[0:3], v[80:83], a[216:219], a[0:3]
ds_read_b64_tr_b16 v[104:105], v70 offset:8192
ds_read_b64_tr_b16 v[106:107], v70 offset:12288
s_add_u32 s29, s88, 8192
s_waitcnt vmcnt(24)
v_mfma_f32_16x16x32_bf16 a[4:7], v[80:83], a[220:223], a[4:7]
buffer_load_dwordx4 a[176:179], v2, s[84:87], s29 offen offset:0 sc1
s_lshl_b32 s24, s92, 2
s_waitcnt lgkmcnt(10)
v_mfma_f32_16x16x32_bf16 a[8:11], v[84:87], a[216:219], a[8:11]
ds_read_b64_tr_b16 v[108:109], v71 offset:8192
ds_read_b64_tr_b16 v[110:111], v71 offset:12288
v_mfma_f32_16x16x32_bf16 a[12:15], v[84:87], a[220:223], a[12:15]
s_add_u32 s24, s24, s93
s_barrier
s_waitcnt lgkmcnt(10)
v_mfma_f32_16x16x32_bf16 a[16:19], v[88:91], a[216:219], a[16:19]
ds_read_b64_tr_b16 v[80:81], v64 offset:16384
ds_read_b64_tr_b16 v[82:83], v64 offset:20480
s_mov_b32 s28, 262144
v_mfma_f32_16x16x32_bf16 a[20:23], v[88:91], a[220:223], a[20:23]
s_add_u32 m0, s89, 32768
s_nop 0
buffer_load_dwordx4 v32, s[8:11], s28 offen lds
s_lshl_b32 s24, s24, 17
s_waitcnt lgkmcnt(10)
v_mfma_f32_16x16x32_bf16 a[24:27], v[92:95], a[216:219], a[24:27]
ds_read_b64_tr_b16 v[84:85], v65 offset:16384
v_mfma_f32_16x16x32_bf16 a[28:31], v[92:95], a[220:223], a[28:31]
ds_read_b64_tr_b16 v[86:87], v65 offset:20480
s_waitcnt lgkmcnt(10)
v_mfma_f32_16x16x32_bf16 a[32:35], v[96:99], a[216:219], a[32:35]
ds_read_b64_tr_b16 v[88:89], v66 offset:16384
ds_read_b64_tr_b16 v[90:91], v66 offset:20480
v_mfma_f32_16x16x32_bf16 a[36:39], v[96:99], a[220:223], a[36:39]
buffer_load_dwordx4 v33, s[8:11], s28 offen offset:1024 lds
s_lshl_b32 s23, s7, 11
s_waitcnt lgkmcnt(10)
v_mfma_f32_16x16x32_bf16 a[40:43], v[100:103], a[216:219], a[40:43]
ds_read_b64_tr_b16 v[92:93], v67 offset:16384
ds_read_b64_tr_b16 v[94:95], v67 offset:20480
v_mfma_f32_16x16x32_bf16 a[44:47], v[100:103], a[220:223], a[44:47]
s_add_u32 s24, s24, s23
s_waitcnt lgkmcnt(10)
v_mfma_f32_16x16x32_bf16 a[48:51], v[104:107], a[216:219], a[48:51]
ds_read_b64_tr_b16 v[96:97], v68 offset:16384
ds_read_b64_tr_b16 v[98:99], v68 offset:20480
v_mfma_f32_16x16x32_bf16 a[52:55], v[104:107], a[220:223], a[52:55]
buffer_load_dwordx4 v34, s[8:11], s28 offen offset:2048 lds
s_add_u32 s24, s24, 62914560
s_waitcnt lgkmcnt(10)
v_mfma_f32_16x16x32_bf16 a[56:59], v[108:111], a[216:219], a[56:59]
ds_read_b64_tr_b16 v[100:101], v69 offset:16384
ds_read_b64_tr_b16 v[102:103], v69 offset:20480
v_mfma_f32_16x16x32_bf16 a[60:63], v[108:111], a[220:223], a[60:63]
buffer_load_dwordx4 v35, s[8:11], s28 offen offset:3072 lds
s_waitcnt vmcnt(24) lgkmcnt(10)
v_mfma_f32_16x16x32_bf16 a[0:3], v[80:83], a[224:227], a[0:3]
ds_read_b64_tr_b16 v[104:105], v70 offset:16384
ds_read_b64_tr_b16 v[106:107], v70 offset:20480
s_waitcnt vmcnt(23)
v_mfma_f32_16x16x32_bf16 a[4:7], v[80:83], a[228:231], a[4:7]
s_add_u32 s46, s48, s24
s_addc_u32 s47, s49, 0
s_waitcnt lgkmcnt(10)
v_mfma_f32_16x16x32_bf16 a[8:11], v[84:87], a[224:227], a[8:11]
ds_read_b64_tr_b16 v[108:109], v71 offset:16384
ds_read_b64_tr_b16 v[110:111], v71 offset:20480
s_mov_b32 s28, 327680
v_mfma_f32_16x16x32_bf16 a[12:15], v[84:87], a[228:231], a[12:15]
s_add_u32 m0, s89, 49152
s_nop 0
buffer_load_dwordx4 v32, s[8:11], s28 offen lds
s_mov_b32 s16, s46
s_waitcnt lgkmcnt(10)
v_mfma_f32_16x16x32_bf16 a[16:19], v[88:91], a[224:227], a[16:19]
ds_read_b64_tr_b16 v[80:81], v64 offset:24576
ds_read_b64_tr_b16 v[82:83], v64 offset:28672
v_mfma_f32_16x16x32_bf16 a[20:23], v[88:91], a[228:231], a[20:23]
s_and_b32 s17, s47, 65535
s_waitcnt lgkmcnt(10)
v_mfma_f32_16x16x32_bf16 a[24:27], v[92:95], a[224:227], a[24:27]
ds_read_b64_tr_b16 v[84:85], v65 offset:24576
ds_read_b64_tr_b16 v[86:87], v65 offset:28672
v_mfma_f32_16x16x32_bf16 a[28:31], v[92:95], a[228:231], a[28:31]
buffer_load_dwordx4 v33, s[8:11], s28 offen offset:1024 lds
s_waitcnt lgkmcnt(10)
v_mfma_f32_16x16x32_bf16 a[32:35], v[96:99], a[224:227], a[32:35]
ds_read_b64_tr_b16 v[88:89], v66 offset:24576
ds_read_b64_tr_b16 v[90:91], v66 offset:28672
v_mfma_f32_16x16x32_bf16 a[36:39], v[96:99], a[228:231], a[36:39]
s_mov_b32 s18, 131072
s_waitcnt lgkmcnt(10)
v_mfma_f32_16x16x32_bf16 a[40:43], v[100:103], a[224:227], a[40:43]
ds_read_b64_tr_b16 v[92:93], v67 offset:24576
ds_read_b64_tr_b16 v[94:95], v67 offset:28672
v_mfma_f32_16x16x32_bf16 a[44:47], v[100:103], a[228:231], a[44:47]
buffer_load_dwordx4 v34, s[8:11], s28 offen offset:2048 lds
s_mov_b32 s19, 131072
s_waitcnt lgkmcnt(10)
v_mfma_f32_16x16x32_bf16 a[48:51], v[104:107], a[224:227], a[48:51]
ds_read_b64_tr_b16 v[96:97], v68 offset:24576
ds_read_b64_tr_b16 v[98:99], v68 offset:28672
v_mfma_f32_16x16x32_bf16 a[52:55], v[104:107], a[228:231], a[52:55]
buffer_load_dwordx4 v35, s[8:11], s28 offen offset:3072 lds
s_waitcnt lgkmcnt(10)
v_mfma_f32_16x16x32_bf16 a[56:59], v[108:111], a[224:227], a[56:59]
ds_read_b64_tr_b16 v[100:101], v69 offset:24576
ds_read_b64_tr_b16 v[102:103], v69 offset:28672
v_mfma_f32_16x16x32_bf16 a[60:63], v[108:111], a[228:231], a[60:63]
s_add_u32 s23, s91, 1
s_waitcnt vmcnt(26) lgkmcnt(10)
v_mfma_f32_16x16x32_bf16 a[0:3], v[80:83], a[232:235], a[0:3]
ds_read_b64_tr_b16 v[104:105], v70 offset:24576
ds_read_b64_tr_b16 v[106:107], v70 offset:28672
s_waitcnt vmcnt(25)
v_mfma_f32_16x16x32_bf16 a[4:7], v[80:83], a[236:239], a[4:7]
buffer_load_dwordx4 a[180:183], v18, s[84:87], s29 offen offset:0 sc1
s_and_b32 s23, s23, 3
s_waitcnt lgkmcnt(10)
v_mfma_f32_16x16x32_bf16 a[8:11], v[84:87], a[232:235], a[8:11]
ds_read_b64_tr_b16 v[108:109], v71 offset:24576
ds_read_b64_tr_b16 v[110:111], v71 offset:28672
v_mfma_f32_16x16x32_bf16 a[12:15], v[84:87], a[236:239], a[12:15]
s_lshl_b32 s24, s23, 15
s_waitcnt vmcnt(14)
s_barrier
s_cmp_eq_u32 s7, 0
s_cbranch_scc0 L_FS_D
s_lshl_b32 s31, s6, 6
s_add_u32 s31, s31, 65536
v_mov_b32 v244, s31
v_mov_b32 v242, s98
v_mov_b32 v243, s99
s_mov_b64 exec, 1
global_store_dwordx2 v244, v[242:243], s[48:49] sc1
s_mov_b64 exec, -1
L_FS_D:
s_waitcnt lgkmcnt(10)
s_nop 2
v_mfma_f32_16x16x32_bf16 a[16:19], v[88:91], a[232:235], a[16:19]
ds_read_b64_tr_b16 v[80:81], v72 offset:0
ds_read_b64_tr_b16 v[82:83], v72 offset:4096
s_mov_b32 s28, 393216
v_mfma_f32_16x16x32_bf16 a[20:23], v[88:91], a[236:239], a[20:23]
s_add_u32 m0, s89, 98304
s_nop 0
buffer_load_dwordx4 v32, s[8:11], s28 offen lds
s_waitcnt lgkmcnt(10)
v_mfma_f32_16x16x32_bf16 a[24:27], v[92:95], a[232:235], a[24:27]
ds_read_b64_tr_b16 v[84:85], v73 offset:0
ds_read_b64_tr_b16 v[86:87], v73 offset:4096
v_mfma_f32_16x16x32_bf16 a[28:31], v[92:95], a[236:239], a[28:31]
s_lshl_b32 s25, s91, 13
s_waitcnt lgkmcnt(10)
v_mfma_f32_16x16x32_bf16 a[32:35], v[96:99], a[232:235], a[32:35]
ds_read_b64_tr_b16 v[88:89], v74 offset:0
ds_read_b64_tr_b16 v[90:91], v74 offset:4096
v_mfma_f32_16x16x32_bf16 a[36:39], v[96:99], a[236:239], a[36:39]
buffer_load_dwordx4 v33, s[8:11], s28 offen offset:1024 lds
s_add_u32 s60, s24, s25
s_waitcnt lgkmcnt(10)
v_mfma_f32_16x16x32_bf16 a[40:43], v[100:103], a[232:235], a[40:43]
ds_read_b64_tr_b16 v[92:93], v75 offset:0
ds_read_b64_tr_b16 v[94:95], v75 offset:4096
v_mfma_f32_16x16x32_bf16 a[44:47], v[100:103], a[236:239], a[44:47]
buffer_load_dwordx4 v34, s[8:11], s28 offen offset:2048 lds
s_lshl_b32 s24, s91, 15
s_waitcnt lgkmcnt(10)
v_mfma_f32_16x16x32_bf16 a[48:51], v[104:107], a[232:235], a[48:51]
ds_read_b64_tr_b16 v[96:97], v76 offset:0
v_mfma_f32_16x16x32_bf16 a[52:55], v[104:107], a[236:239], a[52:55]
ds_read_b64_tr_b16 v[98:99], v76 offset:4096
s_waitcnt lgkmcnt(10)
)ASM"
R"ASM(v_mfma_f32_16x16x32_bf16 a[56:59], v[108:111], a[232:235], a[56:59]
ds_read_b64_tr_b16 v[100:101], v77 offset:0
ds_read_b64_tr_b16 v[102:103], v77 offset:4096
v_mfma_f32_16x16x32_bf16 a[60:63], v[108:111], a[236:239], a[60:63]
buffer_load_dwordx4 v35, s[8:11], s28 offen offset:3072 lds
s_lshl_b32 s25, s23, 13
s_waitcnt lgkmcnt(10)
v_mfma_f32_16x16x32_bf16 a[0:3], v[80:83], a[240:243], a[0:3]
ds_read_b64_tr_b16 v[104:105], v78 offset:0
ds_read_b64_tr_b16 v[106:107], v78 offset:4096
v_mfma_f32_16x16x32_bf16 a[4:7], v[80:83], a[244:247], a[4:7]
s_add_u32 s64, s24, s25
s_waitcnt lgkmcnt(10)
v_mfma_f32_16x16x32_bf16 a[8:11], v[84:87], a[240:243], a[8:11]
ds_read_b64_tr_b16 v[108:109], v79 offset:0
ds_read_b64_tr_b16 v[110:111], v79 offset:4096
v_mfma_f32_16x16x32_bf16 a[12:15], v[84:87], a[244:247], a[12:15]
buffer_load_dwordx4 a[184:187], v2, s[84:87], s29 offen offset:1024 sc1
s_waitcnt lgkmcnt(10)
v_mfma_f32_16x16x32_bf16 a[16:19], v[88:91], a[240:243], a[16:19]
ds_read_b64_tr_b16 v[80:81], v72 offset:8192
ds_read_b64_tr_b16 v[82:83], v72 offset:12288
v_mfma_f32_16x16x32_bf16 a[20:23], v[88:91], a[244:247], a[20:23]
s_add_u32 s23, s91, 2
s_waitcnt lgkmcnt(10)
v_mfma_f32_16x16x32_bf16 a[24:27], v[92:95], a[240:243], a[24:27]
ds_read_b64_tr_b16 v[84:85], v73 offset:8192
ds_read_b64_tr_b16 v[86:87], v73 offset:12288
v_mfma_f32_16x16x32_bf16 a[28:31], v[92:95], a[244:247], a[28:31]
buffer_load_dwordx4 a[188:191], v18, s[84:87], s29 offen offset:1024 sc1
s_and_b32 s23, s23, 3
s_waitcnt lgkmcnt(10)
v_mfma_f32_16x16x32_bf16 a[32:35], v[96:99], a[240:243], a[32:35]
ds_read_b64_tr_b16 v[88:89], v74 offset:8192
ds_read_b64_tr_b16 v[90:91], v74 offset:12288
v_mfma_f32_16x16x32_bf16 a[36:39], v[96:99], a[244:247], a[36:39]
buffer_load_dwordx4 a[128:131], v2, s[84:87], s29 offen offset:2048 sc1
s_lshl_b32 s24, s23, 15
s_waitcnt lgkmcnt(10)
v_mfma_f32_16x16x32_bf16 a[40:43], v[100:103], a[240:243], a[40:43]
ds_read_b64_tr_b16 v[92:93], v75 offset:8192
v_mfma_f32_16x16x32_bf16 a[44:47], v[100:103], a[244:247], a[44:47]
ds_read_b64_tr_b16 v[94:95], v75 offset:12288
s_waitcnt lgkmcnt(10)
v_mfma_f32_16x16x32_bf16 a[48:51], v[104:107], a[240:243], a[48:51]
ds_read_b64_tr_b16 v[96:97], v76 offset:8192
ds_read_b64_tr_b16 v[98:99], v76 offset:12288
v_mfma_f32_16x16x32_bf16 a[52:55], v[104:107], a[244:247], a[52:55]
buffer_load_dwordx4 a[132:135], v18, s[84:87], s29 offen offset:2048 sc1
s_lshl_b32 s25, s91, 13
s_waitcnt lgkmcnt(10)
v_mfma_f32_16x16x32_bf16 a[56:59], v[108:111], a[240:243], a[56:59]
ds_read_b64_tr_b16 v[100:101], v77 offset:8192
ds_read_b64_tr_b16 v[102:103], v77 offset:12288
v_mfma_f32_16x16x32_bf16 a[60:63], v[108:111], a[244:247], a[60:63]
s_add_u32 s61, s24, s25
s_waitcnt lgkmcnt(10)
v_mfma_f32_16x16x32_bf16 a[0:3], v[80:83], a[248:251], a[0:3]
ds_read_b64_tr_b16 v[104:105], v78 offset:8192
ds_read_b64_tr_b16 v[106:107], v78 offset:12288
s_mov_b32 s28, 0
v_mfma_f32_16x16x32_bf16 a[4:7], v[80:83], a[252:255], a[4:7]
s_add_u32 m0, s89, 114688
s_nop 0
buffer_load_dwordx4 v32, s[52:55], s28 offen lds
s_lshl_b32 s24, s91, 15
s_waitcnt lgkmcnt(10)
v_mfma_f32_16x16x32_bf16 a[8:11], v[84:87], a[248:251], a[8:11]
ds_read_b64_tr_b16 v[108:109], v79 offset:8192
v_mfma_f32_16x16x32_bf16 a[12:15], v[84:87], a[252:255], a[12:15]
ds_read_b64_tr_b16 v[110:111], v79 offset:12288
s_barrier
s_waitcnt lgkmcnt(10)
v_mfma_f32_16x16x32_bf16 a[16:19], v[88:91], a[248:251], a[16:19]
ds_read_b64_tr_b16 v[80:81], v72 offset:16384
ds_read_b64_tr_b16 v[82:83], v72 offset:20480
v_mfma_f32_16x16x32_bf16 a[20:23], v[88:91], a[252:255], a[20:23]
buffer_load_dwordx4 v33, s[52:55], s28 offen offset:1024 lds
s_lshl_b32 s25, s23, 13
s_waitcnt lgkmcnt(10)
v_mfma_f32_16x16x32_bf16 a[24:27], v[92:95], a[248:251], a[24:27]
ds_read_b64_tr_b16 v[84:85], v73 offset:16384
ds_read_b64_tr_b16 v[86:87], v73 offset:20480
v_mfma_f32_16x16x32_bf16 a[28:31], v[92:95], a[252:255], a[28:31]
buffer_load_dwordx4 v34, s[52:55], s28 offen offset:2048 lds
s_add_u32 s65, s24, s25
s_waitcnt lgkmcnt(10)
v_mfma_f32_16x16x32_bf16 a[32:35], v[96:99], a[248:251], a[32:35]
ds_read_b64_tr_b16 v[88:89], v74 offset:16384
v_mfma_f32_16x16x32_bf16 a[36:39], v[96:99], a[252:255], a[36:39]
ds_read_b64_tr_b16 v[90:91], v74 offset:20480
s_waitcnt lgkmcnt(10)
v_mfma_f32_16x16x32_bf16 a[40:43], v[100:103], a[248:251], a[40:43]
ds_read_b64_tr_b16 v[92:93], v75 offset:16384
ds_read_b64_tr_b16 v[94:95], v75 offset:20480
v_mfma_f32_16x16x32_bf16 a[44:47], v[100:103], a[252:255], a[44:47]
buffer_load_dwordx4 v35, s[52:55], s28 offen offset:3072 lds
s_add_u32 s23, s91, 3
s_waitcnt lgkmcnt(10)
v_mfma_f32_16x16x32_bf16 a[48:51], v[104:107], a[248:251], a[48:51]
ds_read_b64_tr_b16 v[96:97], v76 offset:16384
ds_read_b64_tr_b16 v[98:99], v76 offset:20480
v_mfma_f32_16x16x32_bf16 a[52:55], v[104:107], a[252:255], a[52:55]
s_and_b32 s23, s23, 3
s_waitcnt lgkmcnt(10)
v_mfma_f32_16x16x32_bf16 a[56:59], v[108:111], a[248:251], a[56:59]
ds_read_b64_tr_b16 v[100:101], v77 offset:16384
ds_read_b64_tr_b16 v[102:103], v77 offset:20480
v_mfma_f32_16x16x32_bf16 a[60:63], v[108:111], a[252:255], a[60:63]
buffer_load_dwordx4 a[136:139], v2, s[84:87], s29 offen offset:3072 sc1
s_lshl_b32 s24, s23, 15
s_waitcnt vmcnt(26) lgkmcnt(10)
v_mfma_f32_16x16x32_bf16 a[0:3], v[80:83], a[160:163], a[0:3]
ds_read_b64_tr_b16 v[104:105], v78 offset:16384
s_waitcnt vmcnt(25)
v_mfma_f32_16x16x32_bf16 a[4:7], v[80:83], a[164:167], a[4:7]
ds_read_b64_tr_b16 v[106:107], v78 offset:20480
s_waitcnt lgkmcnt(10)
v_mfma_f32_16x16x32_bf16 a[8:11], v[84:87], a[160:163], a[8:11]
ds_read_b64_tr_b16 v[108:109], v79 offset:16384
ds_read_b64_tr_b16 v[110:111], v79 offset:20480
v_mfma_f32_16x16x32_bf16 a[12:15], v[84:87], a[164:167], a[12:15]
buffer_load_dwordx4 a[140:143], v18, s[84:87], s29 offen offset:3072 sc1
s_lshl_b32 s25, s91, 13
s_waitcnt lgkmcnt(10)
v_mfma_f32_16x16x32_bf16 a[16:19], v[88:91], a[160:163], a[16:19]
ds_read_b64_tr_b16 v[80:81], v72 offset:24576
ds_read_b64_tr_b16 v[82:83], v72 offset:28672
s_add_u32 s29, s88, 12288
v_mfma_f32_16x16x32_bf16 a[20:23], v[88:91], a[164:167], a[20:23]
buffer_load_dwordx4 a[144:147], v2, s[84:87], s29 offen offset:0 sc1
s_add_u32 s62, s24, s25
s_waitcnt lgkmcnt(10)
v_mfma_f32_16x16x32_bf16 a[24:27], v[92:95], a[160:163], a[24:27]
ds_read_b64_tr_b16 v[84:85], v73 offset:24576
ds_read_b64_tr_b16 v[86:87], v73 offset:28672
v_mfma_f32_16x16x32_bf16 a[28:31], v[92:95], a[164:167], a[28:31]
s_lshl_b32 s24, s91, 15
s_waitcnt lgkmcnt(10)
v_mfma_f32_16x16x32_bf16 a[32:35], v[96:99], a[160:163], a[32:35]
ds_read_b64_tr_b16 v[88:89], v74 offset:24576
ds_read_b64_tr_b16 v[90:91], v74 offset:28672
v_mfma_f32_16x16x32_bf16 a[36:39], v[96:99], a[164:167], a[36:39]
buffer_load_dwordx4 a[148:151], v18, s[84:87], s29 offen offset:0 sc1
s_waitcnt lgkmcnt(10)
v_mfma_f32_16x16x32_bf16 a[40:43], v[100:103], a[160:163], a[40:43]
ds_read_b64_tr_b16 v[92:93], v75 offset:24576
ds_read_b64_tr_b16 v[94:95], v75 offset:28672
v_mfma_f32_16x16x32_bf16 a[44:47], v[100:103], a[164:167], a[44:47]
s_lshl_b32 s25, s23, 13
s_waitcnt lgkmcnt(10)
v_mfma_f32_16x16x32_bf16 a[48:51], v[104:107], a[160:163], a[48:51]
ds_read_b64_tr_b16 v[96:97], v76 offset:24576
ds_read_b64_tr_b16 v[98:99], v76 offset:28672
s_mov_b32 s28, 65536
v_mfma_f32_16x16x32_bf16 a[52:55], v[104:107], a[164:167], a[52:55]
s_add_u32 m0, s89, 0
s_nop 0
buffer_load_dwordx4 v32, s[52:55], s28 offen lds
s_add_u32 s66, s24, s25
s_waitcnt lgkmcnt(10)
v_mfma_f32_16x16x32_bf16 a[56:59], v[108:111], a[160:163], a[56:59]
ds_read_b64_tr_b16 v[100:101], v77 offset:24576
v_mfma_f32_16x16x32_bf16 a[60:63], v[108:111], a[164:167], a[60:63]
ds_read_b64_tr_b16 v[102:103], v77 offset:28672
s_waitcnt vmcnt(28) lgkmcnt(10)
v_mfma_f32_16x16x32_bf16 a[0:3], v[80:83], a[168:171], a[0:3]
ds_read_b64_tr_b16 v[104:105], v78 offset:24576
ds_read_b64_tr_b16 v[106:107], v78 offset:28672
s_waitcnt vmcnt(27)
v_mfma_f32_16x16x32_bf16 a[4:7], v[80:83], a[172:175], a[4:7]
buffer_load_dwordx4 v33, s[52:55], s28 offen offset:1024 lds
s_waitcnt lgkmcnt(10)
v_mfma_f32_16x16x32_bf16 a[8:11], v[84:87], a[168:171], a[8:11]
ds_read_b64_tr_b16 v[108:109], v79 offset:24576
ds_read_b64_tr_b16 v[110:111], v79 offset:28672
v_mfma_f32_16x16x32_bf16 a[12:15], v[84:87], a[172:175], a[12:15]
buffer_load_dwordx4 v34, s[52:55], s28 offen offset:2048 lds
s_waitcnt vmcnt(24)
s_barrier
s_waitcnt lgkmcnt(10)
v_mfma_f32_16x16x32_bf16 a[16:19], v[88:91], a[168:171], a[16:19]
ds_read_b64_tr_b16 v[80:81], v64 offset:32768
v_mfma_f32_16x16x32_bf16 a[20:23], v[88:91], a[172:175], a[20:23]
ds_read_b64_tr_b16 v[82:83], v64 offset:36864
s_waitcnt lgkmcnt(10)
v_mfma_f32_16x16x32_bf16 a[24:27], v[92:95], a[168:171], a[24:27]
ds_read_b64_tr_b16 v[84:85], v65 offset:32768
ds_read_b64_tr_b16 v[86:87], v65 offset:36864
v_mfma_f32_16x16x32_bf16 a[28:31], v[92:95], a[172:175], a[28:31]
buffer_load_dwordx4 v35, s[52:55], s28 offen offset:3072 lds
s_waitcnt lgkmcnt(10)
v_mfma_f32_16x16x32_bf16 a[32:35], v[96:99], a[168:171], a[32:35]
ds_read_b64_tr_b16 v[88:89], v66 offset:32768
ds_read_b64_tr_b16 v[90:91], v66 offset:36864
v_mfma_f32_16x16x32_bf16 a[36:39], v[96:99], a[172:175], a[36:39]
s_lshl_b32 s22, s7, 11
s_waitcnt lgkmcnt(10)
v_mfma_f32_16x16x32_bf16 a[40:43], v[100:103], a[168:171], a[40:43]
ds_read_b64_tr_b16 v[92:93], v67 offset:32768
ds_read_b64_tr_b16 v[94:95], v67 offset:36864
v_mfma_f32_16x16x32_bf16 a[44:47], v[100:103], a[172:175], a[44:47]
buffer_load_dwordx4 a[152:155], v2, s[84:87], s29 offen offset:1024 sc1
s_add_u32 s22, s22, 135168
s_waitcnt lgkmcnt(10)
v_mfma_f32_16x16x32_bf16 a[48:51], v[104:107], a[168:171], a[48:51]
ds_read_b64_tr_b16 v[96:97], v68 offset:32768
v_mfma_f32_16x16x32_bf16 a[52:55], v[104:107], a[172:175], a[52:55]
ds_read_b64_tr_b16 v[98:99], v68 offset:36864
s_waitcnt lgkmcnt(10)
v_mfma_f32_16x16x32_bf16 a[56:59], v[108:111], a[168:171], a[56:59]
ds_read_b64_tr_b16 v[100:101], v69 offset:32768
ds_read_b64_tr_b16 v[102:103], v69 offset:36864
v_mfma_f32_16x16x32_bf16 a[60:63], v[108:111], a[172:175], a[60:63]
buffer_load_dwordx4 a[156:159], v18, s[84:87], s29 offen offset:1024 sc1
v_lshlrev_b32 v136, 4, v1
s_waitcnt lgkmcnt(10)
v_mfma_f32_16x16x32_bf16 a[0:3], v[80:83], a[176:179], a[0:3]
ds_read_b64_tr_b16 v[104:105], v70 offset:32768
ds_read_b64_tr_b16 v[106:107], v70 offset:36864
s_mov_b32 s28, 131072
s_waitcnt vmcnt(22)
v_mfma_f32_16x16x32_bf16 a[4:7], v[80:83], a[180:183], a[4:7]
s_add_u32 m0, s89, 16384
s_nop 0
buffer_load_dwordx4 v32, s[52:55], s28 offen lds
s_waitcnt lgkmcnt(10)
v_mfma_f32_16x16x32_bf16 a[8:11], v[84:87], a[176:179], a[8:11]
ds_read_b64_tr_b16 v[108:109], v71 offset:32768
ds_read_b64_tr_b16 v[110:111], v71 offset:36864
v_mfma_f32_16x16x32_bf16 a[12:15], v[84:87], a[180:183], a[12:15]
v_add_u32 v136, s22, v136
s_waitcnt lgkmcnt(10)
v_mfma_f32_16x16x32_bf16 a[16:19], v[88:91], a[176:179], a[16:19]
ds_read_b64_tr_b16 v[80:81], v64 offset:40960
ds_read_b64_tr_b16 v[82:83], v64 offset:45056
v_mfma_f32_16x16x32_bf16 a[20:23], v[88:91], a[180:183], a[20:23]
buffer_load_dwordx4 v33, s[52:55], s28 offen offset:1024 lds
s_waitcnt lgkmcnt(10)
v_mfma_f32_16x16x32_bf16 a[24:27], v[92:95], a[176:179], a[24:27]
ds_read_b64_tr_b16 v[84:85], v65 offset:40960
ds_read_b64_tr_b16 v[86:87], v65 offset:45056
v_mfma_f32_16x16x32_bf16 a[28:31], v[92:95], a[180:183], a[28:31]
v_and_b32 v137, 15, v1
s_waitcnt lgkmcnt(10)
v_mfma_f32_16x16x32_bf16 a[32:35], v[96:99], a[176:179], a[32:35]
ds_read_b64_tr_b16 v[88:89], v66 offset:40960
ds_read_b64_tr_b16 v[90:91], v66 offset:45056
v_mfma_f32_16x16x32_bf16 a[36:39], v[96:99], a[180:183], a[36:39]
buffer_load_dwordx4 v34, s[52:55], s28 offen offset:2048 lds
s_waitcnt lgkmcnt(10)
v_mfma_f32_16x16x32_bf16 a[40:43], v[100:103], a[176:179], a[40:43]
ds_read_b64_tr_b16 v[92:93], v67 offset:40960
ds_read_b64_tr_b16 v[94:95], v67 offset:45056
v_mfma_f32_16x16x32_bf16 a[44:47], v[100:103], a[180:183], a[44:47]
v_lshlrev_b32 v137, 6, v137
s_waitcnt lgkmcnt(10)
v_mfma_f32_16x16x32_bf16 a[48:51], v[104:107], a[176:179], a[48:51]
ds_read_b64_tr_b16 v[96:97], v68 offset:40960
ds_read_b64_tr_b16 v[98:99], v68 offset:45056
v_mfma_f32_16x16x32_bf16 a[52:55], v[104:107], a[180:183], a[52:55]
buffer_load_dwordx4 v35, s[52:55], s28 offen offset:3072 lds
s_waitcnt lgkmcnt(10)
v_mfma_f32_16x16x32_bf16 a[56:59], v[108:111], a[176:179], a[56:59]
ds_read_b64_tr_b16 v[100:101], v69 offset:40960
ds_read_b64_tr_b16 v[102:103], v69 offset:45056
s_mov_b32 s28, 196608
v_mfma_f32_16x16x32_bf16 a[60:63], v[108:111], a[180:183], a[60:63]
s_add_u32 m0, s89, 65536
s_nop 0
buffer_load_dwordx4 v32, s[52:55], s28 offen lds
v_lshrrev_b32 v146, 4, v1
s_waitcnt vmcnt(22) lgkmcnt(10)
v_mfma_f32_16x16x32_bf16 a[0:3], v[80:83], a[184:187], a[0:3]
ds_read_b64_tr_b16 v[104:105], v70 offset:40960
s_waitcnt vmcnt(21)
v_mfma_f32_16x16x32_bf16 a[4:7], v[80:83], a[188:191], a[4:7]
ds_read_b64_tr_b16 v[106:107], v70 offset:45056
s_waitcnt lgkmcnt(10)
v_mfma_f32_16x16x32_bf16 a[8:11], v[84:87], a[184:187], a[8:11]
ds_read_b64_tr_b16 v[108:109], v71 offset:40960
ds_read_b64_tr_b16 v[110:111], v71 offset:45056
v_mfma_f32_16x16x32_bf16 a[12:15], v[84:87], a[188:191], a[12:15]
buffer_load_dwordx4 v33, s[52:55], s28 offen offset:1024 lds
v_lshlrev_b32 v146, 3, v146
s_barrier
s_waitcnt lgkmcnt(10)
v_mfma_f32_16x16x32_bf16 a[16:19], v[88:91], a[184:187], a[16:19]
ds_read_b64_tr_b16 v[80:81], v64 offset:49152
v_mfma_f32_16x16x32_bf16 a[20:23], v[88:91], a[188:191], a[20:23]
ds_read_b64_tr_b16 v[82:83], v64 offset:53248
s_waitcnt lgkmcnt(10)
v_mfma_f32_16x16x32_bf16 a[24:27], v[92:95], a[184:187], a[24:27]
ds_read_b64_tr_b16 v[84:85], v65 offset:49152
ds_read_b64_tr_b16 v[86:87], v65 offset:53248
v_mfma_f32_16x16x32_bf16 a[28:31], v[92:95], a[188:191], a[28:31]
buffer_load_dwordx4 v34, s[52:55], s28 offen offset:2048 lds
v_add_u32 v137, v137, v146
s_waitcnt lgkmcnt(10)
v_mfma_f32_16x16x32_bf16 a[32:35], v[96:99], a[184:187], a[32:35]
ds_read_b64_tr_b16 v[88:89], v66 offset:49152
ds_read_b64_tr_b16 v[90:91], v66 offset:53248
v_mfma_f32_16x16x32_bf16 a[36:39], v[96:99], a[188:191], a[36:39]
v_add_u32 v137, s22, v137
s_waitcnt lgkmcnt(10)
v_mfma_f32_16x16x32_bf16 a[40:43], v[100:103], a[184:187], a[40:43]
ds_read_b64_tr_b16 v[92:93], v67 offset:49152
ds_read_b64_tr_b16 v[94:95], v67 offset:53248
v_mfma_f32_16x16x32_bf16 a[44:47], v[100:103], a[188:191], a[44:47]
buffer_load_dwordx4 v35, s[52:55], s28 offen offset:3072 lds
s_waitcnt lgkmcnt(10)
v_mfma_f32_16x16x32_bf16 a[48:51], v[104:107], a[184:187], a[48:51]
ds_read_b64_tr_b16 v[96:97], v68 offset:49152
ds_read_b64_tr_b16 v[98:99], v68 offset:53248
s_mov_b32 s28, 262144
v_mfma_f32_16x16x32_bf16 a[52:55], v[104:107], a[188:191], a[52:55]
s_add_u32 m0, s89, 81920
s_nop 0
buffer_load_dwordx4 v32, s[52:55], s28 offen lds
ds_write_b128 v136, v[10:13] offset:0
s_waitcnt lgkmcnt(11)
v_mfma_f32_16x16x32_bf16 a[56:59], v[108:111], a[184:187], a[56:59]
ds_read_b64_tr_b16 v[100:101], v69 offset:49152
v_mfma_f32_16x16x32_bf16 a[60:63], v[108:111], a[188:191], a[60:63]
ds_read_b64_tr_b16 v[102:103], v69 offset:53248
global_load_dwordx2 v[240:241], v38, s[48:49] sc1
s_waitcnt vmcnt(25) lgkmcnt(11)
v_mfma_f32_16x16x32_bf16 a[0:3], v[80:83], a[128:131], a[0:3]
ds_read_b64_tr_b16 v[104:105], v70 offset:49152
ds_read_b64_tr_b16 v[106:107], v70 offset:53248
s_waitcnt vmcnt(24)
v_mfma_f32_16x16x32_bf16 a[4:7], v[80:83], a[132:135], a[4:7]
buffer_load_dwordx4 v33, s[52:55], s28 offen offset:1024 lds
ds_write_b128 v136, v[14:17] offset:1024
s_waitcnt lgkmcnt(12)
v_mfma_f32_16x16x32_bf16 a[8:11], v[84:87], a[128:131], a[8:11]
ds_read_b64_tr_b16 v[108:109], v71 offset:49152
v_mfma_f32_16x16x32_bf16 a[12:15], v[84:87], a[132:135], a[12:15]
ds_read_b64_tr_b16 v[110:111], v71 offset:53248
s_waitcnt lgkmcnt(12)
v_mfma_f32_16x16x32_bf16 a[16:19], v[88:91], a[128:131], a[16:19]
ds_read_b64_tr_b16 v[80:81], v64 offset:57344
ds_read_b64_tr_b16 v[82:83], v64 offset:61440
v_mfma_f32_16x16x32_bf16 a[20:23], v[88:91], a[132:135], a[20:23]
buffer_load_dwordx4 v34, s[52:55], s28 offen offset:2048 lds
ds_read_b64 v[138:139], v137 offset:0
s_waitcnt lgkmcnt(13)
v_mfma_f32_16x16x32_bf16 a[24:27], v[92:95], a[128:131], a[24:27]
ds_read_b64_tr_b16 v[84:85], v65 offset:57344
v_mfma_f32_16x16x32_bf16 a[28:31], v[92:95], a[132:135], a[28:31]
ds_read_b64_tr_b16 v[86:87], v65 offset:61440
s_waitcnt lgkmcnt(13)
v_mfma_f32_16x16x32_bf16 a[32:35], v[96:99], a[128:131], a[32:35]
ds_read_b64_tr_b16 v[88:89], v66 offset:57344
ds_read_b64_tr_b16 v[90:91], v66 offset:61440
v_mfma_f32_16x16x32_bf16 a[36:39], v[96:99], a[132:135], a[36:39]
buffer_load_dwordx4 v35, s[52:55], s28 offen offset:3072 lds
ds_read_b64 v[140:141], v137 offset:32
s_waitcnt lgkmcnt(13)
v_mfma_f32_16x16x32_bf16 a[40:43], v[100:103], a[128:131], a[40:43]
ds_read_b64_tr_b16 v[92:93], v67 offset:57344
v_mfma_f32_16x16x32_bf16 a[44:47], v[100:103], a[132:135], a[44:47]
ds_read_b64_tr_b16 v[94:95], v67 offset:61440
s_waitcnt lgkmcnt(13)
v_mfma_f32_16x16x32_bf16 a[48:51], v[104:107], a[128:131], a[48:51]
ds_read_b64_tr_b16 v[96:97], v68 offset:57344
ds_read_b64_tr_b16 v[98:99], v68 offset:61440
v_mfma_f32_16x16x32_bf16 a[52:55], v[104:107], a[132:135], a[52:55]
ds_read_b64 v[142:143], v137 offset:1024
s_waitcnt lgkmcnt(13)
v_mfma_f32_16x16x32_bf16 a[56:59], v[108:111], a[128:131], a[56:59]
ds_read_b64_tr_b16 v[100:101], v69 offset:57344
v_mfma_f32_16x16x32_bf16 a[60:63], v[108:111], a[132:135], a[60:63]
ds_read_b64_tr_b16 v[102:103], v69 offset:61440
s_waitcnt vmcnt(22) lgkmcnt(13)
v_mfma_f32_16x16x32_bf16 a[0:3], v[80:83], a[136:139], a[0:3]
ds_read_b64_tr_b16 v[104:105], v70 offset:57344
ds_read_b64_tr_b16 v[106:107], v70 offset:61440
s_waitcnt vmcnt(21)
v_mfma_f32_16x16x32_bf16 a[4:7], v[80:83], a[140:143], a[4:7]
ds_read_b64 v[144:145], v137 offset:1056
s_waitcnt lgkmcnt(13)
v_mfma_f32_16x16x32_bf16 a[8:11], v[84:87], a[136:139], a[8:11]
ds_read_b64_tr_b16 v[108:109], v71 offset:57344
v_mfma_f32_16x16x32_bf16 a[12:15], v[84:87], a[140:143], a[12:15]
ds_read_b64_tr_b16 v[110:111], v71 offset:61440
s_barrier
s_waitcnt lgkmcnt(13)
v_mfma_f32_16x16x32_bf16 a[16:19], v[88:91], a[136:139], a[16:19]
ds_read_b64_tr_b16 v[80:81], v72 offset:32768
ds_read_b64_tr_b16 v[82:83], v72 offset:36864
s_mov_b32 s28, 327680
v_mfma_f32_16x16x32_bf16 a[20:23], v[88:91], a[140:143], a[20:23]
s_add_u32 m0, s89, 32768
s_nop 0
buffer_load_dwordx4 v32, s[52:55], s28 offen lds
s_waitcnt lgkmcnt(12)
v_mfma_f32_16x16x32_bf16 a[24:27], v[92:95], a[136:139], a[24:27]
ds_read_b64_tr_b16 v[84:85], v73 offset:32768
ds_read_b64_tr_b16 v[86:87], v73 offset:36864
v_mfma_f32_16x16x32_bf16 a[28:31], v[92:95], a[140:143], a[28:31]
buffer_load_dwordx4 v33, s[52:55], s28 offen offset:1024 lds
s_waitcnt lgkmcnt(12)
v_mfma_f32_16x16x32_bf16 a[32:35], v[96:99], a[136:139], a[32:35]
ds_read_b64_tr_b16 v[88:89], v74 offset:32768
v_mfma_f32_16x16x32_bf16 a[36:39], v[96:99], a[140:143], a[36:39]
ds_read_b64_tr_b16 v[90:91], v74 offset:36864
s_waitcnt lgkmcnt(11)
v_mfma_f32_16x16x32_bf16 a[40:43], v[100:103], a[136:139], a[40:43]
ds_read_b64_tr_b16 v[92:93], v75 offset:32768
ds_read_b64_tr_b16 v[94:95], v75 offset:36864
v_mfma_f32_16x16x32_bf16 a[44:47], v[100:103], a[140:143], a[44:47]
buffer_load_dwordx4 v34, s[52:55], s28 offen offset:2048 lds
s_waitcnt lgkmcnt(11)
v_mfma_f32_16x16x32_bf16 a[48:51], v[104:107], a[136:139], a[48:51]
ds_read_b64_tr_b16 v[96:97], v76 offset:32768
v_mfma_f32_16x16x32_bf16 a[52:55], v[104:107], a[140:143], a[52:55]
ds_read_b64_tr_b16 v[98:99], v76 offset:36864
s_waitcnt lgkmcnt(10)
v_mfma_f32_16x16x32_bf16 a[56:59], v[108:111], a[136:139], a[56:59]
ds_read_b64_tr_b16 v[100:101], v77 offset:32768
ds_read_b64_tr_b16 v[102:103], v77 offset:36864
v_mfma_f32_16x16x32_bf16 a[60:63], v[108:111], a[140:143], a[60:63]
buffer_load_dwordx4 v35, s[52:55], s28 offen offset:3072 lds
L_PW_ED:
s_waitcnt vmcnt(7)
v_cmp_ne_u64 vcc, s[98:99], v[240:241]
s_nop 4
s_cbranch_vccz L_PK_ED
s_mov_b32 s31, 1048576
L_PL_ED:
s_sleep 2
global_load_dwordx2 v[240:241], v38, s[48:49] sc1
s_waitcnt vmcnt(0)
v_cmp_ne_u64 vcc, s[98:99], v[240:241]
s_sub_u32 s31, s31, 1
s_cbranch_scc1 L_PK_ED
s_nop 4
s_cbranch_vccnz L_PL_ED
L_PK_ED:
s_waitcnt lgkmcnt(10)
v_mfma_f32_16x16x32_bf16 a[0:3], v[80:83], a[144:147], a[0:3]
ds_read_b64_tr_b16 v[104:105], v78 offset:32768
v_mfma_f32_16x16x32_bf16 a[4:7], v[80:83], a[148:151], a[4:7]
ds_read_b64_tr_b16 v[106:107], v78 offset:36864
s_waitcnt lgkmcnt(10)
v_mfma_f32_16x16x32_bf16 a[8:11], v[84:87], a[144:147], a[8:11]
ds_read_b64_tr_b16 v[108:109], v79 offset:32768
ds_read_b64_tr_b16 v[110:111], v79 offset:36864
v_mfma_f32_16x16x32_bf16 a[12:15], v[84:87], a[148:151], a[12:15]
s_add_u32 s29, s90, 0
buffer_load_dwordx4 a[192:195], v2, s[84:87], s29 offen offset:0 sc1
s_waitcnt lgkmcnt(10)
v_mfma_f32_16x16x32_bf16 a[16:19], v[88:91], a[144:147], a[16:19]
ds_read_b64_tr_b16 v[80:81], v72 offset:40960
v_mfma_f32_16x16x32_bf16 a[20:23], v[88:91], a[148:151], a[20:23]
ds_read_b64_tr_b16 v[82:83], v72 offset:45056
s_waitcnt lgkmcnt(10)
v_mfma_f32_16x16x32_bf16 a[24:27], v[92:95], a[144:147], a[24:27]
ds_read_b64_tr_b16 v[84:85], v73 offset:40960
ds_read_b64_tr_b16 v[86:87], v73 offset:45056
v_mfma_f32_16x16x32_bf16 a[28:31], v[92:95], a[148:151], a[28:31]
buffer_load_dwordx4 a[196:199], v18, s[84:87], s29 offen offset:0 sc1
s_waitcnt lgkmcnt(10)
v_mfma_f32_16x16x32_bf16 a[32:35], v[96:99], a[144:147], a[32:35]
ds_read_b64_tr_b16 v[88:89], v74 offset:40960
v_mfma_f32_16x16x32_bf16 a[36:39], v[96:99], a[148:151], a[36:39]
ds_read_b64_tr_b16 v[90:91], v74 offset:45056
s_waitcnt lgkmcnt(10)
v_mfma_f32_16x16x32_bf16 a[40:43], v[100:103], a[144:147], a[40:43]
ds_read_b64_tr_b16 v[92:93], v75 offset:40960
ds_read_b64_tr_b16 v[94:95], v75 offset:45056
v_mfma_f32_16x16x32_bf16 a[44:47], v[100:103], a[148:151], a[44:47]
buffer_load_dwordx4 a[200:203], v2, s[84:87], s29 offen offset:1024 sc1
s_waitcnt lgkmcnt(10)
v_mfma_f32_16x16x32_bf16 a[48:51], v[104:107], a[144:147], a[48:51]
ds_read_b64_tr_b16 v[96:97], v76 offset:40960
v_mfma_f32_16x16x32_bf16 a[52:55], v[104:107], a[148:151], a[52:55]
ds_read_b64_tr_b16 v[98:99], v76 offset:45056
s_waitcnt lgkmcnt(10)
v_mfma_f32_16x16x32_bf16 a[56:59], v[108:111], a[144:147], a[56:59]
ds_read_b64_tr_b16 v[100:101], v77 offset:40960
ds_read_b64_tr_b16 v[102:103], v77 offset:45056
v_mfma_f32_16x16x32_bf16 a[60:63], v[108:111], a[148:151], a[60:63]
buffer_load_dwordx4 a[204:207], v18, s[84:87], s29 offen offset:1024 sc1
s_waitcnt lgkmcnt(10)
v_mfma_f32_16x16x32_bf16 a[0:3], v[80:83], a[152:155], a[0:3]
ds_read_b64_tr_b16 v[104:105], v78 offset:40960
v_mfma_f32_16x16x32_bf16 a[4:7], v[80:83], a[156:159], a[4:7]
ds_read_b64_tr_b16 v[106:107], v78 offset:45056
s_waitcnt lgkmcnt(10)
v_mfma_f32_16x16x32_bf16 a[8:11], v[84:87], a[152:155], a[8:11]
ds_read_b64_tr_b16 v[108:109], v79 offset:40960
ds_read_b64_tr_b16 v[110:111], v79 offset:45056
v_mfma_f32_16x16x32_bf16 a[12:15], v[84:87], a[156:159], a[12:15]
buffer_load_dwordx4 a[64:67], v2, s[84:87], s29 offen offset:2048 sc1
s_barrier
s_waitcnt lgkmcnt(10)
v_mfma_f32_16x16x32_bf16 a[16:19], v[88:91], a[152:155], a[16:19]
ds_read_b64_tr_b16 v[80:81], v72 offset:49152
v_mfma_f32_16x16x32_bf16 a[20:23], v[88:91], a[156:159], a[20:23]
ds_read_b64_tr_b16 v[82:83], v72 offset:53248
s_waitcnt lgkmcnt(10)
v_mfma_f32_16x16x32_bf16 a[24:27], v[92:95], a[152:155], a[24:27]
ds_read_b64_tr_b16 v[84:85], v73 offset:49152
ds_read_b64_tr_b16 v[86:87], v73 offset:53248
v_mfma_f32_16x16x32_bf16 a[28:31], v[92:95], a[156:159], a[28:31]
buffer_load_dwordx4 a[68:71], v18, s[84:87], s29 offen offset:2048 sc1
s_waitcnt lgkmcnt(10)
v_mfma_f32_16x16x32_bf16 a[32:35], v[96:99], a[152:155], a[32:35]
ds_read_b64_tr_b16 v[88:89], v74 offset:49152
v_mfma_f32_16x16x32_bf16 a[36:39], v[96:99], a[156:159], a[36:39]
ds_read_b64_tr_b16 v[90:91], v74 offset:53248
s_waitcnt lgkmcnt(10)
v_mfma_f32_16x16x32_bf16 a[40:43], v[100:103], a[152:155], a[40:43]
ds_read_b64_tr_b16 v[92:93], v75 offset:49152
ds_read_b64_tr_b16 v[94:95], v75 offset:53248
v_mfma_f32_16x16x32_bf16 a[44:47], v[100:103], a[156:159], a[44:47]
buffer_load_dwordx4 a[72:75], v2, s[84:87], s29 offen offset:3072 sc1
s_waitcnt lgkmcnt(10)
v_mfma_f32_16x16x32_bf16 a[48:51], v[104:107], a[152:155], a[48:51]
ds_read_b64_tr_b16 v[96:97], v76 offset:49152
v_mfma_f32_16x16x32_bf16 a[52:55], v[104:107], a[156:159], a[52:55]
ds_read_b64_tr_b16 v[98:99], v76 offset:53248
s_waitcnt lgkmcnt(10)
v_mfma_f32_16x16x32_bf16 a[56:59], v[108:111], a[152:155], a[56:59]
ds_read_b64_tr_b16 v[100:101], v77 offset:49152
ds_read_b64_tr_b16 v[102:103], v77 offset:53248
v_mfma_f32_16x16x32_bf16 a[60:63], v[108:111], a[156:159], a[60:63]
buffer_load_dwordx4 a[76:79], v18, s[84:87], s29 offen offset:3072 sc1
s_waitcnt vmcnt(7) lgkmcnt(10)
v_mfma_f32_16x16x32_bf16 a[0:3], v[80:83], a[192:195], a[0:3]
ds_read_b64_tr_b16 v[104:105], v78 offset:49152
s_waitcnt vmcnt(6)
v_mfma_f32_16x16x32_bf16 a[4:7], v[80:83], a[196:199], a[4:7]
ds_read_b64_tr_b16 v[106:107], v78 offset:53248
s_waitcnt lgkmcnt(10)
v_mfma_f32_16x16x32_bf16 a[8:11], v[84:87], a[192:195], a[8:11]
ds_read_b64_tr_b16 v[108:109], v79 offset:49152
ds_read_b64_tr_b16 v[110:111], v79 offset:53248
v_mfma_f32_16x16x32_bf16 a[12:15], v[84:87], a[196:199], a[12:15]
s_add_u32 s29, s90, 4096
buffer_load_dwordx4 a[80:83], v2, s[84:87], s29 offen offset:0 sc1
s_waitcnt lgkmcnt(10)
v_mfma_f32_16x16x32_bf16 a[16:19], v[88:91], a[192:195], a[16:19]
ds_read_b64_tr_b16 v[80:81], v72 offset:57344
v_mfma_f32_16x16x32_bf16 a[20:23], v[88:91], a[196:199], a[20:23]
ds_read_b64_tr_b16 v[82:83], v72 offset:61440
s_waitcnt lgkmcnt(10)
v_mfma_f32_16x16x32_bf16 a[24:27], v[92:95], a[192:195], a[24:27]
ds_read_b64_tr_b16 v[84:85], v73 offset:57344
ds_read_b64_tr_b16 v[86:87], v73 offset:61440
v_mfma_f32_16x16x32_bf16 a[28:31], v[92:95], a[196:199], a[28:31]
buffer_load_dwordx4 a[84:87], v18, s[84:87], s29 offen offset:0 sc1
s_waitcnt lgkmcnt(10)
v_mfma_f32_16x16x32_bf16 a[32:35], v[96:99], a[192:195], a[32:35]
ds_read_b64_tr_b16 v[88:89], v74 offset:57344
v_mfma_f32_16x16x32_bf16 a[36:39], v[96:99], a[196:199], a[36:39]
ds_read_b64_tr_b16 v[90:91], v74 offset:61440
s_waitcnt lgkmcnt(10)
v_mfma_f32_16x16x32_bf16 a[40:43], v[100:103], a[192:195], a[40:43]
ds_read_b64_tr_b16 v[92:93], v75 offset:57344
ds_read_b64_tr_b16 v[94:95], v75 offset:61440
v_mfma_f32_16x16x32_bf16 a[44:47], v[100:103], a[196:199], a[44:47]
buffer_load_dwordx4 a[88:91], v2, s[84:87], s29 offen offset:1024 sc1
s_waitcnt lgkmcnt(10)
v_mfma_f32_16x16x32_bf16 a[48:51], v[104:107], a[192:195], a[48:51]
ds_read_b64_tr_b16 v[96:97], v76 offset:57344
v_mfma_f32_16x16x32_bf16 a[52:55], v[104:107], a[196:199], a[52:55]
ds_read_b64_tr_b16 v[98:99], v76 offset:61440
s_waitcnt lgkmcnt(10)
v_mfma_f32_16x16x32_bf16 a[56:59], v[108:111], a[192:195], a[56:59]
ds_read_b64_tr_b16 v[100:101], v77 offset:57344
ds_read_b64_tr_b16 v[102:103], v77 offset:61440
v_mfma_f32_16x16x32_bf16 a[60:63], v[108:111], a[196:199], a[60:63]
buffer_load_dwordx4 a[92:95], v18, s[84:87], s29 offen offset:1024 sc1
s_waitcnt vmcnt(9) lgkmcnt(10)
v_mfma_f32_16x16x32_bf16 a[0:3], v[80:83], a[200:203], a[0:3]
ds_read_b64_tr_b16 v[104:105], v78 offset:57344
s_waitcnt vmcnt(8)
v_mfma_f32_16x16x32_bf16 a[4:7], v[80:83], a[204:207], a[4:7]
ds_read_b64_tr_b16 v[106:107], v78 offset:61440
s_waitcnt lgkmcnt(10)
v_mfma_f32_16x16x32_bf16 a[8:11], v[84:87], a[200:203], a[8:11]
ds_read_b64_tr_b16 v[108:109], v79 offset:57344
ds_read_b64_tr_b16 v[110:111], v79 offset:61440
v_mfma_f32_16x16x32_bf16 a[12:15], v[84:87], a[204:207], a[12:15]
buffer_load_dwordx4 a[96:99], v2, s[84:87], s29 offen offset:2048 sc1
s_barrier
s_waitcnt lgkmcnt(10)
v_mfma_f32_16x16x32_bf16 a[16:19], v[88:91], a[200:203], a[16:19]
ds_read_b64_tr_b16 v[80:81], v64 offset:0
v_mfma_f32_16x16x32_bf16 a[20:23], v[88:91], a[204:207], a[20:23]
ds_read_b64_tr_b16 v[82:83], v64 offset:4096
s_waitcnt lgkmcnt(10)
v_mfma_f32_16x16x32_bf16 a[24:27], v[92:95], a[200:203], a[24:27]
ds_read_b64_tr_b16 v[84:85], v65 offset:0
ds_read_b64_tr_b16 v[86:87], v65 offset:4096
v_mfma_f32_16x16x32_bf16 a[28:31], v[92:95], a[204:207], a[28:31]
buffer_load_dwordx4 a[100:103], v18, s[84:87], s29 offen offset:2048 sc1
s_waitcnt lgkmcnt(10)
v_mfma_f32_16x16x32_bf16 a[32:35], v[96:99], a[200:203], a[32:35]
ds_read_b64_tr_b16 v[88:89], v66 offset:0
v_mfma_f32_16x16x32_bf16 a[36:39], v[96:99], a[204:207], a[36:39]
ds_read_b64_tr_b16 v[90:91], v66 offset:4096
s_waitcnt lgkmcnt(10)
v_mfma_f32_16x16x32_bf16 a[40:43], v[100:103], a[200:203], a[40:43]
ds_read_b64_tr_b16 v[92:93], v67 offset:0
ds_read_b64_tr_b16 v[94:95], v67 offset:4096
v_mfma_f32_16x16x32_bf16 a[44:47], v[100:103], a[204:207], a[44:47]
buffer_load_dwordx4 a[104:107], v2, s[84:87], s29 offen offset:3072 sc1
s_waitcnt lgkmcnt(10)
v_mfma_f32_16x16x32_bf16 a[48:51], v[104:107], a[200:203], a[48:51]
ds_read_b64_tr_b16 v[96:97], v68 offset:0
v_mfma_f32_16x16x32_bf16 a[52:55], v[104:107], a[204:207], a[52:55]
ds_read_b64_tr_b16 v[98:99], v68 offset:4096
s_waitcnt lgkmcnt(10)
v_mfma_f32_16x16x32_bf16 a[56:59], v[108:111], a[200:203], a[56:59]
ds_read_b64_tr_b16 v[100:101], v69 offset:0
ds_read_b64_tr_b16 v[102:103], v69 offset:4096
v_mfma_f32_16x16x32_bf16 a[60:63], v[108:111], a[204:207], a[60:63]
buffer_load_dwordx4 a[108:111], v18, s[84:87], s29 offen offset:3072 sc1
s_waitcnt vmcnt(11) lgkmcnt(10)
v_mfma_f32_16x16x32_bf16 a[0:3], v[80:83], a[64:67], a[0:3]
ds_read_b64_tr_b16 v[104:105], v70 offset:0
s_waitcnt vmcnt(10)
v_mfma_f32_16x16x32_bf16 a[4:7], v[80:83], a[68:71], a[4:7]
ds_read_b64_tr_b16 v[106:107], v70 offset:4096
s_waitcnt lgkmcnt(10)
v_mfma_f32_16x16x32_bf16 a[8:11], v[84:87], a[64:67], a[8:11]
ds_read_b64_tr_b16 v[108:109], v71 offset:0
ds_read_b64_tr_b16 v[110:111], v71 offset:4096
v_mfma_f32_16x16x32_bf16 a[12:15], v[84:87], a[68:71], a[12:15]
s_add_u32 s29, s90, 8192
buffer_load_dwordx4 a[112:115], v2, s[84:87], s29 offen offset:0 sc1
s_waitcnt lgkmcnt(10)
v_mfma_f32_16x16x32_bf16 a[16:19], v[88:91], a[64:67], a[16:19]
ds_read_b64_tr_b16 v[80:81], v64 offset:8192
v_mfma_f32_16x16x32_bf16 a[20:23], v[88:91], a[68:71], a[20:23]
)ASM"
R"ASM(ds_read_b64_tr_b16 v[82:83], v64 offset:12288
s_waitcnt lgkmcnt(10)
v_mfma_f32_16x16x32_bf16 a[24:27], v[92:95], a[64:67], a[24:27]
ds_read_b64_tr_b16 v[84:85], v65 offset:8192
ds_read_b64_tr_b16 v[86:87], v65 offset:12288
v_mfma_f32_16x16x32_bf16 a[28:31], v[92:95], a[68:71], a[28:31]
buffer_load_dwordx4 a[116:119], v18, s[84:87], s29 offen offset:0 sc1
s_waitcnt lgkmcnt(10)
v_mfma_f32_16x16x32_bf16 a[32:35], v[96:99], a[64:67], a[32:35]
ds_read_b64_tr_b16 v[88:89], v66 offset:8192
v_mfma_f32_16x16x32_bf16 a[36:39], v[96:99], a[68:71], a[36:39]
ds_read_b64_tr_b16 v[90:91], v66 offset:12288
s_waitcnt lgkmcnt(10)
v_mfma_f32_16x16x32_bf16 a[40:43], v[100:103], a[64:67], a[40:43]
ds_read_b64_tr_b16 v[92:93], v67 offset:8192
ds_read_b64_tr_b16 v[94:95], v67 offset:12288
s_mov_b32 s28, 393216
v_mfma_f32_16x16x32_bf16 a[44:47], v[100:103], a[68:71], a[44:47]
s_add_u32 m0, s89, 49152
s_nop 0
buffer_load_dwordx4 v32, s[52:55], s28 offen lds
s_waitcnt lgkmcnt(10)
v_mfma_f32_16x16x32_bf16 a[48:51], v[104:107], a[64:67], a[48:51]
ds_read_b64_tr_b16 v[96:97], v68 offset:8192
v_mfma_f32_16x16x32_bf16 a[52:55], v[104:107], a[68:71], a[52:55]
ds_read_b64_tr_b16 v[98:99], v68 offset:12288
s_waitcnt lgkmcnt(10)
v_mfma_f32_16x16x32_bf16 a[56:59], v[108:111], a[64:67], a[56:59]
ds_read_b64_tr_b16 v[100:101], v69 offset:8192
ds_read_b64_tr_b16 v[102:103], v69 offset:12288
v_mfma_f32_16x16x32_bf16 a[60:63], v[108:111], a[68:71], a[60:63]
buffer_load_dwordx4 v33, s[52:55], s28 offen offset:1024 lds
s_waitcnt vmcnt(13) lgkmcnt(10)
v_mfma_f32_16x16x32_bf16 a[0:3], v[80:83], a[72:75], a[0:3]
ds_read_b64_tr_b16 v[104:105], v70 offset:8192
s_waitcnt vmcnt(12)
v_mfma_f32_16x16x32_bf16 a[4:7], v[80:83], a[76:79], a[4:7]
ds_read_b64_tr_b16 v[106:107], v70 offset:12288
s_waitcnt lgkmcnt(10)
v_mfma_f32_16x16x32_bf16 a[8:11], v[84:87], a[72:75], a[8:11]
ds_read_b64_tr_b16 v[108:109], v71 offset:8192
ds_read_b64_tr_b16 v[110:111], v71 offset:12288
v_mfma_f32_16x16x32_bf16 a[12:15], v[84:87], a[76:79], a[12:15]
buffer_load_dwordx4 v34, s[52:55], s28 offen offset:2048 lds
s_barrier
s_waitcnt lgkmcnt(10)
v_mfma_f32_16x16x32_bf16 a[16:19], v[88:91], a[72:75], a[16:19]
ds_read_b64_tr_b16 v[80:81], v64 offset:16384
v_mfma_f32_16x16x32_bf16 a[20:23], v[88:91], a[76:79], a[20:23]
ds_read_b64_tr_b16 v[82:83], v64 offset:20480
s_waitcnt lgkmcnt(10)
v_mfma_f32_16x16x32_bf16 a[24:27], v[92:95], a[72:75], a[24:27]
ds_read_b64_tr_b16 v[84:85], v65 offset:16384
ds_read_b64_tr_b16 v[86:87], v65 offset:20480
v_mfma_f32_16x16x32_bf16 a[28:31], v[92:95], a[76:79], a[28:31]
buffer_load_dwordx4 v35, s[52:55], s28 offen offset:3072 lds
s_waitcnt lgkmcnt(10)
v_mfma_f32_16x16x32_bf16 a[32:35], v[96:99], a[72:75], a[32:35]
ds_read_b64_tr_b16 v[88:89], v66 offset:16384
v_mfma_f32_16x16x32_bf16 a[36:39], v[96:99], a[76:79], a[36:39]
ds_read_b64_tr_b16 v[90:91], v66 offset:20480
s_waitcnt lgkmcnt(10)
v_mfma_f32_16x16x32_bf16 a[40:43], v[100:103], a[72:75], a[40:43]
ds_read_b64_tr_b16 v[92:93], v67 offset:16384
ds_read_b64_tr_b16 v[94:95], v67 offset:20480
v_mfma_f32_16x16x32_bf16 a[44:47], v[100:103], a[76:79], a[44:47]
buffer_load_dwordx4 a[120:123], v2, s[84:87], s29 offen offset:1024 sc1
s_waitcnt lgkmcnt(10)
v_mfma_f32_16x16x32_bf16 a[48:51], v[104:107], a[72:75], a[48:51]
ds_read_b64_tr_b16 v[96:97], v68 offset:16384
v_mfma_f32_16x16x32_bf16 a[52:55], v[104:107], a[76:79], a[52:55]
ds_read_b64_tr_b16 v[98:99], v68 offset:20480
s_waitcnt lgkmcnt(10)
v_mfma_f32_16x16x32_bf16 a[56:59], v[108:111], a[72:75], a[56:59]
ds_read_b64_tr_b16 v[100:101], v69 offset:16384
ds_read_b64_tr_b16 v[102:103], v69 offset:20480
v_mfma_f32_16x16x32_bf16 a[60:63], v[108:111], a[76:79], a[60:63]
buffer_load_dwordx4 a[124:127], v18, s[84:87], s29 offen offset:1024 sc1
s_waitcnt vmcnt(15) lgkmcnt(10)
v_mfma_f32_16x16x32_bf16 a[0:3], v[80:83], a[80:83], a[0:3]
ds_read_b64_tr_b16 v[104:105], v70 offset:16384
s_waitcnt vmcnt(14)
v_mfma_f32_16x16x32_bf16 a[4:7], v[80:83], a[84:87], a[4:7]
ds_read_b64_tr_b16 v[106:107], v70 offset:20480
s_waitcnt lgkmcnt(10)
v_mfma_f32_16x16x32_bf16 a[8:11], v[84:87], a[80:83], a[8:11]
ds_read_b64_tr_b16 v[108:109], v71 offset:16384
ds_read_b64_tr_b16 v[110:111], v71 offset:20480
v_mfma_f32_16x16x32_bf16 a[12:15], v[84:87], a[84:87], a[12:15]
buffer_load_dwordx4 a[208:211], v2, s[84:87], s29 offen offset:2048 sc1
s_waitcnt lgkmcnt(10)
v_mfma_f32_16x16x32_bf16 a[16:19], v[88:91], a[80:83], a[16:19]
ds_read_b64_tr_b16 v[80:81], v64 offset:24576
v_mfma_f32_16x16x32_bf16 a[20:23], v[88:91], a[84:87], a[20:23]
ds_read_b64_tr_b16 v[82:83], v64 offset:28672
s_waitcnt lgkmcnt(10)
v_mfma_f32_16x16x32_bf16 a[24:27], v[92:95], a[80:83], a[24:27]
ds_read_b64_tr_b16 v[84:85], v65 offset:24576
ds_read_b64_tr_b16 v[86:87], v65 offset:28672
v_mfma_f32_16x16x32_bf16 a[28:31], v[92:95], a[84:87], a[28:31]
buffer_load_dwordx4 a[212:215], v18, s[84:87], s29 offen offset:2048 sc1
s_waitcnt lgkmcnt(10)
v_mfma_f32_16x16x32_bf16 a[32:35], v[96:99], a[80:83], a[32:35]
ds_read_b64_tr_b16 v[88:89], v66 offset:24576
v_mfma_f32_16x16x32_bf16 a[36:39], v[96:99], a[84:87], a[36:39]
ds_read_b64_tr_b16 v[90:91], v66 offset:28672
s_waitcnt lgkmcnt(10)
v_mfma_f32_16x16x32_bf16 a[40:43], v[100:103], a[80:83], a[40:43]
ds_read_b64_tr_b16 v[92:93], v67 offset:24576
ds_read_b64_tr_b16 v[94:95], v67 offset:28672
s_mov_b32 s28, 458752
v_mfma_f32_16x16x32_bf16 a[44:47], v[100:103], a[84:87], a[44:47]
s_add_u32 m0, s89, 98304
s_nop 0
buffer_load_dwordx4 v32, s[52:55], s28 offen lds
s_waitcnt lgkmcnt(10)
v_mfma_f32_16x16x32_bf16 a[48:51], v[104:107], a[80:83], a[48:51]
ds_read_b64_tr_b16 v[96:97], v68 offset:24576
v_mfma_f32_16x16x32_bf16 a[52:55], v[104:107], a[84:87], a[52:55]
ds_read_b64_tr_b16 v[98:99], v68 offset:28672
s_waitcnt lgkmcnt(10)
v_mfma_f32_16x16x32_bf16 a[56:59], v[108:111], a[80:83], a[56:59]
ds_read_b64_tr_b16 v[100:101], v69 offset:24576
ds_read_b64_tr_b16 v[102:103], v69 offset:28672
v_mfma_f32_16x16x32_bf16 a[60:63], v[108:111], a[84:87], a[60:63]
buffer_load_dwordx4 v33, s[52:55], s28 offen offset:1024 lds
s_waitcnt vmcnt(17) lgkmcnt(10)
v_mfma_f32_16x16x32_bf16 a[0:3], v[80:83], a[88:91], a[0:3]
ds_read_b64_tr_b16 v[104:105], v70 offset:24576
s_waitcnt vmcnt(16)
v_mfma_f32_16x16x32_bf16 a[4:7], v[80:83], a[92:95], a[4:7]
ds_read_b64_tr_b16 v[106:107], v70 offset:28672
s_waitcnt lgkmcnt(10)
v_mfma_f32_16x16x32_bf16 a[8:11], v[84:87], a[88:91], a[8:11]
ds_read_b64_tr_b16 v[108:109], v71 offset:24576
ds_read_b64_tr_b16 v[110:111], v71 offset:28672
v_mfma_f32_16x16x32_bf16 a[12:15], v[84:87], a[92:95], a[12:15]
buffer_load_dwordx4 v34, s[52:55], s28 offen offset:2048 lds
s_barrier
s_waitcnt lgkmcnt(10)
v_mfma_f32_16x16x32_bf16 a[16:19], v[88:91], a[88:91], a[16:19]
ds_read_b64_tr_b16 v[80:81], v72 offset:0
v_mfma_f32_16x16x32_bf16 a[20:23], v[88:91], a[92:95], a[20:23]
ds_read_b64_tr_b16 v[82:83], v72 offset:4096
s_waitcnt lgkmcnt(10)
v_mfma_f32_16x16x32_bf16 a[24:27], v[92:95], a[88:91], a[24:27]
ds_read_b64_tr_b16 v[84:85], v73 offset:0
ds_read_b64_tr_b16 v[86:87], v73 offset:4096
v_mfma_f32_16x16x32_bf16 a[28:31], v[92:95], a[92:95], a[28:31]
buffer_load_dwordx4 v35, s[52:55], s28 offen offset:3072 lds
s_waitcnt lgkmcnt(10)
v_mfma_f32_16x16x32_bf16 a[32:35], v[96:99], a[88:91], a[32:35]
ds_read_b64_tr_b16 v[88:89], v74 offset:0
v_mfma_f32_16x16x32_bf16 a[36:39], v[96:99], a[92:95], a[36:39]
ds_read_b64_tr_b16 v[90:91], v74 offset:4096
s_waitcnt lgkmcnt(10)
v_mfma_f32_16x16x32_bf16 a[40:43], v[100:103], a[88:91], a[40:43]
ds_read_b64_tr_b16 v[92:93], v75 offset:0
ds_read_b64_tr_b16 v[94:95], v75 offset:4096
v_mfma_f32_16x16x32_bf16 a[44:47], v[100:103], a[92:95], a[44:47]
buffer_load_dwordx4 a[216:219], v2, s[84:87], s29 offen offset:3072 sc1
s_waitcnt lgkmcnt(10)
v_mfma_f32_16x16x32_bf16 a[48:51], v[104:107], a[88:91], a[48:51]
ds_read_b64_tr_b16 v[96:97], v76 offset:0
v_mfma_f32_16x16x32_bf16 a[52:55], v[104:107], a[92:95], a[52:55]
ds_read_b64_tr_b16 v[98:99], v76 offset:4096
s_waitcnt lgkmcnt(10)
v_mfma_f32_16x16x32_bf16 a[56:59], v[108:111], a[88:91], a[56:59]
ds_read_b64_tr_b16 v[100:101], v77 offset:0
ds_read_b64_tr_b16 v[102:103], v77 offset:4096
v_mfma_f32_16x16x32_bf16 a[60:63], v[108:111], a[92:95], a[60:63]
buffer_load_dwordx4 a[220:223], v18, s[84:87], s29 offen offset:3072 sc1
s_waitcnt vmcnt(19) lgkmcnt(10)
v_mfma_f32_16x16x32_bf16 a[0:3], v[80:83], a[96:99], a[0:3]
ds_read_b64_tr_b16 v[104:105], v78 offset:0
s_waitcnt vmcnt(18)
v_mfma_f32_16x16x32_bf16 a[4:7], v[80:83], a[100:103], a[4:7]
ds_read_b64_tr_b16 v[106:107], v78 offset:4096
s_waitcnt lgkmcnt(10)
v_mfma_f32_16x16x32_bf16 a[8:11], v[84:87], a[96:99], a[8:11]
ds_read_b64_tr_b16 v[108:109], v79 offset:0
ds_read_b64_tr_b16 v[110:111], v79 offset:4096
v_mfma_f32_16x16x32_bf16 a[12:15], v[84:87], a[100:103], a[12:15]
s_add_u32 s29, s90, 12288
buffer_load_dwordx4 a[224:227], v2, s[84:87], s29 offen offset:0 sc1
s_waitcnt lgkmcnt(10)
v_mfma_f32_16x16x32_bf16 a[16:19], v[88:91], a[96:99], a[16:19]
ds_read_b64_tr_b16 v[80:81], v72 offset:8192
v_mfma_f32_16x16x32_bf16 a[20:23], v[88:91], a[100:103], a[20:23]
ds_read_b64_tr_b16 v[82:83], v72 offset:12288
s_waitcnt lgkmcnt(10)
v_mfma_f32_16x16x32_bf16 a[24:27], v[92:95], a[96:99], a[24:27]
ds_read_b64_tr_b16 v[84:85], v73 offset:8192
ds_read_b64_tr_b16 v[86:87], v73 offset:12288
v_mfma_f32_16x16x32_bf16 a[28:31], v[92:95], a[100:103], a[28:31]
buffer_load_dwordx4 a[228:231], v18, s[84:87], s29 offen offset:0 sc1
s_waitcnt lgkmcnt(10)
v_mfma_f32_16x16x32_bf16 a[32:35], v[96:99], a[96:99], a[32:35]
ds_read_b64_tr_b16 v[88:89], v74 offset:8192
v_mfma_f32_16x16x32_bf16 a[36:39], v[96:99], a[100:103], a[36:39]
ds_read_b64_tr_b16 v[90:91], v74 offset:12288
s_waitcnt lgkmcnt(10)
v_mfma_f32_16x16x32_bf16 a[40:43], v[100:103], a[96:99], a[40:43]
ds_read_b64_tr_b16 v[92:93], v75 offset:8192
ds_read_b64_tr_b16 v[94:95], v75 offset:12288
s_mov_b32 s28, 524288
v_mfma_f32_16x16x32_bf16 a[44:47], v[100:103], a[100:103], a[44:47]
s_add_u32 m0, s89, 114688
s_nop 0
buffer_load_dwordx4 v32, s[52:55], s28 offen lds
s_waitcnt lgkmcnt(10)
v_mfma_f32_16x16x32_bf16 a[48:51], v[104:107], a[96:99], a[48:51]
ds_read_b64_tr_b16 v[96:97], v76 offset:8192
v_mfma_f32_16x16x32_bf16 a[52:55], v[104:107], a[100:103], a[52:55]
ds_read_b64_tr_b16 v[98:99], v76 offset:12288
s_waitcnt lgkmcnt(10)
v_mfma_f32_16x16x32_bf16 a[56:59], v[108:111], a[96:99], a[56:59]
ds_read_b64_tr_b16 v[100:101], v77 offset:8192
ds_read_b64_tr_b16 v[102:103], v77 offset:12288
v_mfma_f32_16x16x32_bf16 a[60:63], v[108:111], a[100:103], a[60:63]
buffer_load_dwordx4 v33, s[52:55], s28 offen offset:1024 lds
s_waitcnt vmcnt(21) lgkmcnt(10)
v_mfma_f32_16x16x32_bf16 a[0:3], v[80:83], a[104:107], a[0:3]
ds_read_b64_tr_b16 v[104:105], v78 offset:8192
s_waitcnt vmcnt(20)
v_mfma_f32_16x16x32_bf16 a[4:7], v[80:83], a[108:111], a[4:7]
ds_read_b64_tr_b16 v[106:107], v78 offset:12288
s_waitcnt lgkmcnt(10)
v_mfma_f32_16x16x32_bf16 a[8:11], v[84:87], a[104:107], a[8:11]
ds_read_b64_tr_b16 v[108:109], v79 offset:8192
ds_read_b64_tr_b16 v[110:111], v79 offset:12288
v_mfma_f32_16x16x32_bf16 a[12:15], v[84:87], a[108:111], a[12:15]
buffer_load_dwordx4 v34, s[52:55], s28 offen offset:2048 lds
s_barrier
s_waitcnt lgkmcnt(10)
v_mfma_f32_16x16x32_bf16 a[16:19], v[88:91], a[104:107], a[16:19]
ds_read_b64_tr_b16 v[80:81], v72 offset:16384
v_mfma_f32_16x16x32_bf16 a[20:23], v[88:91], a[108:111], a[20:23]
ds_read_b64_tr_b16 v[82:83], v72 offset:20480
s_waitcnt lgkmcnt(10)
v_mfma_f32_16x16x32_bf16 a[24:27], v[92:95], a[104:107], a[24:27]
ds_read_b64_tr_b16 v[84:85], v73 offset:16384
ds_read_b64_tr_b16 v[86:87], v73 offset:20480
v_mfma_f32_16x16x32_bf16 a[28:31], v[92:95], a[108:111], a[28:31]
buffer_load_dwordx4 v35, s[52:55], s28 offen offset:3072 lds
s_waitcnt lgkmcnt(10)
v_mfma_f32_16x16x32_bf16 a[32:35], v[96:99], a[104:107], a[32:35]
ds_read_b64_tr_b16 v[88:89], v74 offset:16384
v_mfma_f32_16x16x32_bf16 a[36:39], v[96:99], a[108:111], a[36:39]
ds_read_b64_tr_b16 v[90:91], v74 offset:20480
s_waitcnt lgkmcnt(10)
v_mfma_f32_16x16x32_bf16 a[40:43], v[100:103], a[104:107], a[40:43]
ds_read_b64_tr_b16 v[92:93], v75 offset:16384
ds_read_b64_tr_b16 v[94:95], v75 offset:20480
v_mfma_f32_16x16x32_bf16 a[44:47], v[100:103], a[108:111], a[44:47]
buffer_load_dwordx4 a[232:235], v2, s[84:87], s29 offen offset:1024 sc1
s_waitcnt lgkmcnt(10)
v_mfma_f32_16x16x32_bf16 a[48:51], v[104:107], a[104:107], a[48:51]
ds_read_b64_tr_b16 v[96:97], v76 offset:16384
v_mfma_f32_16x16x32_bf16 a[52:55], v[104:107], a[108:111], a[52:55]
ds_read_b64_tr_b16 v[98:99], v76 offset:20480
s_waitcnt lgkmcnt(10)
v_mfma_f32_16x16x32_bf16 a[56:59], v[108:111], a[104:107], a[56:59]
ds_read_b64_tr_b16 v[100:101], v77 offset:16384
ds_read_b64_tr_b16 v[102:103], v77 offset:20480
v_mfma_f32_16x16x32_bf16 a[60:63], v[108:111], a[108:111], a[60:63]
buffer_load_dwordx4 a[236:239], v18, s[84:87], s29 offen offset:1024 sc1
s_waitcnt vmcnt(23) lgkmcnt(10)
v_mfma_f32_16x16x32_bf16 a[0:3], v[80:83], a[112:115], a[0:3]
ds_read_b64_tr_b16 v[104:105], v78 offset:16384
s_waitcnt vmcnt(22)
v_mfma_f32_16x16x32_bf16 a[4:7], v[80:83], a[116:119], a[4:7]
ds_read_b64_tr_b16 v[106:107], v78 offset:20480
s_waitcnt lgkmcnt(10)
v_mfma_f32_16x16x32_bf16 a[8:11], v[84:87], a[112:115], a[8:11]
ds_read_b64_tr_b16 v[108:109], v79 offset:16384
ds_read_b64_tr_b16 v[110:111], v79 offset:20480
v_mfma_f32_16x16x32_bf16 a[12:15], v[84:87], a[116:119], a[12:15]
buffer_load_dwordx4 a[240:243], v2, s[84:87], s29 offen offset:2048 sc1
s_waitcnt lgkmcnt(10)
v_mfma_f32_16x16x32_bf16 a[16:19], v[88:91], a[112:115], a[16:19]
ds_read_b64_tr_b16 v[80:81], v72 offset:24576
v_mfma_f32_16x16x32_bf16 a[20:23], v[88:91], a[116:119], a[20:23]
ds_read_b64_tr_b16 v[82:83], v72 offset:28672
s_waitcnt lgkmcnt(10)
v_mfma_f32_16x16x32_bf16 a[24:27], v[92:95], a[112:115], a[24:27]
ds_read_b64_tr_b16 v[84:85], v73 offset:24576
ds_read_b64_tr_b16 v[86:87], v73 offset:28672
v_mfma_f32_16x16x32_bf16 a[28:31], v[92:95], a[116:119], a[28:31]
buffer_load_dwordx4 a[244:247], v18, s[84:87], s29 offen offset:2048 sc1
s_waitcnt lgkmcnt(10)
v_mfma_f32_16x16x32_bf16 a[32:35], v[96:99], a[112:115], a[32:35]
ds_read_b64_tr_b16 v[88:89], v74 offset:24576
v_mfma_f32_16x16x32_bf16 a[36:39], v[96:99], a[116:119], a[36:39]
ds_read_b64_tr_b16 v[90:91], v74 offset:28672
s_waitcnt lgkmcnt(10)
v_mfma_f32_16x16x32_bf16 a[40:43], v[100:103], a[112:115], a[40:43]
ds_read_b64_tr_b16 v[92:93], v75 offset:24576
ds_read_b64_tr_b16 v[94:95], v75 offset:28672
s_mov_b32 s28, 589824
v_mfma_f32_16x16x32_bf16 a[44:47], v[100:103], a[116:119], a[44:47]
s_add_u32 m0, s89, 0
s_nop 0
buffer_load_dwordx4 v32, s[52:55], s28 offen lds
s_waitcnt lgkmcnt(10)
v_mfma_f32_16x16x32_bf16 a[48:51], v[104:107], a[112:115], a[48:51]
ds_read_b64_tr_b16 v[96:97], v76 offset:24576
v_mfma_f32_16x16x32_bf16 a[52:55], v[104:107], a[116:119], a[52:55]
ds_read_b64_tr_b16 v[98:99], v76 offset:28672
s_waitcnt lgkmcnt(10)
v_mfma_f32_16x16x32_bf16 a[56:59], v[108:111], a[112:115], a[56:59]
ds_read_b64_tr_b16 v[100:101], v77 offset:24576
ds_read_b64_tr_b16 v[102:103], v77 offset:28672
v_mfma_f32_16x16x32_bf16 a[60:63], v[108:111], a[116:119], a[60:63]
buffer_load_dwordx4 v33, s[52:55], s28 offen offset:1024 lds
s_waitcnt vmcnt(21) lgkmcnt(10)
v_mfma_f32_16x16x32_bf16 a[0:3], v[80:83], a[120:123], a[0:3]
ds_read_b64_tr_b16 v[104:105], v78 offset:24576
s_waitcnt vmcnt(20)
v_mfma_f32_16x16x32_bf16 a[4:7], v[80:83], a[124:127], a[4:7]
ds_read_b64_tr_b16 v[106:107], v78 offset:28672
s_waitcnt lgkmcnt(10)
v_mfma_f32_16x16x32_bf16 a[8:11], v[84:87], a[120:123], a[8:11]
ds_read_b64_tr_b16 v[108:109], v79 offset:24576
ds_read_b64_tr_b16 v[110:111], v79 offset:28672
v_mfma_f32_16x16x32_bf16 a[12:15], v[84:87], a[124:127], a[12:15]
buffer_load_dwordx4 v34, s[52:55], s28 offen offset:2048 lds
s_barrier
s_waitcnt lgkmcnt(10)
v_mfma_f32_16x16x32_bf16 a[16:19], v[88:91], a[120:123], a[16:19]
ds_read_b64_tr_b16 v[80:81], v64 offset:32768
v_mfma_f32_16x16x32_bf16 a[20:23], v[88:91], a[124:127], a[20:23]
ds_read_b64_tr_b16 v[82:83], v64 offset:36864
s_waitcnt lgkmcnt(10)
v_mfma_f32_16x16x32_bf16 a[24:27], v[92:95], a[120:123], a[24:27]
ds_read_b64_tr_b16 v[84:85], v65 offset:32768
ds_read_b64_tr_b16 v[86:87], v65 offset:36864
v_mfma_f32_16x16x32_bf16 a[28:31], v[92:95], a[124:127], a[28:31]
buffer_load_dwordx4 v35, s[52:55], s28 offen offset:3072 lds
s_waitcnt lgkmcnt(10)
v_mfma_f32_16x16x32_bf16 a[32:35], v[96:99], a[120:123], a[32:35]
ds_read_b64_tr_b16 v[88:89], v66 offset:32768
v_mfma_f32_16x16x32_bf16 a[36:39], v[96:99], a[124:127], a[36:39]
ds_read_b64_tr_b16 v[90:91], v66 offset:36864
s_waitcnt lgkmcnt(10)
v_mfma_f32_16x16x32_bf16 a[40:43], v[100:103], a[120:123], a[40:43]
ds_read_b64_tr_b16 v[92:93], v67 offset:32768
ds_read_b64_tr_b16 v[94:95], v67 offset:36864
v_mfma_f32_16x16x32_bf16 a[44:47], v[100:103], a[124:127], a[44:47]
buffer_load_dwordx4 a[248:251], v2, s[84:87], s29 offen offset:3072 sc1
s_waitcnt lgkmcnt(10)
v_mfma_f32_16x16x32_bf16 a[48:51], v[104:107], a[120:123], a[48:51]
ds_read_b64_tr_b16 v[96:97], v68 offset:32768
v_mfma_f32_16x16x32_bf16 a[52:55], v[104:107], a[124:127], a[52:55]
ds_read_b64_tr_b16 v[98:99], v68 offset:36864
s_waitcnt lgkmcnt(10)
v_mfma_f32_16x16x32_bf16 a[56:59], v[108:111], a[120:123], a[56:59]
ds_read_b64_tr_b16 v[100:101], v69 offset:32768
ds_read_b64_tr_b16 v[102:103], v69 offset:36864
v_mfma_f32_16x16x32_bf16 a[60:63], v[108:111], a[124:127], a[60:63]
buffer_load_dwordx4 a[252:255], v18, s[84:87], s29 offen offset:3072 sc1
s_waitcnt vmcnt(23) lgkmcnt(10)
v_mfma_f32_16x16x32_bf16 a[0:3], v[80:83], a[208:211], a[0:3]
ds_read_b64_tr_b16 v[104:105], v70 offset:32768
s_waitcnt vmcnt(22)
v_mfma_f32_16x16x32_bf16 a[4:7], v[80:83], a[212:215], a[4:7]
ds_read_b64_tr_b16 v[106:107], v70 offset:36864
s_waitcnt lgkmcnt(10)
v_mfma_f32_16x16x32_bf16 a[8:11], v[84:87], a[208:211], a[8:11]
ds_read_b64_tr_b16 v[108:109], v71 offset:32768
ds_read_b64_tr_b16 v[110:111], v71 offset:36864
v_mfma_f32_16x16x32_bf16 a[12:15], v[84:87], a[212:215], a[12:15]
s_add_u32 s29, s90, 16384
buffer_load_dwordx4 a[160:163], v2, s[84:87], s29 offen offset:0 sc1
s_waitcnt lgkmcnt(10)
v_mfma_f32_16x16x32_bf16 a[16:19], v[88:91], a[208:211], a[16:19]
ds_read_b64_tr_b16 v[80:81], v64 offset:40960
v_mfma_f32_16x16x32_bf16 a[20:23], v[88:91], a[212:215], a[20:23]
ds_read_b64_tr_b16 v[82:83], v64 offset:45056
s_waitcnt lgkmcnt(10)
v_mfma_f32_16x16x32_bf16 a[24:27], v[92:95], a[208:211], a[24:27]
ds_read_b64_tr_b16 v[84:85], v65 offset:40960
ds_read_b64_tr_b16 v[86:87], v65 offset:45056
v_mfma_f32_16x16x32_bf16 a[28:31], v[92:95], a[212:215], a[28:31]
buffer_load_dwordx4 a[164:167], v18, s[84:87], s29 offen offset:0 sc1
s_waitcnt lgkmcnt(10)
v_mfma_f32_16x16x32_bf16 a[32:35], v[96:99], a[208:211], a[32:35]
ds_read_b64_tr_b16 v[88:89], v66 offset:40960
v_mfma_f32_16x16x32_bf16 a[36:39], v[96:99], a[212:215], a[36:39]
ds_read_b64_tr_b16 v[90:91], v66 offset:45056
s_waitcnt lgkmcnt(10)
v_mfma_f32_16x16x32_bf16 a[40:43], v[100:103], a[208:211], a[40:43]
ds_read_b64_tr_b16 v[92:93], v67 offset:40960
ds_read_b64_tr_b16 v[94:95], v67 offset:45056
s_mov_b32 s28, 655360
v_mfma_f32_16x16x32_bf16 a[44:47], v[100:103], a[212:215], a[44:47]
s_add_u32 m0, s89, 16384
s_nop 0
buffer_load_dwordx4 v32, s[52:55], s28 offen lds
s_waitcnt lgkmcnt(10)
v_mfma_f32_16x16x32_bf16 a[48:51], v[104:107], a[208:211], a[48:51]
ds_read_b64_tr_b16 v[96:97], v68 offset:40960
v_mfma_f32_16x16x32_bf16 a[52:55], v[104:107], a[212:215], a[52:55]
ds_read_b64_tr_b16 v[98:99], v68 offset:45056
s_waitcnt lgkmcnt(10)
v_mfma_f32_16x16x32_bf16 a[56:59], v[108:111], a[208:211], a[56:59]
ds_read_b64_tr_b16 v[100:101], v69 offset:40960
ds_read_b64_tr_b16 v[102:103], v69 offset:45056
v_mfma_f32_16x16x32_bf16 a[60:63], v[108:111], a[212:215], a[60:63]
buffer_load_dwordx4 v33, s[52:55], s28 offen offset:1024 lds
s_waitcnt vmcnt(21) lgkmcnt(10)
v_mfma_f32_16x16x32_bf16 a[0:3], v[80:83], a[216:219], a[0:3]
ds_read_b64_tr_b16 v[104:105], v70 offset:40960
s_waitcnt vmcnt(20)
v_mfma_f32_16x16x32_bf16 a[4:7], v[80:83], a[220:223], a[4:7]
ds_read_b64_tr_b16 v[106:107], v70 offset:45056
s_waitcnt lgkmcnt(10)
v_mfma_f32_16x16x32_bf16 a[8:11], v[84:87], a[216:219], a[8:11]
ds_read_b64_tr_b16 v[108:109], v71 offset:40960
ds_read_b64_tr_b16 v[110:111], v71 offset:45056
v_mfma_f32_16x16x32_bf16 a[12:15], v[84:87], a[220:223], a[12:15]
buffer_load_dwordx4 v34, s[52:55], s28 offen offset:2048 lds
s_barrier
s_waitcnt lgkmcnt(10)
v_mfma_f32_16x16x32_bf16 a[16:19], v[88:91], a[216:219], a[16:19]
ds_read_b64_tr_b16 v[80:81], v64 offset:49152
v_mfma_f32_16x16x32_bf16 a[20:23], v[88:91], a[220:223], a[20:23]
ds_read_b64_tr_b16 v[82:83], v64 offset:53248
s_waitcnt lgkmcnt(10)
v_mfma_f32_16x16x32_bf16 a[24:27], v[92:95], a[216:219], a[24:27]
ds_read_b64_tr_b16 v[84:85], v65 offset:49152
ds_read_b64_tr_b16 v[86:87], v65 offset:53248
v_mfma_f32_16x16x32_bf16 a[28:31], v[92:95], a[220:223], a[28:31]
buffer_load_dwordx4 v35, s[52:55], s28 offen offset:3072 lds
s_waitcnt lgkmcnt(10)
v_mfma_f32_16x16x32_bf16 a[32:35], v[96:99], a[216:219], a[32:35]
ds_read_b64_tr_b16 v[88:89], v66 offset:49152
v_mfma_f32_16x16x32_bf16 a[36:39], v[96:99], a[220:223], a[36:39]
ds_read_b64_tr_b16 v[90:91], v66 offset:53248
s_waitcnt lgkmcnt(10)
v_mfma_f32_16x16x32_bf16 a[40:43], v[100:103], a[216:219], a[40:43]
ds_read_b64_tr_b16 v[92:93], v67 offset:49152
ds_read_b64_tr_b16 v[94:95], v67 offset:53248
v_mfma_f32_16x16x32_bf16 a[44:47], v[100:103], a[220:223], a[44:47]
buffer_load_dwordx4 a[168:171], v2, s[84:87], s29 offen offset:1024 sc1
s_waitcnt lgkmcnt(10)
v_mfma_f32_16x16x32_bf16 a[48:51], v[104:107], a[216:219], a[48:51]
ds_read_b64_tr_b16 v[96:97], v68 offset:49152
v_mfma_f32_16x16x32_bf16 a[52:55], v[104:107], a[220:223], a[52:55]
ds_read_b64_tr_b16 v[98:99], v68 offset:53248
s_waitcnt lgkmcnt(10)
v_mfma_f32_16x16x32_bf16 a[56:59], v[108:111], a[216:219], a[56:59]
ds_read_b64_tr_b16 v[100:101], v69 offset:49152
ds_read_b64_tr_b16 v[102:103], v69 offset:53248
v_mfma_f32_16x16x32_bf16 a[60:63], v[108:111], a[220:223], a[60:63]
buffer_load_dwordx4 a[172:175], v18, s[84:87], s29 offen offset:1024 sc1
s_waitcnt vmcnt(23) lgkmcnt(10)
v_mfma_f32_16x16x32_bf16 a[0:3], v[80:83], a[224:227], a[0:3]
ds_read_b64_tr_b16 v[104:105], v70 offset:49152
s_waitcnt vmcnt(22)
v_mfma_f32_16x16x32_bf16 a[4:7], v[80:83], a[228:231], a[4:7]
ds_read_b64_tr_b16 v[106:107], v70 offset:53248
s_waitcnt lgkmcnt(10)
v_mfma_f32_16x16x32_bf16 a[8:11], v[84:87], a[224:227], a[8:11]
ds_read_b64_tr_b16 v[108:109], v71 offset:49152
ds_read_b64_tr_b16 v[110:111], v71 offset:53248
v_mfma_f32_16x16x32_bf16 a[12:15], v[84:87], a[228:231], a[12:15]
buffer_load_dwordx4 a[176:179], v2, s[84:87], s29 offen offset:2048 sc1
s_waitcnt lgkmcnt(10)
v_mfma_f32_16x16x32_bf16 a[16:19], v[88:91], a[224:227], a[16:19]
ds_read_b64_tr_b16 v[80:81], v64 offset:57344
v_mfma_f32_16x16x32_bf16 a[20:23], v[88:91], a[228:231], a[20:23]
ds_read_b64_tr_b16 v[82:83], v64 offset:61440
s_waitcnt lgkmcnt(10)
v_mfma_f32_16x16x32_bf16 a[24:27], v[92:95], a[224:227], a[24:27]
ds_read_b64_tr_b16 v[84:85], v65 offset:57344
ds_read_b64_tr_b16 v[86:87], v65 offset:61440
v_mfma_f32_16x16x32_bf16 a[28:31], v[92:95], a[228:231], a[28:31]
buffer_load_dwordx4 a[180:183], v18, s[84:87], s29 offen offset:2048 sc1
s_waitcnt lgkmcnt(10)
v_mfma_f32_16x16x32_bf16 a[32:35], v[96:99], a[224:227], a[32:35]
ds_read_b64_tr_b16 v[88:89], v66 offset:57344
v_mfma_f32_16x16x32_bf16 a[36:39], v[96:99], a[228:231], a[36:39]
ds_read_b64_tr_b16 v[90:91], v66 offset:61440
s_waitcnt lgkmcnt(10)
v_mfma_f32_16x16x32_bf16 a[40:43], v[100:103], a[224:227], a[40:43]
ds_read_b64_tr_b16 v[92:93], v67 offset:57344
ds_read_b64_tr_b16 v[94:95], v67 offset:61440
s_mov_b32 s28, 720896
v_mfma_f32_16x16x32_bf16 a[44:47], v[100:103], a[228:231], a[44:47]
s_add_u32 m0, s89, 65536
s_nop 0
buffer_load_dwordx4 v32, s[52:55], s28 offen lds
s_waitcnt lgkmcnt(10)
v_mfma_f32_16x16x32_bf16 a[48:51], v[104:107], a[224:227], a[48:51]
ds_read_b64_tr_b16 v[96:97], v68 offset:57344
v_mfma_f32_16x16x32_bf16 a[52:55], v[104:107], a[228:231], a[52:55]
ds_read_b64_tr_b16 v[98:99], v68 offset:61440
s_waitcnt lgkmcnt(10)
v_mfma_f32_16x16x32_bf16 a[56:59], v[108:111], a[224:227], a[56:59]
ds_read_b64_tr_b16 v[100:101], v69 offset:57344
ds_read_b64_tr_b16 v[102:103], v69 offset:61440
v_mfma_f32_16x16x32_bf16 a[60:63], v[108:111], a[228:231], a[60:63]
buffer_load_dwordx4 v33, s[52:55], s28 offen offset:1024 lds
s_waitcnt vmcnt(21) lgkmcnt(10)
v_mfma_f32_16x16x32_bf16 a[0:3], v[80:83], a[232:235], a[0:3]
ds_read_b64_tr_b16 v[104:105], v70 offset:57344
s_waitcnt vmcnt(20)
v_mfma_f32_16x16x32_bf16 a[4:7], v[80:83], a[236:239], a[4:7]
ds_read_b64_tr_b16 v[106:107], v70 offset:61440
s_waitcnt lgkmcnt(10)
v_mfma_f32_16x16x32_bf16 a[8:11], v[84:87], a[232:235], a[8:11]
ds_read_b64_tr_b16 v[108:109], v71 offset:57344
ds_read_b64_tr_b16 v[110:111], v71 offset:61440
v_mfma_f32_16x16x32_bf16 a[12:15], v[84:87], a[236:239], a[12:15]
buffer_load_dwordx4 v34, s[52:55], s28 offen offset:2048 lds
s_barrier
s_waitcnt lgkmcnt(10)
v_mfma_f32_16x16x32_bf16 a[16:19], v[88:91], a[232:235], a[16:19]
ds_read_b64_tr_b16 v[80:81], v72 offset:32768
v_mfma_f32_16x16x32_bf16 a[20:23], v[88:91], a[236:239], a[20:23]
ds_read_b64_tr_b16 v[82:83], v72 offset:36864
s_waitcnt lgkmcnt(10)
v_mfma_f32_16x16x32_bf16 a[24:27], v[92:95], a[232:235], a[24:27]
ds_read_b64_tr_b16 v[84:85], v73 offset:32768
ds_read_b64_tr_b16 v[86:87], v73 offset:36864
v_mfma_f32_16x16x32_bf16 a[28:31], v[92:95], a[236:239], a[28:31]
buffer_load_dwordx4 v35, s[52:55], s28 offen offset:3072 lds
s_waitcnt lgkmcnt(10)
v_mfma_f32_16x16x32_bf16 a[32:35], v[96:99], a[232:235], a[32:35]
ds_read_b64_tr_b16 v[88:89], v74 offset:32768
v_mfma_f32_16x16x32_bf16 a[36:39], v[96:99], a[236:239], a[36:39]
ds_read_b64_tr_b16 v[90:91], v74 offset:36864
s_waitcnt lgkmcnt(10)
v_mfma_f32_16x16x32_bf16 a[40:43], v[100:103], a[232:235], a[40:43]
ds_read_b64_tr_b16 v[92:93], v75 offset:32768
ds_read_b64_tr_b16 v[94:95], v75 offset:36864
v_mfma_f32_16x16x32_bf16 a[44:47], v[100:103], a[236:239], a[44:47]
buffer_load_dwordx4 a[184:187], v2, s[84:87], s29 offen offset:3072 sc1
s_waitcnt lgkmcnt(10)
v_mfma_f32_16x16x32_bf16 a[48:51], v[104:107], a[232:235], a[48:51]
ds_read_b64_tr_b16 v[96:97], v76 offset:32768
v_mfma_f32_16x16x32_bf16 a[52:55], v[104:107], a[236:239], a[52:55]
ds_read_b64_tr_b16 v[98:99], v76 offset:36864
s_waitcnt lgkmcnt(10)
v_mfma_f32_16x16x32_bf16 a[56:59], v[108:111], a[232:235], a[56:59]
ds_read_b64_tr_b16 v[100:101], v77 offset:32768
ds_read_b64_tr_b16 v[102:103], v77 offset:36864
v_mfma_f32_16x16x32_bf16 a[60:63], v[108:111], a[236:239], a[60:63]
buffer_load_dwordx4 a[188:191], v18, s[84:87], s29 offen offset:3072 sc1
s_waitcnt vmcnt(23) lgkmcnt(10)
v_mfma_f32_16x16x32_bf16 a[0:3], v[80:83], a[240:243], a[0:3]
ds_read_b64_tr_b16 v[104:105], v78 offset:32768
s_waitcnt vmcnt(22)
v_mfma_f32_16x16x32_bf16 a[4:7], v[80:83], a[244:247], a[4:7]
ds_read_b64_tr_b16 v[106:107], v78 offset:36864
s_waitcnt lgkmcnt(10)
v_mfma_f32_16x16x32_bf16 a[8:11], v[84:87], a[240:243], a[8:11]
ds_read_b64_tr_b16 v[108:109], v79 offset:32768
ds_read_b64_tr_b16 v[110:111], v79 offset:36864
v_mfma_f32_16x16x32_bf16 a[12:15], v[84:87], a[244:247], a[12:15]
s_add_u32 s29, s90, 20480
buffer_load_dwordx4 a[128:131], v2, s[84:87], s29 offen offset:0 sc1
s_waitcnt lgkmcnt(10)
v_mfma_f32_16x16x32_bf16 a[16:19], v[88:91], a[240:243], a[16:19]
ds_read_b64_tr_b16 v[80:81], v72 offset:40960
v_mfma_f32_16x16x32_bf16 a[20:23], v[88:91], a[244:247], a[20:23]
ds_read_b64_tr_b16 v[82:83], v72 offset:45056
s_waitcnt lgkmcnt(10)
v_mfma_f32_16x16x32_bf16 a[24:27], v[92:95], a[240:243], a[24:27]
ds_read_b64_tr_b16 v[84:85], v73 offset:40960
ds_read_b64_tr_b16 v[86:87], v73 offset:45056
v_mfma_f32_16x16x32_bf16 a[28:31], v[92:95], a[244:247], a[28:31]
buffer_load_dwordx4 a[132:135], v18, s[84:87], s29 offen offset:0 sc1
s_waitcnt lgkmcnt(10)
v_mfma_f32_16x16x32_bf16 a[32:35], v[96:99], a[240:243], a[32:35]
ds_read_b64_tr_b16 v[88:89], v74 offset:40960
v_mfma_f32_16x16x32_bf16 a[36:39], v[96:99], a[244:247], a[36:39]
ds_read_b64_tr_b16 v[90:91], v74 offset:45056
s_waitcnt lgkmcnt(10)
v_mfma_f32_16x16x32_bf16 a[40:43], v[100:103], a[240:243], a[40:43]
ds_read_b64_tr_b16 v[92:93], v75 offset:40960
ds_read_b64_tr_b16 v[94:95], v75 offset:45056
s_mov_b32 s28, 786432
v_mfma_f32_16x16x32_bf16 a[44:47], v[100:103], a[244:247], a[44:47]
s_add_u32 m0, s89, 81920
s_nop 0
buffer_load_dwordx4 v32, s[52:55], s28 offen lds
s_waitcnt lgkmcnt(10)
v_mfma_f32_16x16x32_bf16 a[48:51], v[104:107], a[240:243], a[48:51]
ds_read_b64_tr_b16 v[96:97], v76 offset:40960
v_mfma_f32_16x16x32_bf16 a[52:55], v[104:107], a[244:247], a[52:55]
ds_read_b64_tr_b16 v[98:99], v76 offset:45056
s_waitcnt lgkmcnt(10)
v_mfma_f32_16x16x32_bf16 a[56:59], v[108:111], a[240:243], a[56:59]
ds_read_b64_tr_b16 v[100:101], v77 offset:40960
ds_read_b64_tr_b16 v[102:103], v77 offset:45056
v_mfma_f32_16x16x32_bf16 a[60:63], v[108:111], a[244:247], a[60:63]
buffer_load_dwordx4 v33, s[52:55], s28 offen offset:1024 lds
s_waitcnt vmcnt(21) lgkmcnt(10)
v_mfma_f32_16x16x32_bf16 a[0:3], v[80:83], a[248:251], a[0:3]
ds_read_b64_tr_b16 v[104:105], v78 offset:40960
s_waitcnt vmcnt(20)
v_mfma_f32_16x16x32_bf16 a[4:7], v[80:83], a[252:255], a[4:7]
ds_read_b64_tr_b16 v[106:107], v78 offset:45056
s_waitcnt lgkmcnt(10)
v_mfma_f32_16x16x32_bf16 a[8:11], v[84:87], a[248:251], a[8:11]
ds_read_b64_tr_b16 v[108:109], v79 offset:40960
ds_read_b64_tr_b16 v[110:111], v79 offset:45056
v_mfma_f32_16x16x32_bf16 a[12:15], v[84:87], a[252:255], a[12:15]
buffer_load_dwordx4 v34, s[52:55], s28 offen offset:2048 lds
s_barrier
s_waitcnt lgkmcnt(10)
v_mfma_f32_16x16x32_bf16 a[16:19], v[88:91], a[248:251], a[16:19]
ds_read_b64_tr_b16 v[80:81], v72 offset:49152
v_mfma_f32_16x16x32_bf16 a[20:23], v[88:91], a[252:255], a[20:23]
ds_read_b64_tr_b16 v[82:83], v72 offset:53248
s_waitcnt lgkmcnt(10)
v_mfma_f32_16x16x32_bf16 a[24:27], v[92:95], a[248:251], a[24:27]
ds_read_b64_tr_b16 v[84:85], v73 offset:49152
)ASM"
R"ASM(ds_read_b64_tr_b16 v[86:87], v73 offset:53248
v_mfma_f32_16x16x32_bf16 a[28:31], v[92:95], a[252:255], a[28:31]
buffer_load_dwordx4 v35, s[52:55], s28 offen offset:3072 lds
s_waitcnt lgkmcnt(10)
v_mfma_f32_16x16x32_bf16 a[32:35], v[96:99], a[248:251], a[32:35]
ds_read_b64_tr_b16 v[88:89], v74 offset:49152
v_mfma_f32_16x16x32_bf16 a[36:39], v[96:99], a[252:255], a[36:39]
ds_read_b64_tr_b16 v[90:91], v74 offset:53248
s_waitcnt lgkmcnt(10)
v_mfma_f32_16x16x32_bf16 a[40:43], v[100:103], a[248:251], a[40:43]
ds_read_b64_tr_b16 v[92:93], v75 offset:49152
ds_read_b64_tr_b16 v[94:95], v75 offset:53248
v_mfma_f32_16x16x32_bf16 a[44:47], v[100:103], a[252:255], a[44:47]
buffer_load_dwordx4 a[136:139], v2, s[84:87], s29 offen offset:1024 sc1
s_waitcnt lgkmcnt(10)
v_mfma_f32_16x16x32_bf16 a[48:51], v[104:107], a[248:251], a[48:51]
ds_read_b64_tr_b16 v[96:97], v76 offset:49152
v_mfma_f32_16x16x32_bf16 a[52:55], v[104:107], a[252:255], a[52:55]
ds_read_b64_tr_b16 v[98:99], v76 offset:53248
s_waitcnt lgkmcnt(10)
v_mfma_f32_16x16x32_bf16 a[56:59], v[108:111], a[248:251], a[56:59]
ds_read_b64_tr_b16 v[100:101], v77 offset:49152
ds_read_b64_tr_b16 v[102:103], v77 offset:53248
v_mfma_f32_16x16x32_bf16 a[60:63], v[108:111], a[252:255], a[60:63]
buffer_load_dwordx4 a[140:143], v18, s[84:87], s29 offen offset:1024 sc1
s_waitcnt vmcnt(23) lgkmcnt(10)
v_mfma_f32_16x16x32_bf16 a[0:3], v[80:83], a[160:163], a[0:3]
ds_read_b64_tr_b16 v[104:105], v72 offset:57344
s_waitcnt vmcnt(22)
v_mfma_f32_16x16x32_bf16 a[4:7], v[80:83], a[164:167], a[4:7]
ds_read_b64_tr_b16 v[106:107], v72 offset:61440
s_waitcnt lgkmcnt(10)
v_mfma_f32_16x16x32_bf16 a[8:11], v[84:87], a[160:163], a[8:11]
ds_read_b64_tr_b16 v[108:109], v73 offset:57344
ds_read_b64_tr_b16 v[110:111], v73 offset:61440
v_mfma_f32_16x16x32_bf16 a[12:15], v[84:87], a[164:167], a[12:15]
buffer_load_dwordx4 a[144:147], v2, s[84:87], s29 offen offset:2048 sc1
s_waitcnt lgkmcnt(10)
v_mfma_f32_16x16x32_bf16 a[16:19], v[88:91], a[160:163], a[16:19]
ds_read_b64_tr_b16 v[80:81], v74 offset:57344
v_mfma_f32_16x16x32_bf16 a[20:23], v[88:91], a[164:167], a[20:23]
ds_read_b64_tr_b16 v[82:83], v74 offset:61440
s_waitcnt lgkmcnt(10)
v_mfma_f32_16x16x32_bf16 a[24:27], v[92:95], a[160:163], a[24:27]
ds_read_b64_tr_b16 v[84:85], v75 offset:57344
ds_read_b64_tr_b16 v[86:87], v75 offset:61440
v_mfma_f32_16x16x32_bf16 a[28:31], v[92:95], a[164:167], a[28:31]
buffer_load_dwordx4 a[148:151], v18, s[84:87], s29 offen offset:2048 sc1
s_waitcnt lgkmcnt(10)
v_mfma_f32_16x16x32_bf16 a[32:35], v[96:99], a[160:163], a[32:35]
ds_read_b64_tr_b16 v[88:89], v76 offset:57344
v_mfma_f32_16x16x32_bf16 a[36:39], v[96:99], a[164:167], a[36:39]
ds_read_b64_tr_b16 v[90:91], v76 offset:61440
s_waitcnt lgkmcnt(10)
v_mfma_f32_16x16x32_bf16 a[40:43], v[100:103], a[160:163], a[40:43]
ds_read_b64_tr_b16 v[92:93], v77 offset:57344
ds_read_b64_tr_b16 v[94:95], v77 offset:61440
s_mov_b32 s28, 851968
v_mfma_f32_16x16x32_bf16 a[44:47], v[100:103], a[164:167], a[44:47]
s_add_u32 m0, s89, 32768
s_nop 0
buffer_load_dwordx4 v32, s[52:55], s28 offen lds
s_barrier
s_waitcnt vmcnt(20) lgkmcnt(10)
v_mfma_f32_16x16x32_bf16 a[0:3], v[104:107], a[168:171], a[0:3]
ds_read_b64_tr_b16 v[96:97], v64 offset:0
s_waitcnt vmcnt(19)
v_mfma_f32_16x16x32_bf16 a[4:7], v[104:107], a[172:175], a[4:7]
ds_read_b64_tr_b16 v[98:99], v64 offset:4096
s_waitcnt lgkmcnt(10)
v_mfma_f32_16x16x32_bf16 a[8:11], v[108:111], a[168:171], a[8:11]
ds_read_b64_tr_b16 v[100:101], v65 offset:0
ds_read_b64_tr_b16 v[102:103], v65 offset:4096
v_mfma_f32_16x16x32_bf16 a[12:15], v[108:111], a[172:175], a[12:15]
buffer_load_dwordx4 v33, s[52:55], s28 offen offset:1024 lds
s_waitcnt lgkmcnt(10)
v_mfma_f32_16x16x32_bf16 a[16:19], v[80:83], a[168:171], a[16:19]
ds_read_b64_tr_b16 v[104:105], v66 offset:0
v_mfma_f32_16x16x32_bf16 a[20:23], v[80:83], a[172:175], a[20:23]
ds_read_b64_tr_b16 v[106:107], v66 offset:4096
s_waitcnt lgkmcnt(10)
v_mfma_f32_16x16x32_bf16 a[24:27], v[84:87], a[168:171], a[24:27]
ds_read_b64_tr_b16 v[108:109], v67 offset:0
ds_read_b64_tr_b16 v[110:111], v67 offset:4096
v_mfma_f32_16x16x32_bf16 a[28:31], v[84:87], a[172:175], a[28:31]
buffer_load_dwordx4 v34, s[52:55], s28 offen offset:2048 lds
s_waitcnt lgkmcnt(10)
v_mfma_f32_16x16x32_bf16 a[32:35], v[88:91], a[168:171], a[32:35]
ds_read_b64_tr_b16 v[80:81], v68 offset:0
v_mfma_f32_16x16x32_bf16 a[36:39], v[88:91], a[172:175], a[36:39]
ds_read_b64_tr_b16 v[82:83], v68 offset:4096
s_waitcnt lgkmcnt(10)
v_mfma_f32_16x16x32_bf16 a[40:43], v[92:95], a[168:171], a[40:43]
ds_read_b64_tr_b16 v[84:85], v69 offset:0
ds_read_b64_tr_b16 v[86:87], v69 offset:4096
v_mfma_f32_16x16x32_bf16 a[44:47], v[92:95], a[172:175], a[44:47]
buffer_load_dwordx4 v35, s[52:55], s28 offen offset:3072 lds
s_waitcnt vmcnt(21) lgkmcnt(10)
v_mfma_f32_16x16x32_bf16 a[0:3], v[96:99], a[176:179], a[0:3]
ds_read_b64_tr_b16 v[88:89], v64 offset:8192
s_waitcnt vmcnt(20)
v_mfma_f32_16x16x32_bf16 a[4:7], v[96:99], a[180:183], a[4:7]
ds_read_b64_tr_b16 v[90:91], v64 offset:12288
s_waitcnt lgkmcnt(10)
v_mfma_f32_16x16x32_bf16 a[8:11], v[100:103], a[176:179], a[8:11]
ds_read_b64_tr_b16 v[92:93], v65 offset:8192
ds_read_b64_tr_b16 v[94:95], v65 offset:12288
v_mfma_f32_16x16x32_bf16 a[12:15], v[100:103], a[180:183], a[12:15]
buffer_load_dwordx4 a[152:155], v2, s[84:87], s29 offen offset:3072 sc1
s_waitcnt lgkmcnt(10)
v_mfma_f32_16x16x32_bf16 a[16:19], v[104:107], a[176:179], a[16:19]
ds_read_b64_tr_b16 v[96:97], v66 offset:8192
v_mfma_f32_16x16x32_bf16 a[20:23], v[104:107], a[180:183], a[20:23]
ds_read_b64_tr_b16 v[98:99], v66 offset:12288
s_waitcnt lgkmcnt(10)
v_mfma_f32_16x16x32_bf16 a[24:27], v[108:111], a[176:179], a[24:27]
ds_read_b64_tr_b16 v[100:101], v67 offset:8192
ds_read_b64_tr_b16 v[102:103], v67 offset:12288
v_mfma_f32_16x16x32_bf16 a[28:31], v[108:111], a[180:183], a[28:31]
buffer_load_dwordx4 a[156:159], v18, s[84:87], s29 offen offset:3072 sc1
s_waitcnt lgkmcnt(10)
v_mfma_f32_16x16x32_bf16 a[32:35], v[80:83], a[176:179], a[32:35]
ds_read_b64_tr_b16 v[104:105], v68 offset:8192
v_mfma_f32_16x16x32_bf16 a[36:39], v[80:83], a[180:183], a[36:39]
ds_read_b64_tr_b16 v[106:107], v68 offset:12288
s_waitcnt lgkmcnt(10)
v_mfma_f32_16x16x32_bf16 a[40:43], v[84:87], a[176:179], a[40:43]
ds_read_b64_tr_b16 v[108:109], v69 offset:8192
ds_read_b64_tr_b16 v[110:111], v69 offset:12288
v_mfma_f32_16x16x32_bf16 a[44:47], v[84:87], a[180:183], a[44:47]
s_add_u32 s29, s90, 24576
buffer_load_dwordx4 a[192:195], v2, s[84:87], s29 offen offset:0 sc1
s_barrier
s_waitcnt vmcnt(18) lgkmcnt(10)
v_mfma_f32_16x16x32_bf16 a[0:3], v[88:91], a[184:187], a[0:3]
ds_read_b64_tr_b16 v[80:81], v64 offset:16384
s_waitcnt vmcnt(17)
v_mfma_f32_16x16x32_bf16 a[4:7], v[88:91], a[188:191], a[4:7]
ds_read_b64_tr_b16 v[82:83], v64 offset:20480
s_waitcnt lgkmcnt(10)
v_mfma_f32_16x16x32_bf16 a[8:11], v[92:95], a[184:187], a[8:11]
ds_read_b64_tr_b16 v[84:85], v65 offset:16384
ds_read_b64_tr_b16 v[86:87], v65 offset:20480
v_mfma_f32_16x16x32_bf16 a[12:15], v[92:95], a[188:191], a[12:15]
buffer_load_dwordx4 a[196:199], v18, s[84:87], s29 offen offset:0 sc1
s_waitcnt lgkmcnt(10)
v_mfma_f32_16x16x32_bf16 a[16:19], v[96:99], a[184:187], a[16:19]
ds_read_b64_tr_b16 v[88:89], v66 offset:16384
v_mfma_f32_16x16x32_bf16 a[20:23], v[96:99], a[188:191], a[20:23]
ds_read_b64_tr_b16 v[90:91], v66 offset:20480
s_waitcnt lgkmcnt(10)
v_mfma_f32_16x16x32_bf16 a[24:27], v[100:103], a[184:187], a[24:27]
ds_read_b64_tr_b16 v[92:93], v67 offset:16384
ds_read_b64_tr_b16 v[94:95], v67 offset:20480
s_mov_b32 s28, 917504
v_mfma_f32_16x16x32_bf16 a[28:31], v[100:103], a[188:191], a[28:31]
s_add_u32 m0, s89, 49152
s_nop 0
buffer_load_dwordx4 v32, s[52:55], s28 offen lds
s_waitcnt lgkmcnt(10)
v_mfma_f32_16x16x32_bf16 a[32:35], v[104:107], a[184:187], a[32:35]
ds_read_b64_tr_b16 v[96:97], v68 offset:16384
v_mfma_f32_16x16x32_bf16 a[36:39], v[104:107], a[188:191], a[36:39]
ds_read_b64_tr_b16 v[98:99], v68 offset:20480
s_waitcnt lgkmcnt(10)
v_mfma_f32_16x16x32_bf16 a[40:43], v[108:111], a[184:187], a[40:43]
ds_read_b64_tr_b16 v[100:101], v69 offset:16384
ds_read_b64_tr_b16 v[102:103], v69 offset:20480
v_mfma_f32_16x16x32_bf16 a[44:47], v[108:111], a[188:191], a[44:47]
buffer_load_dwordx4 v33, s[52:55], s28 offen offset:1024 lds
s_waitcnt vmcnt(19) lgkmcnt(10)
v_mfma_f32_16x16x32_bf16 a[0:3], v[80:83], a[128:131], a[0:3]
ds_read_b64_tr_b16 v[104:105], v64 offset:24576
s_waitcnt vmcnt(18)
v_mfma_f32_16x16x32_bf16 a[4:7], v[80:83], a[132:135], a[4:7]
ds_read_b64_tr_b16 v[106:107], v64 offset:28672
s_waitcnt lgkmcnt(10)
v_mfma_f32_16x16x32_bf16 a[8:11], v[84:87], a[128:131], a[8:11]
ds_read_b64_tr_b16 v[108:109], v65 offset:24576
ds_read_b64_tr_b16 v[110:111], v65 offset:28672
v_mfma_f32_16x16x32_bf16 a[12:15], v[84:87], a[132:135], a[12:15]
buffer_load_dwordx4 v34, s[52:55], s28 offen offset:2048 lds
s_waitcnt lgkmcnt(10)
v_mfma_f32_16x16x32_bf16 a[16:19], v[88:91], a[128:131], a[16:19]
ds_read_b64_tr_b16 v[80:81], v66 offset:24576
v_mfma_f32_16x16x32_bf16 a[20:23], v[88:91], a[132:135], a[20:23]
ds_read_b64_tr_b16 v[82:83], v66 offset:28672
s_waitcnt lgkmcnt(10)
v_mfma_f32_16x16x32_bf16 a[24:27], v[92:95], a[128:131], a[24:27]
ds_read_b64_tr_b16 v[84:85], v67 offset:24576
ds_read_b64_tr_b16 v[86:87], v67 offset:28672
v_mfma_f32_16x16x32_bf16 a[28:31], v[92:95], a[132:135], a[28:31]
buffer_load_dwordx4 v35, s[52:55], s28 offen offset:3072 lds
s_waitcnt lgkmcnt(10)
v_mfma_f32_16x16x32_bf16 a[32:35], v[96:99], a[128:131], a[32:35]
ds_read_b64_tr_b16 v[88:89], v68 offset:24576
v_mfma_f32_16x16x32_bf16 a[36:39], v[96:99], a[132:135], a[36:39]
ds_read_b64_tr_b16 v[90:91], v68 offset:28672
s_waitcnt lgkmcnt(10)
v_mfma_f32_16x16x32_bf16 a[40:43], v[100:103], a[128:131], a[40:43]
ds_read_b64_tr_b16 v[92:93], v69 offset:24576
ds_read_b64_tr_b16 v[94:95], v69 offset:28672
v_mfma_f32_16x16x32_bf16 a[44:47], v[100:103], a[132:135], a[44:47]
buffer_load_dwordx4 a[200:203], v2, s[84:87], s29 offen offset:1024 sc1
s_barrier
s_waitcnt vmcnt(16) lgkmcnt(10)
v_mfma_f32_16x16x32_bf16 a[0:3], v[104:107], a[136:139], a[0:3]
ds_read_b64_tr_b16 v[96:97], v72 offset:0
s_waitcnt vmcnt(15)
v_mfma_f32_16x16x32_bf16 a[4:7], v[104:107], a[140:143], a[4:7]
ds_read_b64_tr_b16 v[98:99], v72 offset:4096
s_waitcnt lgkmcnt(10)
v_mfma_f32_16x16x32_bf16 a[8:11], v[108:111], a[136:139], a[8:11]
ds_read_b64_tr_b16 v[100:101], v73 offset:0
ds_read_b64_tr_b16 v[102:103], v73 offset:4096
v_mfma_f32_16x16x32_bf16 a[12:15], v[108:111], a[140:143], a[12:15]
buffer_load_dwordx4 a[204:207], v18, s[84:87], s29 offen offset:1024 sc1
s_waitcnt lgkmcnt(10)
v_mfma_f32_16x16x32_bf16 a[16:19], v[80:83], a[136:139], a[16:19]
ds_read_b64_tr_b16 v[104:105], v74 offset:0
v_mfma_f32_16x16x32_bf16 a[20:23], v[80:83], a[140:143], a[20:23]
ds_read_b64_tr_b16 v[106:107], v74 offset:4096
s_waitcnt lgkmcnt(10)
v_mfma_f32_16x16x32_bf16 a[24:27], v[84:87], a[136:139], a[24:27]
ds_read_b64_tr_b16 v[108:109], v75 offset:0
ds_read_b64_tr_b16 v[110:111], v75 offset:4096
v_mfma_f32_16x16x32_bf16 a[28:31], v[84:87], a[140:143], a[28:31]
buffer_load_dwordx4 a[64:67], v2, s[84:87], s29 offen offset:2048 sc1
s_waitcnt lgkmcnt(10)
v_mfma_f32_16x16x32_bf16 a[32:35], v[88:91], a[136:139], a[32:35]
ds_read_b64_tr_b16 v[80:81], v76 offset:0
v_mfma_f32_16x16x32_bf16 a[36:39], v[88:91], a[140:143], a[36:39]
ds_read_b64_tr_b16 v[82:83], v76 offset:4096
s_waitcnt lgkmcnt(10)
v_mfma_f32_16x16x32_bf16 a[40:43], v[92:95], a[136:139], a[40:43]
ds_read_b64_tr_b16 v[84:85], v77 offset:0
ds_read_b64_tr_b16 v[86:87], v77 offset:4096
v_mfma_f32_16x16x32_bf16 a[44:47], v[92:95], a[140:143], a[44:47]
buffer_load_dwordx4 a[68:71], v18, s[84:87], s29 offen offset:2048 sc1
s_waitcnt vmcnt(17) lgkmcnt(10)
v_mfma_f32_16x16x32_bf16 a[0:3], v[96:99], a[144:147], a[0:3]
ds_read_b64_tr_b16 v[88:89], v72 offset:8192
s_waitcnt vmcnt(16)
v_mfma_f32_16x16x32_bf16 a[4:7], v[96:99], a[148:151], a[4:7]
ds_read_b64_tr_b16 v[90:91], v72 offset:12288
s_waitcnt lgkmcnt(10)
v_mfma_f32_16x16x32_bf16 a[8:11], v[100:103], a[144:147], a[8:11]
ds_read_b64_tr_b16 v[92:93], v73 offset:8192
ds_read_b64_tr_b16 v[94:95], v73 offset:12288
s_mov_b32 s28, 983040
v_mfma_f32_16x16x32_bf16 a[12:15], v[100:103], a[148:151], a[12:15]
s_add_u32 m0, s89, 98304
s_nop 0
buffer_load_dwordx4 v32, s[52:55], s28 offen lds
s_waitcnt lgkmcnt(10)
v_mfma_f32_16x16x32_bf16 a[16:19], v[104:107], a[144:147], a[16:19]
ds_read_b64_tr_b16 v[96:97], v74 offset:8192
v_mfma_f32_16x16x32_bf16 a[20:23], v[104:107], a[148:151], a[20:23]
ds_read_b64_tr_b16 v[98:99], v74 offset:12288
s_waitcnt lgkmcnt(10)
v_mfma_f32_16x16x32_bf16 a[24:27], v[108:111], a[144:147], a[24:27]
ds_read_b64_tr_b16 v[100:101], v75 offset:8192
ds_read_b64_tr_b16 v[102:103], v75 offset:12288
v_mfma_f32_16x16x32_bf16 a[28:31], v[108:111], a[148:151], a[28:31]
buffer_load_dwordx4 v33, s[52:55], s28 offen offset:1024 lds
s_waitcnt lgkmcnt(10)
v_mfma_f32_16x16x32_bf16 a[32:35], v[80:83], a[144:147], a[32:35]
ds_read_b64_tr_b16 v[104:105], v76 offset:8192
v_mfma_f32_16x16x32_bf16 a[36:39], v[80:83], a[148:151], a[36:39]
ds_read_b64_tr_b16 v[106:107], v76 offset:12288
s_waitcnt lgkmcnt(10)
v_mfma_f32_16x16x32_bf16 a[40:43], v[84:87], a[144:147], a[40:43]
ds_read_b64_tr_b16 v[108:109], v77 offset:8192
ds_read_b64_tr_b16 v[110:111], v77 offset:12288
v_mfma_f32_16x16x32_bf16 a[44:47], v[84:87], a[148:151], a[44:47]
buffer_load_dwordx4 v34, s[52:55], s28 offen offset:2048 lds
s_barrier
s_waitcnt vmcnt(14) lgkmcnt(10)
v_mfma_f32_16x16x32_bf16 a[0:3], v[88:91], a[152:155], a[0:3]
ds_read_b64_tr_b16 v[80:81], v72 offset:16384
s_waitcnt vmcnt(13)
v_mfma_f32_16x16x32_bf16 a[4:7], v[88:91], a[156:159], a[4:7]
ds_read_b64_tr_b16 v[82:83], v72 offset:20480
s_waitcnt lgkmcnt(10)
v_mfma_f32_16x16x32_bf16 a[8:11], v[92:95], a[152:155], a[8:11]
ds_read_b64_tr_b16 v[84:85], v73 offset:16384
ds_read_b64_tr_b16 v[86:87], v73 offset:20480
v_mfma_f32_16x16x32_bf16 a[12:15], v[92:95], a[156:159], a[12:15]
buffer_load_dwordx4 v35, s[52:55], s28 offen offset:3072 lds
s_waitcnt lgkmcnt(10)
v_mfma_f32_16x16x32_bf16 a[16:19], v[96:99], a[152:155], a[16:19]
ds_read_b64_tr_b16 v[88:89], v74 offset:16384
v_mfma_f32_16x16x32_bf16 a[20:23], v[96:99], a[156:159], a[20:23]
ds_read_b64_tr_b16 v[90:91], v74 offset:20480
s_waitcnt lgkmcnt(10)
v_mfma_f32_16x16x32_bf16 a[24:27], v[100:103], a[152:155], a[24:27]
ds_read_b64_tr_b16 v[92:93], v75 offset:16384
ds_read_b64_tr_b16 v[94:95], v75 offset:20480
v_mfma_f32_16x16x32_bf16 a[28:31], v[100:103], a[156:159], a[28:31]
buffer_load_dwordx4 a[72:75], v2, s[84:87], s29 offen offset:3072 sc1
s_waitcnt lgkmcnt(10)
v_mfma_f32_16x16x32_bf16 a[32:35], v[104:107], a[152:155], a[32:35]
ds_read_b64_tr_b16 v[96:97], v76 offset:16384
v_mfma_f32_16x16x32_bf16 a[36:39], v[104:107], a[156:159], a[36:39]
ds_read_b64_tr_b16 v[98:99], v76 offset:20480
s_waitcnt lgkmcnt(10)
v_mfma_f32_16x16x32_bf16 a[40:43], v[108:111], a[152:155], a[40:43]
ds_read_b64_tr_b16 v[100:101], v77 offset:16384
ds_read_b64_tr_b16 v[102:103], v77 offset:20480
v_mfma_f32_16x16x32_bf16 a[44:47], v[108:111], a[156:159], a[44:47]
buffer_load_dwordx4 a[76:79], v18, s[84:87], s29 offen offset:3072 sc1
s_waitcnt vmcnt(15) lgkmcnt(10)
v_mfma_f32_16x16x32_bf16 a[0:3], v[80:83], a[192:195], a[0:3]
ds_read_b64_tr_b16 v[104:105], v72 offset:24576
s_waitcnt vmcnt(14)
v_mfma_f32_16x16x32_bf16 a[4:7], v[80:83], a[196:199], a[4:7]
ds_read_b64_tr_b16 v[106:107], v72 offset:28672
s_waitcnt lgkmcnt(10)
v_mfma_f32_16x16x32_bf16 a[8:11], v[84:87], a[192:195], a[8:11]
ds_read_b64_tr_b16 v[108:109], v73 offset:24576
ds_read_b64_tr_b16 v[110:111], v73 offset:28672
v_mfma_f32_16x16x32_bf16 a[12:15], v[84:87], a[196:199], a[12:15]
s_add_u32 s29, s90, 28672
buffer_load_dwordx4 a[80:83], v2, s[84:87], s29 offen offset:0 sc1
s_waitcnt lgkmcnt(10)
v_mfma_f32_16x16x32_bf16 a[16:19], v[88:91], a[192:195], a[16:19]
ds_read_b64_tr_b16 v[80:81], v74 offset:24576
v_mfma_f32_16x16x32_bf16 a[20:23], v[88:91], a[196:199], a[20:23]
ds_read_b64_tr_b16 v[82:83], v74 offset:28672
s_waitcnt lgkmcnt(10)
v_mfma_f32_16x16x32_bf16 a[24:27], v[92:95], a[192:195], a[24:27]
ds_read_b64_tr_b16 v[84:85], v75 offset:24576
ds_read_b64_tr_b16 v[86:87], v75 offset:28672
v_mfma_f32_16x16x32_bf16 a[28:31], v[92:95], a[196:199], a[28:31]
buffer_load_dwordx4 a[84:87], v18, s[84:87], s29 offen offset:0 sc1
s_waitcnt lgkmcnt(10)
v_mfma_f32_16x16x32_bf16 a[32:35], v[96:99], a[192:195], a[32:35]
ds_read_b64_tr_b16 v[88:89], v76 offset:24576
v_mfma_f32_16x16x32_bf16 a[36:39], v[96:99], a[196:199], a[36:39]
ds_read_b64_tr_b16 v[90:91], v76 offset:28672
s_waitcnt lgkmcnt(10)
v_mfma_f32_16x16x32_bf16 a[40:43], v[100:103], a[192:195], a[40:43]
ds_read_b64_tr_b16 v[92:93], v77 offset:24576
ds_read_b64_tr_b16 v[94:95], v77 offset:28672
v_mfma_f32_16x16x32_bf16 a[44:47], v[100:103], a[196:199], a[44:47]
buffer_load_dwordx4 a[88:91], v2, s[84:87], s29 offen offset:1024 sc1
s_barrier
s_waitcnt vmcnt(12) lgkmcnt(10)
v_mfma_f32_16x16x32_bf16 a[0:3], v[104:107], a[200:203], a[0:3]
ds_read_b64_tr_b16 v[96:97], v64 offset:32768
s_waitcnt vmcnt(11)
v_mfma_f32_16x16x32_bf16 a[4:7], v[104:107], a[204:207], a[4:7]
ds_read_b64_tr_b16 v[98:99], v64 offset:36864
s_waitcnt lgkmcnt(10)
v_mfma_f32_16x16x32_bf16 a[8:11], v[108:111], a[200:203], a[8:11]
ds_read_b64_tr_b16 v[100:101], v65 offset:32768
ds_read_b64_tr_b16 v[102:103], v65 offset:36864
v_mfma_f32_16x16x32_bf16 a[12:15], v[108:111], a[204:207], a[12:15]
buffer_load_dwordx4 a[92:95], v18, s[84:87], s29 offen offset:1024 sc1
s_waitcnt lgkmcnt(10)
v_mfma_f32_16x16x32_bf16 a[16:19], v[80:83], a[200:203], a[16:19]
ds_read_b64_tr_b16 v[104:105], v66 offset:32768
v_mfma_f32_16x16x32_bf16 a[20:23], v[80:83], a[204:207], a[20:23]
ds_read_b64_tr_b16 v[106:107], v66 offset:36864
s_waitcnt lgkmcnt(10)
v_mfma_f32_16x16x32_bf16 a[24:27], v[84:87], a[200:203], a[24:27]
ds_read_b64_tr_b16 v[108:109], v67 offset:32768
ds_read_b64_tr_b16 v[110:111], v67 offset:36864
v_mfma_f32_16x16x32_bf16 a[28:31], v[84:87], a[204:207], a[28:31]
buffer_load_dwordx4 a[96:99], v2, s[84:87], s29 offen offset:2048 sc1
s_waitcnt lgkmcnt(10)
v_mfma_f32_16x16x32_bf16 a[32:35], v[88:91], a[200:203], a[32:35]
ds_read_b64_tr_b16 v[80:81], v68 offset:32768
v_mfma_f32_16x16x32_bf16 a[36:39], v[88:91], a[204:207], a[36:39]
ds_read_b64_tr_b16 v[82:83], v68 offset:36864
s_waitcnt lgkmcnt(10)
v_mfma_f32_16x16x32_bf16 a[40:43], v[92:95], a[200:203], a[40:43]
ds_read_b64_tr_b16 v[84:85], v69 offset:32768
ds_read_b64_tr_b16 v[86:87], v69 offset:36864
v_mfma_f32_16x16x32_bf16 a[44:47], v[92:95], a[204:207], a[44:47]
buffer_load_dwordx4 a[100:103], v18, s[84:87], s29 offen offset:2048 sc1
s_waitcnt vmcnt(13) lgkmcnt(10)
v_mfma_f32_16x16x32_bf16 a[0:3], v[96:99], a[64:67], a[0:3]
ds_read_b64_tr_b16 v[88:89], v64 offset:40960
s_waitcnt vmcnt(12)
v_mfma_f32_16x16x32_bf16 a[4:7], v[96:99], a[68:71], a[4:7]
ds_read_b64_tr_b16 v[90:91], v64 offset:45056
s_waitcnt lgkmcnt(10)
v_mfma_f32_16x16x32_bf16 a[8:11], v[100:103], a[64:67], a[8:11]
ds_read_b64_tr_b16 v[92:93], v65 offset:40960
ds_read_b64_tr_b16 v[94:95], v65 offset:45056
v_mfma_f32_16x16x32_bf16 a[12:15], v[100:103], a[68:71], a[12:15]
buffer_load_dwordx4 a[104:107], v2, s[84:87], s29 offen offset:3072 sc1
s_waitcnt lgkmcnt(10)
v_mfma_f32_16x16x32_bf16 a[16:19], v[104:107], a[64:67], a[16:19]
ds_read_b64_tr_b16 v[96:97], v66 offset:40960
v_mfma_f32_16x16x32_bf16 a[20:23], v[104:107], a[68:71], a[20:23]
ds_read_b64_tr_b16 v[98:99], v66 offset:45056
s_waitcnt lgkmcnt(10)
v_mfma_f32_16x16x32_bf16 a[24:27], v[108:111], a[64:67], a[24:27]
ds_read_b64_tr_b16 v[100:101], v67 offset:40960
ds_read_b64_tr_b16 v[102:103], v67 offset:45056
v_mfma_f32_16x16x32_bf16 a[28:31], v[108:111], a[68:71], a[28:31]
buffer_load_dwordx4 a[108:111], v18, s[84:87], s29 offen offset:3072 sc1
s_waitcnt lgkmcnt(10)
v_mfma_f32_16x16x32_bf16 a[32:35], v[80:83], a[64:67], a[32:35]
ds_read_b64_tr_b16 v[104:105], v68 offset:40960
v_mfma_f32_16x16x32_bf16 a[36:39], v[80:83], a[68:71], a[36:39]
ds_read_b64_tr_b16 v[106:107], v68 offset:45056
s_waitcnt lgkmcnt(10)
v_mfma_f32_16x16x32_bf16 a[40:43], v[84:87], a[64:67], a[40:43]
ds_read_b64_tr_b16 v[108:109], v69 offset:40960
v_mfma_f32_16x16x32_bf16 a[44:47], v[84:87], a[68:71], a[44:47]
ds_read_b64_tr_b16 v[110:111], v69 offset:45056
s_barrier
s_waitcnt vmcnt(9) lgkmcnt(10)
v_mfma_f32_16x16x32_bf16 a[0:3], v[88:91], a[72:75], a[0:3]
ds_read_b64_tr_b16 v[80:81], v64 offset:49152
s_waitcnt vmcnt(8)
v_mfma_f32_16x16x32_bf16 a[4:7], v[88:91], a[76:79], a[4:7]
ds_read_b64_tr_b16 v[82:83], v64 offset:53248
s_waitcnt lgkmcnt(10)
v_mfma_f32_16x16x32_bf16 a[8:11], v[92:95], a[72:75], a[8:11]
ds_read_b64_tr_b16 v[84:85], v65 offset:49152
v_mfma_f32_16x16x32_bf16 a[12:15], v[92:95], a[76:79], a[12:15]
ds_read_b64_tr_b16 v[86:87], v65 offset:53248
s_waitcnt lgkmcnt(10)
v_mfma_f32_16x16x32_bf16 a[16:19], v[96:99], a[72:75], a[16:19]
ds_read_b64_tr_b16 v[88:89], v66 offset:49152
v_mfma_f32_16x16x32_bf16 a[20:23], v[96:99], a[76:79], a[20:23]
ds_read_b64_tr_b16 v[90:91], v66 offset:53248
s_waitcnt lgkmcnt(10)
v_mfma_f32_16x16x32_bf16 a[24:27], v[100:103], a[72:75], a[24:27]
ds_read_b64_tr_b16 v[92:93], v67 offset:49152
v_mfma_f32_16x16x32_bf16 a[28:31], v[100:103], a[76:79], a[28:31]
ds_read_b64_tr_b16 v[94:95], v67 offset:53248
s_waitcnt lgkmcnt(10)
v_mfma_f32_16x16x32_bf16 a[32:35], v[104:107], a[72:75], a[32:35]
ds_read_b64_tr_b16 v[96:97], v68 offset:49152
v_mfma_f32_16x16x32_bf16 a[36:39], v[104:107], a[76:79], a[36:39]
ds_read_b64_tr_b16 v[98:99], v68 offset:53248
s_waitcnt lgkmcnt(10)
v_mfma_f32_16x16x32_bf16 a[40:43], v[108:111], a[72:75], a[40:43]
ds_read_b64_tr_b16 v[100:101], v69 offset:49152
v_mfma_f32_16x16x32_bf16 a[44:47], v[108:111], a[76:79], a[44:47]
ds_read_b64_tr_b16 v[102:103], v69 offset:53248
s_waitcnt vmcnt(7) lgkmcnt(10)
v_mfma_f32_16x16x32_bf16 a[0:3], v[80:83], a[80:83], a[0:3]
ds_read_b64_tr_b16 v[104:105], v64 offset:57344
s_waitcnt vmcnt(6)
v_mfma_f32_16x16x32_bf16 a[4:7], v[80:83], a[84:87], a[4:7]
ds_read_b64_tr_b16 v[106:107], v64 offset:61440
s_waitcnt lgkmcnt(10)
v_mfma_f32_16x16x32_bf16 a[8:11], v[84:87], a[80:83], a[8:11]
ds_read_b64_tr_b16 v[108:109], v65 offset:57344
v_mfma_f32_16x16x32_bf16 a[12:15], v[84:87], a[84:87], a[12:15]
ds_read_b64_tr_b16 v[110:111], v65 offset:61440
s_waitcnt lgkmcnt(10)
v_mfma_f32_16x16x32_bf16 a[16:19], v[88:91], a[80:83], a[16:19]
ds_read_b64_tr_b16 v[80:81], v66 offset:57344
v_mfma_f32_16x16x32_bf16 a[20:23], v[88:91], a[84:87], a[20:23]
ds_read_b64_tr_b16 v[82:83], v66 offset:61440
s_waitcnt lgkmcnt(10)
v_mfma_f32_16x16x32_bf16 a[24:27], v[92:95], a[80:83], a[24:27]
ds_read_b64_tr_b16 v[84:85], v67 offset:57344
v_mfma_f32_16x16x32_bf16 a[28:31], v[92:95], a[84:87], a[28:31]
ds_read_b64_tr_b16 v[86:87], v67 offset:61440
s_waitcnt lgkmcnt(10)
v_mfma_f32_16x16x32_bf16 a[32:35], v[96:99], a[80:83], a[32:35]
ds_read_b64_tr_b16 v[88:89], v68 offset:57344
v_mfma_f32_16x16x32_bf16 a[36:39], v[96:99], a[84:87], a[36:39]
ds_read_b64_tr_b16 v[90:91], v68 offset:61440
s_waitcnt lgkmcnt(10)
v_mfma_f32_16x16x32_bf16 a[40:43], v[100:103], a[80:83], a[40:43]
ds_read_b64_tr_b16 v[92:93], v69 offset:57344
v_mfma_f32_16x16x32_bf16 a[44:47], v[100:103], a[84:87], a[44:47]
ds_read_b64_tr_b16 v[94:95], v69 offset:61440
s_barrier
s_waitcnt vmcnt(5) lgkmcnt(10)
v_mfma_f32_16x16x32_bf16 a[0:3], v[104:107], a[88:91], a[0:3]
ds_read_b64_tr_b16 v[96:97], v72 offset:32768
s_waitcnt vmcnt(4)
v_mfma_f32_16x16x32_bf16 a[4:7], v[104:107], a[92:95], a[4:7]
ds_read_b64_tr_b16 v[98:99], v72 offset:36864
s_waitcnt lgkmcnt(10)
v_mfma_f32_16x16x32_bf16 a[8:11], v[108:111], a[88:91], a[8:11]
ds_read_b64_tr_b16 v[100:101], v73 offset:32768
v_mfma_f32_16x16x32_bf16 a[12:15], v[108:111], a[92:95], a[12:15]
ds_read_b64_tr_b16 v[102:103], v73 offset:36864
s_waitcnt lgkmcnt(10)
v_mfma_f32_16x16x32_bf16 a[16:19], v[80:83], a[88:91], a[16:19]
ds_read_b64_tr_b16 v[104:105], v74 offset:32768
v_mfma_f32_16x16x32_bf16 a[20:23], v[80:83], a[92:95], a[20:23]
ds_read_b64_tr_b16 v[106:107], v74 offset:36864
s_waitcnt lgkmcnt(10)
v_mfma_f32_16x16x32_bf16 a[24:27], v[84:87], a[88:91], a[24:27]
ds_read_b64_tr_b16 v[108:109], v75 offset:32768
v_mfma_f32_16x16x32_bf16 a[28:31], v[84:87], a[92:95], a[28:31]
ds_read_b64_tr_b16 v[110:111], v75 offset:36864
s_waitcnt lgkmcnt(10)
v_mfma_f32_16x16x32_bf16 a[32:35], v[88:91], a[88:91], a[32:35]
ds_read_b64_tr_b16 v[80:81], v76 offset:32768
v_mfma_f32_16x16x32_bf16 a[36:39], v[88:91], a[92:95], a[36:39]
ds_read_b64_tr_b16 v[82:83], v76 offset:36864
s_waitcnt lgkmcnt(10)
v_mfma_f32_16x16x32_bf16 a[40:43], v[92:95], a[88:91], a[40:43]
ds_read_b64_tr_b16 v[84:85], v77 offset:32768
v_mfma_f32_16x16x32_bf16 a[44:47], v[92:95], a[92:95], a[44:47]
ds_read_b64_tr_b16 v[86:87], v77 offset:36864
s_waitcnt vmcnt(3) lgkmcnt(10)
v_mfma_f32_16x16x32_bf16 a[0:3], v[96:99], a[96:99], a[0:3]
ds_read_b64_tr_b16 v[88:89], v72 offset:40960
s_waitcnt vmcnt(2)
v_mfma_f32_16x16x32_bf16 a[4:7], v[96:99], a[100:103], a[4:7]
ds_read_b64_tr_b16 v[90:91], v72 offset:45056
s_waitcnt lgkmcnt(10)
v_mfma_f32_16x16x32_bf16 a[8:11], v[100:103], a[96:99], a[8:11]
ds_read_b64_tr_b16 v[92:93], v73 offset:40960
v_mfma_f32_16x16x32_bf16 a[12:15], v[100:103], a[100:103], a[12:15]
ds_read_b64_tr_b16 v[94:95], v73 offset:45056
s_waitcnt lgkmcnt(10)
v_mfma_f32_16x16x32_bf16 a[16:19], v[104:107], a[96:99], a[16:19]
ds_read_b64_tr_b16 v[96:97], v74 offset:40960
v_mfma_f32_16x16x32_bf16 a[20:23], v[104:107], a[100:103], a[20:23]
ds_read_b64_tr_b16 v[98:99], v74 offset:45056
s_waitcnt lgkmcnt(10)
v_mfma_f32_16x16x32_bf16 a[24:27], v[108:111], a[96:99], a[24:27]
ds_read_b64_tr_b16 v[100:101], v75 offset:40960
v_mfma_f32_16x16x32_bf16 a[28:31], v[108:111], a[100:103], a[28:31]
ds_read_b64_tr_b16 v[102:103], v75 offset:45056
s_waitcnt lgkmcnt(10)
v_mfma_f32_16x16x32_bf16 a[32:35], v[80:83], a[96:99], a[32:35]
ds_read_b64_tr_b16 v[104:105], v76 offset:40960
v_mfma_f32_16x16x32_bf16 a[36:39], v[80:83], a[100:103], a[36:39]
ds_read_b64_tr_b16 v[106:107], v76 offset:45056
s_waitcnt lgkmcnt(10)
v_mfma_f32_16x16x32_bf16 a[40:43], v[84:87], a[96:99], a[40:43]
ds_read_b64_tr_b16 v[108:109], v77 offset:40960
v_mfma_f32_16x16x32_bf16 a[44:47], v[84:87], a[100:103], a[44:47]
ds_read_b64_tr_b16 v[110:111], v77 offset:45056
s_waitcnt vmcnt(1) lgkmcnt(10)
v_mfma_f32_16x16x32_bf16 a[0:3], v[88:91], a[104:107], a[0:3]
ds_read_b64_tr_b16 v[80:81], v78 offset:49152
s_waitcnt vmcnt(0)
v_mfma_f32_16x16x32_bf16 a[4:7], v[88:91], a[108:111], a[4:7]
ds_read_b64_tr_b16 v[82:83], v78 offset:53248
s_waitcnt lgkmcnt(10)
v_mfma_f32_16x16x32_bf16 a[8:11], v[92:95], a[104:107], a[8:11]
ds_read_b64_tr_b16 v[84:85], v79 offset:49152
v_mfma_f32_16x16x32_bf16 a[12:15], v[92:95], a[108:111], a[12:15]
ds_read_b64_tr_b16 v[86:87], v79 offset:53248
s_waitcnt lgkmcnt(10)
v_mfma_f32_16x16x32_bf16 a[16:19], v[96:99], a[104:107], a[16:19]
ds_read_b64_tr_b16 v[88:89], v78 offset:57344
v_mfma_f32_16x16x32_bf16 a[20:23], v[96:99], a[108:111], a[20:23]
ds_read_b64_tr_b16 v[90:91], v78 offset:61440
s_waitcnt lgkmcnt(10)
v_mfma_f32_16x16x32_bf16 a[24:27], v[100:103], a[104:107], a[24:27]
ds_read_b64_tr_b16 v[92:93], v79 offset:57344
v_mfma_f32_16x16x32_bf16 a[28:31], v[100:103], a[108:111], a[28:31]
ds_read_b64_tr_b16 v[94:95], v79 offset:61440
s_waitcnt lgkmcnt(10)
v_mfma_f32_16x16x32_bf16 a[32:35], v[104:107], a[104:107], a[32:35]
ds_read_b64_tr_b16 v[96:97], v70 offset:0
v_mfma_f32_16x16x32_bf16 a[36:39], v[104:107], a[108:111], a[36:39]
ds_read_b64_tr_b16 v[98:99], v70 offset:4096
s_waitcnt lgkmcnt(10)
v_mfma_f32_16x16x32_bf16 a[40:43], v[108:111], a[104:107], a[40:43]
ds_read_b64_tr_b16 v[100:101], v71 offset:0
v_mfma_f32_16x16x32_bf16 a[44:47], v[108:111], a[108:111], a[44:47]
ds_read_b64_tr_b16 v[102:103], v71 offset:4096
s_waitcnt lgkmcnt(10)
v_mfma_f32_16x16x32_bf16 a[48:51], v[80:83], a[160:163], a[48:51]
ds_read_b64_tr_b16 v[104:105], v70 offset:8192
ds_read_b64_tr_b16 v[106:107], v70 offset:12288
v_accvgpr_read_b32 v112, a0
v_mfma_f32_16x16x32_bf16 a[52:55], v[80:83], a[164:167], a[52:55]
v_accvgpr_read_b32 v113, a1
v_accvgpr_read_b32 v114, a2
v_accvgpr_read_b32 v115, a3
s_waitcnt lgkmcnt(10)
v_mfma_f32_16x16x32_bf16 a[56:59], v[84:87], a[160:163], a[56:59]
ds_read_b64_tr_b16 v[108:109], v71 offset:8192
ds_read_b64_tr_b16 v[110:111], v71 offset:12288
v_accvgpr_read_b32 v116, a8
v_mfma_f32_16x16x32_bf16 a[60:63], v[84:87], a[164:167], a[60:63]
v_accvgpr_read_b32 v117, a9
v_accvgpr_read_b32 v118, a10
s_waitcnt lgkmcnt(10)
v_mfma_f32_16x16x32_bf16 a[48:51], v[88:91], a[168:171], a[48:51]
ds_read_b64_tr_b16 v[80:81], v70 offset:16384
ds_read_b64_tr_b16 v[82:83], v70 offset:20480
v_accvgpr_read_b32 v119, a11
v_mfma_f32_16x16x32_bf16 a[52:55], v[88:91], a[172:175], a[52:55]
v_cvt_pk_bf16_f32 v120, v112, v113
v_cvt_pk_bf16_f32 v121, v114, v115
s_waitcnt lgkmcnt(10)
v_mfma_f32_16x16x32_bf16 a[56:59], v[92:95], a[168:171], a[56:59]
ds_read_b64_tr_b16 v[84:85], v71 offset:16384
ds_read_b64_tr_b16 v[86:87], v71 offset:20480
v_cvt_pk_bf16_f32 v122, v116, v117
v_mfma_f32_16x16x32_bf16 a[60:63], v[92:95], a[172:175], a[60:63]
v_cvt_pk_bf16_f32 v123, v118, v119
v_min_u32 v120, -2, v120
s_waitcnt lgkmcnt(10)
v_mfma_f32_16x16x32_bf16 a[48:51], v[96:99], a[176:179], a[48:51]
ds_read_b64_tr_b16 v[88:89], v70 offset:24576
ds_read_b64_tr_b16 v[90:91], v70 offset:28672
v_min_u32 v121, -2, v121
v_mfma_f32_16x16x32_bf16 a[52:55], v[96:99], a[180:183], a[52:55]
v_min_u32 v122, -2, v122
v_min_u32 v123, -2, v123
s_waitcnt lgkmcnt(10)
v_mfma_f32_16x16x32_bf16 a[56:59], v[100:103], a[176:179], a[56:59]
ds_read_b64_tr_b16 v[92:93], v71 offset:24576
ds_read_b64_tr_b16 v[94:95], v71 offset:28672
buffer_store_dwordx4 v[120:123], v2, s[16:19], s60 offen offset:0 sc1
v_mfma_f32_16x16x32_bf16 a[60:63], v[100:103], a[180:183], a[60:63]
v_accvgpr_read_b32 v124, a4
v_accvgpr_read_b32 v125, a5
v_accvgpr_read_b32 v126, a6
s_waitcnt lgkmcnt(10)
v_mfma_f32_16x16x32_bf16 a[48:51], v[104:107], a[184:187], a[48:51]
ds_read_b64_tr_b16 v[96:97], v78 offset:0
ds_read_b64_tr_b16 v[98:99], v78 offset:4096
)ASM"
R"ASM(v_accvgpr_read_b32 v127, a7
v_mfma_f32_16x16x32_bf16 a[52:55], v[104:107], a[188:191], a[52:55]
v_accvgpr_read_b32 v128, a12
v_accvgpr_read_b32 v129, a13
s_waitcnt lgkmcnt(10)
v_mfma_f32_16x16x32_bf16 a[56:59], v[108:111], a[184:187], a[56:59]
ds_read_b64_tr_b16 v[100:101], v79 offset:0
ds_read_b64_tr_b16 v[102:103], v79 offset:4096
v_accvgpr_read_b32 v130, a14
v_mfma_f32_16x16x32_bf16 a[60:63], v[108:111], a[188:191], a[60:63]
v_accvgpr_read_b32 v131, a15
v_cvt_pk_bf16_f32 v132, v124, v125
s_waitcnt lgkmcnt(10)
v_mfma_f32_16x16x32_bf16 a[48:51], v[80:83], a[128:131], a[48:51]
ds_read_b64_tr_b16 v[104:105], v78 offset:8192
ds_read_b64_tr_b16 v[106:107], v78 offset:12288
v_cvt_pk_bf16_f32 v133, v126, v127
v_mfma_f32_16x16x32_bf16 a[52:55], v[80:83], a[132:135], a[52:55]
v_cvt_pk_bf16_f32 v134, v128, v129
v_cvt_pk_bf16_f32 v135, v130, v131
s_waitcnt lgkmcnt(10)
v_mfma_f32_16x16x32_bf16 a[56:59], v[84:87], a[128:131], a[56:59]
ds_read_b64_tr_b16 v[108:109], v79 offset:8192
ds_read_b64_tr_b16 v[110:111], v79 offset:12288
v_min_u32 v132, -2, v132
v_mfma_f32_16x16x32_bf16 a[60:63], v[84:87], a[132:135], a[60:63]
v_min_u32 v133, -2, v133
v_min_u32 v134, -2, v134
s_waitcnt lgkmcnt(10)
v_mfma_f32_16x16x32_bf16 a[48:51], v[88:91], a[136:139], a[48:51]
ds_read_b64_tr_b16 v[80:81], v78 offset:16384
ds_read_b64_tr_b16 v[82:83], v78 offset:20480
v_min_u32 v135, -2, v135
v_mfma_f32_16x16x32_bf16 a[52:55], v[88:91], a[140:143], a[52:55]
buffer_store_dwordx4 v[132:135], v2, s[16:19], s60 offen offset:1024 sc1
v_accvgpr_read_b32 v112, a16
v_accvgpr_read_b32 v113, a17
s_waitcnt lgkmcnt(10)
v_mfma_f32_16x16x32_bf16 a[56:59], v[92:95], a[136:139], a[56:59]
ds_read_b64_tr_b16 v[84:85], v79 offset:16384
ds_read_b64_tr_b16 v[86:87], v79 offset:20480
v_accvgpr_read_b32 v114, a18
v_mfma_f32_16x16x32_bf16 a[60:63], v[92:95], a[140:143], a[60:63]
v_accvgpr_read_b32 v115, a19
v_accvgpr_read_b32 v116, a24
s_waitcnt lgkmcnt(10)
v_mfma_f32_16x16x32_bf16 a[48:51], v[96:99], a[144:147], a[48:51]
ds_read_b64_tr_b16 v[88:89], v78 offset:24576
ds_read_b64_tr_b16 v[90:91], v78 offset:28672
v_accvgpr_read_b32 v117, a25
v_mfma_f32_16x16x32_bf16 a[52:55], v[96:99], a[148:151], a[52:55]
v_accvgpr_read_b32 v118, a26
v_accvgpr_read_b32 v119, a27
s_waitcnt lgkmcnt(10)
v_mfma_f32_16x16x32_bf16 a[56:59], v[100:103], a[144:147], a[56:59]
ds_read_b64_tr_b16 v[92:93], v79 offset:24576
ds_read_b64_tr_b16 v[94:95], v79 offset:28672
v_cvt_pk_bf16_f32 v120, v112, v113
v_mfma_f32_16x16x32_bf16 a[60:63], v[100:103], a[148:151], a[60:63]
v_cvt_pk_bf16_f32 v121, v114, v115
v_cvt_pk_bf16_f32 v122, v116, v117
s_waitcnt lgkmcnt(10)
v_mfma_f32_16x16x32_bf16 a[48:51], v[104:107], a[152:155], a[48:51]
ds_read_b64_tr_b16 v[96:97], v70 offset:32768
ds_read_b64_tr_b16 v[98:99], v70 offset:36864
v_cvt_pk_bf16_f32 v123, v118, v119
v_mfma_f32_16x16x32_bf16 a[52:55], v[104:107], a[156:159], a[52:55]
v_min_u32 v120, -2, v120
v_min_u32 v121, -2, v121
s_waitcnt lgkmcnt(10)
v_mfma_f32_16x16x32_bf16 a[56:59], v[108:111], a[152:155], a[56:59]
ds_read_b64_tr_b16 v[100:101], v71 offset:32768
ds_read_b64_tr_b16 v[102:103], v71 offset:36864
v_min_u32 v122, -2, v122
v_mfma_f32_16x16x32_bf16 a[60:63], v[108:111], a[156:159], a[60:63]
v_min_u32 v123, -2, v123
buffer_store_dwordx4 v[120:123], v2, s[16:19], s61 offen offset:0 sc1
s_waitcnt lgkmcnt(10)
v_mfma_f32_16x16x32_bf16 a[48:51], v[80:83], a[192:195], a[48:51]
ds_read_b64_tr_b16 v[104:105], v70 offset:40960
ds_read_b64_tr_b16 v[106:107], v70 offset:45056
v_accvgpr_read_b32 v124, a20
v_mfma_f32_16x16x32_bf16 a[52:55], v[80:83], a[196:199], a[52:55]
v_accvgpr_read_b32 v125, a21
v_accvgpr_read_b32 v126, a22
v_accvgpr_read_b32 v127, a23
s_waitcnt lgkmcnt(10)
v_mfma_f32_16x16x32_bf16 a[56:59], v[84:87], a[192:195], a[56:59]
ds_read_b64_tr_b16 v[108:109], v71 offset:40960
ds_read_b64_tr_b16 v[110:111], v71 offset:45056
v_accvgpr_read_b32 v128, a28
v_mfma_f32_16x16x32_bf16 a[60:63], v[84:87], a[196:199], a[60:63]
v_accvgpr_read_b32 v129, a29
v_accvgpr_read_b32 v130, a30
s_waitcnt lgkmcnt(10)
v_mfma_f32_16x16x32_bf16 a[48:51], v[88:91], a[200:203], a[48:51]
ds_read_b64_tr_b16 v[80:81], v70 offset:49152
ds_read_b64_tr_b16 v[82:83], v70 offset:53248
v_accvgpr_read_b32 v131, a31
v_mfma_f32_16x16x32_bf16 a[52:55], v[88:91], a[204:207], a[52:55]
v_cvt_pk_bf16_f32 v132, v124, v125
v_cvt_pk_bf16_f32 v133, v126, v127
s_waitcnt lgkmcnt(10)
v_mfma_f32_16x16x32_bf16 a[56:59], v[92:95], a[200:203], a[56:59]
ds_read_b64_tr_b16 v[84:85], v71 offset:49152
ds_read_b64_tr_b16 v[86:87], v71 offset:53248
v_cvt_pk_bf16_f32 v134, v128, v129
v_mfma_f32_16x16x32_bf16 a[60:63], v[92:95], a[204:207], a[60:63]
v_cvt_pk_bf16_f32 v135, v130, v131
v_min_u32 v132, -2, v132
s_waitcnt lgkmcnt(10)
v_mfma_f32_16x16x32_bf16 a[48:51], v[96:99], a[64:67], a[48:51]
ds_read_b64_tr_b16 v[88:89], v70 offset:57344
ds_read_b64_tr_b16 v[90:91], v70 offset:61440
v_min_u32 v133, -2, v133
v_mfma_f32_16x16x32_bf16 a[52:55], v[96:99], a[68:71], a[52:55]
v_min_u32 v134, -2, v134
v_min_u32 v135, -2, v135
s_waitcnt lgkmcnt(10)
v_mfma_f32_16x16x32_bf16 a[56:59], v[100:103], a[64:67], a[56:59]
ds_read_b64_tr_b16 v[92:93], v71 offset:57344
ds_read_b64_tr_b16 v[94:95], v71 offset:61440
buffer_store_dwordx4 v[132:135], v2, s[16:19], s61 offen offset:1024 sc1
v_mfma_f32_16x16x32_bf16 a[60:63], v[100:103], a[68:71], a[60:63]
v_accvgpr_read_b32 v112, a32
v_accvgpr_read_b32 v113, a33
v_accvgpr_read_b32 v114, a34
s_waitcnt lgkmcnt(10)
v_mfma_f32_16x16x32_bf16 a[48:51], v[104:107], a[72:75], a[48:51]
ds_read_b64_tr_b16 v[96:97], v78 offset:32768
ds_read_b64_tr_b16 v[98:99], v78 offset:36864
v_accvgpr_read_b32 v115, a35
v_mfma_f32_16x16x32_bf16 a[52:55], v[104:107], a[76:79], a[52:55]
v_accvgpr_read_b32 v116, a40
v_accvgpr_read_b32 v117, a41
s_waitcnt lgkmcnt(10)
v_mfma_f32_16x16x32_bf16 a[56:59], v[108:111], a[72:75], a[56:59]
ds_read_b64_tr_b16 v[100:101], v79 offset:32768
ds_read_b64_tr_b16 v[102:103], v79 offset:36864
v_accvgpr_read_b32 v118, a42
v_mfma_f32_16x16x32_bf16 a[60:63], v[108:111], a[76:79], a[60:63]
v_accvgpr_read_b32 v119, a43
v_cvt_pk_bf16_f32 v120, v112, v113
s_waitcnt lgkmcnt(10)
v_mfma_f32_16x16x32_bf16 a[48:51], v[80:83], a[80:83], a[48:51]
ds_read_b64_tr_b16 v[104:105], v78 offset:40960
ds_read_b64_tr_b16 v[106:107], v78 offset:45056
v_cvt_pk_bf16_f32 v121, v114, v115
v_mfma_f32_16x16x32_bf16 a[52:55], v[80:83], a[84:87], a[52:55]
v_cvt_pk_bf16_f32 v122, v116, v117
v_cvt_pk_bf16_f32 v123, v118, v119
s_waitcnt lgkmcnt(10)
v_mfma_f32_16x16x32_bf16 a[56:59], v[84:87], a[80:83], a[56:59]
ds_read_b64_tr_b16 v[108:109], v79 offset:40960
ds_read_b64_tr_b16 v[110:111], v79 offset:45056
v_min_u32 v120, -2, v120
v_mfma_f32_16x16x32_bf16 a[60:63], v[84:87], a[84:87], a[60:63]
v_min_u32 v121, -2, v121
v_min_u32 v122, -2, v122
s_waitcnt lgkmcnt(10)
v_mfma_f32_16x16x32_bf16 a[48:51], v[88:91], a[88:91], a[48:51]
v_min_u32 v123, -2, v123
buffer_store_dwordx4 v[120:123], v2, s[16:19], s62 offen offset:0 sc1
v_mfma_f32_16x16x32_bf16 a[52:55], v[88:91], a[92:95], a[52:55]
v_accvgpr_read_b32 v124, a36
v_accvgpr_read_b32 v125, a37
s_waitcnt lgkmcnt(8)
v_mfma_f32_16x16x32_bf16 a[56:59], v[92:95], a[88:91], a[56:59]
v_accvgpr_read_b32 v126, a38
v_accvgpr_read_b32 v127, a39
v_mfma_f32_16x16x32_bf16 a[60:63], v[92:95], a[92:95], a[60:63]
v_accvgpr_read_b32 v128, a44
s_waitcnt lgkmcnt(6)
v_mfma_f32_16x16x32_bf16 a[48:51], v[96:99], a[96:99], a[48:51]
v_accvgpr_read_b32 v129, a45
v_accvgpr_read_b32 v130, a46
v_mfma_f32_16x16x32_bf16 a[52:55], v[96:99], a[100:103], a[52:55]
v_accvgpr_read_b32 v131, a47
s_waitcnt lgkmcnt(4)
v_mfma_f32_16x16x32_bf16 a[56:59], v[100:103], a[96:99], a[56:59]
v_cvt_pk_bf16_f32 v132, v124, v125
v_cvt_pk_bf16_f32 v133, v126, v127
v_mfma_f32_16x16x32_bf16 a[60:63], v[100:103], a[100:103], a[60:63]
v_cvt_pk_bf16_f32 v134, v128, v129
s_waitcnt lgkmcnt(2)
v_mfma_f32_16x16x32_bf16 a[48:51], v[104:107], a[104:107], a[48:51]
v_cvt_pk_bf16_f32 v135, v130, v131
v_min_u32 v132, -2, v132
v_mfma_f32_16x16x32_bf16 a[52:55], v[104:107], a[108:111], a[52:55]
v_min_u32 v133, -2, v133
s_waitcnt lgkmcnt(0)
v_mfma_f32_16x16x32_bf16 a[56:59], v[108:111], a[104:107], a[56:59]
v_min_u32 v134, -2, v134
v_min_u32 v135, -2, v135
v_mfma_f32_16x16x32_bf16 a[60:63], v[108:111], a[108:111], a[60:63]
buffer_store_dwordx4 v[132:135], v2, s[16:19], s62 offen offset:1024 sc1
L_EM1:
L_RX:
buffer_load_dwordx4 v[148:151], v2, s[16:19], s64 offen offset:0 sc1
buffer_load_dwordx4 v[152:155], v2, s[16:19], s64 offen offset:1024 sc1
buffer_load_dwordx4 v[156:159], v2, s[16:19], s65 offen offset:0 sc1
buffer_load_dwordx4 v[160:163], v2, s[16:19], s65 offen offset:1024 sc1
buffer_load_dwordx4 v[164:167], v2, s[16:19], s66 offen offset:0 sc1
buffer_load_dwordx4 v[168:171], v2, s[16:19], s66 offen offset:1024 sc1
L_RX_OWN:
s_nop 9
v_accvgpr_read_b32 v172, a48
v_accvgpr_read_b32 v173, a49
v_accvgpr_read_b32 v174, a50
v_accvgpr_read_b32 v175, a51
v_lshlrev_b32 v146, 16, v138
v_add_f32 v172, v172, v146
v_and_b32 v146, 0xffff0000, v138
v_add_f32 v173, v173, v146
v_lshlrev_b32 v146, 16, v139
v_add_f32 v174, v174, v146
v_and_b32 v146, 0xffff0000, v139
v_add_f32 v175, v175, v146
v_accvgpr_read_b32 v176, a56
v_accvgpr_read_b32 v177, a57
v_accvgpr_read_b32 v178, a58
v_accvgpr_read_b32 v179, a59
v_lshlrev_b32 v146, 16, v140
v_add_f32 v176, v176, v146
v_and_b32 v146, 0xffff0000, v140
v_add_f32 v177, v177, v146
v_lshlrev_b32 v146, 16, v141
v_add_f32 v178, v178, v146
v_and_b32 v146, 0xffff0000, v141
v_add_f32 v179, v179, v146
v_accvgpr_read_b32 v180, a52
v_accvgpr_read_b32 v181, a53
v_accvgpr_read_b32 v182, a54
v_accvgpr_read_b32 v183, a55
v_lshlrev_b32 v146, 16, v142
v_add_f32 v180, v180, v146
v_and_b32 v146, 0xffff0000, v142
v_add_f32 v181, v181, v146
v_lshlrev_b32 v146, 16, v143
v_add_f32 v182, v182, v146
v_and_b32 v146, 0xffff0000, v143
v_add_f32 v183, v183, v146
v_accvgpr_read_b32 v184, a60
v_accvgpr_read_b32 v185, a61
v_accvgpr_read_b32 v186, a62
v_accvgpr_read_b32 v187, a63
v_lshlrev_b32 v146, 16, v144
v_add_f32 v184, v184, v146
v_and_b32 v146, 0xffff0000, v144
v_add_f32 v185, v185, v146
v_lshlrev_b32 v146, 16, v145
v_add_f32 v186, v186, v146
v_and_b32 v146, 0xffff0000, v145
v_add_f32 v187, v187, v146
s_mov_b32 s30, 1048576
L_RX_CHK:
s_waitcnt vmcnt(0)
v_max3_u32 v147, v148, v149, v150
v_max3_u32 v147, v147, v151, v152
v_max3_u32 v147, v147, v153, v154
v_max3_u32 v147, v147, v155, v156
v_max3_u32 v147, v147, v157, v158
v_max3_u32 v147, v147, v159, v160
v_max3_u32 v147, v147, v161, v162
v_max3_u32 v147, v147, v163, v164
v_max3_u32 v147, v147, v165, v166
v_max3_u32 v147, v147, v167, v168
v_max3_u32 v147, v147, v169, v170
v_max_u32 v147, v147, v171
v_cmp_eq_u32 vcc, -1, v147
s_nop 4
s_cbranch_vccz L_RX_OK
s_sub_u32 s30, s30, 1
s_cbranch_scc1 L_RX_OK
s_sleep 1
buffer_load_dwordx4 v[148:151], v2, s[16:19], s64 offen offset:0 sc1
buffer_load_dwordx4 v[152:155], v2, s[16:19], s64 offen offset:1024 sc1
buffer_load_dwordx4 v[156:159], v2, s[16:19], s65 offen offset:0 sc1
buffer_load_dwordx4 v[160:163], v2, s[16:19], s65 offen offset:1024 sc1
buffer_load_dwordx4 v[164:167], v2, s[16:19], s66 offen offset:0 sc1
buffer_load_dwordx4 v[168:171], v2, s[16:19], s66 offen offset:1024 sc1
s_branch L_RX_CHK
L_RX_OK:
v_lshlrev_b32 v146, 16, v148
v_add_f32 v172, v172, v146
v_and_b32 v146, 0xffff0000, v148
v_add_f32 v173, v173, v146
v_lshlrev_b32 v146, 16, v156
v_add_f32 v172, v172, v146
v_and_b32 v146, 0xffff0000, v156
v_add_f32 v173, v173, v146
v_lshlrev_b32 v146, 16, v164
v_add_f32 v172, v172, v146
v_and_b32 v146, 0xffff0000, v164
v_add_f32 v173, v173, v146
v_lshlrev_b32 v146, 16, v149
v_add_f32 v174, v174, v146
v_and_b32 v146, 0xffff0000, v149
v_add_f32 v175, v175, v146
v_lshlrev_b32 v146, 16, v157
v_add_f32 v174, v174, v146
v_and_b32 v146, 0xffff0000, v157
v_add_f32 v175, v175, v146
v_lshlrev_b32 v146, 16, v165
v_add_f32 v174, v174, v146
v_and_b32 v146, 0xffff0000, v165
v_add_f32 v175, v175, v146
v_cvt_pk_bf16_f32 v188, v172, v173
v_cvt_pk_bf16_f32 v189, v174, v175
ds_write_b64 v137, v[188:189] offset:0
v_lshlrev_b32 v146, 16, v150
v_add_f32 v176, v176, v146
v_and_b32 v146, 0xffff0000, v150
v_add_f32 v177, v177, v146
v_lshlrev_b32 v146, 16, v158
v_add_f32 v176, v176, v146
v_and_b32 v146, 0xffff0000, v158
v_add_f32 v177, v177, v146
v_lshlrev_b32 v146, 16, v166
v_add_f32 v176, v176, v146
v_and_b32 v146, 0xffff0000, v166
v_add_f32 v177, v177, v146
v_lshlrev_b32 v146, 16, v151
v_add_f32 v178, v178, v146
v_and_b32 v146, 0xffff0000, v151
v_add_f32 v179, v179, v146
v_lshlrev_b32 v146, 16, v159
v_add_f32 v178, v178, v146
v_and_b32 v146, 0xffff0000, v159
v_add_f32 v179, v179, v146
v_lshlrev_b32 v146, 16, v167
v_add_f32 v178, v178, v146
v_and_b32 v146, 0xffff0000, v167
v_add_f32 v179, v179, v146
v_cvt_pk_bf16_f32 v190, v176, v177
v_cvt_pk_bf16_f32 v191, v178, v179
ds_write_b64 v137, v[190:191] offset:32
v_lshlrev_b32 v146, 16, v152
v_add_f32 v180, v180, v146
v_and_b32 v146, 0xffff0000, v152
v_add_f32 v181, v181, v146
v_lshlrev_b32 v146, 16, v160
v_add_f32 v180, v180, v146
v_and_b32 v146, 0xffff0000, v160
v_add_f32 v181, v181, v146
v_lshlrev_b32 v146, 16, v168
v_add_f32 v180, v180, v146
v_and_b32 v146, 0xffff0000, v168
v_add_f32 v181, v181, v146
v_lshlrev_b32 v146, 16, v153
v_add_f32 v182, v182, v146
v_and_b32 v146, 0xffff0000, v153
v_add_f32 v183, v183, v146
v_lshlrev_b32 v146, 16, v161
v_add_f32 v182, v182, v146
v_and_b32 v146, 0xffff0000, v161
v_add_f32 v183, v183, v146
v_lshlrev_b32 v146, 16, v169
v_add_f32 v182, v182, v146
v_and_b32 v146, 0xffff0000, v169
v_add_f32 v183, v183, v146
v_cvt_pk_bf16_f32 v192, v180, v181
v_cvt_pk_bf16_f32 v193, v182, v183
ds_write_b64 v137, v[192:193] offset:1024
v_lshlrev_b32 v146, 16, v154
v_add_f32 v184, v184, v146
v_and_b32 v146, 0xffff0000, v154
v_add_f32 v185, v185, v146
v_lshlrev_b32 v146, 16, v162
v_add_f32 v184, v184, v146
v_and_b32 v146, 0xffff0000, v162
v_add_f32 v185, v185, v146
v_lshlrev_b32 v146, 16, v170
v_add_f32 v184, v184, v146
v_and_b32 v146, 0xffff0000, v170
v_add_f32 v185, v185, v146
v_lshlrev_b32 v146, 16, v155
v_add_f32 v186, v186, v146
v_and_b32 v146, 0xffff0000, v155
v_add_f32 v187, v187, v146
v_lshlrev_b32 v146, 16, v163
v_add_f32 v186, v186, v146
v_and_b32 v146, 0xffff0000, v163
v_add_f32 v187, v187, v146
v_lshlrev_b32 v146, 16, v171
v_add_f32 v186, v186, v146
v_and_b32 v146, 0xffff0000, v171
v_add_f32 v187, v187, v146
v_cvt_pk_bf16_f32 v194, v184, v185
v_cvt_pk_bf16_f32 v195, v186, v187
ds_write_b64 v137, v[194:195] offset:1056
ds_read_b128 v[10:13], v136 offset:0
ds_read_b128 v[14:17], v136 offset:1024
s_waitcnt lgkmcnt(1)
global_store_dwordx4 v39, v[10:13], s[80:81]
s_waitcnt lgkmcnt(0)
global_store_dwordx4 v9, v[14:17], s[80:81]
s_waitcnt vmcnt(0) lgkmcnt(0)
s_endpgm
L_QONLY:
s_waitcnt vmcnt(0) lgkmcnt(0)
s_nop 15
s_nop 3
s_barrier
ds_read_b64_tr_b16 v[40:41], v10 offset:0
ds_read_b64_tr_b16 v[42:43], v10 offset:1024
ds_read_b64_tr_b16 v[44:45], v11 offset:0
ds_read_b64_tr_b16 v[46:47], v11 offset:1024
ds_read_b64_tr_b16 v[48:49], v12 offset:0
ds_read_b64_tr_b16 v[50:51], v12 offset:1024
ds_read_b64_tr_b16 v[52:53], v13 offset:0
ds_read_b64_tr_b16 v[54:55], v13 offset:1024
s_waitcnt lgkmcnt(6)
v_mfma_f32_16x16x32_bf16 a[128:131], v[40:43], v[88:91], 0
ds_read_b64_tr_b16 v[56:57], v14 offset:0
ds_read_b64_tr_b16 v[58:59], v14 offset:1024
buffer_load_dwordx4 v[64:67], v28, s[52:55], s8 offen offset:3072 sc1
v_mfma_f32_16x16x32_bf16 a[132:135], v[40:43], v[92:95], 0
s_mov_b32 s31, 8192
buffer_store_dwordx4 v[160:163], v28, s[92:95], s31 offen offset:0 sc1
v_mfma_f32_16x16x32_bf16 a[136:139], v[40:43], v[96:99], 0
s_lshl_b32 s35, s44, 1
s_add_u32 s35, s35, 2
v_mfma_f32_16x16x32_bf16 a[140:143], v[40:43], v[100:103], 0
s_cmp_lt_u32 s7, 2
s_cselect_b32 s36, 1, 0
s_waitcnt lgkmcnt(6)
v_mfma_f32_16x16x32_bf16 a[144:147], v[44:47], v[88:91], 0
ds_read_b64_tr_b16 v[60:61], v15 offset:0
ds_read_b64_tr_b16 v[62:63], v15 offset:1024
v_mfma_f32_16x16x32_bf16 a[148:151], v[44:47], v[92:95], 0
buffer_load_dwordx4 v[68:71], v28, s[52:55], s9 offen offset:3072 sc1
v_mfma_f32_16x16x32_bf16 a[152:155], v[44:47], v[96:99], 0
v_lshlrev_b32 v2, 4, v1
v_mfma_f32_16x16x32_bf16 a[156:159], v[44:47], v[100:103], 0
v_add_u32 v9, 8192, v2
s_waitcnt lgkmcnt(6)
v_mfma_f32_16x16x32_bf16 a[160:163], v[48:51], v[88:91], 0
ds_read_b64_tr_b16 v[40:41], v16 offset:0
ds_read_b64_tr_b16 v[42:43], v16 offset:1024
v_mfma_f32_16x16x32_bf16 a[164:167], v[48:51], v[92:95], 0
buffer_load_dwordx4 v[120:123], v28, s[52:55], s10 offen offset:3072 sc1
v_mfma_f32_16x16x32_bf16 a[168:171], v[48:51], v[96:99], 0
v_lshrrev_b32 v22, 4, v1
v_mfma_f32_16x16x32_bf16 a[172:175], v[48:51], v[100:103], 0
v_lshlrev_b32 v22, 2, v22
s_waitcnt lgkmcnt(6)
v_mfma_f32_16x16x32_bf16 a[176:179], v[52:55], v[88:91], 0
ds_read_b64_tr_b16 v[44:45], v17 offset:0
ds_read_b64_tr_b16 v[46:47], v17 offset:1024
v_mfma_f32_16x16x32_bf16 a[180:183], v[52:55], v[92:95], 0
buffer_load_dwordx4 v[124:127], v28, s[52:55], s11 offen offset:3072 sc1
v_mfma_f32_16x16x32_bf16 a[184:187], v[52:55], v[96:99], 0
v_and_b32 v23, 15, v1
v_mfma_f32_16x16x32_bf16 a[188:191], v[52:55], v[100:103], 0
v_sub_u32 v6, v22, v23
s_barrier
s_waitcnt lgkmcnt(6)
v_mfma_f32_16x16x32_bf16 a[192:195], v[56:59], v[88:91], 0
ds_read_b64_tr_b16 v[48:49], v10 offset:16384
ds_read_b64_tr_b16 v[50:51], v10 offset:17408
v_mfma_f32_16x16x32_bf16 a[196:199], v[56:59], v[92:95], 0
s_add_u32 s28, s71, 786432
s_add_u32 m0, s70, 49152
v_mfma_f32_16x16x32_bf16 a[200:203], v[56:59], v[96:99], 0
buffer_load_dwordx4 v29, s[76:79], s28 offen lds
buffer_store_dwordx4 v[164:167], v28, s[92:95], s31 offen offset:1024 sc1
v_mfma_f32_16x16x32_bf16 a[204:207], v[56:59], v[100:103], 0
v_mov_b32 v7, 0xff800000
s_lshr_b32 s24, s7, 1
s_waitcnt lgkmcnt(6)
v_mfma_f32_16x16x32_bf16 a[208:211], v[60:63], v[88:91], 0
ds_read_b64_tr_b16 v[52:53], v11 offset:16384
ds_read_b64_tr_b16 v[54:55], v11 offset:17408
v_mfma_f32_16x16x32_bf16 a[212:215], v[60:63], v[92:95], 0
s_add_u32 s28, s28, 31744
v_mfma_f32_16x16x32_bf16 a[216:219], v[60:63], v[96:99], 0
buffer_load_dwordx4 v29, s[76:79], s28 offen offset:1024 lds
s_lshl_b32 s24, s24, 16
v_mfma_f32_16x16x32_bf16 a[220:223], v[60:63], v[100:103], 0
v_add_u32 v3, s24, v2
s_waitcnt lgkmcnt(6)
v_mfma_f32_16x16x32_bf16 a[224:227], v[40:43], v[88:91], 0
ds_read_b64_tr_b16 v[56:57], v12 offset:16384
v_mfma_f32_16x16x32_bf16 a[228:231], v[40:43], v[92:95], 0
ds_read_b64_tr_b16 v[58:59], v12 offset:17408
v_mfma_f32_16x16x32_bf16 a[232:235], v[40:43], v[96:99], 0
v_add_u32 v4, 49152, v3
v_mfma_f32_16x16x32_bf16 a[236:239], v[40:43], v[100:103], 0
s_lshl_b32 s25, s75, 12
s_waitcnt lgkmcnt(6)
v_mfma_f32_16x16x32_bf16 a[240:243], v[44:47], v[88:91], 0
ds_read_b64_tr_b16 v[60:61], v13 offset:16384
v_mfma_f32_16x16x32_bf16 a[244:247], v[44:47], v[92:95], 0
ds_read_b64_tr_b16 v[62:63], v13 offset:17408
v_mfma_f32_16x16x32_bf16 a[248:251], v[44:47], v[96:99], 0
s_lshl_b32 s27, s44, 1
v_mfma_f32_16x16x32_bf16 a[252:255], v[44:47], v[100:103], 0
s_sub_u32 s22, 13, s27
s_waitcnt lgkmcnt(6)
v_mfma_f32_16x16x32_bf16 a[128:131], v[48:51], v[72:75], a[128:131]
ds_read_b64_tr_b16 v[40:41], v14 offset:16384
ds_read_b64_tr_b16 v[42:43], v14 offset:17408
s_mov_b32 s8, 4096
v_mfma_f32_16x16x32_bf16 a[132:135], v[48:51], v[76:79], a[132:135]
buffer_load_dwordx4 v[88:91], v28, s[52:55], s8 offen offset:0 sc1
s_mov_b32 s9, 36864
v_mfma_f32_16x16x32_bf16 a[136:139], v[48:51], v[80:83], a[136:139]
buffer_load_dwordx4 v[92:95], v28, s[52:55], s9 offen offset:0 sc1
buffer_store_dwordx4 v[168:171], v28, s[92:95], s31 offen offset:2048 sc1
s_cmp_lt_u32 s7, 2
v_mfma_f32_16x16x32_bf16 a[140:143], v[48:51], v[84:87], a[140:143]
s_cselect_b32 s22, s22, -2
s_lshl_b32 s23, s44, 2
s_waitcnt lgkmcnt(6)
v_mfma_f32_16x16x32_bf16 a[144:147], v[52:55], v[72:75], a[144:147]
ds_read_b64_tr_b16 v[44:45], v15 offset:16384
ds_read_b64_tr_b16 v[46:47], v15 offset:17408
v_mfma_f32_16x16x32_bf16 a[148:151], v[52:55], v[76:79], a[148:151]
s_mov_b32 s10, 69632
buffer_load_dwordx4 v[96:99], v28, s[52:55], s10 offen offset:0 sc1
v_mfma_f32_16x16x32_bf16 a[152:155], v[52:55], v[80:83], a[152:155]
s_sub_u32 s23, 30, s23
s_cmp_lt_u32 s7, 2
v_mfma_f32_16x16x32_bf16 a[156:159], v[52:55], v[84:87], a[156:159]
s_cselect_b32 s23, 0, s23
s_waitcnt lgkmcnt(6)
v_mfma_f32_16x16x32_bf16 a[160:163], v[56:59], v[72:75], a[160:163]
ds_read_b64_tr_b16 v[48:49], v16 offset:16384
ds_read_b64_tr_b16 v[50:51], v16 offset:17408
v_mfma_f32_16x16x32_bf16 a[164:167], v[56:59], v[76:79], a[164:167]
s_mov_b32 s11, 102400
buffer_load_dwordx4 v[100:103], v28, s[52:55], s11 offen offset:0 sc1
v_mfma_f32_16x16x32_bf16 a[168:171], v[56:59], v[80:83], a[168:171]
v_mov_b32 v22, s23
v_cmp_le_u32 vcc, s27, v1
v_mfma_f32_16x16x32_bf16 a[172:175], v[56:59], v[84:87], a[172:175]
s_nop 0
v_cndmask_b32 v22, 0, v22
s_waitcnt lgkmcnt(6)
v_mfma_f32_16x16x32_bf16 a[176:179], v[60:63], v[72:75], a[176:179]
ds_read_b64_tr_b16 v[52:53], v17 offset:16384
ds_read_b64_tr_b16 v[54:55], v17 offset:17408
v_mfma_f32_16x16x32_bf16 a[180:183], v[60:63], v[76:79], a[180:183]
v_mov_b32 v23, s22
v_mfma_f32_16x16x32_bf16 a[184:187], v[60:63], v[80:83], a[184:187]
v_cmp_le_u32 vcc, s35, v1
v_mfma_f32_16x16x32_bf16 a[188:191], v[60:63], v[84:87], a[188:191]
s_nop 0
v_cndmask_b32 v22, v22, v23
s_barrier
s_waitcnt lgkmcnt(6)
v_mfma_f32_16x16x32_bf16 a[192:195], v[40:43], v[72:75], a[192:195]
ds_read_b64_tr_b16 v[56:57], v10 offset:32768
ds_read_b64_tr_b16 v[58:59], v10 offset:33792
s_add_u32 s28, s71, 1048576
v_mfma_f32_16x16x32_bf16 a[196:199], v[40:43], v[76:79], a[196:199]
s_add_u32 m0, s70, 0
s_nop 0
buffer_load_dwordx4 v29, s[76:79], s28 offen lds
v_mfma_f32_16x16x32_bf16 a[200:203], v[40:43], v[80:83], a[200:203]
s_add_u32 s28, s28, 31744
buffer_load_dwordx4 v29, s[76:79], s28 offen offset:1024 lds
v_mfma_f32_16x16x32_bf16 a[204:207], v[40:43], v[84:87], a[204:207]
v_add_u32 v22, v22, v1
v_lshlrev_b32 v22, 14, v22
s_waitcnt lgkmcnt(6)
v_mfma_f32_16x16x32_bf16 a[208:211], v[44:47], v[72:75], a[208:211]
ds_read_b64_tr_b16 v[60:61], v11 offset:32768
ds_read_b64_tr_b16 v[62:63], v11 offset:33792
v_mfma_f32_16x16x32_bf16 a[212:215], v[44:47], v[76:79], a[212:215]
buffer_store_dwordx4 v[172:175], v28, s[92:95], s31 offen offset:3072 sc1
v_mfma_f32_16x16x32_bf16 a[216:219], v[44:47], v[80:83], a[216:219]
v_add_u32 v5, s25, v22
v_mfma_f32_16x16x32_bf16 a[220:223], v[44:47], v[84:87], a[220:223]
v_mov_b32 v22, 1073741824
s_waitcnt lgkmcnt(6)
v_mfma_f32_16x16x32_bf16 a[224:227], v[48:51], v[72:75], a[224:227]
ds_read_b64_tr_b16 v[40:41], v12 offset:32768
ds_read_b64_tr_b16 v[42:43], v12 offset:33792
v_mfma_f32_16x16x32_bf16 a[228:231], v[48:51], v[76:79], a[228:231]
v_cmp_lt_u32 vcc, 16, v1
v_mfma_f32_16x16x32_bf16 a[232:235], v[48:51], v[80:83], a[232:235]
s_nop 0
v_cndmask_b32 v5, v5, v22
v_mfma_f32_16x16x32_bf16 a[236:239], v[48:51], v[84:87], a[236:239]
s_lshl_b32 s22, 1, s35
s_waitcnt lgkmcnt(6)
v_mfma_f32_16x16x32_bf16 a[240:243], v[52:55], v[72:75], a[240:243]
ds_read_b64_tr_b16 v[44:45], v13 offset:32768
ds_read_b64_tr_b16 v[46:47], v13 offset:33792
v_mfma_f32_16x16x32_bf16 a[244:247], v[52:55], v[76:79], a[244:247]
s_lshr_b32 s23, s22, 3
v_mfma_f32_16x16x32_bf16 a[248:251], v[52:55], v[80:83], a[248:251]
s_cmp_lt_u32 s7, 2
v_mfma_f32_16x16x32_bf16 a[252:255], v[52:55], v[84:87], a[252:255]
s_cselect_b32 s22, s22, 0
s_waitcnt lgkmcnt(6)
v_mfma_f32_16x16x32_bf16 a[128:131], v[56:59], v[104:107], a[128:131]
ds_read_b64_tr_b16 v[48:49], v14 offset:32768
ds_read_b64_tr_b16 v[50:51], v14 offset:33792
v_mfma_f32_16x16x32_bf16 a[132:135], v[56:59], v[108:111], a[132:135]
buffer_load_dwordx4 v[72:75], v28, s[52:55], s8 offen offset:1024 sc1
v_mfma_f32_16x16x32_bf16 a[136:139], v[56:59], v[112:115], a[136:139]
buffer_load_dwordx4 v[76:79], v28, s[52:55], s9 offen offset:1024 sc1
s_lshl_b32 s22, 1, s35
v_mfma_f32_16x16x32_bf16 a[140:143], v[56:59], v[116:119], a[140:143]
s_or_b32 s51, s22, 65536
s_waitcnt lgkmcnt(6)
v_mfma_f32_16x16x32_bf16 a[144:147], v[60:63], v[104:107], a[144:147]
ds_read_b64_tr_b16 v[52:53], v15 offset:32768
ds_read_b64_tr_b16 v[54:55], v15 offset:33792
v_mfma_f32_16x16x32_bf16 a[148:151], v[60:63], v[108:111], a[148:151]
buffer_load_dwordx4 v[80:83], v28, s[52:55], s10 offen offset:1024 sc1
s_mov_b32 s31, 12288
v_mfma_f32_16x16x32_bf16 a[152:155], v[60:63], v[112:115], a[152:155]
buffer_store_dwordx4 v[176:179], v28, s[92:95], s31 offen offset:0 sc1
s_or_b32 s60, s51, s23
v_mfma_f32_16x16x32_bf16 a[156:159], v[60:63], v[116:119], a[156:159]
s_lshl_b32 s22, s45, 19
s_waitcnt lgkmcnt(6)
v_mfma_f32_16x16x32_bf16 a[160:163], v[40:43], v[104:107], a[160:163]
ds_read_b64_tr_b16 v[56:57], v16 offset:32768
ds_read_b64_tr_b16 v[58:59], v16 offset:33792
v_mfma_f32_16x16x32_bf16 a[164:167], v[40:43], v[108:111], a[164:167]
buffer_load_dwordx4 v[84:87], v28, s[52:55], s11 offen offset:1024 sc1
v_mfma_f32_16x16x32_bf16 a[168:171], v[40:43], v[112:115], a[168:171]
s_add_u32 s23, s22, 5242880
s_add_u32 s46, s48, s23
v_mfma_f32_16x16x32_bf16 a[172:175], v[40:43], v[116:119], a[172:175]
s_addc_u32 s47, s49, 0
s_waitcnt lgkmcnt(6)
v_mfma_f32_16x16x32_bf16 a[176:179], v[44:47], v[104:107], a[176:179]
ds_read_b64_tr_b16 v[60:61], v17 offset:32768
v_mfma_f32_16x16x32_bf16 a[180:183], v[44:47], v[108:111], a[180:183]
ds_read_b64_tr_b16 v[62:63], v17 offset:33792
v_mfma_f32_16x16x32_bf16 a[184:187], v[44:47], v[112:115], a[184:187]
s_mov_b32 s12, s46
v_mfma_f32_16x16x32_bf16 a[188:191], v[44:47], v[116:119], a[188:191]
s_and_b32 s13, s47, 65535
s_waitcnt vmcnt(13)
s_barrier
s_waitcnt lgkmcnt(6)
v_mfma_f32_16x16x32_bf16 a[192:195], v[48:51], v[104:107], a[192:195]
ds_read_b64_tr_b16 v[40:41], v10 offset:49152
ds_read_b64_tr_b16 v[42:43], v10 offset:50176
s_add_u32 s28, s71, 1310720
v_mfma_f32_16x16x32_bf16 a[196:199], v[48:51], v[108:111], a[196:199]
s_add_u32 m0, s70, 16384
s_nop 0
buffer_load_dwordx4 v29, s[76:79], s28 offen lds
v_mfma_f32_16x16x32_bf16 a[200:203], v[48:51], v[112:115], a[200:203]
s_add_u32 s28, s28, 31744
buffer_load_dwordx4 v29, s[76:79], s28 offen offset:1024 lds
v_mfma_f32_16x16x32_bf16 a[204:207], v[48:51], v[116:119], a[204:207]
s_mov_b32 s14, 524288
s_mov_b32 s15, 131072
s_waitcnt lgkmcnt(6)
v_mfma_f32_16x16x32_bf16 a[208:211], v[52:55], v[104:107], a[208:211]
ds_read_b64_tr_b16 v[44:45], v11 offset:49152
ds_read_b64_tr_b16 v[46:47], v11 offset:50176
v_mfma_f32_16x16x32_bf16 a[212:215], v[52:55], v[108:111], a[212:215]
buffer_store_dwordx4 v[180:183], v28, s[92:95], s31 offen offset:1024 sc1
v_mfma_f32_16x16x32_bf16 a[216:219], v[52:55], v[112:115], a[216:219]
s_add_u32 s23, s22, 22020096
s_add_u32 s46, s48, s23
v_mfma_f32_16x16x32_bf16 a[220:223], v[52:55], v[116:119], a[220:223]
s_addc_u32 s47, s49, 0
s_waitcnt lgkmcnt(6)
v_mfma_f32_16x16x32_bf16 a[224:227], v[56:59], v[104:107], a[224:227]
ds_read_b64_tr_b16 v[48:49], v12 offset:49152
v_mfma_f32_16x16x32_bf16 a[228:231], v[56:59], v[108:111], a[228:231]
ds_read_b64_tr_b16 v[50:51], v12 offset:50176
v_mfma_f32_16x16x32_bf16 a[232:235], v[56:59], v[112:115], a[232:235]
s_mov_b32 s16, s46
v_mfma_f32_16x16x32_bf16 a[236:239], v[56:59], v[116:119], a[236:239]
s_and_b32 s17, s47, 65535
s_waitcnt lgkmcnt(6)
v_mfma_f32_16x16x32_bf16 a[240:243], v[60:63], v[104:107], a[240:243]
ds_read_b64_tr_b16 v[52:53], v13 offset:49152
v_mfma_f32_16x16x32_bf16 a[244:247], v[60:63], v[108:111], a[244:247]
ds_read_b64_tr_b16 v[54:55], v13 offset:50176
v_mfma_f32_16x16x32_bf16 a[248:251], v[60:63], v[112:115], a[248:251]
s_mov_b32 s18, 524288
v_mfma_f32_16x16x32_bf16 a[252:255], v[60:63], v[116:119], a[252:255]
s_mov_b32 s19, 131072
s_waitcnt lgkmcnt(6)
v_mfma_f32_16x16x32_bf16 a[128:131], v[40:43], v[64:67], a[128:131]
ds_read_b64_tr_b16 v[56:57], v14 offset:49152
ds_read_b64_tr_b16 v[58:59], v14 offset:50176
v_mfma_f32_16x16x32_bf16 a[132:135], v[40:43], v[68:71], a[132:135]
buffer_load_dwordx4 v[104:107], v28, s[52:55], s8 offen offset:2048 sc1
v_mfma_f32_16x16x32_bf16 a[136:139], v[40:43], v[120:123], a[136:139]
buffer_load_dwordx4 v[108:111], v28, s[52:55], s9 offen offset:2048 sc1
s_lshr_b32 s22, s50, 3
v_mfma_f32_16x16x32_bf16 a[140:143], v[40:43], v[124:127], a[140:143]
s_lshl_b32 s22, s22, 15
s_waitcnt lgkmcnt(6)
v_mfma_f32_16x16x32_bf16 a[144:147], v[44:47], v[64:67], a[144:147]
ds_read_b64_tr_b16 v[60:61], v15 offset:49152
ds_read_b64_tr_b16 v[62:63], v15 offset:50176
v_mfma_f32_16x16x32_bf16 a[148:151], v[44:47], v[68:71], a[148:151]
buffer_load_dwordx4 v[112:115], v28, s[52:55], s10 offen offset:2048 sc1
v_mfma_f32_16x16x32_bf16 a[152:155], v[44:47], v[120:123], a[152:155]
s_lshl_b32 s23, s75, 12
v_mfma_f32_16x16x32_bf16 a[156:159], v[44:47], v[124:127], a[156:159]
s_add_u32 s37, s22, s23
s_waitcnt lgkmcnt(6)
v_mfma_f32_16x16x32_bf16 a[160:163], v[48:51], v[64:67], a[160:163]
ds_read_b64_tr_b16 v[40:41], v16 offset:49152
ds_read_b64_tr_b16 v[42:43], v16 offset:50176
v_mfma_f32_16x16x32_bf16 a[164:167], v[48:51], v[68:71], a[164:167]
buffer_load_dwordx4 v[116:119], v28, s[52:55], s11 offen offset:2048 sc1
v_mfma_f32_16x16x32_bf16 a[168:171], v[48:51], v[120:123], a[168:171]
buffer_store_dwordx4 v[184:187], v28, s[92:95], s31 offen offset:2048 sc1
s_mul_i32 s22, s50, 184
v_mfma_f32_16x16x32_bf16 a[172:175], v[48:51], v[124:127], a[172:175]
s_lshl_b32 s23, s45, 2
s_waitcnt lgkmcnt(6)
v_mfma_f32_16x16x32_bf16 a[176:179], v[52:55], v[64:67], a[176:179]
ds_read_b64_tr_b16 v[44:45], v17 offset:49152
v_mfma_f32_16x16x32_bf16 a[180:183], v[52:55], v[68:71], a[180:183]
ds_read_b64_tr_b16 v[46:47], v17 offset:50176
v_mfma_f32_16x16x32_bf16 a[184:187], v[52:55], v[120:123], a[184:187]
s_add_u32 s22, s22, s23
v_mfma_f32_16x16x32_bf16 a[188:191], v[52:55], v[124:127], a[188:191]
s_lshl_b32 s22, s22, 10
s_waitcnt vmcnt(14)
s_barrier
s_waitcnt lgkmcnt(6)
v_mfma_f32_16x16x32_bf16 a[192:195], v[56:59], v[64:67], a[192:195]
ds_read_b64_tr_b16 v[48:49], v10 offset:0
ds_read_b64_tr_b16 v[50:51], v10 offset:1024
s_add_u32 s28, s71, 1572864
v_mfma_f32_16x16x32_bf16 a[196:199], v[56:59], v[68:71], a[196:199]
s_add_u32 m0, s70, 32768
s_nop 0
buffer_load_dwordx4 v29, s[76:79], s28 offen lds
v_mfma_f32_16x16x32_bf16 a[200:203], v[56:59], v[120:123], a[200:203]
)ASM"
R"ASM(s_add_u32 s28, s28, 31744
buffer_load_dwordx4 v29, s[76:79], s28 offen offset:1024 lds
s_add_u32 s22, s22, 38797312
v_mfma_f32_16x16x32_bf16 a[204:207], v[56:59], v[124:127], a[204:207]
s_add_u32 s46, s48, s22
s_addc_u32 s47, s49, 0
s_waitcnt lgkmcnt(6)
v_mfma_f32_16x16x32_bf16 a[208:211], v[60:63], v[64:67], a[208:211]
ds_read_b64_tr_b16 v[52:53], v11 offset:0
v_mfma_f32_16x16x32_bf16 a[212:215], v[60:63], v[68:71], a[212:215]
ds_read_b64_tr_b16 v[54:55], v11 offset:1024
v_mfma_f32_16x16x32_bf16 a[216:219], v[60:63], v[120:123], a[216:219]
s_mov_b32 s56, s46
v_mfma_f32_16x16x32_bf16 a[220:223], v[60:63], v[124:127], a[220:223]
s_and_b32 s57, s47, 65535
s_waitcnt lgkmcnt(6)
v_mfma_f32_16x16x32_bf16 a[224:227], v[40:43], v[64:67], a[224:227]
ds_read_b64_tr_b16 v[56:57], v12 offset:0
ds_read_b64_tr_b16 v[58:59], v12 offset:1024
v_mfma_f32_16x16x32_bf16 a[228:231], v[40:43], v[68:71], a[228:231]
buffer_store_dwordx4 v[188:191], v28, s[92:95], s31 offen offset:3072 sc1
v_mfma_f32_16x16x32_bf16 a[232:235], v[40:43], v[120:123], a[232:235]
s_mov_b32 s58, 1318912
v_mfma_f32_16x16x32_bf16 a[236:239], v[40:43], v[124:127], a[236:239]
s_mov_b32 s59, 131072
s_waitcnt lgkmcnt(6)
v_mfma_f32_16x16x32_bf16 a[240:243], v[44:47], v[64:67], a[240:243]
ds_read_b64_tr_b16 v[60:61], v13 offset:0
ds_read_b64_tr_b16 v[62:63], v13 offset:1024
v_mfma_f32_16x16x32_bf16 a[244:247], v[44:47], v[68:71], a[244:247]
s_getpc_b64 s[46:47]
L_CP_X5:
v_mfma_f32_16x16x32_bf16 a[248:251], v[44:47], v[120:123], a[248:251]
s_add_u32 s46, s46, L_XM-L_CP_X5
s_addc_u32 s47, s47, -1
v_mfma_f32_16x16x32_bf16 a[252:255], v[44:47], v[124:127], a[252:255]
s_lshr_b32 s22, s6, 3
s_waitcnt lgkmcnt(6)
v_mfma_f32_16x16x32_bf16 a[128:131], v[48:51], v[88:91], a[128:131]
ds_read_b64_tr_b16 v[40:41], v14 offset:0
ds_read_b64_tr_b16 v[42:43], v14 offset:1024
v_mfma_f32_16x16x32_bf16 a[132:135], v[48:51], v[92:95], a[132:135]
buffer_load_dwordx4 v[64:67], v28, s[52:55], s8 offen offset:3072 sc1
v_mfma_f32_16x16x32_bf16 a[136:139], v[48:51], v[96:99], a[136:139]
buffer_load_dwordx4 v[68:71], v28, s[52:55], s9 offen offset:3072 sc1
s_lshl_b32 s22, s22, 2
v_mfma_f32_16x16x32_bf16 a[140:143], v[48:51], v[100:103], a[140:143]
s_add_u32 s22, s22, s7
s_waitcnt lgkmcnt(6)
v_mfma_f32_16x16x32_bf16 a[144:147], v[52:55], v[88:91], a[144:147]
ds_read_b64_tr_b16 v[44:45], v15 offset:0
ds_read_b64_tr_b16 v[46:47], v15 offset:1024
v_mfma_f32_16x16x32_bf16 a[148:151], v[52:55], v[92:95], a[148:151]
buffer_load_dwordx4 v[120:123], v28, s[52:55], s10 offen offset:3072 sc1
v_mfma_f32_16x16x32_bf16 a[152:155], v[52:55], v[96:99], a[152:155]
s_mul_i32 s22, s22, 256
v_min_u32 v22, 1, v1
v_mfma_f32_16x16x32_bf16 a[156:159], v[52:55], v[100:103], a[156:159]
v_lshlrev_b32 v22, 7, v22
s_waitcnt lgkmcnt(6)
v_mfma_f32_16x16x32_bf16 a[160:163], v[56:59], v[88:91], a[160:163]
ds_read_b64_tr_b16 v[48:49], v16 offset:0
ds_read_b64_tr_b16 v[50:51], v16 offset:1024
v_mfma_f32_16x16x32_bf16 a[164:167], v[56:59], v[92:95], a[164:167]
buffer_load_dwordx4 v[124:127], v28, s[52:55], s11 offen offset:3072 sc1
s_mov_b32 s31, 524288
v_mfma_f32_16x16x32_bf16 a[168:171], v[56:59], v[96:99], a[168:171]
buffer_store_dwordx4 v[192:195], v28, s[92:95], s31 offen offset:0 sc1
v_add_u32 v22, s22, v22
v_mfma_f32_16x16x32_bf16 a[172:175], v[56:59], v[100:103], a[172:175]
v_min_u32 v22, L_CODE_END-L_XM-4, v22
s_waitcnt lgkmcnt(6)
v_mfma_f32_16x16x32_bf16 a[176:179], v[60:63], v[88:91], a[176:179]
ds_read_b64_tr_b16 v[52:53], v17 offset:0
v_mfma_f32_16x16x32_bf16 a[180:183], v[60:63], v[92:95], a[180:183]
ds_read_b64_tr_b16 v[54:55], v17 offset:1024
v_mfma_f32_16x16x32_bf16 a[184:187], v[60:63], v[96:99], a[184:187]
global_load_dword v8, v22, s[46:47]
v_mfma_f32_16x16x32_bf16 a[188:191], v[60:63], v[100:103], a[188:191]
v_and_b32 v24, 7, v1
s_waitcnt vmcnt(15)
s_barrier
s_waitcnt lgkmcnt(6)
v_mfma_f32_16x16x32_bf16 a[192:195], v[40:43], v[88:91], a[192:195]
ds_read_b64_tr_b16 v[56:57], v10 offset:16384
ds_read_b64_tr_b16 v[58:59], v10 offset:17408
s_add_u32 s28, s71, 1835008
v_mfma_f32_16x16x32_bf16 a[196:199], v[40:43], v[92:95], a[196:199]
s_add_u32 m0, s70, 49152
s_nop 0
buffer_load_dwordx4 v29, s[76:79], s28 offen lds
v_mfma_f32_16x16x32_bf16 a[200:203], v[40:43], v[96:99], a[200:203]
s_add_u32 s28, s28, 31744
buffer_load_dwordx4 v29, s[76:79], s28 offen offset:1024 lds
v_mfma_f32_16x16x32_bf16 a[204:207], v[40:43], v[100:103], a[204:207]
v_lshrrev_b32 v25, 3, v1
v_and_b32 v25, 1, v25
s_waitcnt lgkmcnt(6)
v_mfma_f32_16x16x32_bf16 a[208:211], v[44:47], v[88:91], a[208:211]
ds_read_b64_tr_b16 v[60:61], v11 offset:16384
v_mfma_f32_16x16x32_bf16 a[212:215], v[44:47], v[92:95], a[212:215]
ds_read_b64_tr_b16 v[62:63], v11 offset:17408
v_mfma_f32_16x16x32_bf16 a[216:219], v[44:47], v[96:99], a[216:219]
v_lshlrev_b32 v25, 4, v25
v_mfma_f32_16x16x32_bf16 a[220:223], v[44:47], v[100:103], a[220:223]
v_add_u32 v24, v24, v25
s_waitcnt lgkmcnt(6)
v_mfma_f32_16x16x32_bf16 a[224:227], v[48:51], v[88:91], a[224:227]
ds_read_b64_tr_b16 v[40:41], v12 offset:16384
v_mfma_f32_16x16x32_bf16 a[228:231], v[48:51], v[92:95], a[228:231]
ds_read_b64_tr_b16 v[42:43], v12 offset:17408
v_mfma_f32_16x16x32_bf16 a[232:235], v[48:51], v[96:99], a[232:235]
s_and_b32 s22, s45, 3
v_mfma_f32_16x16x32_bf16 a[236:239], v[48:51], v[100:103], a[236:239]
s_lshr_b32 s22, s22, 1
s_waitcnt lgkmcnt(6)
v_mfma_f32_16x16x32_bf16 a[240:243], v[52:55], v[88:91], a[240:243]
ds_read_b64_tr_b16 v[44:45], v13 offset:16384
ds_read_b64_tr_b16 v[46:47], v13 offset:17408
v_mfma_f32_16x16x32_bf16 a[244:247], v[52:55], v[92:95], a[244:247]
buffer_store_dwordx4 v[196:199], v28, s[92:95], s31 offen offset:1024 sc1
v_mfma_f32_16x16x32_bf16 a[248:251], v[52:55], v[96:99], a[248:251]
s_lshl_b32 s22, s22, 3
v_mfma_f32_16x16x32_bf16 a[252:255], v[52:55], v[100:103], a[252:255]
v_add_u32 v24, s22, v24
s_waitcnt lgkmcnt(6)
v_mfma_f32_16x16x32_bf16 a[128:131], v[56:59], v[72:75], a[128:131]
ds_read_b64_tr_b16 v[48:49], v14 offset:16384
ds_read_b64_tr_b16 v[50:51], v14 offset:17408
v_mfma_f32_16x16x32_bf16 a[132:135], v[56:59], v[76:79], a[132:135]
s_mov_b32 s8, 8192
buffer_load_dwordx4 v[88:91], v28, s[52:55], s8 offen offset:0 sc1
v_mfma_f32_16x16x32_bf16 a[136:139], v[56:59], v[80:83], a[136:139]
s_mov_b32 s9, 40960
buffer_load_dwordx4 v[92:95], v28, s[52:55], s9 offen offset:0 sc1
v_mfma_f32_16x16x32_bf16 a[140:143], v[56:59], v[84:87], a[140:143]
v_lshlrev_b32 v24, 3, v24
s_and_b32 s22, s6, 7
s_waitcnt lgkmcnt(6)
v_mfma_f32_16x16x32_bf16 a[144:147], v[60:63], v[72:75], a[144:147]
ds_read_b64_tr_b16 v[52:53], v15 offset:16384
ds_read_b64_tr_b16 v[54:55], v15 offset:17408
v_mfma_f32_16x16x32_bf16 a[148:151], v[60:63], v[76:79], a[148:151]
s_mov_b32 s10, 73728
v_mfma_f32_16x16x32_bf16 a[152:155], v[60:63], v[80:83], a[152:155]
buffer_load_dwordx4 v[96:99], v28, s[52:55], s10 offen offset:0 sc1
v_add_u32 v24, s22, v24
v_mfma_f32_16x16x32_bf16 a[156:159], v[60:63], v[84:87], a[156:159]
v_lshlrev_b32 v24, 6, v24
s_waitcnt lgkmcnt(6)
v_mfma_f32_16x16x32_bf16 a[160:163], v[40:43], v[72:75], a[160:163]
ds_read_b64_tr_b16 v[56:57], v16 offset:16384
ds_read_b64_tr_b16 v[58:59], v16 offset:17408
v_mfma_f32_16x16x32_bf16 a[164:167], v[40:43], v[76:79], a[164:167]
s_mov_b32 s11, 106496
v_mfma_f32_16x16x32_bf16 a[168:171], v[40:43], v[80:83], a[168:171]
buffer_load_dwordx4 v[100:103], v28, s[52:55], s11 offen offset:0 sc1
v_add_u32 v24, 32768, v24
v_mfma_f32_16x16x32_bf16 a[172:175], v[40:43], v[84:87], a[172:175]
s_and_b32 s20, s6, 7
s_waitcnt lgkmcnt(6)
v_mfma_f32_16x16x32_bf16 a[176:179], v[44:47], v[72:75], a[176:179]
ds_read_b64_tr_b16 v[60:61], v17 offset:16384
ds_read_b64_tr_b16 v[62:63], v17 offset:17408
v_mfma_f32_16x16x32_bf16 a[180:183], v[44:47], v[76:79], a[180:183]
buffer_store_dwordx4 v[200:203], v28, s[92:95], s31 offen offset:2048 sc1
v_mfma_f32_16x16x32_bf16 a[184:187], v[44:47], v[80:83], a[184:187]
s_lshr_b32 s21, s6, 3
v_mfma_f32_16x16x32_bf16 a[188:191], v[44:47], v[84:87], a[188:191]
s_and_b32 s25, s20, 3
s_waitcnt vmcnt(15)
s_barrier
s_waitcnt lgkmcnt(6)
v_mfma_f32_16x16x32_bf16 a[192:195], v[48:51], v[72:75], a[192:195]
ds_read_b64_tr_b16 v[40:41], v10 offset:32768
ds_read_b64_tr_b16 v[42:43], v10 offset:33792
s_add_u32 s28, s71, 2097152
v_mfma_f32_16x16x32_bf16 a[196:199], v[48:51], v[76:79], a[196:199]
s_add_u32 m0, s70, 0
s_nop 0
buffer_load_dwordx4 v29, s[76:79], s28 offen lds
v_mfma_f32_16x16x32_bf16 a[200:203], v[48:51], v[80:83], a[200:203]
s_add_u32 s28, s28, 31744
buffer_load_dwordx4 v29, s[76:79], s28 offen offset:1024 lds
v_mfma_f32_16x16x32_bf16 a[204:207], v[48:51], v[84:87], a[204:207]
s_lshr_b32 s22, s20, 2
s_lshl_b32 s22, s22, 3
s_waitcnt lgkmcnt(6)
v_mfma_f32_16x16x32_bf16 a[208:211], v[52:55], v[72:75], a[208:211]
ds_read_b64_tr_b16 v[44:45], v11 offset:32768
v_mfma_f32_16x16x32_bf16 a[212:215], v[52:55], v[76:79], a[212:215]
ds_read_b64_tr_b16 v[46:47], v11 offset:33792
v_mfma_f32_16x16x32_bf16 a[216:219], v[52:55], v[80:83], a[216:219]
s_lshr_b32 s23, s21, 2
v_mfma_f32_16x16x32_bf16 a[220:223], v[52:55], v[84:87], a[220:223]
s_add_u32 s26, s22, s23
s_waitcnt lgkmcnt(6)
v_mfma_f32_16x16x32_bf16 a[224:227], v[56:59], v[72:75], a[224:227]
ds_read_b64_tr_b16 v[48:49], v12 offset:32768
v_mfma_f32_16x16x32_bf16 a[228:231], v[56:59], v[76:79], a[228:231]
ds_read_b64_tr_b16 v[50:51], v12 offset:33792
v_mfma_f32_16x16x32_bf16 a[232:235], v[56:59], v[80:83], a[232:235]
s_and_b32 s27, s21, 3
v_mfma_f32_16x16x32_bf16 a[236:239], v[56:59], v[84:87], a[236:239]
v_lshrrev_b32 v26, 4, v1
s_waitcnt lgkmcnt(6)
v_mfma_f32_16x16x32_bf16 a[240:243], v[60:63], v[72:75], a[240:243]
ds_read_b64_tr_b16 v[52:53], v13 offset:32768
ds_read_b64_tr_b16 v[54:55], v13 offset:33792
v_mfma_f32_16x16x32_bf16 a[244:247], v[60:63], v[76:79], a[244:247]
buffer_store_dwordx4 v[204:207], v28, s[92:95], s31 offen offset:3072 sc1
v_mfma_f32_16x16x32_bf16 a[248:251], v[60:63], v[80:83], a[248:251]
s_lshl_b32 s24, s7, 14
v_mfma_f32_16x16x32_bf16 a[252:255], v[60:63], v[84:87], a[252:255]
s_add_u32 s23, s25, 1
s_waitcnt lgkmcnt(6)
v_mfma_f32_16x16x32_bf16 a[128:131], v[40:43], v[104:107], a[128:131]
ds_read_b64_tr_b16 v[56:57], v14 offset:32768
ds_read_b64_tr_b16 v[58:59], v14 offset:33792
v_mfma_f32_16x16x32_bf16 a[132:135], v[40:43], v[108:111], a[132:135]
buffer_load_dwordx4 v[72:75], v28, s[52:55], s8 offen offset:1024 sc1
v_mfma_f32_16x16x32_bf16 a[136:139], v[40:43], v[112:115], a[136:139]
buffer_load_dwordx4 v[76:79], v28, s[52:55], s9 offen offset:1024 sc1
s_lshl_b32 s23, s23, 2
v_mfma_f32_16x16x32_bf16 a[140:143], v[40:43], v[116:119], a[140:143]
v_add_u32 v22, 0, v26
s_waitcnt lgkmcnt(6)
v_mfma_f32_16x16x32_bf16 a[144:147], v[44:47], v[104:107], a[144:147]
ds_read_b64_tr_b16 v[60:61], v15 offset:32768
ds_read_b64_tr_b16 v[62:63], v15 offset:33792
v_mfma_f32_16x16x32_bf16 a[148:151], v[44:47], v[108:111], a[148:151]
buffer_load_dwordx4 v[80:83], v28, s[52:55], s10 offen offset:1024 sc1
v_mfma_f32_16x16x32_bf16 a[152:155], v[44:47], v[112:115], a[152:155]
v_lshlrev_b32 v22, 1, v22
v_mfma_f32_16x16x32_bf16 a[156:159], v[44:47], v[116:119], a[156:159]
v_and_b32 v23, 15, v1
s_waitcnt lgkmcnt(6)
v_mfma_f32_16x16x32_bf16 a[160:163], v[48:51], v[104:107], a[160:163]
ds_read_b64_tr_b16 v[40:41], v16 offset:32768
ds_read_b64_tr_b16 v[42:43], v16 offset:33792
v_mfma_f32_16x16x32_bf16 a[164:167], v[48:51], v[108:111], a[164:167]
buffer_load_dwordx4 v[84:87], v28, s[52:55], s11 offen offset:1024 sc1
v_mfma_f32_16x16x32_bf16 a[168:171], v[48:51], v[112:115], a[168:171]
v_xor_b32 v23, v23, v22
v_mfma_f32_16x16x32_bf16 a[172:175], v[48:51], v[116:119], a[172:175]
v_add_u32 v23, s23, v23
s_waitcnt lgkmcnt(6)
v_mfma_f32_16x16x32_bf16 a[176:179], v[52:55], v[104:107], a[176:179]
ds_read_b64_tr_b16 v[44:45], v17 offset:32768
v_mfma_f32_16x16x32_bf16 a[180:183], v[52:55], v[108:111], a[180:183]
ds_read_b64_tr_b16 v[46:47], v17 offset:33792
v_mfma_f32_16x16x32_bf16 a[184:187], v[52:55], v[112:115], a[184:187]
v_and_b32 v23, 15, v23
v_mfma_f32_16x16x32_bf16 a[188:191], v[52:55], v[116:119], a[188:191]
v_lshlrev_b32 v23, 4, v23
s_waitcnt vmcnt(13)
s_barrier
s_waitcnt lgkmcnt(6)
v_mfma_f32_16x16x32_bf16 a[192:195], v[56:59], v[104:107], a[192:195]
ds_read_b64_tr_b16 v[48:49], v10 offset:49152
ds_read_b64_tr_b16 v[50:51], v10 offset:50176
s_add_u32 s28, s71, 2359296
v_mfma_f32_16x16x32_bf16 a[196:199], v[56:59], v[108:111], a[196:199]
s_add_u32 m0, s70, 16384
s_nop 0
buffer_load_dwordx4 v29, s[76:79], s28 offen lds
s_add_u32 s28, s28, 31744
v_mfma_f32_16x16x32_bf16 a[200:203], v[56:59], v[112:115], a[200:203]
buffer_load_dwordx4 v29, s[76:79], s28 offen offset:1024 lds
s_mov_b32 s31, 528384
buffer_store_dwordx4 v[208:211], v28, s[92:95], s31 offen offset:0 sc1
v_mfma_f32_16x16x32_bf16 a[204:207], v[56:59], v[116:119], a[204:207]
v_lshlrev_b32 v31, 10, v26
v_add_u32 v23, v23, v31
s_waitcnt lgkmcnt(6)
v_mfma_f32_16x16x32_bf16 a[208:211], v[60:63], v[104:107], a[208:211]
ds_read_b64_tr_b16 v[52:53], v11 offset:49152
v_mfma_f32_16x16x32_bf16 a[212:215], v[60:63], v[108:111], a[212:215]
ds_read_b64_tr_b16 v[54:55], v11 offset:50176
v_mfma_f32_16x16x32_bf16 a[216:219], v[60:63], v[112:115], a[216:219]
v_add_u32 v23, 0, v23
v_mfma_f32_16x16x32_bf16 a[220:223], v[60:63], v[116:119], a[220:223]
v_add_u32 v32, s24, v23
s_waitcnt lgkmcnt(6)
v_mfma_f32_16x16x32_bf16 a[224:227], v[40:43], v[104:107], a[224:227]
ds_read_b64_tr_b16 v[56:57], v12 offset:49152
v_mfma_f32_16x16x32_bf16 a[228:231], v[40:43], v[108:111], a[228:231]
ds_read_b64_tr_b16 v[58:59], v12 offset:50176
v_mfma_f32_16x16x32_bf16 a[232:235], v[40:43], v[112:115], a[232:235]
v_add_u32 v22, 4, v26
v_mfma_f32_16x16x32_bf16 a[236:239], v[40:43], v[116:119], a[236:239]
v_lshlrev_b32 v22, 1, v22
s_waitcnt lgkmcnt(6)
v_mfma_f32_16x16x32_bf16 a[240:243], v[44:47], v[104:107], a[240:243]
ds_read_b64_tr_b16 v[60:61], v13 offset:49152
v_mfma_f32_16x16x32_bf16 a[244:247], v[44:47], v[108:111], a[244:247]
ds_read_b64_tr_b16 v[62:63], v13 offset:50176
v_mfma_f32_16x16x32_bf16 a[248:251], v[44:47], v[112:115], a[248:251]
v_and_b32 v23, 15, v1
v_mfma_f32_16x16x32_bf16 a[252:255], v[44:47], v[116:119], a[252:255]
v_xor_b32 v23, v23, v22
s_waitcnt lgkmcnt(6)
v_mfma_f32_16x16x32_bf16 a[128:131], v[48:51], v[64:67], a[128:131]
ds_read_b64_tr_b16 v[40:41], v14 offset:49152
ds_read_b64_tr_b16 v[42:43], v14 offset:50176
v_mfma_f32_16x16x32_bf16 a[132:135], v[48:51], v[68:71], a[132:135]
buffer_load_dwordx4 v[104:107], v28, s[52:55], s8 offen offset:2048 sc1
buffer_load_dwordx4 v[108:111], v28, s[52:55], s9 offen offset:2048 sc1
v_mfma_f32_16x16x32_bf16 a[136:139], v[48:51], v[120:123], a[136:139]
buffer_store_dwordx4 v[212:215], v28, s[92:95], s31 offen offset:1024 sc1
v_add_u32 v23, s23, v23
v_mfma_f32_16x16x32_bf16 a[140:143], v[48:51], v[124:127], a[140:143]
v_and_b32 v23, 15, v23
s_waitcnt lgkmcnt(6)
v_mfma_f32_16x16x32_bf16 a[144:147], v[52:55], v[64:67], a[144:147]
ds_read_b64_tr_b16 v[44:45], v15 offset:49152
ds_read_b64_tr_b16 v[46:47], v15 offset:50176
v_mfma_f32_16x16x32_bf16 a[148:151], v[52:55], v[68:71], a[148:151]
buffer_load_dwordx4 v[112:115], v28, s[52:55], s10 offen offset:2048 sc1
v_mfma_f32_16x16x32_bf16 a[152:155], v[52:55], v[120:123], a[152:155]
v_lshlrev_b32 v23, 4, v23
v_mfma_f32_16x16x32_bf16 a[156:159], v[52:55], v[124:127], a[156:159]
v_lshlrev_b32 v31, 10, v26
s_waitcnt lgkmcnt(6)
v_mfma_f32_16x16x32_bf16 a[160:163], v[56:59], v[64:67], a[160:163]
ds_read_b64_tr_b16 v[48:49], v16 offset:49152
ds_read_b64_tr_b16 v[50:51], v16 offset:50176
v_mfma_f32_16x16x32_bf16 a[164:167], v[56:59], v[68:71], a[164:167]
buffer_load_dwordx4 v[116:119], v28, s[52:55], s11 offen offset:2048 sc1
v_mfma_f32_16x16x32_bf16 a[168:171], v[56:59], v[120:123], a[168:171]
v_add_u32 v23, v23, v31
v_mfma_f32_16x16x32_bf16 a[172:175], v[56:59], v[124:127], a[172:175]
v_add_u32 v23, 3072, v23
s_waitcnt lgkmcnt(6)
v_mfma_f32_16x16x32_bf16 a[176:179], v[60:63], v[64:67], a[176:179]
ds_read_b64_tr_b16 v[52:53], v17 offset:49152
v_mfma_f32_16x16x32_bf16 a[180:183], v[60:63], v[68:71], a[180:183]
ds_read_b64_tr_b16 v[54:55], v17 offset:50176
v_mfma_f32_16x16x32_bf16 a[184:187], v[60:63], v[120:123], a[184:187]
v_add_u32 v33, s24, v23
v_mfma_f32_16x16x32_bf16 a[188:191], v[60:63], v[124:127], a[188:191]
v_add_u32 v22, 0, v26
s_waitcnt vmcnt(13)
s_barrier
s_waitcnt lgkmcnt(6)
v_mfma_f32_16x16x32_bf16 a[192:195], v[40:43], v[64:67], a[192:195]
ds_read_b64_tr_b16 v[56:57], v10 offset:0
ds_read_b64_tr_b16 v[58:59], v10 offset:1024
s_add_u32 s28, s71, 2621440
v_mfma_f32_16x16x32_bf16 a[196:199], v[40:43], v[68:71], a[196:199]
s_add_u32 m0, s70, 32768
s_nop 0
buffer_load_dwordx4 v29, s[76:79], s28 offen lds
v_mfma_f32_16x16x32_bf16 a[200:203], v[40:43], v[120:123], a[200:203]
s_add_u32 s28, s28, 31744
buffer_load_dwordx4 v29, s[76:79], s28 offen offset:1024 lds
buffer_store_dwordx4 v[216:219], v28, s[92:95], s31 offen offset:2048 sc1
v_mfma_f32_16x16x32_bf16 a[204:207], v[40:43], v[124:127], a[204:207]
v_lshlrev_b32 v22, 1, v22
v_and_b32 v23, 15, v1
s_waitcnt lgkmcnt(6)
v_mfma_f32_16x16x32_bf16 a[208:211], v[44:47], v[64:67], a[208:211]
ds_read_b64_tr_b16 v[60:61], v11 offset:0
v_mfma_f32_16x16x32_bf16 a[212:215], v[44:47], v[68:71], a[212:215]
ds_read_b64_tr_b16 v[62:63], v11 offset:1024
v_mfma_f32_16x16x32_bf16 a[216:219], v[44:47], v[120:123], a[216:219]
v_xor_b32 v23, v23, v22
v_mfma_f32_16x16x32_bf16 a[220:223], v[44:47], v[124:127], a[220:223]
v_add_u32 v23, s23, v23
s_waitcnt lgkmcnt(6)
v_mfma_f32_16x16x32_bf16 a[224:227], v[48:51], v[64:67], a[224:227]
ds_read_b64_tr_b16 v[40:41], v12 offset:0
v_mfma_f32_16x16x32_bf16 a[228:231], v[48:51], v[68:71], a[228:231]
ds_read_b64_tr_b16 v[42:43], v12 offset:1024
v_mfma_f32_16x16x32_bf16 a[232:235], v[48:51], v[120:123], a[232:235]
v_and_b32 v23, 15, v23
v_mfma_f32_16x16x32_bf16 a[236:239], v[48:51], v[124:127], a[236:239]
v_lshlrev_b32 v23, 4, v23
s_waitcnt lgkmcnt(6)
v_mfma_f32_16x16x32_bf16 a[240:243], v[52:55], v[64:67], a[240:243]
ds_read_b64_tr_b16 v[44:45], v13 offset:0
v_mfma_f32_16x16x32_bf16 a[244:247], v[52:55], v[68:71], a[244:247]
ds_read_b64_tr_b16 v[46:47], v13 offset:1024
v_mfma_f32_16x16x32_bf16 a[248:251], v[52:55], v[120:123], a[248:251]
v_lshlrev_b32 v31, 10, v26
v_mfma_f32_16x16x32_bf16 a[252:255], v[52:55], v[124:127], a[252:255]
v_add_u32 v23, v23, v31
s_waitcnt lgkmcnt(6)
v_mfma_f32_16x16x32_bf16 a[128:131], v[56:59], v[88:91], a[128:131]
ds_read_b64_tr_b16 v[48:49], v14 offset:0
ds_read_b64_tr_b16 v[50:51], v14 offset:1024
v_mfma_f32_16x16x32_bf16 a[132:135], v[56:59], v[92:95], a[132:135]
buffer_load_dwordx4 v[64:67], v28, s[52:55], s8 offen offset:3072 sc1
v_mfma_f32_16x16x32_bf16 a[136:139], v[56:59], v[96:99], a[136:139]
buffer_load_dwordx4 v[68:71], v28, s[52:55], s9 offen offset:3072 sc1
v_add_u32 v23, 6144, v23
v_mfma_f32_16x16x32_bf16 a[140:143], v[56:59], v[100:103], a[140:143]
v_add_u32 v34, s24, v23
s_waitcnt lgkmcnt(6)
v_mfma_f32_16x16x32_bf16 a[144:147], v[60:63], v[88:91], a[144:147]
ds_read_b64_tr_b16 v[52:53], v15 offset:0
ds_read_b64_tr_b16 v[54:55], v15 offset:1024
v_mfma_f32_16x16x32_bf16 a[148:151], v[60:63], v[92:95], a[148:151]
buffer_load_dwordx4 v[120:123], v28, s[52:55], s10 offen offset:3072 sc1
v_mfma_f32_16x16x32_bf16 a[152:155], v[60:63], v[96:99], a[152:155]
buffer_store_dwordx4 v[220:223], v28, s[92:95], s31 offen offset:3072 sc1
v_add_u32 v22, 4, v26
v_mfma_f32_16x16x32_bf16 a[156:159], v[60:63], v[100:103], a[156:159]
v_lshlrev_b32 v22, 1, v22
s_waitcnt lgkmcnt(6)
v_mfma_f32_16x16x32_bf16 a[160:163], v[40:43], v[88:91], a[160:163]
ds_read_b64_tr_b16 v[56:57], v16 offset:0
ds_read_b64_tr_b16 v[58:59], v16 offset:1024
v_mfma_f32_16x16x32_bf16 a[164:167], v[40:43], v[92:95], a[164:167]
buffer_load_dwordx4 v[124:127], v28, s[52:55], s11 offen offset:3072 sc1
v_mfma_f32_16x16x32_bf16 a[168:171], v[40:43], v[96:99], a[168:171]
v_and_b32 v23, 15, v1
v_xor_b32 v23, v23, v22
v_mfma_f32_16x16x32_bf16 a[172:175], v[40:43], v[100:103], a[172:175]
v_add_u32 v23, s23, v23
s_waitcnt lgkmcnt(6)
v_mfma_f32_16x16x32_bf16 a[176:179], v[44:47], v[88:91], a[176:179]
ds_read_b64_tr_b16 v[60:61], v17 offset:0
v_mfma_f32_16x16x32_bf16 a[180:183], v[44:47], v[92:95], a[180:183]
ds_read_b64_tr_b16 v[62:63], v17 offset:1024
v_mfma_f32_16x16x32_bf16 a[184:187], v[44:47], v[96:99], a[184:187]
v_and_b32 v23, 15, v23
v_mfma_f32_16x16x32_bf16 a[188:191], v[44:47], v[100:103], a[188:191]
v_lshlrev_b32 v23, 4, v23
s_waitcnt vmcnt(14)
s_barrier
s_waitcnt lgkmcnt(6)
v_mfma_f32_16x16x32_bf16 a[192:195], v[48:51], v[88:91], a[192:195]
ds_read_b64_tr_b16 v[40:41], v10 offset:16384
ds_read_b64_tr_b16 v[42:43], v10 offset:17408
s_add_u32 s28, s71, 2883584
v_mfma_f32_16x16x32_bf16 a[196:199], v[48:51], v[92:95], a[196:199]
s_add_u32 m0, s70, 49152
s_nop 0
buffer_load_dwordx4 v29, s[76:79], s28 offen lds
v_mfma_f32_16x16x32_bf16 a[200:203], v[48:51], v[96:99], a[200:203]
s_add_u32 s28, s28, 31744
buffer_load_dwordx4 v29, s[76:79], s28 offen offset:1024 lds
v_mfma_f32_16x16x32_bf16 a[204:207], v[48:51], v[100:103], a[204:207]
v_lshlrev_b32 v31, 10, v26
v_add_u32 v23, v23, v31
s_waitcnt lgkmcnt(6)
v_mfma_f32_16x16x32_bf16 a[208:211], v[52:55], v[88:91], a[208:211]
ds_read_b64_tr_b16 v[44:45], v11 offset:16384
ds_read_b64_tr_b16 v[46:47], v11 offset:17408
v_mfma_f32_16x16x32_bf16 a[212:215], v[52:55], v[92:95], a[212:215]
s_mov_b32 s31, 532480
v_mfma_f32_16x16x32_bf16 a[216:219], v[52:55], v[96:99], a[216:219]
buffer_store_dwordx4 v[224:227], v28, s[92:95], s31 offen offset:0 sc1
v_add_u32 v23, 9216, v23
v_mfma_f32_16x16x32_bf16 a[220:223], v[52:55], v[100:103], a[220:223]
v_add_u32 v35, s24, v23
s_waitcnt lgkmcnt(6)
v_mfma_f32_16x16x32_bf16 a[224:227], v[56:59], v[88:91], a[224:227]
ds_read_b64_tr_b16 v[48:49], v12 offset:16384
v_mfma_f32_16x16x32_bf16 a[228:231], v[56:59], v[92:95], a[228:231]
ds_read_b64_tr_b16 v[50:51], v12 offset:17408
v_mfma_f32_16x16x32_bf16 a[232:235], v[56:59], v[96:99], a[232:235]
v_and_b32 v27, 15, v1
v_mfma_f32_16x16x32_bf16 a[236:239], v[56:59], v[100:103], a[236:239]
v_lshrrev_b32 v27, 2, v27
s_waitcnt lgkmcnt(6)
v_mfma_f32_16x16x32_bf16 a[240:243], v[60:63], v[88:91], a[240:243]
ds_read_b64_tr_b16 v[52:53], v13 offset:16384
v_mfma_f32_16x16x32_bf16 a[244:247], v[60:63], v[92:95], a[244:247]
ds_read_b64_tr_b16 v[54:55], v13 offset:17408
v_mfma_f32_16x16x32_bf16 a[248:251], v[60:63], v[96:99], a[248:251]
v_lshlrev_b32 v22, 2, v26
v_mfma_f32_16x16x32_bf16 a[252:255], v[60:63], v[100:103], a[252:255]
v_add_u32 v22, v22, v27
s_waitcnt lgkmcnt(6)
v_mfma_f32_16x16x32_bf16 a[128:131], v[40:43], v[72:75], a[128:131]
ds_read_b64_tr_b16 v[56:57], v14 offset:16384
ds_read_b64_tr_b16 v[58:59], v14 offset:17408
v_mfma_f32_16x16x32_bf16 a[132:135], v[40:43], v[76:79], a[132:135]
s_mov_b32 s8, 12288
buffer_load_dwordx4 v[88:91], v28, s[52:55], s8 offen offset:0 sc1
v_mfma_f32_16x16x32_bf16 a[136:139], v[40:43], v[80:83], a[136:139]
s_mov_b32 s9, 45056
buffer_load_dwordx4 v[92:95], v28, s[52:55], s9 offen offset:0 sc1
v_mfma_f32_16x16x32_bf16 a[140:143], v[40:43], v[84:87], a[140:143]
v_and_b32 v25, 7, v22
v_lshlrev_b32 v22, 8, v22
s_waitcnt lgkmcnt(6)
v_mfma_f32_16x16x32_bf16 a[144:147], v[44:47], v[72:75], a[144:147]
ds_read_b64_tr_b16 v[60:61], v15 offset:16384
ds_read_b64_tr_b16 v[62:63], v15 offset:17408
v_mfma_f32_16x16x32_bf16 a[148:151], v[44:47], v[76:79], a[148:151]
s_mov_b32 s10, 77824
buffer_load_dwordx4 v[96:99], v28, s[52:55], s10 offen offset:0 sc1
v_mfma_f32_16x16x32_bf16 a[152:155], v[44:47], v[80:83], a[152:155]
buffer_store_dwordx4 v[228:231], v28, s[92:95], s31 offen offset:1024 sc1
v_and_b32 v23, 3, v1
v_mfma_f32_16x16x32_bf16 a[156:159], v[44:47], v[84:87], a[156:159]
v_lshlrev_b32 v23, 3, v23
s_waitcnt lgkmcnt(6)
v_mfma_f32_16x16x32_bf16 a[160:163], v[48:51], v[72:75], a[160:163]
ds_read_b64_tr_b16 v[40:41], v16 offset:16384
ds_read_b64_tr_b16 v[42:43], v16 offset:17408
v_mfma_f32_16x16x32_bf16 a[164:167], v[48:51], v[76:79], a[164:167]
s_mov_b32 s11, 110592
v_mfma_f32_16x16x32_bf16 a[168:171], v[48:51], v[80:83], a[168:171]
buffer_load_dwordx4 v[100:103], v28, s[52:55], s11 offen offset:0 sc1
v_add_u32 v22, v22, v23
v_mfma_f32_16x16x32_bf16 a[172:175], v[48:51], v[84:87], a[172:175]
v_lshlrev_b32 v23, 5, v25
s_waitcnt lgkmcnt(6)
v_mfma_f32_16x16x32_bf16 a[176:179], v[52:55], v[72:75], a[176:179]
ds_read_b64_tr_b16 v[44:45], v17 offset:16384
v_mfma_f32_16x16x32_bf16 a[180:183], v[52:55], v[76:79], a[180:183]
ds_read_b64_tr_b16 v[46:47], v17 offset:17408
v_mfma_f32_16x16x32_bf16 a[184:187], v[52:55], v[80:83], a[184:187]
v_add_u32 v36, v22, v23
v_mfma_f32_16x16x32_bf16 a[188:191], v[52:55], v[84:87], a[188:191]
v_and_b32 v22, 7, v1
s_waitcnt vmcnt(14)
s_barrier
s_waitcnt lgkmcnt(6)
v_mfma_f32_16x16x32_bf16 a[192:195], v[56:59], v[72:75], a[192:195]
ds_read_b64_tr_b16 v[48:49], v10 offset:32768
ds_read_b64_tr_b16 v[50:51], v10 offset:33792
s_add_u32 s28, s71, 3145728
v_mfma_f32_16x16x32_bf16 a[196:199], v[56:59], v[76:79], a[196:199]
s_add_u32 m0, s70, 0
s_nop 0
buffer_load_dwordx4 v29, s[76:79], s28 offen lds
v_mfma_f32_16x16x32_bf16 a[200:203], v[56:59], v[80:83], a[200:203]
s_add_u32 s28, s28, 31744
buffer_load_dwordx4 v29, s[76:79], s28 offen offset:1024 lds
v_mfma_f32_16x16x32_bf16 a[204:207], v[56:59], v[84:87], a[204:207]
v_min_u32 v23, 6, v22
s_mul_i32 s22, s25, 7
s_waitcnt lgkmcnt(6)
v_mfma_f32_16x16x32_bf16 a[208:211], v[60:63], v[72:75], a[208:211]
ds_read_b64_tr_b16 v[52:53], v11 offset:32768
v_mfma_f32_16x16x32_bf16 a[212:215], v[60:63], v[76:79], a[212:215]
ds_read_b64_tr_b16 v[54:55], v11 offset:33792
v_mfma_f32_16x16x32_bf16 a[216:219], v[60:63], v[80:83], a[216:219]
v_add_u32 v23, s22, v23
v_mfma_f32_16x16x32_bf16 a[220:223], v[60:63], v[84:87], a[220:223]
s_sub_u32 s22, 15, s26
s_waitcnt lgkmcnt(6)
v_mfma_f32_16x16x32_bf16 a[224:227], v[40:43], v[72:75], a[224:227]
ds_read_b64_tr_b16 v[56:57], v12 offset:32768
ds_read_b64_tr_b16 v[58:59], v12 offset:33792
v_mfma_f32_16x16x32_bf16 a[228:231], v[40:43], v[76:79], a[228:231]
buffer_store_dwordx4 v[232:235], v28, s[92:95], s31 offen offset:2048 sc1
v_mfma_f32_16x16x32_bf16 a[232:235], v[40:43], v[80:83], a[232:235]
s_min_u32 s22, s22, s26
v_mfma_f32_16x16x32_bf16 a[236:239], v[40:43], v[84:87], a[236:239]
s_and_b32 s23, s22, 4
s_waitcnt lgkmcnt(6)
v_mfma_f32_16x16x32_bf16 a[240:243], v[44:47], v[72:75], a[240:243]
ds_read_b64_tr_b16 v[60:61], v13 offset:32768
v_mfma_f32_16x16x32_bf16 a[244:247], v[44:47], v[76:79], a[244:247]
ds_read_b64_tr_b16 v[62:63], v13 offset:33792
v_mfma_f32_16x16x32_bf16 a[248:251], v[44:47], v[80:83], a[248:251]
s_xor_b32 s23, s23, 4
v_mfma_f32_16x16x32_bf16 a[252:255], v[44:47], v[84:87], a[252:255]
v_add_u32 v37, s23, v23
s_waitcnt lgkmcnt(6)
v_mfma_f32_16x16x32_bf16 a[128:131], v[48:51], v[104:107], a[128:131]
ds_read_b64_tr_b16 v[40:41], v14 offset:32768
ds_read_b64_tr_b16 v[42:43], v14 offset:33792
v_mfma_f32_16x16x32_bf16 a[132:135], v[48:51], v[108:111], a[132:135]
buffer_load_dwordx4 v[72:75], v28, s[52:55], s8 offen offset:1024 sc1
v_mfma_f32_16x16x32_bf16 a[136:139], v[48:51], v[112:115], a[136:139]
buffer_load_dwordx4 v[76:79], v28, s[52:55], s9 offen offset:1024 sc1
v_mul_u32_u24 v37, 37, v37
v_mfma_f32_16x16x32_bf16 a[140:143], v[48:51], v[116:119], a[140:143]
v_lshrrev_b32 v37, 8, v37
s_waitcnt lgkmcnt(6)
v_mfma_f32_16x16x32_bf16 a[144:147], v[52:55], v[104:107], a[144:147]
ds_read_b64_tr_b16 v[44:45], v15 offset:32768
ds_read_b64_tr_b16 v[46:47], v15 offset:33792
v_mfma_f32_16x16x32_bf16 a[148:151], v[52:55], v[108:111], a[148:151]
buffer_load_dwordx4 v[80:83], v28, s[52:55], s10 offen offset:1024 sc1
v_mfma_f32_16x16x32_bf16 a[152:155], v[52:55], v[112:115], a[152:155]
v_add_u32 v23, v23, v37
v_mfma_f32_16x16x32_bf16 a[156:159], v[52:55], v[116:119], a[156:159]
v_and_b32 v37, 3, v23
s_waitcnt lgkmcnt(6)
v_mfma_f32_16x16x32_bf16 a[160:163], v[56:59], v[104:107], a[160:163]
ds_read_b64_tr_b16 v[48:49], v16 offset:32768
ds_read_b64_tr_b16 v[50:51], v16 offset:33792
v_mfma_f32_16x16x32_bf16 a[164:167], v[56:59], v[108:111], a[164:167]
buffer_load_dwordx4 v[84:87], v28, s[52:55], s11 offen offset:1024 sc1
v_mfma_f32_16x16x32_bf16 a[168:171], v[56:59], v[112:115], a[168:171]
buffer_store_dwordx4 v[236:239], v28, s[92:95], s31 offen offset:3072 sc1
v_lshlrev_b32 v37, 3, v37
v_mfma_f32_16x16x32_bf16 a[172:175], v[56:59], v[116:119], a[172:175]
v_add_u32 v37, s22, v37
s_waitcnt lgkmcnt(6)
v_mfma_f32_16x16x32_bf16 a[176:179], v[60:63], v[104:107], a[176:179]
ds_read_b64_tr_b16 v[52:53], v17 offset:32768
v_mfma_f32_16x16x32_bf16 a[180:183], v[60:63], v[108:111], a[180:183]
ds_read_b64_tr_b16 v[54:55], v17 offset:33792
v_mfma_f32_16x16x32_bf16 a[184:187], v[60:63], v[112:115], a[184:187]
v_lshlrev_b32 v37, 3, v37
v_mfma_f32_16x16x32_bf16 a[188:191], v[60:63], v[116:119], a[188:191]
v_lshrrev_b32 v23, 2, v23
s_waitcnt vmcnt(14)
s_barrier
s_waitcnt lgkmcnt(6)
v_mfma_f32_16x16x32_bf16 a[192:195], v[40:43], v[104:107], a[192:195]
ds_read_b64_tr_b16 v[56:57], v10 offset:49152
ds_read_b64_tr_b16 v[58:59], v10 offset:50176
s_add_u32 s28, s71, 3407872
v_mfma_f32_16x16x32_bf16 a[196:199], v[40:43], v[108:111], a[196:199]
s_add_u32 m0, s70, 16384
s_nop 0
buffer_load_dwordx4 v29, s[76:79], s28 offen lds
v_mfma_f32_16x16x32_bf16 a[200:203], v[40:43], v[112:115], a[200:203]
s_add_u32 s28, s28, 31744
buffer_load_dwordx4 v29, s[76:79], s28 offen offset:1024 lds
v_mfma_f32_16x16x32_bf16 a[204:207], v[40:43], v[116:119], a[204:207]
v_add_u32 v23, v23, v37
v_lshlrev_b32 v23, 6, v23
s_waitcnt lgkmcnt(6)
v_mfma_f32_16x16x32_bf16 a[208:211], v[44:47], v[104:107], a[208:211]
ds_read_b64_tr_b16 v[60:61], v11 offset:49152
v_mfma_f32_16x16x32_bf16 a[212:215], v[44:47], v[108:111], a[212:215]
ds_read_b64_tr_b16 v[62:63], v11 offset:50176
v_mfma_f32_16x16x32_bf16 a[216:219], v[44:47], v[112:115], a[216:219]
v_add_u32 v37, 49152, v23
v_mfma_f32_16x16x32_bf16 a[220:223], v[44:47], v[116:119], a[220:223]
s_lshl_b32 s23, s25, 3
s_waitcnt lgkmcnt(6)
v_mfma_f32_16x16x32_bf16 a[224:227], v[48:51], v[104:107], a[224:227]
ds_read_b64_tr_b16 v[40:41], v12 offset:49152
)ASM"
R"ASM(ds_read_b64_tr_b16 v[42:43], v12 offset:50176
v_mfma_f32_16x16x32_bf16 a[228:231], v[48:51], v[108:111], a[228:231]
s_mov_b32 s31, 536576
v_mfma_f32_16x16x32_bf16 a[232:235], v[48:51], v[112:115], a[232:235]
buffer_store_dwordx4 v[240:243], v28, s[92:95], s31 offen offset:0 sc1
v_add_u32 v22, s23, v22
v_mfma_f32_16x16x32_bf16 a[236:239], v[48:51], v[116:119], a[236:239]
v_and_b32 v23, 3, v22
s_waitcnt lgkmcnt(6)
v_mfma_f32_16x16x32_bf16 a[240:243], v[52:55], v[104:107], a[240:243]
ds_read_b64_tr_b16 v[44:45], v13 offset:49152
v_mfma_f32_16x16x32_bf16 a[244:247], v[52:55], v[108:111], a[244:247]
ds_read_b64_tr_b16 v[46:47], v13 offset:50176
v_mfma_f32_16x16x32_bf16 a[248:251], v[52:55], v[112:115], a[248:251]
v_lshlrev_b32 v23, 3, v23
v_mfma_f32_16x16x32_bf16 a[252:255], v[52:55], v[116:119], a[252:255]
v_add_u32 v23, s22, v23
s_waitcnt lgkmcnt(6)
v_mfma_f32_16x16x32_bf16 a[128:131], v[56:59], v[64:67], a[128:131]
ds_read_b64_tr_b16 v[48:49], v14 offset:49152
ds_read_b64_tr_b16 v[50:51], v14 offset:50176
v_mfma_f32_16x16x32_bf16 a[132:135], v[56:59], v[68:71], a[132:135]
buffer_load_dwordx4 v[104:107], v28, s[52:55], s8 offen offset:2048 sc1
v_mfma_f32_16x16x32_bf16 a[136:139], v[56:59], v[120:123], a[136:139]
buffer_load_dwordx4 v[108:111], v28, s[52:55], s9 offen offset:2048 sc1
v_lshlrev_b32 v23, 3, v23
v_mfma_f32_16x16x32_bf16 a[140:143], v[56:59], v[124:127], a[140:143]
v_lshrrev_b32 v22, 2, v22
s_waitcnt lgkmcnt(6)
v_mfma_f32_16x16x32_bf16 a[144:147], v[60:63], v[64:67], a[144:147]
ds_read_b64_tr_b16 v[52:53], v15 offset:49152
ds_read_b64_tr_b16 v[54:55], v15 offset:50176
v_mfma_f32_16x16x32_bf16 a[148:151], v[60:63], v[68:71], a[148:151]
buffer_load_dwordx4 v[112:115], v28, s[52:55], s10 offen offset:2048 sc1
v_mfma_f32_16x16x32_bf16 a[152:155], v[60:63], v[120:123], a[152:155]
v_add_u32 v23, v23, v22
v_mfma_f32_16x16x32_bf16 a[156:159], v[60:63], v[124:127], a[156:159]
v_lshlrev_b32 v23, 6, v23
s_waitcnt lgkmcnt(6)
v_mfma_f32_16x16x32_bf16 a[160:163], v[40:43], v[64:67], a[160:163]
ds_read_b64_tr_b16 v[56:57], v16 offset:49152
ds_read_b64_tr_b16 v[58:59], v16 offset:50176
v_mfma_f32_16x16x32_bf16 a[164:167], v[40:43], v[68:71], a[164:167]
buffer_load_dwordx4 v[116:119], v28, s[52:55], s11 offen offset:2048 sc1
v_mfma_f32_16x16x32_bf16 a[168:171], v[40:43], v[120:123], a[168:171]
v_add_u32 v38, 65536, v23
v_mfma_f32_16x16x32_bf16 a[172:175], v[40:43], v[124:127], a[172:175]
v_and_b32 v22, 3, v1
s_waitcnt lgkmcnt(6)
v_mfma_f32_16x16x32_bf16 a[176:179], v[44:47], v[64:67], a[176:179]
ds_read_b64_tr_b16 v[60:61], v17 offset:49152
ds_read_b64_tr_b16 v[62:63], v17 offset:50176
v_mfma_f32_16x16x32_bf16 a[180:183], v[44:47], v[68:71], a[180:183]
buffer_store_dwordx4 v[244:247], v28, s[92:95], s31 offen offset:1024 sc1
v_mfma_f32_16x16x32_bf16 a[184:187], v[44:47], v[120:123], a[184:187]
s_and_b32 s22, s6, 0xfffffffc
v_mfma_f32_16x16x32_bf16 a[188:191], v[44:47], v[124:127], a[188:191]
v_or_b32 v22, s22, v22
s_waitcnt vmcnt(14)
s_barrier
s_waitcnt lgkmcnt(6)
v_mfma_f32_16x16x32_bf16 a[192:195], v[48:51], v[64:67], a[192:195]
ds_read_b64_tr_b16 v[40:41], v10 offset:0
ds_read_b64_tr_b16 v[42:43], v10 offset:1024
s_add_u32 s28, s71, 3670016
v_mfma_f32_16x16x32_bf16 a[196:199], v[48:51], v[68:71], a[196:199]
s_add_u32 m0, s70, 32768
s_nop 0
buffer_load_dwordx4 v29, s[76:79], s28 offen lds
v_mfma_f32_16x16x32_bf16 a[200:203], v[48:51], v[120:123], a[200:203]
s_add_u32 s28, s28, 31744
buffer_load_dwordx4 v29, s[76:79], s28 offen offset:1024 lds
v_mfma_f32_16x16x32_bf16 a[204:207], v[48:51], v[124:127], a[204:207]
v_lshlrev_b32 v22, 6, v22
v_add_u32 v22, 49152, v22
s_waitcnt lgkmcnt(6)
v_mfma_f32_16x16x32_bf16 a[208:211], v[52:55], v[64:67], a[208:211]
ds_read_b64_tr_b16 v[44:45], v11 offset:0
ds_read_b64_tr_b16 v[46:47], v11 offset:1024
v_mfma_f32_16x16x32_bf16 a[212:215], v[52:55], v[68:71], a[212:215]
v_cmp_gt_u32 vcc, 8, v1
v_mfma_f32_16x16x32_bf16 a[216:219], v[52:55], v[120:123], a[216:219]
s_nop 0
v_cndmask_b32 v38, v22, v38
v_mfma_f32_16x16x32_bf16 a[220:223], v[52:55], v[124:127], a[220:223]
v_lshrrev_b32 v22, 2, v1
s_waitcnt lgkmcnt(6)
v_mfma_f32_16x16x32_bf16 a[224:227], v[56:59], v[64:67], a[224:227]
ds_read_b64_tr_b16 v[48:49], v12 offset:0
v_mfma_f32_16x16x32_bf16 a[228:231], v[56:59], v[68:71], a[228:231]
ds_read_b64_tr_b16 v[50:51], v12 offset:1024
v_mfma_f32_16x16x32_bf16 a[232:235], v[56:59], v[120:123], a[232:235]
v_lshlrev_b32 v22, 10, v22
v_mfma_f32_16x16x32_bf16 a[236:239], v[56:59], v[124:127], a[236:239]
v_and_b32 v23, 3, v1
s_waitcnt lgkmcnt(6)
v_mfma_f32_16x16x32_bf16 a[240:243], v[60:63], v[64:67], a[240:243]
ds_read_b64_tr_b16 v[52:53], v13 offset:0
ds_read_b64_tr_b16 v[54:55], v13 offset:1024
v_mfma_f32_16x16x32_bf16 a[244:247], v[60:63], v[68:71], a[244:247]
buffer_store_dwordx4 v[248:251], v28, s[92:95], s31 offen offset:2048 sc1
v_mfma_f32_16x16x32_bf16 a[248:251], v[60:63], v[120:123], a[248:251]
v_lshlrev_b32 v23, 4, v23
v_mfma_f32_16x16x32_bf16 a[252:255], v[60:63], v[124:127], a[252:255]
v_add_u32 v22, v22, v23
s_waitcnt lgkmcnt(6)
v_mfma_f32_16x16x32_bf16 a[128:131], v[40:43], v[88:91], a[128:131]
ds_read_b64_tr_b16 v[56:57], v14 offset:0
ds_read_b64_tr_b16 v[58:59], v14 offset:1024
v_mfma_f32_16x16x32_bf16 a[132:135], v[40:43], v[92:95], a[132:135]
buffer_load_dwordx4 v[64:67], v28, s[52:55], s8 offen offset:3072 sc1
v_mfma_f32_16x16x32_bf16 a[136:139], v[40:43], v[96:99], a[136:139]
buffer_load_dwordx4 v[68:71], v28, s[52:55], s9 offen offset:3072 sc1
s_lshl_b32 s22, s26, 17
v_mfma_f32_16x16x32_bf16 a[140:143], v[40:43], v[100:103], a[140:143]
s_lshl_b32 s23, s7, 15
s_waitcnt lgkmcnt(6)
v_mfma_f32_16x16x32_bf16 a[144:147], v[44:47], v[88:91], a[144:147]
ds_read_b64_tr_b16 v[60:61], v15 offset:0
ds_read_b64_tr_b16 v[62:63], v15 offset:1024
v_mfma_f32_16x16x32_bf16 a[148:151], v[44:47], v[92:95], a[148:151]
buffer_load_dwordx4 v[120:123], v28, s[52:55], s10 offen offset:3072 sc1
v_mfma_f32_16x16x32_bf16 a[152:155], v[44:47], v[96:99], a[152:155]
s_add_u32 s22, s22, s23
v_mfma_f32_16x16x32_bf16 a[156:159], v[44:47], v[100:103], a[156:159]
s_lshl_b32 s23, s27, 8
s_waitcnt lgkmcnt(6)
v_mfma_f32_16x16x32_bf16 a[160:163], v[48:51], v[88:91], a[160:163]
ds_read_b64_tr_b16 v[40:41], v16 offset:0
ds_read_b64_tr_b16 v[42:43], v16 offset:1024
v_mfma_f32_16x16x32_bf16 a[164:167], v[48:51], v[92:95], a[164:167]
buffer_load_dwordx4 v[124:127], v28, s[52:55], s11 offen offset:3072 sc1
v_mfma_f32_16x16x32_bf16 a[168:171], v[48:51], v[96:99], a[168:171]
s_add_u32 s22, s22, s23
v_mfma_f32_16x16x32_bf16 a[172:175], v[48:51], v[100:103], a[172:175]
s_lshl_b32 s23, s25, 6
s_waitcnt lgkmcnt(6)
v_mfma_f32_16x16x32_bf16 a[176:179], v[52:55], v[88:91], a[176:179]
ds_read_b64_tr_b16 v[44:45], v17 offset:0
ds_read_b64_tr_b16 v[46:47], v17 offset:1024
v_mfma_f32_16x16x32_bf16 a[180:183], v[52:55], v[92:95], a[180:183]
buffer_store_dwordx4 v[252:255], v28, s[92:95], s31 offen offset:3072 sc1
v_mfma_f32_16x16x32_bf16 a[184:187], v[52:55], v[96:99], a[184:187]
s_add_u32 s22, s22, s23
v_mfma_f32_16x16x32_bf16 a[188:191], v[52:55], v[100:103], a[188:191]
v_add_u32 v39, s22, v22
s_waitcnt vmcnt(14)
s_barrier
s_waitcnt lgkmcnt(6)
v_mfma_f32_16x16x32_bf16 a[192:195], v[56:59], v[88:91], a[192:195]
ds_read_b64_tr_b16 v[48:49], v10 offset:16384
ds_read_b64_tr_b16 v[50:51], v10 offset:17408
v_mfma_f32_16x16x32_bf16 a[196:199], v[56:59], v[92:95], a[196:199]
s_add_u32 s28, s71, 3932160
s_add_u32 m0, s70, 49152
v_mfma_f32_16x16x32_bf16 a[200:203], v[56:59], v[96:99], a[200:203]
buffer_load_dwordx4 v29, s[76:79], s28 offen lds
s_add_u32 s28, s28, 31744
v_mfma_f32_16x16x32_bf16 a[204:207], v[56:59], v[100:103], a[204:207]
buffer_load_dwordx4 v29, s[76:79], s28 offen offset:1024 lds
s_waitcnt lgkmcnt(6)
v_mfma_f32_16x16x32_bf16 a[208:211], v[60:63], v[88:91], a[208:211]
ds_read_b64_tr_b16 v[52:53], v11 offset:16384
v_mfma_f32_16x16x32_bf16 a[212:215], v[60:63], v[92:95], a[212:215]
v_mfma_f32_16x16x32_bf16 a[216:219], v[60:63], v[96:99], a[216:219]
ds_read_b64_tr_b16 v[54:55], v11 offset:17408
v_mfma_f32_16x16x32_bf16 a[220:223], v[60:63], v[100:103], a[220:223]
s_waitcnt lgkmcnt(6)
v_mfma_f32_16x16x32_bf16 a[224:227], v[40:43], v[88:91], a[224:227]
ds_read_b64_tr_b16 v[56:57], v12 offset:16384
v_mfma_f32_16x16x32_bf16 a[228:231], v[40:43], v[92:95], a[228:231]
v_mfma_f32_16x16x32_bf16 a[232:235], v[40:43], v[96:99], a[232:235]
ds_read_b64_tr_b16 v[58:59], v12 offset:17408
v_mfma_f32_16x16x32_bf16 a[236:239], v[40:43], v[100:103], a[236:239]
s_waitcnt lgkmcnt(6)
v_mfma_f32_16x16x32_bf16 a[240:243], v[44:47], v[88:91], a[240:243]
ds_read_b64_tr_b16 v[60:61], v13 offset:16384
v_mfma_f32_16x16x32_bf16 a[244:247], v[44:47], v[92:95], a[244:247]
v_mfma_f32_16x16x32_bf16 a[248:251], v[44:47], v[96:99], a[248:251]
ds_read_b64_tr_b16 v[62:63], v13 offset:17408
v_mfma_f32_16x16x32_bf16 a[252:255], v[44:47], v[100:103], a[252:255]
s_waitcnt lgkmcnt(6)
v_mfma_f32_16x16x32_bf16 a[128:131], v[48:51], v[72:75], a[128:131]
ds_read_b64_tr_b16 v[40:41], v14 offset:16384
v_mfma_f32_16x16x32_bf16 a[132:135], v[48:51], v[76:79], a[132:135]
ds_read_b64_tr_b16 v[42:43], v14 offset:17408
v_mfma_f32_16x16x32_bf16 a[136:139], v[48:51], v[80:83], a[136:139]
s_and_b32 s20, s6, 7
v_mfma_f32_16x16x32_bf16 a[140:143], v[48:51], v[84:87], a[140:143]
s_lshr_b32 s21, s6, 3
s_waitcnt lgkmcnt(6)
v_mfma_f32_16x16x32_bf16 a[144:147], v[52:55], v[72:75], a[144:147]
ds_read_b64_tr_b16 v[44:45], v15 offset:16384
v_mfma_f32_16x16x32_bf16 a[148:151], v[52:55], v[76:79], a[148:151]
ds_read_b64_tr_b16 v[46:47], v15 offset:17408
v_mfma_f32_16x16x32_bf16 a[152:155], v[52:55], v[80:83], a[152:155]
s_and_b32 s25, s20, 3
v_mfma_f32_16x16x32_bf16 a[156:159], v[52:55], v[84:87], a[156:159]
s_lshr_b32 s22, s20, 2
s_waitcnt lgkmcnt(6)
v_mfma_f32_16x16x32_bf16 a[160:163], v[56:59], v[72:75], a[160:163]
ds_read_b64_tr_b16 v[48:49], v16 offset:16384
v_mfma_f32_16x16x32_bf16 a[164:167], v[56:59], v[76:79], a[164:167]
ds_read_b64_tr_b16 v[50:51], v16 offset:17408
v_mfma_f32_16x16x32_bf16 a[168:171], v[56:59], v[80:83], a[168:171]
s_lshl_b32 s22, s22, 3
v_mfma_f32_16x16x32_bf16 a[172:175], v[56:59], v[84:87], a[172:175]
s_lshr_b32 s23, s21, 2
s_waitcnt lgkmcnt(6)
v_mfma_f32_16x16x32_bf16 a[176:179], v[60:63], v[72:75], a[176:179]
ds_read_b64_tr_b16 v[52:53], v17 offset:16384
v_mfma_f32_16x16x32_bf16 a[180:183], v[60:63], v[76:79], a[180:183]
ds_read_b64_tr_b16 v[54:55], v17 offset:17408
v_mfma_f32_16x16x32_bf16 a[184:187], v[60:63], v[80:83], a[184:187]
s_add_u32 s26, s22, s23
v_mfma_f32_16x16x32_bf16 a[188:191], v[60:63], v[84:87], a[188:191]
s_and_b32 s27, s21, 3
s_waitcnt vmcnt(8)
s_barrier
s_waitcnt lgkmcnt(6)
v_mfma_f32_16x16x32_bf16 a[192:195], v[40:43], v[72:75], a[192:195]
ds_read_b64_tr_b16 v[56:57], v10 offset:32768
v_mfma_f32_16x16x32_bf16 a[196:199], v[40:43], v[76:79], a[196:199]
ds_read_b64_tr_b16 v[58:59], v10 offset:33792
v_mfma_f32_16x16x32_bf16 a[200:203], v[40:43], v[80:83], a[200:203]
s_lshl_b32 s24, s26, 2
v_mfma_f32_16x16x32_bf16 a[204:207], v[40:43], v[84:87], a[204:207]
s_add_u32 s24, s24, s27
s_waitcnt lgkmcnt(6)
v_mfma_f32_16x16x32_bf16 a[208:211], v[44:47], v[72:75], a[208:211]
ds_read_b64_tr_b16 v[60:61], v11 offset:32768
v_mfma_f32_16x16x32_bf16 a[212:215], v[44:47], v[76:79], a[212:215]
ds_read_b64_tr_b16 v[62:63], v11 offset:33792
v_mfma_f32_16x16x32_bf16 a[216:219], v[44:47], v[80:83], a[216:219]
s_lshl_b32 s24, s24, 17
v_mfma_f32_16x16x32_bf16 a[220:223], v[44:47], v[84:87], a[220:223]
s_lshl_b32 s23, s7, 11
s_waitcnt lgkmcnt(6)
v_mfma_f32_16x16x32_bf16 a[224:227], v[48:51], v[72:75], a[224:227]
ds_read_b64_tr_b16 v[40:41], v12 offset:32768
v_mfma_f32_16x16x32_bf16 a[228:231], v[48:51], v[76:79], a[228:231]
ds_read_b64_tr_b16 v[42:43], v12 offset:33792
v_mfma_f32_16x16x32_bf16 a[232:235], v[48:51], v[80:83], a[232:235]
s_add_u32 s24, s24, s23
v_mfma_f32_16x16x32_bf16 a[236:239], v[48:51], v[84:87], a[236:239]
s_add_u32 s24, s24, 62914560
s_waitcnt lgkmcnt(6)
v_mfma_f32_16x16x32_bf16 a[240:243], v[52:55], v[72:75], a[240:243]
ds_read_b64_tr_b16 v[44:45], v13 offset:32768
ds_read_b64_tr_b16 v[46:47], v13 offset:33792
v_mfma_f32_16x16x32_bf16 a[244:247], v[52:55], v[76:79], a[244:247]
s_add_u32 s92, s48, s24
v_mfma_f32_16x16x32_bf16 a[248:251], v[52:55], v[80:83], a[248:251]
s_addc_u32 s93, s49, 0
v_mfma_f32_16x16x32_bf16 a[252:255], v[52:55], v[84:87], a[252:255]
s_lshl_b32 s22, s25, 15
s_waitcnt vmcnt(2)
s_waitcnt lgkmcnt(6)
v_mfma_f32_16x16x32_bf16 a[128:131], v[56:59], v[104:107], a[128:131]
ds_read_b64_tr_b16 v[48:49], v14 offset:32768
v_mfma_f32_16x16x32_bf16 a[132:135], v[56:59], v[108:111], a[132:135]
ds_read_b64_tr_b16 v[50:51], v14 offset:33792
v_mfma_f32_16x16x32_bf16 a[136:139], v[56:59], v[112:115], a[136:139]
v_mov_b32 v192, -1
v_mfma_f32_16x16x32_bf16 a[140:143], v[56:59], v[116:119], a[140:143]
v_mov_b32 v193, -1
s_waitcnt lgkmcnt(6)
v_mfma_f32_16x16x32_bf16 a[144:147], v[60:63], v[104:107], a[144:147]
ds_read_b64_tr_b16 v[52:53], v15 offset:32768
v_mfma_f32_16x16x32_bf16 a[148:151], v[60:63], v[108:111], a[148:151]
ds_read_b64_tr_b16 v[54:55], v15 offset:33792
v_mfma_f32_16x16x32_bf16 a[152:155], v[60:63], v[112:115], a[152:155]
v_mov_b32 v194, -1
v_mfma_f32_16x16x32_bf16 a[156:159], v[60:63], v[116:119], a[156:159]
v_mov_b32 v195, -1
s_waitcnt lgkmcnt(6)
v_mfma_f32_16x16x32_bf16 a[160:163], v[40:43], v[104:107], a[160:163]
ds_read_b64_tr_b16 v[56:57], v16 offset:32768
v_mfma_f32_16x16x32_bf16 a[164:167], v[40:43], v[108:111], a[164:167]
ds_read_b64_tr_b16 v[58:59], v16 offset:33792
v_mfma_f32_16x16x32_bf16 a[168:171], v[40:43], v[112:115], a[168:171]
s_add_u32 s23, s25, 1
v_mfma_f32_16x16x32_bf16 a[172:175], v[40:43], v[116:119], a[172:175]
s_and_b32 s23, s23, 3
s_waitcnt lgkmcnt(6)
v_mfma_f32_16x16x32_bf16 a[176:179], v[44:47], v[104:107], a[176:179]
ds_read_b64_tr_b16 v[60:61], v17 offset:32768
v_mfma_f32_16x16x32_bf16 a[180:183], v[44:47], v[108:111], a[180:183]
ds_read_b64_tr_b16 v[62:63], v17 offset:33792
v_mfma_f32_16x16x32_bf16 a[184:187], v[44:47], v[112:115], a[184:187]
s_lshl_b32 s23, s23, 13
v_mfma_f32_16x16x32_bf16 a[188:191], v[44:47], v[116:119], a[188:191]
s_add_u32 s23, s23, s22
s_waitcnt vmcnt(0)
s_barrier
s_waitcnt lgkmcnt(6)
v_mfma_f32_16x16x32_bf16 a[192:195], v[48:51], v[104:107], a[192:195]
ds_read_b64_tr_b16 v[40:41], v10 offset:49152
v_mfma_f32_16x16x32_bf16 a[196:199], v[48:51], v[108:111], a[196:199]
ds_read_b64_tr_b16 v[42:43], v10 offset:50176
v_mfma_f32_16x16x32_bf16 a[200:203], v[48:51], v[112:115], a[200:203]
v_add_u32 v196, s23, v28
v_mfma_f32_16x16x32_bf16 a[204:207], v[48:51], v[116:119], a[204:207]
global_store_dwordx4 v196, v[192:195], s[92:93] offset:0 sc1
s_waitcnt lgkmcnt(6)
v_mfma_f32_16x16x32_bf16 a[208:211], v[52:55], v[104:107], a[208:211]
ds_read_b64_tr_b16 v[44:45], v11 offset:49152
v_mfma_f32_16x16x32_bf16 a[212:215], v[52:55], v[108:111], a[212:215]
ds_read_b64_tr_b16 v[46:47], v11 offset:50176
v_mfma_f32_16x16x32_bf16 a[216:219], v[52:55], v[112:115], a[216:219]
global_store_dwordx4 v196, v[192:195], s[92:93] offset:1024 sc1
v_mfma_f32_16x16x32_bf16 a[220:223], v[52:55], v[116:119], a[220:223]
s_add_u32 s23, s25, 2
s_waitcnt lgkmcnt(6)
v_mfma_f32_16x16x32_bf16 a[224:227], v[56:59], v[104:107], a[224:227]
ds_read_b64_tr_b16 v[48:49], v12 offset:49152
v_mfma_f32_16x16x32_bf16 a[228:231], v[56:59], v[108:111], a[228:231]
ds_read_b64_tr_b16 v[50:51], v12 offset:50176
v_mfma_f32_16x16x32_bf16 a[232:235], v[56:59], v[112:115], a[232:235]
s_and_b32 s23, s23, 3
v_mfma_f32_16x16x32_bf16 a[236:239], v[56:59], v[116:119], a[236:239]
s_lshl_b32 s23, s23, 13
s_waitcnt lgkmcnt(6)
v_mfma_f32_16x16x32_bf16 a[240:243], v[60:63], v[104:107], a[240:243]
ds_read_b64_tr_b16 v[52:53], v13 offset:49152
v_mfma_f32_16x16x32_bf16 a[244:247], v[60:63], v[108:111], a[244:247]
ds_read_b64_tr_b16 v[54:55], v13 offset:50176
v_mfma_f32_16x16x32_bf16 a[248:251], v[60:63], v[112:115], a[248:251]
s_add_u32 s23, s23, s22
v_mfma_f32_16x16x32_bf16 a[252:255], v[60:63], v[116:119], a[252:255]
v_add_u32 v196, s23, v28
s_cmp_eq_u32 s7, 0
s_cbranch_scc0 L_FS_A2
s_lshl_b32 s31, s6, 6
s_add_u32 s31, s31, 32768
v_mov_b32 v197, s31
v_mov_b32 v198, s98
v_mov_b32 v199, s99
s_mov_b64 exec, 1
global_store_dwordx2 v197, v[198:199], s[48:49] sc1
s_mov_b64 exec, -1
L_FS_A2:
s_waitcnt lgkmcnt(6)
s_nop 2
v_mfma_f32_16x16x32_bf16 a[128:131], v[40:43], v[64:67], a[128:131]
ds_read_b64_tr_b16 v[56:57], v14 offset:49152
v_mfma_f32_16x16x32_bf16 a[132:135], v[40:43], v[68:71], a[132:135]
ds_read_b64_tr_b16 v[58:59], v14 offset:50176
v_mfma_f32_16x16x32_bf16 a[136:139], v[40:43], v[120:123], a[136:139]
global_store_dwordx4 v196, v[192:195], s[92:93] offset:0 sc1
v_mfma_f32_16x16x32_bf16 a[140:143], v[40:43], v[124:127], a[140:143]
global_store_dwordx4 v196, v[192:195], s[92:93] offset:1024 sc1
s_waitcnt lgkmcnt(6)
v_mfma_f32_16x16x32_bf16 a[144:147], v[44:47], v[64:67], a[144:147]
ds_read_b64_tr_b16 v[60:61], v15 offset:49152
v_mfma_f32_16x16x32_bf16 a[148:151], v[44:47], v[68:71], a[148:151]
ds_read_b64_tr_b16 v[62:63], v15 offset:50176
v_mfma_f32_16x16x32_bf16 a[152:155], v[44:47], v[120:123], a[152:155]
s_add_u32 s23, s25, 3
v_mfma_f32_16x16x32_bf16 a[156:159], v[44:47], v[124:127], a[156:159]
s_and_b32 s23, s23, 3
s_waitcnt lgkmcnt(6)
v_mfma_f32_16x16x32_bf16 a[160:163], v[48:51], v[64:67], a[160:163]
ds_read_b64_tr_b16 v[40:41], v16 offset:49152
v_mfma_f32_16x16x32_bf16 a[164:167], v[48:51], v[68:71], a[164:167]
ds_read_b64_tr_b16 v[42:43], v16 offset:50176
v_mfma_f32_16x16x32_bf16 a[168:171], v[48:51], v[120:123], a[168:171]
s_lshl_b32 s23, s23, 13
v_mfma_f32_16x16x32_bf16 a[172:175], v[48:51], v[124:127], a[172:175]
s_add_u32 s23, s23, s22
s_waitcnt lgkmcnt(6)
v_mfma_f32_16x16x32_bf16 a[176:179], v[52:55], v[64:67], a[176:179]
ds_read_b64_tr_b16 v[44:45], v17 offset:49152
v_mfma_f32_16x16x32_bf16 a[180:183], v[52:55], v[68:71], a[180:183]
ds_read_b64_tr_b16 v[46:47], v17 offset:50176
v_mfma_f32_16x16x32_bf16 a[184:187], v[52:55], v[120:123], a[184:187]
v_add_u32 v196, s23, v28
v_mfma_f32_16x16x32_bf16 a[188:191], v[52:55], v[124:127], a[188:191]
global_store_dwordx4 v196, v[192:195], s[92:93] offset:0 sc1
s_waitcnt lgkmcnt(6)
v_mfma_f32_16x16x32_bf16 a[192:195], v[56:59], v[64:67], a[192:195]
global_store_dwordx4 v196, v[192:195], s[92:93] offset:1024 sc1
v_mfma_f32_16x16x32_bf16 a[196:199], v[56:59], v[68:71], a[196:199]
v_mfma_f32_16x16x32_bf16 a[200:203], v[56:59], v[120:123], a[200:203]
v_mfma_f32_16x16x32_bf16 a[204:207], v[56:59], v[124:127], a[204:207]
s_waitcnt lgkmcnt(4)
v_mfma_f32_16x16x32_bf16 a[208:211], v[60:63], v[64:67], a[208:211]
v_mfma_f32_16x16x32_bf16 a[212:215], v[60:63], v[68:71], a[212:215]
v_mfma_f32_16x16x32_bf16 a[216:219], v[60:63], v[120:123], a[216:219]
v_mfma_f32_16x16x32_bf16 a[220:223], v[60:63], v[124:127], a[220:223]
s_waitcnt lgkmcnt(2)
v_mfma_f32_16x16x32_bf16 a[224:227], v[40:43], v[64:67], a[224:227]
v_mfma_f32_16x16x32_bf16 a[228:231], v[40:43], v[68:71], a[228:231]
v_mfma_f32_16x16x32_bf16 a[232:235], v[40:43], v[120:123], a[232:235]
v_mfma_f32_16x16x32_bf16 a[236:239], v[40:43], v[124:127], a[236:239]
s_waitcnt lgkmcnt(0)
v_mfma_f32_16x16x32_bf16 a[240:243], v[44:47], v[64:67], a[240:243]
v_mfma_f32_16x16x32_bf16 a[244:247], v[44:47], v[68:71], a[244:247]
v_mfma_f32_16x16x32_bf16 a[248:251], v[44:47], v[120:123], a[248:251]
v_mfma_f32_16x16x32_bf16 a[252:255], v[44:47], v[124:127], a[252:255]
s_waitcnt vmcnt(0) lgkmcnt(0)
s_nop 15
s_nop 3
v_mov_b32 v228, 0
v_mov_b32 v229, 0
v_mov_b32 v230, 0
v_mov_b32 v231, 0
s_nop 3
v_mov_b32 v184, 0x3e0293ee
v_mov_b32 v185, 0x3e0293ee
v_accvgpr_read_b32 v176, a128
v_accvgpr_read_b32 v177, a129
v_accvgpr_read_b32 v178, a130
v_accvgpr_read_b32 v179, a131
v_mfma_f32_16x16x32_bf16 a[0:3], v[228:231], v[228:231], 0
v_accvgpr_read_b32 v180, a144
v_accvgpr_read_b32 v181, a145
v_accvgpr_read_b32 v182, a146
v_accvgpr_read_b32 v183, a147
v_mfma_f32_16x16x32_bf16 a[4:7], v[228:231], v[228:231], 0
v_pk_mul_f32 v[176:177], v[176:177], v[184:185]
v_pk_mul_f32 v[178:179], v[178:179], v[184:185]
v_pk_mul_f32 v[180:181], v[180:181], v[184:185]
v_pk_mul_f32 v[182:183], v[182:183], v[184:185]
v_cvt_pk_bf16_f32 v64, v176, v177
v_cvt_pk_bf16_f32 v65, v178, v179
v_cvt_pk_bf16_f32 v66, v180, v181
v_cvt_pk_bf16_f32 v67, v182, v183
v_accvgpr_read_b32 v176, a160
v_accvgpr_read_b32 v177, a161
v_accvgpr_read_b32 v178, a162
v_accvgpr_read_b32 v179, a163
v_mfma_f32_16x16x32_bf16 a[8:11], v[228:231], v[228:231], 0
v_accvgpr_read_b32 v180, a176
v_accvgpr_read_b32 v181, a177
v_accvgpr_read_b32 v182, a178
v_accvgpr_read_b32 v183, a179
v_mfma_f32_16x16x32_bf16 a[12:15], v[228:231], v[228:231], 0
v_pk_mul_f32 v[176:177], v[176:177], v[184:185]
v_pk_mul_f32 v[178:179], v[178:179], v[184:185]
v_pk_mul_f32 v[180:181], v[180:181], v[184:185]
v_pk_mul_f32 v[182:183], v[182:183], v[184:185]
v_cvt_pk_bf16_f32 v68, v176, v177
v_cvt_pk_bf16_f32 v69, v178, v179
v_cvt_pk_bf16_f32 v70, v180, v181
v_cvt_pk_bf16_f32 v71, v182, v183
v_accvgpr_read_b32 v176, a192
v_accvgpr_read_b32 v177, a193
v_accvgpr_read_b32 v178, a194
v_accvgpr_read_b32 v179, a195
v_mfma_f32_16x16x32_bf16 a[16:19], v[228:231], v[228:231], 0
v_accvgpr_read_b32 v180, a208
v_accvgpr_read_b32 v181, a209
v_accvgpr_read_b32 v182, a210
v_accvgpr_read_b32 v183, a211
v_mfma_f32_16x16x32_bf16 a[20:23], v[228:231], v[228:231], 0
v_pk_mul_f32 v[176:177], v[176:177], v[184:185]
v_pk_mul_f32 v[178:179], v[178:179], v[184:185]
v_pk_mul_f32 v[180:181], v[180:181], v[184:185]
v_pk_mul_f32 v[182:183], v[182:183], v[184:185]
v_cvt_pk_bf16_f32 v72, v176, v177
v_cvt_pk_bf16_f32 v73, v178, v179
v_cvt_pk_bf16_f32 v74, v180, v181
v_cvt_pk_bf16_f32 v75, v182, v183
v_accvgpr_read_b32 v176, a224
v_accvgpr_read_b32 v177, a225
v_accvgpr_read_b32 v178, a226
v_accvgpr_read_b32 v179, a227
v_mfma_f32_16x16x32_bf16 a[24:27], v[228:231], v[228:231], 0
v_accvgpr_read_b32 v180, a240
v_accvgpr_read_b32 v181, a241
v_accvgpr_read_b32 v182, a242
v_accvgpr_read_b32 v183, a243
v_mfma_f32_16x16x32_bf16 a[28:31], v[228:231], v[228:231], 0
v_pk_mul_f32 v[176:177], v[176:177], v[184:185]
v_pk_mul_f32 v[178:179], v[178:179], v[184:185]
v_pk_mul_f32 v[180:181], v[180:181], v[184:185]
v_pk_mul_f32 v[182:183], v[182:183], v[184:185]
v_cvt_pk_bf16_f32 v76, v176, v177
v_cvt_pk_bf16_f32 v77, v178, v179
v_cvt_pk_bf16_f32 v78, v180, v181
v_cvt_pk_bf16_f32 v79, v182, v183
v_accvgpr_read_b32 v176, a132
v_accvgpr_read_b32 v177, a133
v_accvgpr_read_b32 v178, a134
v_accvgpr_read_b32 v179, a135
v_mfma_f32_16x16x32_bf16 a[32:35], v[228:231], v[228:231], 0
v_accvgpr_read_b32 v180, a148
v_accvgpr_read_b32 v181, a149
v_accvgpr_read_b32 v182, a150
v_accvgpr_read_b32 v183, a151
v_mfma_f32_16x16x32_bf16 a[36:39], v[228:231], v[228:231], 0
v_pk_mul_f32 v[176:177], v[176:177], v[184:185]
v_pk_mul_f32 v[178:179], v[178:179], v[184:185]
v_pk_mul_f32 v[180:181], v[180:181], v[184:185]
v_pk_mul_f32 v[182:183], v[182:183], v[184:185]
v_cvt_pk_bf16_f32 v80, v176, v177
v_cvt_pk_bf16_f32 v81, v178, v179
v_cvt_pk_bf16_f32 v82, v180, v181
v_cvt_pk_bf16_f32 v83, v182, v183
v_accvgpr_read_b32 v176, a164
v_accvgpr_read_b32 v177, a165
v_accvgpr_read_b32 v178, a166
v_accvgpr_read_b32 v179, a167
v_mfma_f32_16x16x32_bf16 a[40:43], v[228:231], v[228:231], 0
v_accvgpr_read_b32 v180, a180
v_accvgpr_read_b32 v181, a181
v_accvgpr_read_b32 v182, a182
v_accvgpr_read_b32 v183, a183
v_mfma_f32_16x16x32_bf16 a[44:47], v[228:231], v[228:231], 0
v_pk_mul_f32 v[176:177], v[176:177], v[184:185]
v_pk_mul_f32 v[178:179], v[178:179], v[184:185]
v_pk_mul_f32 v[180:181], v[180:181], v[184:185]
v_pk_mul_f32 v[182:183], v[182:183], v[184:185]
v_cvt_pk_bf16_f32 v84, v176, v177
v_cvt_pk_bf16_f32 v85, v178, v179
v_cvt_pk_bf16_f32 v86, v180, v181
v_cvt_pk_bf16_f32 v87, v182, v183
v_accvgpr_read_b32 v176, a196
v_accvgpr_read_b32 v177, a197
v_accvgpr_read_b32 v178, a198
v_accvgpr_read_b32 v179, a199
v_mfma_f32_16x16x32_bf16 a[48:51], v[228:231], v[228:231], 0
v_accvgpr_read_b32 v180, a212
v_accvgpr_read_b32 v181, a213
v_accvgpr_read_b32 v182, a214
v_accvgpr_read_b32 v183, a215
v_mfma_f32_16x16x32_bf16 a[52:55], v[228:231], v[228:231], 0
v_pk_mul_f32 v[176:177], v[176:177], v[184:185]
v_pk_mul_f32 v[178:179], v[178:179], v[184:185]
v_pk_mul_f32 v[180:181], v[180:181], v[184:185]
v_pk_mul_f32 v[182:183], v[182:183], v[184:185]
v_cvt_pk_bf16_f32 v88, v176, v177
v_cvt_pk_bf16_f32 v89, v178, v179
v_cvt_pk_bf16_f32 v90, v180, v181
v_cvt_pk_bf16_f32 v91, v182, v183
v_accvgpr_read_b32 v176, a228
v_accvgpr_read_b32 v177, a229
v_accvgpr_read_b32 v178, a230
v_accvgpr_read_b32 v179, a231
v_mfma_f32_16x16x32_bf16 a[56:59], v[228:231], v[228:231], 0
v_accvgpr_read_b32 v180, a244
v_accvgpr_read_b32 v181, a245
v_accvgpr_read_b32 v182, a246
v_accvgpr_read_b32 v183, a247
v_mfma_f32_16x16x32_bf16 a[60:63], v[228:231], v[228:231], 0
v_pk_mul_f32 v[176:177], v[176:177], v[184:185]
v_pk_mul_f32 v[178:179], v[178:179], v[184:185]
v_pk_mul_f32 v[180:181], v[180:181], v[184:185]
v_pk_mul_f32 v[182:183], v[182:183], v[184:185]
v_cvt_pk_bf16_f32 v92, v176, v177
v_cvt_pk_bf16_f32 v93, v178, v179
v_cvt_pk_bf16_f32 v94, v180, v181
v_cvt_pk_bf16_f32 v95, v182, v183
v_accvgpr_read_b32 v176, a136
v_accvgpr_read_b32 v177, a137
v_accvgpr_read_b32 v178, a138
v_accvgpr_read_b32 v179, a139
v_mfma_f32_16x16x32_bf16 a[64:67], v[228:231], v[228:231], 0
v_accvgpr_read_b32 v180, a152
v_accvgpr_read_b32 v181, a153
v_accvgpr_read_b32 v182, a154
v_accvgpr_read_b32 v183, a155
v_mfma_f32_16x16x32_bf16 a[68:71], v[228:231], v[228:231], 0
v_pk_mul_f32 v[176:177], v[176:177], v[184:185]
v_pk_mul_f32 v[178:179], v[178:179], v[184:185]
v_pk_mul_f32 v[180:181], v[180:181], v[184:185]
v_pk_mul_f32 v[182:183], v[182:183], v[184:185]
v_cvt_pk_bf16_f32 v96, v176, v177
v_cvt_pk_bf16_f32 v97, v178, v179
v_cvt_pk_bf16_f32 v98, v180, v181
v_cvt_pk_bf16_f32 v99, v182, v183
v_accvgpr_read_b32 v176, a168
v_accvgpr_read_b32 v177, a169
v_accvgpr_read_b32 v178, a170
v_accvgpr_read_b32 v179, a171
v_mfma_f32_16x16x32_bf16 a[72:75], v[228:231], v[228:231], 0
v_accvgpr_read_b32 v180, a184
v_accvgpr_read_b32 v181, a185
v_accvgpr_read_b32 v182, a186
v_accvgpr_read_b32 v183, a187
v_mfma_f32_16x16x32_bf16 a[76:79], v[228:231], v[228:231], 0
v_pk_mul_f32 v[176:177], v[176:177], v[184:185]
v_pk_mul_f32 v[178:179], v[178:179], v[184:185]
v_pk_mul_f32 v[180:181], v[180:181], v[184:185]
v_pk_mul_f32 v[182:183], v[182:183], v[184:185]
v_cvt_pk_bf16_f32 v100, v176, v177
v_cvt_pk_bf16_f32 v101, v178, v179
v_cvt_pk_bf16_f32 v102, v180, v181
v_cvt_pk_bf16_f32 v103, v182, v183
v_accvgpr_read_b32 v176, a200
v_accvgpr_read_b32 v177, a201
v_accvgpr_read_b32 v178, a202
v_accvgpr_read_b32 v179, a203
v_mfma_f32_16x16x32_bf16 a[80:83], v[228:231], v[228:231], 0
v_accvgpr_read_b32 v180, a216
v_accvgpr_read_b32 v181, a217
v_accvgpr_read_b32 v182, a218
v_accvgpr_read_b32 v183, a219
v_mfma_f32_16x16x32_bf16 a[84:87], v[228:231], v[228:231], 0
v_pk_mul_f32 v[176:177], v[176:177], v[184:185]
v_pk_mul_f32 v[178:179], v[178:179], v[184:185]
v_pk_mul_f32 v[180:181], v[180:181], v[184:185]
v_pk_mul_f32 v[182:183], v[182:183], v[184:185]
v_cvt_pk_bf16_f32 v104, v176, v177
v_cvt_pk_bf16_f32 v105, v178, v179
v_cvt_pk_bf16_f32 v106, v180, v181
v_cvt_pk_bf16_f32 v107, v182, v183
v_accvgpr_read_b32 v176, a232
v_accvgpr_read_b32 v177, a233
v_accvgpr_read_b32 v178, a234
v_accvgpr_read_b32 v179, a235
v_mfma_f32_16x16x32_bf16 a[88:91], v[228:231], v[228:231], 0
v_accvgpr_read_b32 v180, a248
v_accvgpr_read_b32 v181, a249
v_accvgpr_read_b32 v182, a250
v_accvgpr_read_b32 v183, a251
v_mfma_f32_16x16x32_bf16 a[92:95], v[228:231], v[228:231], 0
v_pk_mul_f32 v[176:177], v[176:177], v[184:185]
v_pk_mul_f32 v[178:179], v[178:179], v[184:185]
v_pk_mul_f32 v[180:181], v[180:181], v[184:185]
v_pk_mul_f32 v[182:183], v[182:183], v[184:185]
v_cvt_pk_bf16_f32 v108, v176, v177
v_cvt_pk_bf16_f32 v109, v178, v179
v_cvt_pk_bf16_f32 v110, v180, v181
v_cvt_pk_bf16_f32 v111, v182, v183
v_accvgpr_read_b32 v176, a140
v_accvgpr_read_b32 v177, a141
v_accvgpr_read_b32 v178, a142
v_accvgpr_read_b32 v179, a143
v_mfma_f32_16x16x32_bf16 a[96:99], v[228:231], v[228:231], 0
v_accvgpr_read_b32 v180, a156
v_accvgpr_read_b32 v181, a157
v_accvgpr_read_b32 v182, a158
v_accvgpr_read_b32 v183, a159
v_mfma_f32_16x16x32_bf16 a[100:103], v[228:231], v[228:231], 0
v_pk_mul_f32 v[176:177], v[176:177], v[184:185]
v_pk_mul_f32 v[178:179], v[178:179], v[184:185]
v_pk_mul_f32 v[180:181], v[180:181], v[184:185]
v_pk_mul_f32 v[182:183], v[182:183], v[184:185]
v_cvt_pk_bf16_f32 v112, v176, v177
v_cvt_pk_bf16_f32 v113, v178, v179
v_cvt_pk_bf16_f32 v114, v180, v181
v_cvt_pk_bf16_f32 v115, v182, v183
v_accvgpr_read_b32 v176, a172
v_accvgpr_read_b32 v177, a173
v_accvgpr_read_b32 v178, a174
v_accvgpr_read_b32 v179, a175
v_mfma_f32_16x16x32_bf16 a[104:107], v[228:231], v[228:231], 0
v_accvgpr_read_b32 v180, a188
v_accvgpr_read_b32 v181, a189
v_accvgpr_read_b32 v182, a190
v_accvgpr_read_b32 v183, a191
v_mfma_f32_16x16x32_bf16 a[108:111], v[228:231], v[228:231], 0
v_pk_mul_f32 v[176:177], v[176:177], v[184:185]
v_pk_mul_f32 v[178:179], v[178:179], v[184:185]
v_pk_mul_f32 v[180:181], v[180:181], v[184:185]
v_pk_mul_f32 v[182:183], v[182:183], v[184:185]
v_cvt_pk_bf16_f32 v116, v176, v177
v_cvt_pk_bf16_f32 v117, v178, v179
v_cvt_pk_bf16_f32 v118, v180, v181
v_cvt_pk_bf16_f32 v119, v182, v183
v_accvgpr_read_b32 v176, a204
v_accvgpr_read_b32 v177, a205
v_accvgpr_read_b32 v178, a206
v_accvgpr_read_b32 v179, a207
v_mfma_f32_16x16x32_bf16 a[112:115], v[228:231], v[228:231], 0
v_accvgpr_read_b32 v180, a220
v_accvgpr_read_b32 v181, a221
v_accvgpr_read_b32 v182, a222
v_accvgpr_read_b32 v183, a223
v_mfma_f32_16x16x32_bf16 a[116:119], v[228:231], v[228:231], 0
v_pk_mul_f32 v[176:177], v[176:177], v[184:185]
v_pk_mul_f32 v[178:179], v[178:179], v[184:185]
v_pk_mul_f32 v[180:181], v[180:181], v[184:185]
v_pk_mul_f32 v[182:183], v[182:183], v[184:185]
v_cvt_pk_bf16_f32 v120, v176, v177
v_cvt_pk_bf16_f32 v121, v178, v179
v_cvt_pk_bf16_f32 v122, v180, v181
v_cvt_pk_bf16_f32 v123, v182, v183
v_accvgpr_read_b32 v176, a236
v_accvgpr_read_b32 v177, a237
v_accvgpr_read_b32 v178, a238
)ASM"
R"ASM(v_accvgpr_read_b32 v179, a239
v_mfma_f32_16x16x32_bf16 a[120:123], v[228:231], v[228:231], 0
v_accvgpr_read_b32 v180, a252
v_accvgpr_read_b32 v181, a253
v_accvgpr_read_b32 v182, a254
v_accvgpr_read_b32 v183, a255
v_mfma_f32_16x16x32_bf16 a[124:127], v[228:231], v[228:231], 0
v_pk_mul_f32 v[176:177], v[176:177], v[184:185]
v_pk_mul_f32 v[178:179], v[178:179], v[184:185]
v_pk_mul_f32 v[180:181], v[180:181], v[184:185]
v_pk_mul_f32 v[182:183], v[182:183], v[184:185]
v_cvt_pk_bf16_f32 v124, v176, v177
v_cvt_pk_bf16_f32 v125, v178, v179
v_cvt_pk_bf16_f32 v126, v180, v181
v_cvt_pk_bf16_f32 v127, v182, v183
s_waitcnt vmcnt(0) lgkmcnt(0)
s_nop 15
s_nop 3
s_branch L_XM
L_CODE_END:
)ASM"
    :: "s"(__builtin_amdgcn_kernarg_segment_ptr()), "s"(blockIdx.x), "v"(threadIdx.x)
    : "v1", "v2", "v3", "v4", "v5", "v6", "v7", "v8", "v9", "v10", "v11", "v12", "v13", "v14", "v15", "v16", "v17", "v18", "v19", "v20", "v21", "v22", "v23", "v24", "v25", "v26", "v27", "v28", "v29", "v30", "v31", "v32", "v33", "v34", "v35", "v36", "v37", "v38", "v39", "v40", "v41", "v42", "v43", "v44", "v45", "v46", "v47", "v48", "v49", "v50", "v51", "v52", "v53", "v54", "v55", "v56", "v57", "v58", "v59", "v60", "v61", "v62", "v63", "v64", "v65", "v66", "v67", "v68", "v69", "v70", "v71", "v72", "v73", "v74", "v75", "v76", "v77", "v78", "v79", "v80", "v81", "v82", "v83", "v84", "v85", "v86", "v87", "v88", "v89", "v90", "v91", "v92", "v93", "v94", "v95", "v96", "v97", "v98", "v99", "v100", "v101", "v102", "v103", "v104", "v105", "v106", "v107", "v108", "v109", "v110", "v111", "v112", "v113", "v114", "v115", "v116", "v117", "v118", "v119", "v120", "v121", "v122", "v123", "v124", "v125", "v126", "v127", "v128", "v129", "v130", "v131", "v132", "v133", "v134", "v135", "v136", "v137", "v138", "v139", "v140", "v141", "v142", "v143", "v144", "v145", "v146", "v147", "v148", "v149", "v150", "v151", "v152", "v153", "v154", "v155", "v156", "v157", "v158", "v159", "v160", "v161", "v162", "v163", "v164", "v165", "v166", "v167", "v168", "v169", "v170", "v171", "v172", "v173", "v174", "v175", "v176", "v177", "v178", "v179", "v180", "v181", "v182", "v183", "v184", "v185", "v186", "v187", "v188", "v189", "v190", "v191", "v192", "v193", "v194", "v195", "v196", "v197", "v198", "v199", "v200", "v201", "v202", "v203", "v204", "v205", "v206", "v207", "v208", "v209", "v210", "v211", "v212", "v213", "v214", "v215", "v216", "v217", "v218", "v219", "v220", "v221", "v222", "v223", "v224", "v225", "v226", "v227", "v228", "v229", "v230", "v231", "v232", "v233", "v234", "v235", "v236", "v237", "v238", "v239", "v240", "v241", "v242", "v243", "v244", "v245", "v246", "v247", "v248", "v249", "v250", "v251", "v252", "v253", "v254", "v255", "a0", "a1", "a2", "a3", "a4", "a5", "a6", "a7", "a8", "a9", "a10", "a11", "a12", "a13", "a14", "a15", "a16", "a17", "a18", "a19", "a20", "a21", "a22", "a23", "a24", "a25", "a26", "a27", "a28", "a29", "a30", "a31", "a32", "a33", "a34", "a35", "a36", "a37", "a38", "a39", "a40", "a41", "a42", "a43", "a44", "a45", "a46", "a47", "a48", "a49", "a50", "a51", "a52", "a53", "a54", "a55", "a56", "a57", "a58", "a59", "a60", "a61", "a62", "a63", "a64", "a65", "a66", "a67", "a68", "a69", "a70", "a71", "a72", "a73", "a74", "a75", "a76", "a77", "a78", "a79", "a80", "a81", "a82", "a83", "a84", "a85", "a86", "a87", "a88", "a89", "a90", "a91", "a92", "a93", "a94", "a95", "a96", "a97", "a98", "a99", "a100", "a101", "a102", "a103", "a104", "a105", "a106", "a107", "a108", "a109", "a110", "a111", "a112", "a113", "a114", "a115", "a116", "a117", "a118", "a119", "a120", "a121", "a122", "a123", "a124", "a125", "a126", "a127", "a128", "a129", "a130", "a131", "a132", "a133", "a134", "a135", "a136", "a137", "a138", "a139", "a140", "a141", "a142", "a143", "a144", "a145", "a146", "a147", "a148", "a149", "a150", "a151", "a152", "a153", "a154", "a155", "a156", "a157", "a158", "a159", "a160", "a161", "a162", "a163", "a164", "a165", "a166", "a167", "a168", "a169", "a170", "a171", "a172", "a173", "a174", "a175", "a176", "a177", "a178", "a179", "a180", "a181", "a182", "a183", "a184", "a185", "a186", "a187", "a188", "a189", "a190", "a191", "a192", "a193", "a194", "a195", "a196", "a197", "a198", "a199", "a200", "a201", "a202", "a203", "a204", "a205", "a206", "a207", "a208", "a209", "a210", "a211", "a212", "a213", "a214", "a215", "a216", "a217", "a218", "a219", "a220", "a221", "a222", "a223", "a224", "a225", "a226", "a227", "a228", "a229", "a230", "a231", "a232", "a233", "a234", "a235", "a236", "a237", "a238", "a239", "a240", "a241", "a242", "a243", "a244", "a245", "a246", "a247", "a248", "a249", "a250", "a251", "a252", "a253", "a254", "a255", "s4", "s5", "s6", "s7", "s8", "s9", "s10", "s11", "s12", "s13", "s14", "s15", "s16", "s17", "s18", "s19", "s20", "s21", "s22", "s23", "s24", "s25", "s26", "s27", "s28", "s29", "s30", "s31", "s34", "s35", "s36", "s37", "s38", "s39", "s40", "s41", "s42", "s43", "s44", "s45", "s46", "s47", "s48", "s49", "s50", "s51", "s52", "s53", "s54", "s55", "s56", "s57", "s58", "s59", "s60", "s61", "s62", "s63", "s64", "s65", "s66", "s67", "s68", "s69", "s70", "s71", "s72", "s73", "s74", "s75", "s76", "s77", "s78", "s79", "s80", "s81", "s82", "s83", "s84", "s85", "s86", "s87", "s88", "s89", "s90", "s91", "s92", "s93", "s94", "s95", "s96", "s97", "s98", "s99", "vcc", "scc", "memory");
}

extern "C" void kernel_launch(void* const* d_in, const int* in_sizes, int n_in, void* d_out, int out_size, void* d_ws, size_t ws_size, hipStream_t stream) {
    const int lds = 160 * 1024;
    (void)hipFuncSetAttribute((const void*)mega_kernel, hipFuncAttributeMaxDynamicSharedMemorySize, lds);
    mega_kernel<<<256, 256, lds, stream>>>(d_in[0], d_in[1], d_in[2], d_in[3], d_in[4], d_in[5], d_in[7], d_in[8], d_in[9], d_out, d_ws);
}
```
